```python
import torch

def _softmax(x, axis=-1):
    return torch.softmax(x, dim=axis)

def _gelu(x):
    return torch.nn.functional.gelu(x, approximate='tanh')

def _silu(x):
    return torch.nn.functional.silu(x)

def _sigmoid(x):
    return torch.sigmoid(x)

def _layernorm(x, axis=-1, eps=1e-05):
    m = x.mean(dim=axis, keepdim=True)
    v = x.var(dim=axis, keepdim=True, unbiased=False)
    return (x - m) / torch.sqrt(v + eps)

def _scatter_add(tgt, idx, src, axis=0):
    return tgt.clone().index_add_(axis, idx, src)

def _segment_sum(src, seg, n):
    out = torch.zeros((n,) + src.shape[1:], dtype=src.dtype)
    return out.index_add_(0, seg, src)

def _pad(x, pw):
    flat = tuple((v for pair in reversed(pw) for v in pair))
    return torch.nn.functional.pad(x, flat)
kernel_name = 'adaLN_cond__memory_aug_recurrent_mem_tr__dense_activation_gelu__prompt'

def setup_inputs():

    _w = lambda a: (lambda t: t.to(torch.bfloat16) if t.is_floating_point() else t)(torch.as_tensor(a))

    def _A_setup():
        B, S, Dm = (1, 2048, 448)
        H, Dh = (16, 256)
        M, seg = (8, 32)
        seg = min(seg, S)
        S = S // seg * seg
        return {'x': _w((lambda *s: torch.randn(*s, dtype=torch.bfloat16) * 0.05)(B, S, Dm)), 'mem0': _w((lambda *s: torch.randn(*s, dtype=torch.bfloat16) * 0.8)(B, M, Dm)), 'Wq': _w((lambda *s: torch.randn(*s, dtype=torch.bfloat16) * 0.8)(Dm, H, Dh) * Dm ** (-0.5)), 'Wk': _w((lambda *s: torch.randn(*s, dtype=torch.bfloat16) * 0.8)(Dm, H, Dh) * Dm ** (-0.5)), 'Wv': _w((lambda *s: torch.randn(*s, dtype=torch.bfloat16) * 0.8)(Dm, H, Dh) * Dm ** (-0.5)), 'Wo': _w((lambda *s: torch.randn(*s, dtype=torch.bfloat16) * 0.8)(H, Dh, Dm) * (H * Dh) ** (-0.5)), 'seg_len': seg}
    _A_in = _A_setup()

    def _B_setup():
        B, S, D, F = (1, 2048, 448, 1792)
        return {'x': _w((lambda *s: torch.randn(*s, dtype=torch.bfloat16) * 0.05)(B, S, D)), 'w1': _w((lambda *s: torch.randn(*s, dtype=torch.bfloat16) * 0.8)(D, F) * D ** (-0.5)), 'b1': _w((lambda *s: torch.randn(*s, dtype=torch.bfloat16) * 12.8)(F) * 0.01), 'w2': _w((lambda *s: torch.randn(*s, dtype=torch.bfloat16) * 0.8)(F, D) * F ** (-0.5)), 'b2': _w((lambda *s: torch.randn(*s, dtype=torch.bfloat16) * 12.8)(D) * 0.01)}
    _B_in = _B_setup()
    return {'A_x': _A_in['x'], 'A_mem0': _A_in['mem0'], 'A_Wq': _A_in['Wq'], 'A_Wk': _A_in['Wk'], 'A_Wv': _A_in['Wv'], 'A_Wo': _A_in['Wo'], 'A_seg_len': _A_in['seg_len'], 'B_x': _B_in['x'], 'B_w1': _B_in['w1'], 'B_b1': _B_in['b1'], 'B_w2': _B_in['w2'], 'B_b2': _B_in['b2'], 'c': _w((lambda *s: torch.randn(*s, dtype=torch.bfloat16) * 0.1)(1, 448) * 8.0), 'w_mod': _w((lambda *s: torch.randn(*s, dtype=torch.bfloat16) * 0.1)(448, 6 * 448) * 448 ** (-0.5) * 8.0)}

def reference(A_x, A_mem0, A_Wq, A_Wk, A_Wv, A_Wo, A_seg_len, B_x, B_w1, B_b1, B_w2, B_b2, c, w_mod):

    def _A_ref(x, mem0, Wq, Wk, Wv, Wo, seg_len):
        B, S, Dm = x.shape
        H, Dh = (Wq.shape[1], Wq.shape[2])
        M = mem0.shape[1]
        scale = Dh ** (-0.5)
        n_seg = S // seg_len
        L = M + seg_len
        idx = torch.arange(L)
        mask = (idx[:, None] < M) | (idx[:, None] >= idx[None, :])
        mem = mem0
        outs = []
        for s in range(n_seg):
            xs = x[:, s * seg_len:(s + 1) * seg_len, :].clone()
            h = torch.concatenate([mem, xs], axis=1)
            q = torch.einsum('btd,dhe->bhte', h, Wq)
            k = torch.einsum('btd,dhe->bhte', h, Wk)
            v = torch.einsum('btd,dhe->bhte', h, Wv)
            scores = torch.einsum('bhqe,bhke->bhqk', q, k) * scale
            scores = torch.where(mask[None, None], scores, -1000000000.0)
            p = _softmax(scores, axis=-1)
            o = torch.einsum('bhqk,bhke->bhqe', p, v)
            o = torch.einsum('bhte,hed->btd', o, Wo)
            mem = o[:, :M, :].clone()
            outs.append(o[:, M:, :])
        return torch.concatenate(outs, axis=1)

    def _B_ref(x, w1, b1, w2, b2):
        h = torch.einsum('bsd,df->bsf', x, w1) + b1
        h = _gelu(h)
        return torch.einsum('bsf,fd->bsd', h, w2) + b2
    x = A_x
    mod = (_silu(c) @ w_mod).reshape(c.shape[0], 6, 1, -1)
    sa, ca, ga, sb, cb, gb = (mod[:, 0].clone(), mod[:, 1].clone(), mod[:, 2].clone(), mod[:, 3].clone(), mod[:, 4].clone(), mod[:, 5].clone())
    h = _layernorm(x, axis=-1) * (1 + ca) + sa
    x = x + ga * _A_ref(h, A_mem0, A_Wq, A_Wk, A_Wv, A_Wo, A_seg_len)
    h = _layernorm(x, axis=-1) * (1 + cb) + sb
    x = x + gb * _B_ref(h, B_w1, B_b1, B_w2, B_b2)
    return x
```

```cpp
#include <hip/hip_runtime.h>
#include <hip/hip_bf16.h>
#include <stdint.h>

typedef unsigned short u16;
__device__ __forceinline__ float bf2f(u16 v) { return __uint_as_float(((unsigned)v) << 16); }
__device__ __forceinline__ u16 f2bf(float f) { __hip_bfloat16 b = __float2bfloat16(f); return *(u16*)&b; }
__device__ __forceinline__ float rbf(float f) { return f; }

constexpr int S = 2048, D = 448, H = 16, DH = 256, M = 8, SEG = 32, NSEG = 64, F = 1792, L = 40;

__global__ void k_mod(const u16* c, const u16* wmod, float* mod) {
    int j = blockIdx.x * blockDim.x + threadIdx.x;
    if (j >= 6 * D) return;
    float acc = 0;
    for (int k = 0; k < D; ++k) {
        float cv = bf2f(c[k]);
        float s = rbf(cv / (1.f + __expf(-cv)));
        acc += s * bf2f(wmod[k * 6 * D + j]);
    }
    mod[j] = rbf(acc);
}
__global__ void k_ln(const u16* x, const float* mod, int so, int co, u16* h) {
    int row = blockIdx.x; int lane = threadIdx.x;
    float v[7]; float sum = 0;
    for (int i = 0; i < 7; ++i) { v[i] = bf2f(x[row * D + lane + 64 * i]); sum += v[i]; }
    for (int o = 32; o; o >>= 1) sum += __shfl_xor(sum, o);
    float mean = sum / D;
    float sq = 0;
    for (int i = 0; i < 7; ++i) { float d = v[i] - mean; sq += d * d; }
    for (int o = 32; o; o >>= 1) sq += __shfl_xor(sq, o);
    float var = rbf(sq / D);
    float mb = rbf(mean);
    float den = rbf(sqrtf(rbf(var + 1e-5f)));
    for (int i = 0; i < 7; ++i) {
        int c = lane + 64 * i;
        float n = rbf(rbf(v[i] - mb) / den);
        float r = rbf(rbf(n * rbf(1.f + mod[co * D + c])) + mod[so * D + c]);
        h[row * D + c] = f2bf(r);
    }
}
__global__ void k_AB(const u16* Wq, const u16* Wk, const u16* Wv, const u16* Wo, u16* WA, u16* WB) {
    int idx = blockIdx.x * blockDim.x + threadIdx.x;
    if (idx >= H * D * D) return;
    int h = idx / (D * D), r = (idx / D) % D, c = idx % D;
    float a = 0, b = 0;
    for (int f = 0; f < DH; ++f) {
        a += bf2f(Wq[(r * H + h) * DH + f]) * bf2f(Wk[(c * H + h) * DH + f]);
        b += bf2f(Wv[(r * H + h) * DH + f]) * bf2f(Wo[(h * DH + f) * D + c]);
    }
    WA[idx] = f2bf(a * 0.0625f);
    WB[idx] = f2bf(b);
}
__global__ void k_GU(const u16* in, int nrows, const u16* WA, const u16* WB, u16* G, u16* U) {
    int idx = blockIdx.x * blockDim.x + threadIdx.x;
    if (idx >= nrows * H * D) return;
    int t = idx / (H * D), h = (idx / D) % H, c = idx % D;
    float g = 0, u = 0;
    for (int e = 0; e < D; ++e) {
        float xv = bf2f(in[t * D + e]);
        g += xv * bf2f(WA[(h * D + c) * D + e]);
        u += xv * bf2f(WB[(h * D + e) * D + c]);
    }
    G[idx] = f2bf(g); U[idx] = f2bf(u);
}
__global__ void k_att(const u16* mem, const u16* hx, const u16* Gm, const u16* Gx, int seg, float* P) {
    int h = blockIdx.x, q = threadIdx.x;
    if (q >= L) return;
    const u16* qr = q < M ? mem + q * D : hx + (seg * SEG + q - M) * D;
    float s[L]; float mx = -1e30f;
    for (int k = 0; k < L; ++k) {
        const u16* g = k < M ? Gm + (k * H + h) * D : Gx + ((seg * SEG + k - M) * H + h) * D;
        float a = 0;
        for (int d = 0; d < D; ++d) a += bf2f(qr[d]) * bf2f(g[d]);
        a = rbf(a);
        bool ok = (q < M) || (q >= k);
        s[k] = ok ? a : -1e9f;
        mx = fmaxf(mx, s[k]);
    }
    float sum = 0;
    for (int k = 0; k < L; ++k) { s[k] = __expf(s[k] - mx); sum += s[k]; }
    for (int k = 0; k < L; ++k) P[(h * L + q) * L + k] = rbf(s[k] / sum);
}
__global__ void k_out(const float* P, const u16* Ym, const u16* Ux, int seg, u16* memn, u16* att) {
    int idx = blockIdx.x * blockDim.x + threadIdx.x;
    if (idx >= L * D) return;
    int q = idx / D, c = idx % D;
    float a = 0;
    for (int h = 0; h < H; ++h) for (int k = 0; k < L; ++k) {
        float z = k < M ? bf2f(Ym[(k * H + h) * D + c]) : bf2f(Ux[((seg * SEG + k - M) * H + h) * D + c]);
        a += P[(h * L + q) * L + k] * z;
    }
    if (q < M) memn[q * D + c] = f2bf(a); else att[(seg * SEG + q - M) * D + c] = f2bf(a);
}
__global__ void k_res1(const u16* x, const u16* att, const float* mod, u16* x1) {
    int idx = blockIdx.x * blockDim.x + threadIdx.x;
    if (idx >= S * D) return;
    int c = idx % D;
    x1[idx] = f2bf(bf2f(x[idx]) + rbf(mod[2 * D + c] * bf2f(att[idx])));
}
__global__ void k_ffn1(const u16* h2, const u16* w1, const u16* b1, u16* hid) {
    int idx = blockIdx.x * blockDim.x + threadIdx.x;
    if (idx >= S * F) return;
    int t = idx / F, f = idx % F;
    float a = 0;
    for (int d = 0; d < D; ++d) a += bf2f(h2[t * D + d]) * bf2f(w1[d * F + f]);
    a = rbf(rbf(a) + bf2f(b1[f]));
    float u = 0.7978845608028654f * (a + 0.044715f * a * a * a);
    float g = 0.5f * a * (1.f + tanhf(u));
    hid[idx] = f2bf(g);
}
__global__ void k_ffn2(const u16* hid, const u16* w2, const u16* b2, const u16* x1, const float* mod, u16* out) {
    int idx = blockIdx.x * blockDim.x + threadIdx.x;
    if (idx >= S * D) return;
    int t = idx / D, c = idx % D;
    float a = 0;
    for (int f = 0; f < F; ++f) a += bf2f(hid[t * F + f]) * bf2f(w2[f * D + c]);
    a = rbf(rbf(a) + bf2f(b2[c]));
    out[idx] = f2bf(bf2f(x1[idx]) + rbf(mod[5 * D + c] * a));
}

extern "C" void kernel_launch(void* const* d_in, const int* in_sizes, int n_in,
                              void* d_out, int out_size, void* d_ws, size_t ws_size,
                              hipStream_t stream) {
    const u16* x = (const u16*)d_in[0];
    const u16* mem0 = (const u16*)d_in[1];
    const u16* Wq = (const u16*)d_in[2];
    const u16* Wk = (const u16*)d_in[3];
    const u16* Wv = (const u16*)d_in[4];
    const u16* Wo = (const u16*)d_in[5];
    const u16* w1 = (const u16*)d_in[8];
    const u16* b1 = (const u16*)d_in[9];
    const u16* w2 = (const u16*)d_in[10];
    const u16* b2 = (const u16*)d_in[11];
    const u16* c = (const u16*)d_in[12];
    const u16* wmod = (const u16*)d_in[13];
    u16* out = (u16*)d_out;
    char* ws = (char*)d_ws;
    size_t off = 0;
    auto alloc = [&](size_t n) { char* p = ws + off; off += (n + 255) & ~(size_t)255; return p; };
    float* mod = (float*)alloc(6 * D * 4);
    u16* h = (u16*)alloc(S * D * 2);
    u16* WA = (u16*)alloc(H * D * D * 2);
    u16* WB = (u16*)alloc(H * D * D * 2);
    u16* Gx = (u16*)alloc((size_t)S * H * D * 2);
    u16* Ux = (u16*)alloc((size_t)S * H * D * 2);
    u16* Gm = (u16*)alloc(M * H * D * 2);
    u16* Ym = (u16*)alloc(M * H * D * 2);
    u16* memb = (u16*)alloc(2 * M * D * 2);
    float* P = (float*)alloc(H * L * L * 4);
    u16* att = (u16*)alloc(S * D * 2);
    u16* x1 = (u16*)alloc(S * D * 2);
    u16* h2 = (u16*)alloc(S * D * 2);
    u16* hid = (u16*)alloc((size_t)S * F * 2);

    k_mod<<<(6 * D + 255) / 256, 256, 0, stream>>>(c, wmod, mod);
    k_ln<<<S, 64, 0, stream>>>(x, mod, 0, 1, h);
    k_AB<<<(H * D * D + 255) / 256, 256, 0, stream>>>(Wq, Wk, Wv, Wo, WA, WB);
    k_GU<<<(S * H * D + 255) / 256, 256, 0, stream>>>(h, S, WA, WB, Gx, Ux);
    const u16* mem = mem0;
    for (int s = 0; s < NSEG; ++s) {
        u16* memn = memb + (s & 1) * M * D;
        k_GU<<<(M * H * D + 255) / 256, 256, 0, stream>>>(mem, M, WA, WB, Gm, Ym);
        k_att<<<H, 64, 0, stream>>>(mem, h, Gm, Gx, s, P);
        k_out<<<(L * D + 255) / 256, 256, 0, stream>>>(P, Ym, Ux, s, memn, att);
        mem = memn;
    }
    k_res1<<<(S * D + 255) / 256, 256, 0, stream>>>(x, att, mod, x1);
    k_ln<<<S, 64, 0, stream>>>(x1, mod, 3, 4, h2);
    k_ffn1<<<(S * F + 255) / 256, 256, 0, stream>>>(h2, w1, b1, hid);
    k_ffn2<<<(S * D + 255) / 256, 256, 0, stream>>>(hid, w2, b2, x1, mod, out);
}
```

```cpp
#include <hip/hip_runtime.h>
#include <hip/hip_bf16.h>
#include <stdint.h>

typedef unsigned short u16;
typedef unsigned v4u __attribute__((ext_vector_type(4)));
typedef unsigned v2u __attribute__((ext_vector_type(2)));
typedef float f32x4 __attribute__((ext_vector_type(4)));
typedef __bf16 bf16x8 __attribute__((ext_vector_type(8)));
typedef __bf16 bf16x2 __attribute__((ext_vector_type(2)));
typedef __amdgpu_buffer_rsrc_t rsrc_t;
#define LAS __attribute__((address_space(3)))

__device__ __forceinline__ float bf2f(u16 v) { return __uint_as_float(((unsigned)v) << 16); }
__device__ __forceinline__ float bflo(unsigned v) { return __uint_as_float(v << 16); }
__device__ __forceinline__ float bfhi(unsigned v) { return __uint_as_float(v & 0xffff0000u); }
__device__ __forceinline__ unsigned pk2(float lo, float hi) { bf16x2 v; v[0] = (__bf16)lo; v[1] = (__bf16)hi; return __builtin_bit_cast(unsigned, v); }
__device__ __forceinline__ float rbf(float f) { return bflo(pk2(f, 0.f)); }
__device__ __forceinline__ f32x4 mf32(v4u a, v4u b, f32x4 c) {
    return __builtin_amdgcn_mfma_f32_16x16x32_bf16(__builtin_bit_cast(bf16x8, a), __builtin_bit_cast(bf16x8, b), c, 0, 0, 0);
}
__device__ __forceinline__ float add_x32(float x) { unsigned u = __float_as_uint(x); auto r = __builtin_amdgcn_permlane32_swap(u, u, false, false); return __uint_as_float(r[0]) + __uint_as_float(r[1]); }
__device__ __forceinline__ float add_x16(float x) { unsigned u = __float_as_uint(x); auto r = __builtin_amdgcn_permlane16_swap(u, u, false, false); return __uint_as_float(r[0]) + __uint_as_float(r[1]); }
__device__ __forceinline__ float max_x32(float x) { unsigned u = __float_as_uint(x); auto r = __builtin_amdgcn_permlane32_swap(u, u, false, false); return fmaxf(__uint_as_float(r[0]), __uint_as_float(r[1])); }
__device__ __forceinline__ float max_x16(float x) { unsigned u = __float_as_uint(x); auto r = __builtin_amdgcn_permlane16_swap(u, u, false, false); return fmaxf(__uint_as_float(r[0]), __uint_as_float(r[1])); }
template<int CTRL> __device__ __forceinline__ float dppf(float x) { return __int_as_float(__builtin_amdgcn_update_dpp(0, __float_as_int(x), CTRL, 0xf, 0xf, true)); }
__device__ __forceinline__ float wave_sum(float x) {
    x += dppf<0xB1>(x); x += dppf<0x4E>(x); x += dppf<0x141>(x); x += dppf<0x140>(x);
    x = add_x16(x); x = add_x32(x);
    return x;
}

constexpr int S = 2048, D = 448, H = 16, DH = 256, M = 8, SEG = 32, NSEG = 64, F = 1792;

constexpr int STEP_SZ = 42 * 16384;
constexpr int SMM_OFF = 4 * 16384;
constexpr int ML_OFF = 7 * 16384;
constexpr int OX_OFF = 8 * 16384;
constexpr int PLAN_OFF = 7168;
__device__ __forceinline__ constexpr int ox_off(int ct) { return OX_OFF + ct * 16384; }
__device__ __forceinline__ constexpr int yx_off(int ct) { return OX_OFF + ct * 16384 + 4096; }
constexpr unsigned SENT = 0xffffffffu;
constexpr int NHSETS = 2;
constexpr int NREG = 22;
__device__ __forceinline__ int step_base(int s, unsigned parmask) { int q = s / 3; return q * (2 << 20) + (s - 3 * q) * STEP_SZ + (int)((parmask >> q) & 1u) * 8192; }

constexpr size_t O_FLAG = 0;
constexpr size_t FLAG_BYTES = 65536;
constexpr size_t O_MOD = 32768;
constexpr size_t O_HF = 65536;
constexpr size_t O_HN = O_HF + (size_t)S * D * 2;
constexpr size_t O_WAF = O_HN + (size_t)S * D * 2;
constexpr size_t O_WBF = O_WAF + (size_t)H * D * D * 2;
constexpr size_t O_GXF = O_WBF + (size_t)H * D * D * 2;
constexpr size_t O_UXF = O_GXF + (size_t)S * H * D * 2;
constexpr size_t O_GM = O_UXF + (size_t)S * H * D * 2;
constexpr size_t O_PX = O_GM + (size_t)NSEG * H * M * D * 2;
constexpr size_t O_PM = O_PX + (size_t)NSEG * 32 * 1024;
constexpr size_t O_X1 = O_PM + (size_t)NSEG * 16 * 1024;
constexpr size_t O_HID = O_X1 + (size_t)S * D * 2;
constexpr size_t O_FFW = O_HID + (size_t)S * F * 2;
constexpr size_t O_XB = O_FFW + (size_t)14 * 65 * 4096;
constexpr int F_ABORT = 0, F_ROT = 32, F_MOD = 64, F_PAR = 96, F_LN = 128, F_FILL = 160, F_P3 = 192;
constexpr int RDY = 6912;
constexpr int R_MOD = RDY, R_PAR = RDY + 32, R_LN = RDY + 64, R_FILL = RDY + 96, R_P3 = RDY + 128;
constexpr int R_FS = 2816;
constexpr int F_WA = 256;
constexpr int F_WB = 1280;
constexpr int F_WB2 = 1536;
constexpr int F_VOTE = 7424;
constexpr int F_TK = 7488;
constexpr int F_OV = 7808;
constexpr int F_FFW = 7840;
constexpr int F_XG = 2560;
constexpr int F_GU = 5632;

struct Ptrs {
    const u16 *x, *mem0, *Wq, *Wk, *Wv, *Wo, *w1, *b1, *w2, *b2, *c, *wmod;
    u16* out;
    char* ws;
};
#define LD16(rs, off) __builtin_amdgcn_raw_buffer_load_b128(rs, off, 0, 16)
#define LD8(rs, off) __builtin_amdgcn_raw_buffer_load_b64(rs, off, 0, 16)
#define LD4(rs, off) __builtin_amdgcn_raw_buffer_load_b32(rs, off, 0, 16)
#define ST16(v, rs, off) __builtin_amdgcn_raw_buffer_store_b128(v, rs, off, 0, 16)
#define ST8(v, rs, off) __builtin_amdgcn_raw_buffer_store_b64(v, rs, off, 0, 16)
#define ST4(v, rs, off) __builtin_amdgcn_raw_buffer_store_b32(v, rs, off, 0, 16)
__device__ __forceinline__ rsrc_t mkrs(const void* p) { return __builtin_amdgcn_make_buffer_rsrc((void*)p, 0, 0x7fffffff, 0x00020000); }
#define SB() __builtin_amdgcn_sched_barrier(0)
#define DRAIN() asm volatile("s_waitcnt vmcnt(0)" ::: "memory")

__device__ __forceinline__ unsigned poll_ge(unsigned* flags, int idx, unsigned want, int shift) {
    unsigned v = 0;
    for (int spins = 0; spins < (1 << 22); ++spins) {
        v = __builtin_amdgcn_readfirstlane(__hip_atomic_load(flags + idx, __ATOMIC_RELAXED, __HIP_MEMORY_SCOPE_AGENT));
        if ((v >> shift) >= want) return v;
        __builtin_amdgcn_s_sleep(4);
        if ((spins & 255) == 255 && __builtin_amdgcn_readfirstlane(__hip_atomic_load(flags + F_ABORT, __ATOMIC_RELAXED, __HIP_MEMORY_SCOPE_AGENT))) return v;
    }
    __hip_atomic_store(flags + F_ABORT, 1u, __ATOMIC_RELAXED, __HIP_MEMORY_SCOPE_AGENT);
    return v;
}
__device__ __forceinline__ void wg_wait(unsigned* flags, int idx, unsigned want, int w) {
    if (w == 0) poll_ge(flags, idx, want, 0);
    __syncthreads();
}
__device__ __forceinline__ void wg_signal(unsigned* flags, int idx) {
    DRAIN();
    __syncthreads();
    if (threadIdx.x == 0) __hip_atomic_fetch_add(flags + idx, 1u, __ATOMIC_RELAXED, __HIP_MEMORY_SCOPE_AGENT);
}

#define TS(slot) do { } while (0)

__device__ __forceinline__ void arrive(unsigned* flags, int cidx, unsigned target, int ridx) {
    unsigned old = __hip_atomic_fetch_add(flags + cidx, 1u, __ATOMIC_RELAXED, __HIP_MEMORY_SCOPE_AGENT);
    if (old + 1 == target) __hip_atomic_store(flags + ridx, 1u, __ATOMIC_RELAXED, __HIP_MEMORY_SCOPE_AGENT);
}
__device__ __forceinline__ void wg_arrive(unsigned* flags, int cidx, unsigned target, int ridx) {
    DRAIN();
    __syncthreads();
    if (threadIdx.x == 0) arrive(flags, cidx, target, ridx);
}
#define ASM_A2_TEXT \
"v_mov_b64 v[72:73], 0\n\t" \
"v_mov_b64 v[74:75], 0\n\t" \
"v_mov_b64 v[76:77], 0\n\t" \
"v_mov_b64 v[78:79], 0\n\t" \
"v_mov_b64 v[80:81], 0\n\t" \
"v_mov_b64 v[82:83], 0\n\t" \
"v_mov_b64 v[84:85], 0\n\t" \
"v_mov_b64 v[86:87], 0\n\t" \
"v_mov_b64 v[88:89], 0\n\t" \
"v_mov_b64 v[90:91], 0\n\t" \
"v_mov_b64 v[92:93], 0\n\t" \
"v_mov_b64 v[94:95], 0\n\t" \
"v_mov_b64 v[96:97], 0\n\t" \
"v_mov_b64 v[98:99], 0\n\t" \
"v_mov_b64 v[100:101], 0\n\t" \
"v_mov_b64 v[102:103], 0\n\t" \
"v_mov_b64 v[104:105], 0\n\t" \
"v_mov_b64 v[106:107], 0\n\t" \
"v_mov_b64 v[108:109], 0\n\t" \
"v_mov_b64 v[110:111], 0\n\t" \
"v_mov_b64 v[112:113], 0\n\t" \
"v_mov_b64 v[114:115], 0\n\t" \
"v_mov_b64 v[116:117], 0\n\t" \
"v_mov_b64 v[118:119], 0\n\t" \
"v_mov_b64 v[120:121], 0\n\t" \
"v_mov_b64 v[122:123], 0\n\t" \
"v_mov_b64 v[124:125], 0\n\t" \
"v_mov_b64 v[126:127], 0\n\t" \
"v_mov_b64 v[158:159], 0\n\t" \
"v_mov_b64 v[142:143], 0\n\t" \
"Lw%=:\n\t" \
"s_memtime s[88:89]\n\t" \
"s_waitcnt lgkmcnt(0)\n\t" \
"s_sub_u32 s90, s88, %[tgt]\n\t" \
"s_cmp_lt_i32 s90, 0\n\t" \
"s_cbranch_scc1 Lw%=\n\t" \
"s_mov_b32 s92, 0\n\t" \
"Lp%=:\n\t" \
"buffer_load_dwordx4 v[64:67], %[voff], %[rs], 0 offen offset:0 sc1\n\t" \
"buffer_load_dwordx4 v[68:71], %[voff], %[rs], 0 offen offset:1024 sc1\n\t" \
"s_waitcnt vmcnt(1)\n\t" \
"v_smfmac_f32_16x16x64_bf16 v[72:75], v[64:67], a[0:7], %[idx]\n\t" \
"v_max3_u32 v164, v64, v65, v66\n\t" \
"v_smfmac_f32_16x16x64_bf16 v[76:79], v[64:67], a[16:23], %[idx]\n\t" \
"ds_write_b128 %[lw], v[64:67]\n\t" \
"v_smfmac_f32_16x16x64_bf16 v[80:83], v[64:67], a[32:39], %[idx]\n\t" \
"s_waitcnt vmcnt(0)\n\t" \
"v_smfmac_f32_16x16x64_bf16 v[84:87], v[64:67], a[48:55], %[idx]\n\t" \
"v_max3_u32 v165, v67, v68, v69\n\t" \
"v_smfmac_f32_16x16x64_bf16 v[88:91], v[64:67], a[64:71], %[idx]\n\t" \
"v_max3_u32 v164, v164, v70, v71\n\t" \
"v_smfmac_f32_16x16x64_bf16 v[92:95], v[64:67], a[80:87], %[idx]\n\t" \
"v_max_u32 v164, v164, v165\n\t" \
"v_cmp_eq_u32 vcc, -1, v164\n\t" \
"ds_write_b128 %[lw], v[68:71] offset:1024\n\t" \
"s_cbranch_vccnz Lr%=\n\t" \
"Lok%=:\n\t" \
"buffer_load_dword v166, off, %[rs], %[spl] sc1\n\t" \
"v_smfmac_f32_16x16x64_bf16 v[72:75], v[68:71], a[8:15], %[idx]\n\t" \
"v_smfmac_f32_16x16x64_bf16 v[76:79], v[68:71], a[24:31], %[idx]\n\t" \
"v_smfmac_f32_16x16x64_bf16 v[80:83], v[68:71], a[40:47], %[idx]\n\t" \
"v_smfmac_f32_16x16x64_bf16 v[84:87], v[68:71], a[56:63], %[idx]\n\t" \
"v_smfmac_f32_16x16x64_bf16 v[88:91], v[68:71], a[72:79], %[idx]\n\t" \
"v_smfmac_f32_16x16x64_bf16 v[92:95], v[68:71], a[88:95], %[idx]\n\t" \
"v_smfmac_f32_16x16x64_bf16 v[96:99], v[64:67], a[96:103], %[idx]\n\t" \
"v_smfmac_f32_16x16x64_bf16 v[100:103], v[64:67], a[112:119], %[idx]\n\t" \
"s_waitcnt lgkmcnt(0)\n\t" \
"s_barrier\n\t" \
"ds_read_b64 v[144:145], %[lam] offset:0\n\t" \
"ds_read_b64 v[146:147], %[lam] offset:1024\n\t" \
"ds_read_b64 v[148:149], %[lam] offset:2048\n\t" \
"ds_read_b64 v[150:151], %[lam] offset:3072\n\t" \
"ds_read_b64 v[152:153], %[lam] offset:4096\n\t" \
"ds_read_b64 v[154:155], %[lam] offset:5120\n\t" \
"ds_read_b64 v[156:157], %[lam] offset:6144\n\t" \
"v_smfmac_f32_16x16x64_bf16 v[96:99], v[68:71], a[104:111], %[idx]\n\t" \
"v_cvt_pk_bf16_f32 v72, v72, v73\n\t" \
"v_smfmac_f32_16x16x64_bf16 v[100:103], v[68:71], a[120:127], %[idx]\n\t" \
"v_cvt_pk_bf16_f32 v73, v74, v75\n\t" \
"v_smfmac_f32_16x16x64_bf16 v[104:107], v[64:67], a[128:135], %[idx]\n\t" \
"v_cvt_pk_bf16_f32 v74, v76, v77\n\t" \
"v_smfmac_f32_16x16x64_bf16 v[108:111], v[64:67], a[144:151], %[idx]\n\t" \
"v_cvt_pk_bf16_f32 v75, v78, v79\n\t" \
"v_smfmac_f32_16x16x64_bf16 v[104:107], v[68:71], a[136:143], %[idx]\n\t" \
"v_cvt_pk_bf16_f32 v80, v80, v81\n\t" \
"v_mfma_f32_16x16x32_bf16 v[76:79], v[72:75], %[J], 0\n\t" \
"v_cvt_pk_bf16_f32 v81, v82, v83\n\t" \
"v_cvt_pk_bf16_f32 v82, v84, v85\n\t" \
"v_smfmac_f32_16x16x64_bf16 v[108:111], v[68:71], a[152:159], %[idx]\n\t" \
"v_cvt_pk_bf16_f32 v83, v86, v87\n\t" \
"v_smfmac_f32_16x16x64_bf16 v[112:115], v[64:67], a[160:167], %[idx]\n\t" \
"v_cvt_pk_bf16_f32 v88, v88, v89\n\t" \
"v_mfma_f32_16x16x32_bf16 v[84:87], v[80:83], %[J], 0\n\t" \
"v_cvt_pk_bf16_f32 v89, v90, v91\n\t" \
"v_cvt_pk_bf16_f32 v128, v76, v77\n\t" \
"v_smfmac_f32_16x16x64_bf16 v[116:119], v[64:67], a[176:183], %[idx]\n\t" \
"v_cvt_pk_bf16_f32 v129, v78, v79\n\t" \
"v_smfmac_f32_16x16x64_bf16 v[112:115], v[68:71], a[168:175], %[idx]\n\t" \
"v_cvt_pk_bf16_f32 v90, v92, v93\n\t" \
"v_smfmac_f32_16x16x64_bf16 v[116:119], v[68:71], a[184:191], %[idx]\n\t" \
"v_cvt_pk_bf16_f32 v91, v94, v95\n\t" \
"v_smfmac_f32_16x16x64_bf16 v[120:123], v[64:67], a[192:199], %[idx]\n\t" \
"v_cvt_pk_bf16_f32 v130, v84, v85\n\t" \
"v_mfma_f32_16x16x32_bf16 v[92:95], v[88:91], %[J], 0\n\t" \
"v_cvt_pk_bf16_f32 v131, v86, v87\n\t" \
"v_cvt_pk_bf16_f32 v96, v96, v97\n\t" \
"v_smfmac_f32_16x16x64_bf16 v[124:127], v[64:67], a[208:215], %[idx]\n\t" \
"v_cvt_pk_bf16_f32 v97, v98, v99\n\t" \
"v_smfmac_f32_16x16x64_bf16 v[120:123], v[68:71], a[200:207], %[idx]\n\t" \
"v_cvt_pk_bf16_f32 v98, v100, v101\n\t" \
"v_smfmac_f32_16x16x64_bf16 v[124:127], v[68:71], a[216:223], %[idx]\n\t" \
"v_cvt_pk_bf16_f32 v99, v102, v103\n\t" \
"v_cvt_pk_bf16_f32 v132, v92, v93\n\t" \
"v_cvt_pk_bf16_f32 v133, v94, v95\n\t" \
"v_mfma_f32_16x16x32_bf16 v[100:103], v[96:99], %[J], 0\n\t" \
"s_waitcnt lgkmcnt(0)\n\t" \
"v_cvt_pk_bf16_f32 v104, v104, v105\n\t" \
"v_mfma_f32_16x16x32_bf16 v[160:163], v[144:147], v[128:131], 0\n\t" \
"v_cvt_pk_bf16_f32 v105, v106, v107\n\t" \
"v_cvt_pk_bf16_f32 v106, v108, v109\n\t" \
"v_cvt_pk_bf16_f32 v107, v110, v111\n\t" \
"v_cvt_pk_bf16_f32 v112, v112, v113\n\t" \
"v_cvt_pk_bf16_f32 v113, v114, v115\n\t" \
"v_mfma_f32_16x16x32_bf16 v[108:111], v[104:107], %[J], 0\n\t" \
"v_cvt_pk_bf16_f32 v134, v100, v101\n\t" \
"v_cvt_pk_bf16_f32 v135, v102, v103\n\t" \
"v_cvt_pk_bf16_f32 v114, v116, v117\n\t" \
"v_cvt_pk_bf16_f32 v115, v118, v119\n\t" \
"v_mfma_f32_16x16x32_bf16 v[160:163], v[148:151], v[132:135], v[160:163]\n\t" \
"v_cvt_pk_bf16_f32 v120, v120, v121\n\t" \
"v_cvt_pk_bf16_f32 v121, v122, v123\n\t" \
"v_mfma_f32_16x16x32_bf16 v[116:119], v[112:115], %[J], 0\n\t" \
"v_cvt_pk_bf16_f32 v136, v108, v109\n\t" \
"v_cvt_pk_bf16_f32 v137, v110, v111\n\t" \
"v_cvt_pk_bf16_f32 v122, v124, v125\n\t" \
"v_cvt_pk_bf16_f32 v123, v126, v127\n\t" \
"s_nop 0\n\t" \
"s_nop 0\n\t" \
"v_mfma_f32_16x16x32_bf16 v[124:127], v[120:123], %[J], 0\n\t" \
"s_nop 0\n\t" \
"v_cvt_pk_bf16_f32 v138, v116, v117\n\t" \
"v_cvt_pk_bf16_f32 v139, v118, v119\n\t" \
"s_nop 0\n\t" \
"s_nop 0\n\t" \
"v_mfma_f32_16x16x32_bf16 v[160:163], v[152:155], v[136:139], v[160:163]\n\t" \
"s_nop 0\n\t" \
"s_nop 0\n\t" \
"v_cvt_pk_bf16_f32 v140, v124, v125\n\t" \
"v_cvt_pk_bf16_f32 v141, v126, v127\n\t" \
"s_nop 0\n\t" \
"s_nop 0\n\t" \
"v_mfma_f32_16x16x32_bf16 v[160:163], v[156:159], v[140:143], v[160:163]\n\t" \
"s_nop 7\n\t" \
"ds_write_b128 %[lred], v[160:163]\n\t" \
"s_waitcnt vmcnt(0) lgkmcnt(0)\n\t" \
"s_barrier\n\t" \
"s_memtime s[88:89]\n\t" \
"v_readfirstlane_b32 %[plan], v166\n\t" \
"ds_write_b128 %[lg], v[76:79] offset:0\n\t" \
"ds_write_b128 %[lg], v[84:87] offset:1024\n\t" \
"ds_write_b128 %[lg], v[92:95] offset:2048\n\t" \
"ds_write_b128 %[lg], v[100:103] offset:3072\n\t" \
"ds_write_b128 %[lg], v[108:111] offset:4096\n\t" \
"ds_write_b128 %[lg], v[116:119] offset:5120\n\t" \
"ds_write_b128 %[lg], v[124:127] offset:6144\n\t" \
"s_waitcnt lgkmcnt(0)\n\t" \
"s_mov_b32 %[tpub], s88\n\t" \
"s_mov_b32 %[nret], s92\n\t" \
"s_branch Le%=\n\t" \
"Lr%=:\n\t" \
"s_nop 7\n\t" \
"v_mov_b64 v[72:73], 0\n\t" \
"v_mov_b64 v[74:75], 0\n\t" \
"v_mov_b64 v[76:77], 0\n\t" \
"v_mov_b64 v[78:79], 0\n\t" \
"v_mov_b64 v[80:81], 0\n\t" \
"v_mov_b64 v[82:83], 0\n\t" \
"v_mov_b64 v[84:85], 0\n\t" \
"v_mov_b64 v[86:87], 0\n\t" \
"v_mov_b64 v[88:89], 0\n\t" \
"v_mov_b64 v[90:91], 0\n\t" \
"v_mov_b64 v[92:93], 0\n\t" \
"v_mov_b64 v[94:95], 0\n\t" \
"s_add_u32 s92, s92, 1\n\t" \
"s_cmp_lt_u32 s92, 0x40000\n\t" \
"s_cbranch_scc1 Lp%=\n\t" \
"s_branch Lok%=\n\t" \
"Le%=:\n\t"

#define ASM_A2_CLOB "v64", "v65", "v66", "v67", "v68", "v69", "v70", "v71", "v72", "v73", "v74", "v75", "v76", "v77", "v78", "v79", "v80", "v81", "v82", "v83", "v84", "v85", "v86", "v87", "v88", "v89", "v90", "v91", "v92", "v93", "v94", "v95", "v96", "v97", "v98", "v99", "v100", "v101", "v102", "v103", "v104", "v105", "v106", "v107", "v108", "v109", "v110", "v111", "v112", "v113", "v114", "v115", "v116", "v117", "v118", "v119", "v120", "v121", "v122", "v123", "v124", "v125", "v126", "v127", "v128", "v129", "v130", "v131", "v132", "v133", "v134", "v135", "v136", "v137", "v138", "v139", "v140", "v141", "v142", "v143", "v144", "v145", "v146", "v147", "v148", "v149", "v150", "v151", "v152", "v153", "v154", "v155", "v156", "v157", "v158", "v159", "v160", "v161", "v162", "v163", "v164", "v165", "v166", "s84", "s85", "s86", "s87", "s88", "s89", "s90", "s91", "s92", "s93", "vcc", "scc", "memory"

#define ASM_A1_TEXT \
"v_mov_b64 v[72:73], 0\n\t" \
"v_mov_b64 v[74:75], 0\n\t" \
"v_mov_b64 v[76:77], 0\n\t" \
"v_mov_b64 v[78:79], 0\n\t" \
"v_mov_b64 v[80:81], 0\n\t" \
"v_mov_b64 v[82:83], 0\n\t" \
"v_mov_b64 v[84:85], 0\n\t" \
"v_mov_b64 v[86:87], 0\n\t" \
"v_mov_b64 v[88:89], 0\n\t" \
"v_mov_b64 v[90:91], 0\n\t" \
"v_mov_b64 v[92:93], 0\n\t" \
"v_mov_b64 v[94:95], 0\n\t" \
"v_mov_b64 v[96:97], 0\n\t" \
"v_mov_b64 v[98:99], 0\n\t" \
"v_mov_b64 v[100:101], 0\n\t" \
"v_mov_b64 v[102:103], 0\n\t" \
"v_mov_b64 v[104:105], 0\n\t" \
"v_mov_b64 v[106:107], 0\n\t" \
"v_mov_b64 v[108:109], 0\n\t" \
"v_mov_b64 v[110:111], 0\n\t" \
"v_mov_b64 v[112:113], 0\n\t" \
"v_mov_b64 v[114:115], 0\n\t" \
"v_mov_b64 v[116:117], 0\n\t" \
"v_mov_b64 v[118:119], 0\n\t" \
"v_mov_b64 v[120:121], 0\n\t" \
"v_mov_b64 v[122:123], 0\n\t" \
"v_mov_b64 v[124:125], 0\n\t" \
"v_mov_b64 v[126:127], 0\n\t" \
"v_mov_b64 v[158:159], 0\n\t" \
"v_mov_b64 v[142:143], 0\n\t" \
"Lw%=:\n\t" \
"s_memtime s[88:89]\n\t" \
"s_waitcnt lgkmcnt(0)\n\t" \
"s_sub_u32 s90, s88, %[tgt]\n\t" \
"s_cmp_lt_i32 s90, 0\n\t" \
"s_cbranch_scc1 Lw%=\n\t" \
"s_mov_b32 s92, 0\n\t" \
"Lp%=:\n\t" \
"buffer_load_dwordx4 v[64:67], %[voff], %[rs], 0 offen offset:0 sc1\n\t" \
"s_waitcnt vmcnt(0)\n\t" \
"v_smfmac_f32_16x16x64_bf16 v[72:75], v[64:67], a[0:7], %[idx]\n\t" \
"v_max3_u32 v164, v64, v65, v66\n\t" \
"v_smfmac_f32_16x16x64_bf16 v[76:79], v[64:67], a[8:15], %[idx]\n\t" \
"v_max_u32 v164, v164, v67\n\t" \
"v_smfmac_f32_16x16x64_bf16 v[80:83], v[64:67], a[16:23], %[idx]\n\t" \
"v_cmp_eq_u32 vcc, -1, v164\n\t" \
"v_smfmac_f32_16x16x64_bf16 v[84:87], v[64:67], a[24:31], %[idx]\n\t" \
"ds_write_b128 %[lw], v[64:67]\n\t" \
"v_smfmac_f32_16x16x64_bf16 v[88:91], v[64:67], a[32:39], %[idx]\n\t" \
"v_smfmac_f32_16x16x64_bf16 v[92:95], v[64:67], a[40:47], %[idx]\n\t" \
"s_cbranch_vccnz Lr%=\n\t" \
"Lok%=:\n\t" \
"buffer_load_dword v166, off, %[rs], %[spl] sc1\n\t" \
"v_smfmac_f32_16x16x64_bf16 v[96:99], v[64:67], a[48:55], %[idx]\n\t" \
"v_cvt_pk_bf16_f32 v72, v72, v73\n\t" \
"v_smfmac_f32_16x16x64_bf16 v[100:103], v[64:67], a[56:63], %[idx]\n\t" \
"v_cvt_pk_bf16_f32 v73, v74, v75\n\t" \
"v_smfmac_f32_16x16x64_bf16 v[104:107], v[64:67], a[64:71], %[idx]\n\t" \
"v_cvt_pk_bf16_f32 v74, v76, v77\n\t" \
"v_smfmac_f32_16x16x64_bf16 v[108:111], v[64:67], a[72:79], %[idx]\n\t" \
"v_cvt_pk_bf16_f32 v75, v78, v79\n\t" \
"v_smfmac_f32_16x16x64_bf16 v[112:115], v[64:67], a[80:87], %[idx]\n\t" \
"v_cvt_pk_bf16_f32 v80, v80, v81\n\t" \
"v_mfma_f32_16x16x32_bf16 v[76:79], v[72:75], %[J], 0\n\t" \
"v_cvt_pk_bf16_f32 v81, v82, v83\n\t" \
"v_cvt_pk_bf16_f32 v82, v84, v85\n\t" \
"v_smfmac_f32_16x16x64_bf16 v[116:119], v[64:67], a[88:95], %[idx]\n\t" \
"s_waitcnt lgkmcnt(0)\n\t" \
"s_barrier\n\t" \
"ds_read_b64 v[144:145], %[lam] offset:0\n\t" \
"ds_read_b64 v[146:147], %[lam] offset:1024\n\t" \
"ds_read_b64 v[148:149], %[lam] offset:2048\n\t" \
"ds_read_b64 v[150:151], %[lam] offset:3072\n\t" \
"ds_read_b64 v[152:153], %[lam] offset:4096\n\t" \
"ds_read_b64 v[154:155], %[lam] offset:5120\n\t" \
"ds_read_b64 v[156:157], %[lam] offset:6144\n\t" \
"v_smfmac_f32_16x16x64_bf16 v[120:123], v[64:67], a[96:103], %[idx]\n\t" \
"v_cvt_pk_bf16_f32 v128, v76, v77\n\t" \
"v_smfmac_f32_16x16x64_bf16 v[124:127], v[64:67], a[104:111], %[idx]\n\t" \
"v_cvt_pk_bf16_f32 v129, v78, v79\n\t" \
"v_cvt_pk_bf16_f32 v83, v86, v87\n\t" \
"v_cvt_pk_bf16_f32 v88, v88, v89\n\t" \
"v_cvt_pk_bf16_f32 v89, v90, v91\n\t" \
"v_mfma_f32_16x16x32_bf16 v[84:87], v[80:83], %[J], 0\n\t" \
"v_cvt_pk_bf16_f32 v90, v92, v93\n\t" \
"v_cvt_pk_bf16_f32 v91, v94, v95\n\t" \
"v_cvt_pk_bf16_f32 v96, v96, v97\n\t" \
"v_cvt_pk_bf16_f32 v97, v98, v99\n\t" \
"v_mfma_f32_16x16x32_bf16 v[92:95], v[88:91], %[J], 0\n\t" \
"v_cvt_pk_bf16_f32 v98, v100, v101\n\t" \
"v_cvt_pk_bf16_f32 v99, v102, v103\n\t" \
"v_cvt_pk_bf16_f32 v104, v104, v105\n\t" \
"v_cvt_pk_bf16_f32 v130, v84, v85\n\t" \
"v_mfma_f32_16x16x32_bf16 v[100:103], v[96:99], %[J], 0\n\t" \
"v_cvt_pk_bf16_f32 v131, v86, v87\n\t" \
"v_cvt_pk_bf16_f32 v105, v106, v107\n\t" \
"v_cvt_pk_bf16_f32 v106, v108, v109\n\t" \
"v_cvt_pk_bf16_f32 v132, v92, v93\n\t" \
"v_cvt_pk_bf16_f32 v133, v94, v95\n\t" \
"v_cvt_pk_bf16_f32 v107, v110, v111\n\t" \
"v_cvt_pk_bf16_f32 v112, v112, v113\n\t" \
"v_cvt_pk_bf16_f32 v113, v114, v115\n\t" \
"v_mfma_f32_16x16x32_bf16 v[108:111], v[104:107], %[J], 0\n\t" \
"v_cvt_pk_bf16_f32 v134, v100, v101\n\t" \
"v_cvt_pk_bf16_f32 v135, v102, v103\n\t" \
"s_waitcnt lgkmcnt(0)\n\t" \
"v_mfma_f32_16x16x32_bf16 v[160:163], v[144:147], v[128:131], 0\n\t" \
"v_cvt_pk_bf16_f32 v114, v116, v117\n\t" \
"v_cvt_pk_bf16_f32 v115, v118, v119\n\t" \
"v_mfma_f32_16x16x32_bf16 v[160:163], v[148:151], v[132:135], v[160:163]\n\t" \
"v_cvt_pk_bf16_f32 v120, v120, v121\n\t" \
"v_cvt_pk_bf16_f32 v136, v108, v109\n\t" \
"v_mfma_f32_16x16x32_bf16 v[116:119], v[112:115], %[J], 0\n\t" \
"v_cvt_pk_bf16_f32 v137, v110, v111\n\t" \
"v_cvt_pk_bf16_f32 v121, v122, v123\n\t" \
"v_cvt_pk_bf16_f32 v122, v124, v125\n\t" \
"v_cvt_pk_bf16_f32 v123, v126, v127\n\t" \
"s_nop 0\n\t" \
"s_nop 0\n\t" \
"v_mfma_f32_16x16x32_bf16 v[124:127], v[120:123], %[J], 0\n\t" \
"s_nop 0\n\t" \
"v_cvt_pk_bf16_f32 v138, v116, v117\n\t" \
"v_cvt_pk_bf16_f32 v139, v118, v119\n\t" \
"s_nop 0\n\t" \
"s_nop 0\n\t" \
"v_mfma_f32_16x16x32_bf16 v[160:163], v[152:155], v[136:139], v[160:163]\n\t" \
"s_nop 0\n\t" \
"s_nop 0\n\t" \
"v_cvt_pk_bf16_f32 v140, v124, v125\n\t" \
"v_cvt_pk_bf16_f32 v141, v126, v127\n\t" \
"s_nop 0\n\t" \
"s_nop 0\n\t" \
"v_mfma_f32_16x16x32_bf16 v[160:163], v[156:159], v[140:143], v[160:163]\n\t" \
"s_nop 7\n\t" \
"ds_write_b128 %[lred], v[160:163]\n\t" \
"s_waitcnt vmcnt(0) lgkmcnt(0)\n\t" \
"s_barrier\n\t" \
"ds_read2st64_b32 v[200:201], %[lrd] offset1:4\n\t" \
"ds_read2st64_b32 v[202:203], %[lrd] offset0:8 offset1:12\n\t" \
"ds_read2st64_b32 v[204:205], %[lrd2] offset1:4\n\t" \
"ds_read2st64_b32 v[206:207], %[lrd2] offset0:8 offset1:12\n\t" \
"s_waitcnt lgkmcnt(0)\n\t" \
"v_add_f32 v200, v200, v201\n\t" \
"v_add_f32 v202, v202, v203\n\t" \
"v_add_f32 v204, v204, v205\n\t" \
"v_add_f32 v206, v206, v207\n\t" \
"v_add_f32 v200, v200, v202\n\t" \
"v_add_f32 v204, v204, v206\n\t" \
"v_add_f32 v200, v200, v204\n\t" \
"buffer_store_dword v200, %[so], %[rs], 0 offen sc1\n\t" \
"s_memtime s[88:89]\n\t" \
"v_readfirstlane_b32 %[plan], v166\n\t" \
"ds_write_b128 %[lg], v[76:79] offset:0\n\t" \
"ds_write_b128 %[lg], v[84:87] offset:1024\n\t" \
"ds_write_b128 %[lg], v[92:95] offset:2048\n\t" \
"ds_write_b128 %[lg], v[100:103] offset:3072\n\t" \
"ds_write_b128 %[lg], v[108:111] offset:4096\n\t" \
"ds_write_b128 %[lg], v[116:119] offset:5120\n\t" \
"ds_write_b128 %[lg], v[124:127] offset:6144\n\t" \
"s_waitcnt lgkmcnt(0)\n\t" \
"s_mov_b32 %[tpub], s88\n\t" \
"s_mov_b32 %[nret], s92\n\t" \
"s_branch Le%=\n\t" \
"Lr%=:\n\t" \
"s_nop 7\n\t" \
"v_mov_b64 v[72:73], 0\n\t" \
"v_mov_b64 v[74:75], 0\n\t" \
"v_mov_b64 v[76:77], 0\n\t" \
"v_mov_b64 v[78:79], 0\n\t" \
"v_mov_b64 v[80:81], 0\n\t" \
"v_mov_b64 v[82:83], 0\n\t" \
"v_mov_b64 v[84:85], 0\n\t" \
"v_mov_b64 v[86:87], 0\n\t" \
"v_mov_b64 v[88:89], 0\n\t" \
"v_mov_b64 v[90:91], 0\n\t" \
"v_mov_b64 v[92:93], 0\n\t" \
"v_mov_b64 v[94:95], 0\n\t" \
"s_add_u32 s92, s92, 1\n\t" \
"s_cmp_lt_u32 s92, 0x40000\n\t" \
"s_cbranch_scc1 Lp%=\n\t" \
"s_branch Lok%=\n\t" \
"Le%=:\n\t"

#define ASM_A1_CLOB "v64", "v65", "v66", "v67", "v72", "v73", "v74", "v75", "v76", "v77", "v78", "v79", "v80", "v81", "v82", "v83", "v84", "v85", "v86", "v87", "v88", "v89", "v90", "v91", "v92", "v93", "v94", "v95", "v96", "v97", "v98", "v99", "v100", "v101", "v102", "v103", "v104", "v105", "v106", "v107", "v108", "v109", "v110", "v111", "v112", "v113", "v114", "v115", "v116", "v117", "v118", "v119", "v120", "v121", "v122", "v123", "v124", "v125", "v126", "v127", "v128", "v129", "v130", "v131", "v132", "v133", "v134", "v135", "v136", "v137", "v138", "v139", "v140", "v141", "v142", "v143", "v144", "v145", "v146", "v147", "v148", "v149", "v150", "v151", "v152", "v153", "v154", "v155", "v156", "v157", "v158", "v159", "v160", "v161", "v162", "v163", "v164", "v166", "v200", "v201", "v202", "v203", "v204", "v205", "v206", "v207", "s84", "s85", "s86", "s87", "s88", "s89", "s90", "s91", "s92", "s93", "vcc", "scc", "memory"

#define ASM_C0_TEXT \
"Lw%=:\n\t" \
"s_memtime s[88:89]\n\t" \
"s_waitcnt lgkmcnt(0)\n\t" \
"s_sub_u32 s90, s88, %[tgt]\n\t" \
"s_cmp_lt_i32 s90, 0\n\t" \
"s_cbranch_scc1 Lw%=\n\t" \
"s_mov_b32 s92, 0\n\t" \
"s_mov_b32 s91, 0\n\t" \
"Lq%=:\n\t" \
"buffer_load_dwordx4 v[64:67], %[vso], %[rs], 0 offen sc1\n\t" \
"buffer_load_dwordx4 v[68:71], %[vso], %[rs], 0 offen offset:1024 sc1\n\t" \
"buffer_load_dwordx2 v[76:77], %[vml], %[rs], 0 offen sc1\n\t" \
"buffer_load_dwordx4 v[72:75], %[vox], %[rs], 0 offen sc1\n\t" \
"buffer_load_dwordx4 v[152:155], %[vyx], %[rs], 0 offen sc1\n\t" \
"s_waitcnt vmcnt(4)\n\t" \
"v_add_f32_dpp v96, v64, v64 row_ror:8 row_mask:0xf bank_mask:0xf\n\t" \
"v_add_f32_dpp v97, v65, v65 row_ror:8 row_mask:0xf bank_mask:0xf\n\t" \
"v_add_f32_dpp v98, v66, v66 row_ror:8 row_mask:0xf bank_mask:0xf\n\t" \
"v_add_f32_dpp v99, v67, v67 row_ror:8 row_mask:0xf bank_mask:0xf\n\t" \
"v_max3_f32 v80, v96, v97, v98\n\t" \
"s_waitcnt vmcnt(3)\n\t" \
"v_add_f32_dpp v100, v68, v68 row_ror:8 row_mask:0xf bank_mask:0xf\n\t" \
"v_add_f32_dpp v101, v69, v69 row_ror:8 row_mask:0xf bank_mask:0xf\n\t" \
"v_add_f32_dpp v102, v70, v70 row_ror:8 row_mask:0xf bank_mask:0xf\n\t" \
"v_add_f32_dpp v103, v71, v71 row_ror:8 row_mask:0xf bank_mask:0xf\n\t" \
"v_max3_f32 v81, v99, v100, v101\n\t" \
"v_max3_f32 v80, v80, v102, v103\n\t" \
"s_waitcnt vmcnt(2)\n\t" \
"v_max3_f32 v90, v80, v81, v76\n\t" \
"v_sub_f32 v83, v76, v90\n\t" \
"v_sub_f32 v96, v96, v90\n\t" \
"v_sub_f32 v97, v97, v90\n\t" \
"v_sub_f32 v98, v98, v90\n\t" \
"v_sub_f32 v99, v99, v90\n\t" \
"v_sub_f32 v100, v100, v90\n\t" \
"v_sub_f32 v101, v101, v90\n\t" \
"v_sub_f32 v102, v102, v90\n\t" \
"v_sub_f32 v103, v103, v90\n\t" \
"v_exp_f32 v83, v83\n\t" \
"v_exp_f32 v96, v96\n\t" \
"v_exp_f32 v97, v97\n\t" \
"v_exp_f32 v98, v98\n\t" \
"v_exp_f32 v99, v99\n\t" \
"v_exp_f32 v100, v100\n\t" \
"v_exp_f32 v101, v101\n\t" \
"v_exp_f32 v102, v102\n\t" \
"v_exp_f32 v103, v103\n\t" \
"v_add_f32 v84, v96, v97\n\t" \
"v_add_f32 v85, v98, v99\n\t" \
"v_add_f32 v86, v100, v101\n\t" \
"v_fma_f32 v84, v77, v83, v84\n\t" \
"v_add_f32 v87, v102, v103\n\t" \
"v_add_f32 v84, v84, v85\n\t" \
"v_add_f32 v86, v86, v87\n\t" \
"v_add_f32 v84, v84, v86\n\t" \
"v_rcp_f32 v84, v84\n\t" \
"v_max3_u32 v88, v64, v65, v66\n\t" \
"v_mul_f32 v83, v83, v84\n\t" \
"v_cvt_pk_bf16_f32 v83, v83, v83\n\t" \
"v_and_b32 v104, v83, %[dm0]\n\t" \
"v_and_b32 v105, v83, %[dm1]\n\t" \
"v_and_b32 v106, v83, %[dm2]\n\t" \
"v_and_b32 v107, v83, %[dm3]\n\t" \
"v_mul_f32 v96, v96, v84\n\t" \
"v_mul_f32 v97, v97, v84\n\t" \
"s_waitcnt vmcnt(0)\n\t" \
"v_mfma_f32_16x16x32_bf16 v[112:115], v[72:75], v[104:107], 0\n\t" \
"v_mul_f32 v98, v98, v84\n\t" \
"v_mul_f32 v99, v99, v84\n\t" \
"v_mul_f32 v100, v100, v84\n\t" \
"v_mul_f32 v101, v101, v84\n\t" \
"v_mul_f32 v102, v102, v84\n\t" \
"v_mul_f32 v103, v103, v84\n\t" \
"v_cvt_pk_bf16_f32 v108, v96, v97\n\t" \
"v_cvt_pk_bf16_f32 v109, v98, v99\n\t" \
"v_cvt_pk_bf16_f32 v110, v100, v101\n\t" \
"v_cvt_pk_bf16_f32 v111, v102, v103\n\t" \
"v_max3_u32 v89, v67, v68, v69\n\t" \
"v_max3_u32 v88, v88, v70, v71\n\t" \
"v_mfma_f32_16x16x32_bf16 v[112:115], v[152:155], v[108:111], v[112:115]\n\t" \
"v_max3_u32 v89, v89, v72, v73\n\t" \
"v_max3_u32 v88, v88, v74, v75\n\t" \
"v_max3_u32 v89, v89, v76, v77\n\t" \
"v_max3_u32 v88, v88, v152, v153\n\t" \
"v_max3_u32 v89, v89, v154, v155\n\t" \
"v_max_u32 v88, v88, v89\n\t" \
"v_cmp_eq_u32 vcc, -1, v88\n\t" \
"s_cbranch_vccnz Lr%=\n\t" \
"Lo2%=:\n\t" \
"s_lshl_b32 s91, s91, 8\n\t" \
"s_or_b32 s92, s92, s91\n\t" \
"s_mov_b32 s90, %[emask]\n\t" \
"s_mov_b32 s91, 0x00ff00ff\n\t" \
"s_barrier\n\t" \
"s_memtime s[86:87]\n\t" \
"ds_read_b128 v[116:119], %[lred] offset:1024\n\t" \
"ds_read_b128 v[120:123], %[lred] offset:2048\n\t" \
"ds_read_b128 v[124:127], %[lred] offset:3072\n\t" \
"s_waitcnt lgkmcnt(0)\n\t" \
"v_add_f32 v112, v112, v116\n\t" \
"v_add_f32 v113, v113, v117\n\t" \
"v_add_f32 v114, v114, v118\n\t" \
"v_add_f32 v115, v115, v119\n\t" \
"s_add_u32 s86, s86, %[coff]\n\t" \
"s_sub_u32 s87, s86, %[prev]\n\t" \
"s_add_u32 s86, s86, %[pen]\n\t" \
"s_cmp_gt_i32 s87, %[eps]\n\t" \
"s_cselect_b32 s86, s86, %[prev]\n\t" \
"s_mov_b32 %[late], s87\n\t" \
"s_add_u32 s86, s86, %[per]\n\t" \
"s_min_u32 s86, s86, -2\n\t" \
"v_add_f32 v120, v120, v124\n\t" \
"v_add_f32 v121, v121, v125\n\t" \
"v_add_f32 v122, v122, v126\n\t" \
"v_add_f32 v123, v123, v127\n\t" \
"v_add_f32 v112, v112, v120\n\t" \
"v_add_f32 v113, v113, v121\n\t" \
"v_add_f32 v114, v114, v122\n\t" \
"v_add_f32 v115, v115, v123\n\t" \
"v_cvt_pk_bf16_f32 v112, v112, v113\n\t" \
"v_cvt_pk_bf16_f32 v113, v114, v115\n\t" \
"v_min_u32 v112, -2, v112\n\t" \
"v_min_u32 v113, -2, v113\n\t" \
"v_writelane_b32 v112, s86, 8\n\t" \
"s_mov_b64 exec, s[90:91]\n\t" \
"buffer_store_dwordx2 v[112:113], %[vpub], %[rs], 0 offen sc1\n\t" \
"s_mov_b64 exec, -1\n\t" \
"s_mov_b32 %[plan], s86\n\t" \
"s_memtime s[88:89]\n\t" \
"s_waitcnt lgkmcnt(0)\n\t" \
"s_mov_b32 %[tpub], s88\n\t" \
"s_mov_b32 %[nret], s92\n\t" \
"s_branch Le%=\n\t" \
"Lr%=:\n\t" \
"s_nop 7\n\t" \
"s_add_u32 s92, s92, 1\n\t" \
"s_cmp_lt_u32 s92, 0x40000\n\t" \
"s_cbranch_scc1 Lq%=\n\t" \
"s_branch Lo2%=\n\t" \
"Le%=:\n\t" \
"s_nop 1\n\t"

#define ASM_C0_CLOB "v64", "v65", "v66", "v67", "v68", "v69", "v70", "v71", "v72", "v73", "v74", "v75", "v76", "v77", "v80", "v81", "v83", "v84", "v85", "v86", "v87", "v88", "v89", "v90", "v96", "v97", "v98", "v99", "v100", "v101", "v102", "v103", "v104", "v105", "v106", "v107", "v108", "v109", "v110", "v111", "v112", "v113", "v114", "v115", "v116", "v117", "v118", "v119", "v120", "v121", "v122", "v123", "v124", "v125", "v126", "v127", "v152", "v153", "v154", "v155", "s84", "s85", "s86", "s87", "s88", "s89", "s90", "s91", "s92", "s93", "vcc", "scc", "memory"

#define ASM_C1_TEXT \
"Lw%=:\n\t" \
"s_memtime s[88:89]\n\t" \
"s_waitcnt lgkmcnt(0)\n\t" \
"s_sub_u32 s90, s88, %[tgt]\n\t" \
"s_cmp_lt_i32 s90, 0\n\t" \
"s_cbranch_scc1 Lw%=\n\t" \
"s_mov_b32 s92, 0\n\t" \
"s_mov_b32 s91, 0\n\t" \
"Lq%=:\n\t" \
"buffer_load_dwordx4 v[64:67], %[vso], %[rs], 0 offen sc1\n\t" \
"buffer_load_dwordx4 v[68:71], %[vso], %[rs], 0 offen offset:1024 sc1\n\t" \
"buffer_load_dwordx2 v[76:77], %[vml], %[rs], 0 offen sc1\n\t" \
"buffer_load_dwordx4 v[72:75], %[vox], %[rs], 0 offen sc1\n\t" \
"buffer_load_dwordx4 v[152:155], %[vyx], %[rs], 0 offen sc1\n\t" \
"s_waitcnt vmcnt(4)\n\t" \
"v_add_f32_dpp v96, v64, v64 row_ror:8 row_mask:0xf bank_mask:0xf\n\t" \
"v_add_f32_dpp v97, v65, v65 row_ror:8 row_mask:0xf bank_mask:0xf\n\t" \
"v_add_f32_dpp v98, v66, v66 row_ror:8 row_mask:0xf bank_mask:0xf\n\t" \
"v_add_f32_dpp v99, v67, v67 row_ror:8 row_mask:0xf bank_mask:0xf\n\t" \
"v_max3_f32 v80, v96, v97, v98\n\t" \
"s_waitcnt vmcnt(3)\n\t" \
"v_add_f32_dpp v100, v68, v68 row_ror:8 row_mask:0xf bank_mask:0xf\n\t" \
"v_add_f32_dpp v101, v69, v69 row_ror:8 row_mask:0xf bank_mask:0xf\n\t" \
"v_add_f32_dpp v102, v70, v70 row_ror:8 row_mask:0xf bank_mask:0xf\n\t" \
"v_add_f32_dpp v103, v71, v71 row_ror:8 row_mask:0xf bank_mask:0xf\n\t" \
"v_max3_f32 v81, v99, v100, v101\n\t" \
"v_max3_f32 v80, v80, v102, v103\n\t" \
"s_waitcnt vmcnt(2)\n\t" \
"v_max3_f32 v90, v80, v81, v76\n\t" \
"v_sub_f32 v83, v76, v90\n\t" \
"v_sub_f32 v96, v96, v90\n\t" \
"v_sub_f32 v97, v97, v90\n\t" \
"v_sub_f32 v98, v98, v90\n\t" \
"v_sub_f32 v99, v99, v90\n\t" \
"v_sub_f32 v100, v100, v90\n\t" \
"v_sub_f32 v101, v101, v90\n\t" \
"v_sub_f32 v102, v102, v90\n\t" \
"v_sub_f32 v103, v103, v90\n\t" \
"v_exp_f32 v83, v83\n\t" \
"v_exp_f32 v96, v96\n\t" \
"v_exp_f32 v97, v97\n\t" \
"v_exp_f32 v98, v98\n\t" \
"v_exp_f32 v99, v99\n\t" \
"v_exp_f32 v100, v100\n\t" \
"v_exp_f32 v101, v101\n\t" \
"v_exp_f32 v102, v102\n\t" \
"v_exp_f32 v103, v103\n\t" \
"v_add_f32 v84, v96, v97\n\t" \
"v_add_f32 v85, v98, v99\n\t" \
"v_add_f32 v86, v100, v101\n\t" \
"v_fma_f32 v84, v77, v83, v84\n\t" \
"v_add_f32 v87, v102, v103\n\t" \
"v_add_f32 v84, v84, v85\n\t" \
"v_add_f32 v86, v86, v87\n\t" \
"v_add_f32 v84, v84, v86\n\t" \
"v_rcp_f32 v84, v84\n\t" \
"v_max3_u32 v88, v64, v65, v66\n\t" \
"v_mul_f32 v83, v83, v84\n\t" \
"v_cvt_pk_bf16_f32 v83, v83, v83\n\t" \
"v_and_b32 v104, v83, %[dm0]\n\t" \
"v_and_b32 v105, v83, %[dm1]\n\t" \
"v_and_b32 v106, v83, %[dm2]\n\t" \
"v_and_b32 v107, v83, %[dm3]\n\t" \
"v_mul_f32 v96, v96, v84\n\t" \
"v_mul_f32 v97, v97, v84\n\t" \
"s_waitcnt vmcnt(0)\n\t" \
"v_mfma_f32_16x16x32_bf16 v[112:115], v[72:75], v[104:107], 0\n\t" \
"v_mul_f32 v98, v98, v84\n\t" \
"v_mul_f32 v99, v99, v84\n\t" \
"v_mul_f32 v100, v100, v84\n\t" \
"v_mul_f32 v101, v101, v84\n\t" \
"v_mul_f32 v102, v102, v84\n\t" \
"v_mul_f32 v103, v103, v84\n\t" \
"v_cvt_pk_bf16_f32 v108, v96, v97\n\t" \
"v_cvt_pk_bf16_f32 v109, v98, v99\n\t" \
"v_cvt_pk_bf16_f32 v110, v100, v101\n\t" \
"v_cvt_pk_bf16_f32 v111, v102, v103\n\t" \
"v_max3_u32 v89, v67, v68, v69\n\t" \
"v_max3_u32 v88, v88, v70, v71\n\t" \
"v_mfma_f32_16x16x32_bf16 v[112:115], v[152:155], v[108:111], v[112:115]\n\t" \
"v_max3_u32 v89, v89, v72, v73\n\t" \
"v_max3_u32 v88, v88, v74, v75\n\t" \
"v_max3_u32 v89, v89, v76, v77\n\t" \
"v_max3_u32 v88, v88, v152, v153\n\t" \
"v_max3_u32 v89, v89, v154, v155\n\t" \
"v_max_u32 v88, v88, v89\n\t" \
"v_cmp_eq_u32 vcc, -1, v88\n\t" \
"s_cbranch_vccnz Lr%=\n\t" \
"Lo2%=:\n\t" \
"s_lshl_b32 s91, s91, 8\n\t" \
"s_or_b32 s92, s92, s91\n\t" \
"ds_write_b128 %[lred], v[112:115]\n\t" \
"s_waitcnt lgkmcnt(0)\n\t" \
"s_barrier\n\t" \
"s_waitcnt vmcnt(0)\n\t" \
"s_memtime s[88:89]\n\t" \
"s_waitcnt lgkmcnt(0)\n\t" \
"s_mov_b32 %[tpub], s88\n\t" \
"s_mov_b32 %[nret], s92\n\t" \
"s_branch Le%=\n\t" \
"Lr%=:\n\t" \
"s_nop 7\n\t" \
"s_add_u32 s92, s92, 1\n\t" \
"s_cmp_lt_u32 s92, 0x40000\n\t" \
"s_cbranch_scc1 Lq%=\n\t" \
"s_branch Lo2%=\n\t" \
"Le%=:\n\t" \
"s_nop 1\n\t"

#define ASM_C1_CLOB "v64", "v65", "v66", "v67", "v68", "v69", "v70", "v71", "v72", "v73", "v74", "v75", "v76", "v77", "v80", "v81", "v83", "v84", "v85", "v86", "v87", "v88", "v89", "v90", "v96", "v97", "v98", "v99", "v100", "v101", "v102", "v103", "v104", "v105", "v106", "v107", "v108", "v109", "v110", "v111", "v112", "v113", "v114", "v115", "v152", "v153", "v154", "v155", "s84", "s85", "s86", "s87", "s88", "s89", "s90", "s91", "s92", "s93", "vcc", "scc", "memory"

#define ASM_CY_TEXT \
"v_mov_b64 v[96:97], 0\n\t" \
"v_mov_b64 v[98:99], 0\n\t" \
"v_mov_b64 v[100:101], 0\n\t" \
"v_mov_b64 v[102:103], 0\n\t" \
"v_mov_b64 v[104:105], 0\n\t" \
"v_mov_b64 v[106:107], 0\n\t" \
"v_mov_b64 v[108:109], 0\n\t" \
"v_mov_b64 v[110:111], 0\n\t" \
"Lw%=:\n\t" \
"s_memtime s[88:89]\n\t" \
"s_waitcnt lgkmcnt(0)\n\t" \
"s_sub_u32 s90, s88, %[tgt]\n\t" \
"s_cmp_lt_i32 s90, 0\n\t" \
"s_cbranch_scc1 Lw%=\n\t" \
"s_mov_b32 s92, -1\n\t" \
"Lp%=:\n\t" \
"buffer_load_dwordx4 v[112:115], %[vg0], %[rs], 0 offen sc1\n\t" \
"buffer_load_dwordx4 v[116:119], %[vg1], %[rs], 0 offen sc1\n\t" \
"s_add_u32 s92, s92, 1\n\t" \
"s_cmp_lt_u32 s92, 0x40000\n\t" \
"s_waitcnt vmcnt(1)\n\t" \
"ds_write_b128 %[lw0], v[112:115]\n\t" \
"v_max3_u32 v120, v112, v113, v114\n\t" \
"s_waitcnt vmcnt(0)\n\t" \
"ds_write_b128 %[lw1], v[116:119]\n\t" \
"v_max3_u32 v121, v115, v116, v117\n\t" \
"v_max3_u32 v120, v120, v118, v119\n\t" \
"v_max_u32 v120, v120, v121\n\t" \
"v_cmp_eq_u32 vcc, -1, v120\n\t" \
"s_cbranch_scc0 Lok%=\n\t" \
"s_cbranch_vccnz Lp%=\n\t" \
"Lok%=:\n\t" \
"buffer_load_dword v122, off, %[rs], %[spl] sc1\n\t" \
"s_waitcnt lgkmcnt(0)\n\t" \
"s_barrier\n\t" \
"ds_read_b128 v[64:67], %[lm] offset:0\n\t" \
"ds_read_b128 v[68:71], %[lm] offset:1024\n\t" \
"ds_read_b128 v[72:75], %[lm] offset:2048\n\t" \
"ds_read_b128 v[76:79], %[lm] offset:3072\n\t" \
"ds_read_b128 v[80:83], %[lm] offset:4096\n\t" \
"ds_read_b128 v[84:87], %[lm] offset:5120\n\t" \
"ds_read_b128 v[88:91], %[lm] offset:6144\n\t" \
"s_waitcnt lgkmcnt(6)\n\t" \
"v_smfmac_f32_16x16x64_bf16 v[96:99], v[64:67], a[0:7], %[idx]\n\t" \
"v_smfmac_f32_16x16x64_bf16 v[100:103], v[64:67], a[56:63], %[idx]\n\t" \
"v_smfmac_f32_16x16x64_bf16 v[104:107], v[64:67], a[112:119], %[idx]\n\t" \
"v_smfmac_f32_16x16x64_bf16 v[108:111], v[64:67], a[168:175], %[idx]\n\t" \
"s_waitcnt lgkmcnt(5)\n\t" \
"v_smfmac_f32_16x16x64_bf16 v[96:99], v[68:71], a[8:15], %[idx]\n\t" \
"v_smfmac_f32_16x16x64_bf16 v[100:103], v[68:71], a[64:71], %[idx]\n\t" \
"v_smfmac_f32_16x16x64_bf16 v[104:107], v[68:71], a[120:127], %[idx]\n\t" \
"v_smfmac_f32_16x16x64_bf16 v[108:111], v[68:71], a[176:183], %[idx]\n\t" \
"s_waitcnt lgkmcnt(4)\n\t" \
"v_smfmac_f32_16x16x64_bf16 v[96:99], v[72:75], a[16:23], %[idx]\n\t" \
"v_smfmac_f32_16x16x64_bf16 v[100:103], v[72:75], a[72:79], %[idx]\n\t" \
"v_smfmac_f32_16x16x64_bf16 v[104:107], v[72:75], a[128:135], %[idx]\n\t" \
"v_smfmac_f32_16x16x64_bf16 v[108:111], v[72:75], a[184:191], %[idx]\n\t" \
"s_waitcnt lgkmcnt(3)\n\t" \
"v_smfmac_f32_16x16x64_bf16 v[96:99], v[76:79], a[24:31], %[idx]\n\t" \
"v_smfmac_f32_16x16x64_bf16 v[100:103], v[76:79], a[80:87], %[idx]\n\t" \
"v_smfmac_f32_16x16x64_bf16 v[104:107], v[76:79], a[136:143], %[idx]\n\t" \
"v_smfmac_f32_16x16x64_bf16 v[108:111], v[76:79], a[192:199], %[idx]\n\t" \
"s_waitcnt lgkmcnt(2)\n\t" \
"v_smfmac_f32_16x16x64_bf16 v[96:99], v[80:83], a[32:39], %[idx]\n\t" \
"v_smfmac_f32_16x16x64_bf16 v[100:103], v[80:83], a[88:95], %[idx]\n\t" \
"v_smfmac_f32_16x16x64_bf16 v[104:107], v[80:83], a[144:151], %[idx]\n\t" \
"v_smfmac_f32_16x16x64_bf16 v[108:111], v[80:83], a[200:207], %[idx]\n\t" \
"s_waitcnt lgkmcnt(1)\n\t" \
"v_smfmac_f32_16x16x64_bf16 v[96:99], v[84:87], a[40:47], %[idx]\n\t" \
"v_smfmac_f32_16x16x64_bf16 v[100:103], v[84:87], a[96:103], %[idx]\n\t" \
"v_smfmac_f32_16x16x64_bf16 v[104:107], v[84:87], a[152:159], %[idx]\n\t" \
"v_smfmac_f32_16x16x64_bf16 v[108:111], v[84:87], a[208:215], %[idx]\n\t" \
"s_waitcnt lgkmcnt(0)\n\t" \
"v_smfmac_f32_16x16x64_bf16 v[96:99], v[88:91], a[48:55], %[idx]\n\t" \
"v_smfmac_f32_16x16x64_bf16 v[100:103], v[88:91], a[104:111], %[idx]\n\t" \
"v_smfmac_f32_16x16x64_bf16 v[104:107], v[88:91], a[160:167], %[idx]\n\t" \
"v_smfmac_f32_16x16x64_bf16 v[108:111], v[88:91], a[216:223], %[idx]\n\t" \
"s_nop 4\n\t" \
"ds_write_b128 %[lyw], v[96:99] offset:0\n\t" \
"ds_write_b128 %[lyw], v[100:103] offset:1024\n\t" \
"ds_write_b128 %[lyw], v[104:107] offset:2048\n\t" \
"ds_write_b128 %[lyw], v[108:111] offset:3072\n\t" \
"s_waitcnt lgkmcnt(0)\n\t" \
"ds_read_b128 v[64:67], %[lyr] offset:0\n\t" \
"ds_read_b128 v[68:71], %[lyr] offset:256\n\t" \
"ds_read_b128 v[72:75], %[lyr] offset:512\n\t" \
"ds_read_b128 v[76:79], %[lyr] offset:768\n\t" \
"s_waitcnt lgkmcnt(0)\n\t" \
"v_add_f32 v64, v64, v72\n\t" \
"v_add_f32 v65, v65, v73\n\t" \
"v_add_f32 v66, v66, v74\n\t" \
"v_add_f32 v67, v67, v75\n\t" \
"v_add_f32 v68, v68, v76\n\t" \
"v_add_f32 v69, v69, v77\n\t" \
"v_add_f32 v70, v70, v78\n\t" \
"v_add_f32 v71, v71, v79\n\t" \
"v_cvt_pk_bf16_f32 v96, v64, v65\n\t" \
"v_cvt_pk_bf16_f32 v97, v66, v67\n\t" \
"v_cvt_pk_bf16_f32 v98, v68, v69\n\t" \
"v_cvt_pk_bf16_f32 v99, v70, v71\n\t" \
"v_min_u32 v96, -2, v96\n\t" \
"v_min_u32 v97, -2, v97\n\t" \
"v_min_u32 v98, -2, v98\n\t" \
"v_min_u32 v99, -2, v99\n\t" \
"s_waitcnt vmcnt(0)\n\t" \
"buffer_store_dwordx4 v[96:99], %[vyx], %[rs], 0 offen sc1\n\t" \
"v_readfirstlane_b32 %[plan], v122\n\t" \
"s_memtime s[88:89]\n\t" \
"s_waitcnt lgkmcnt(0)\n\t" \
"s_mov_b32 %[tpub], s88\n\t" \
"s_mov_b32 %[nret], s92\n\t" \
"s_nop 1\n\t"

#define ASM_CY_CLOB "v64", "v65", "v66", "v67", "v68", "v69", "v70", "v71", "v72", "v73", "v74", "v75", "v76", "v77", "v78", "v79", "v80", "v81", "v82", "v83", "v84", "v85", "v86", "v87", "v88", "v89", "v90", "v91", "v96", "v97", "v98", "v99", "v100", "v101", "v102", "v103", "v104", "v105", "v106", "v107", "v108", "v109", "v110", "v111", "v112", "v113", "v114", "v115", "v116", "v117", "v118", "v119", "v120", "v121", "v122", "s84", "s85", "s86", "s87", "s88", "s89", "s90", "s91", "s92", "s93", "vcc", "scc", "memory"

#define ASM_H0_TEXT \
"Lw%=:\n\t" \
"s_memtime s[88:89]\n\t" \
"s_waitcnt lgkmcnt(0)\n\t" \
"s_sub_u32 s90, s88, %[tgt]\n\t" \
"s_cmp_lt_i32 s90, 0\n\t" \
"s_cbranch_scc1 Lw%=\n\t" \
"s_mov_b32 s92, 0\n\t" \
"Lp%=:\n\t" \
"buffer_load_dwordx4 v[128:131], %[vm0], %[rs], 0 offen sc1\n\t" \
"buffer_load_dwordx4 v[132:135], %[vm1], %[rs], 0 offen sc1\n\t" \
"s_waitcnt vmcnt(1)\n\t" \
"v_mfma_f32_16x16x32_bf16 v[136:139], %[g000], v[128:131], 0\n\t" \
"v_max3_u32 v216, v128, v129, v130\n\t" \
"v_mfma_f32_16x16x32_bf16 v[140:143], %[g010], v[128:131], 0\n\t" \
"v_mfma_f32_16x16x32_bf16 v[144:147], %[g100], v[128:131], 0\n\t" \
"v_mfma_f32_16x16x32_bf16 v[148:151], %[g110], v[128:131], 0\n\t" \
"s_waitcnt vmcnt(0)\n\t" \
"v_mfma_f32_16x16x32_bf16 v[136:139], %[g001], v[132:135], v[136:139]\n\t" \
"v_max3_u32 v217, v131, v132, v133\n\t" \
"v_max3_u32 v216, v216, v134, v135\n\t" \
"v_mfma_f32_16x16x32_bf16 v[140:143], %[g011], v[132:135], v[140:143]\n\t" \
"v_max_u32 v216, v216, v217\n\t" \
"v_cmp_eq_u32 vcc, -1, v216\n\t" \
"v_mfma_f32_16x16x32_bf16 v[144:147], %[g101], v[132:135], v[144:147]\n\t" \
"v_mfma_f32_16x16x32_bf16 v[148:151], %[g111], v[132:135], v[148:151]\n\t" \
"s_cbranch_vccnz Lr%=\n\t" \
"Lok%=:\n\t" \
"s_nop 2\n\t" \
"v_add_f32_dpp v152, v140, v136 row_shl:8 row_mask:0xf bank_mask:0x3\n\t" \
"v_add_f32_dpp v153, v141, v137 row_shl:8 row_mask:0xf bank_mask:0x3\n\t" \
"v_add_f32_dpp v154, v142, v138 row_shl:8 row_mask:0xf bank_mask:0x3\n\t" \
"v_add_f32_dpp v155, v143, v139 row_shl:8 row_mask:0xf bank_mask:0x3\n\t" \
"v_add_f32_dpp v152, v144, v148 row_shr:8 row_mask:0xf bank_mask:0xc\n\t" \
"v_add_f32_dpp v153, v145, v149 row_shr:8 row_mask:0xf bank_mask:0xc\n\t" \
"v_add_f32_dpp v154, v146, v150 row_shr:8 row_mask:0xf bank_mask:0xc\n\t" \
"v_add_f32_dpp v155, v147, v151 row_shr:8 row_mask:0xf bank_mask:0xc\n\t" \
"ds_write_b128 %[lw], v[152:155]\n\t" \
"s_waitcnt lgkmcnt(0)\n\t" \
"s_barrier\n\t" \
"ds_read_b128 v[156:159], %[lr] offset:0\n\t" \
"ds_read_b128 v[160:163], %[lr] offset:1024\n\t" \
"ds_read_b128 v[164:167], %[lr] offset:2048\n\t" \
"ds_read_b128 v[168:171], %[lr] offset:3072\n\t" \
"s_waitcnt lgkmcnt(2)\n\t" \
"v_add_f32 v156, v156, v160\n\t" \
"v_add_f32 v157, v157, v161\n\t" \
"v_add_f32 v158, v158, v162\n\t" \
"v_add_f32 v159, v159, v163\n\t" \
"s_waitcnt lgkmcnt(0)\n\t" \
"v_add_f32 v164, v164, v168\n\t" \
"v_add_f32 v165, v165, v169\n\t" \
"v_add_f32 v166, v166, v170\n\t" \
"v_add_f32 v167, v167, v171\n\t" \
"v_add_f32 v156, v156, v164\n\t" \
"v_add_f32 v157, v157, v165\n\t" \
"v_add_f32 v158, v158, v166\n\t" \
"v_add_f32 v159, v159, v167\n\t" \
"v_max3_f32 v216, v156, v157, v158\n\t" \
"v_max_f32 v216, v216, v159\n\t" \
"s_nop 1\n\t" \
"v_max_f32_dpp v216, v216, v216 row_ror:8 row_mask:0xf bank_mask:0xf\n\t" \
"v_mov_b32 v217, v216\n\t" \
"s_nop 1\n\t" \
"v_permlane16_swap_b32 v216, v217\n\t" \
"v_max_f32 v216, v216, v217\n\t" \
"v_mov_b32 v217, v216\n\t" \
"s_nop 1\n\t" \
"v_permlane32_swap_b32 v216, v217\n\t" \
"v_max_f32 v216, v216, v217\n\t" \
"v_sub_f32 v156, v156, v216\n\t" \
"v_sub_f32 v157, v157, v216\n\t" \
"v_sub_f32 v158, v158, v216\n\t" \
"v_sub_f32 v159, v159, v216\n\t" \
"v_exp_f32 v156, v156\n\t" \
"v_exp_f32 v157, v157\n\t" \
"v_exp_f32 v158, v158\n\t" \
"v_exp_f32 v159, v159\n\t" \
"v_cvt_pk_bf16_f32 v160, v156, v157\n\t" \
"v_cvt_pk_bf16_f32 v161, v158, v159\n\t" \
"v_cvt_pk_bf16_f32 v164, v156, v157\n\t" \
"v_cvt_pk_bf16_f32 v165, v158, v159\n\t" \
"v_cvt_pk_bf16_f32 v166, v156, v157\n\t" \
"v_cvt_pk_bf16_f32 v167, v158, v159\n\t" \
"v_mov_b32_dpp v164, v160 row_ror:8 row_mask:0xf bank_mask:0xc\n\t" \
"v_mov_b32_dpp v165, v161 row_ror:8 row_mask:0xf bank_mask:0xc\n\t" \
"v_mov_b32_dpp v166, v160 row_ror:8 row_mask:0xf bank_mask:0x3\n\t" \
"v_mov_b32_dpp v167, v161 row_ror:8 row_mask:0xf bank_mask:0x3\n\t" \
"s_nop 1\n\t" \
"v_mfma_f32_16x16x32_bf16 v[184:187], v[164:167], %[u0], 0\n\t" \
"v_add_f32 v218, v156, v157\n\t" \
"v_add_f32 v219, v158, v159\n\t" \
"v_mfma_f32_16x16x32_bf16 v[188:191], v[164:167], %[u1], 0\n\t" \
"v_add_f32 v217, v218, v219\n\t" \
"s_nop 1\n\t" \
"v_add_f32_dpp v217, v217, v217 row_ror:8 row_mask:0xf bank_mask:0xf\n\t" \
"v_mfma_f32_16x16x32_bf16 v[192:195], v[164:167], %[u2], 0\n\t" \
"v_mov_b32 v218, v217\n\t" \
"s_nop 1\n\t" \
"v_permlane16_swap_b32 v217, v218\n\t" \
"v_mfma_f32_16x16x32_bf16 v[196:199], v[164:167], %[u3], 0\n\t" \
"v_add_f32 v217, v217, v218\n\t" \
"v_mov_b32 v218, v217\n\t" \
"v_mfma_f32_16x16x32_bf16 v[200:203], v[164:167], %[u4], 0\n\t" \
"s_nop 0\n\t" \
"v_permlane32_swap_b32 v217, v218\n\t" \
"v_add_f32 v217, v217, v218\n\t" \
"v_mfma_f32_16x16x32_bf16 v[204:207], v[164:167], %[u5], 0\n\t" \
"v_cvt_pk_bf16_f32 v168, v184, v185\n\t" \
"v_cvt_pk_bf16_f32 v169, v186, v187\n\t" \
"v_mfma_f32_16x16x32_bf16 v[208:211], v[164:167], %[u6], 0\n\t" \
"v_cvt_pk_bf16_f32 v172, v192, v193\n\t" \
"v_cvt_pk_bf16_f32 v173, v194, v195\n\t" \
"s_mov_b64 exec, 0xff\n\t" \
"buffer_store_dwordx2 v[216:217], %[vml], %[rs], 0 offen sc1\n\t" \
"s_mov_b64 exec, -1\n\t" \
"v_cvt_pk_bf16_f32 v170, v188, v189\n\t" \
"v_cvt_pk_bf16_f32 v171, v190, v191\n\t" \
"v_cvt_pk_bf16_f32 v174, v196, v197\n\t" \
"v_cvt_pk_bf16_f32 v175, v198, v199\n\t" \
"v_permlane32_swap_b32 v168, v172\n\t" \
"v_permlane32_swap_b32 v169, v173\n\t" \
"v_permlane32_swap_b32 v170, v174\n\t" \
"v_permlane32_swap_b32 v171, v175\n\t" \
"v_cvt_pk_bf16_f32 v176, v200, v201\n\t" \
"v_cvt_pk_bf16_f32 v177, v202, v203\n\t" \
"v_permlane16_swap_b32 v168, v170\n\t" \
"v_permlane16_swap_b32 v169, v171\n\t" \
"v_min_u32 v128, -2, v168\n\t" \
"v_min_u32 v129, -2, v169\n\t" \
"v_min_u32 v130, -2, v170\n\t" \
"v_min_u32 v131, -2, v171\n\t" \
"buffer_store_dwordx4 v[128:131], %[vo0], %[rs], 0 offen sc1\n\t" \
"v_cvt_pk_bf16_f32 v180, v208, v209\n\t" \
"v_cvt_pk_bf16_f32 v181, v210, v211\n\t" \
"v_cvt_pk_bf16_f32 v178, v204, v205\n\t" \
"v_cvt_pk_bf16_f32 v179, v206, v207\n\t" \
"s_mov_b32 s90, -1\n\t" \
"s_mov_b32 s91, 0xffff\n\t" \
"v_permlane32_swap_b32 v176, v180\n\t" \
"v_permlane32_swap_b32 v177, v181\n\t" \
"s_nop 0\n\t" \
"v_permlane16_swap_b32 v176, v178\n\t" \
"v_permlane16_swap_b32 v177, v179\n\t" \
"v_min_u32 v132, -2, v176\n\t" \
"v_min_u32 v133, -2, v177\n\t" \
"v_min_u32 v134, -2, v178\n\t" \
"v_min_u32 v135, -2, v179\n\t" \
"s_mov_b64 exec, s[90:91]\n\t" \
"buffer_store_dwordx4 v[132:135], %[vo1], %[rs], 0 offen sc1\n\t" \
"s_mov_b64 exec, -1\n\t" \
"s_waitcnt lgkmcnt(0)\n\t" \
"s_mov_b32 %[nret], s92\n\t" \
"s_branch Le%=\n\t" \
"Lr%=:\n\t" \
"s_nop 7\n\t" \
"s_add_u32 s92, s92, 1\n\t" \
"s_cmp_lt_u32 s92, 0x40000\n\t" \
"s_cbranch_scc1 Lp%=\n\t" \
"s_branch Lok%=\n\t" \
"Le%=:\n\t" \
"s_nop 1\n\t"

#define ASM_H0_CLOB "v128", "v129", "v130", "v131", "v132", "v133", "v134", "v135", "v136", "v137", "v138", "v139", "v140", "v141", "v142", "v143", "v144", "v145", "v146", "v147", "v148", "v149", "v150", "v151", "v152", "v153", "v154", "v155", "v156", "v157", "v158", "v159", "v160", "v161", "v162", "v163", "v164", "v165", "v166", "v167", "v168", "v169", "v170", "v171", "v172", "v173", "v174", "v175", "v176", "v177", "v178", "v179", "v180", "v181", "v184", "v185", "v186", "v187", "v188", "v189", "v190", "v191", "v192", "v193", "v194", "v195", "v196", "v197", "v198", "v199", "v200", "v201", "v202", "v203", "v204", "v205", "v206", "v207", "v208", "v209", "v210", "v211", "v216", "v217", "v218", "v219", "s84", "s85", "s86", "s87", "s88", "s89", "s90", "s91", "s92", "s93", "vcc", "scc", "memory"

#define ASM_H1_TEXT \
"Lw%=:\n\t" \
"s_memtime s[88:89]\n\t" \
"s_waitcnt lgkmcnt(0)\n\t" \
"s_sub_u32 s90, s88, %[tgt]\n\t" \
"s_cmp_lt_i32 s90, 0\n\t" \
"s_cbranch_scc1 Lw%=\n\t" \
"s_mov_b32 s92, 0\n\t" \
"Lp%=:\n\t" \
"buffer_load_dwordx4 v[128:131], %[vm0], %[rs], 0 offen sc1\n\t" \
"buffer_load_dwordx4 v[132:135], %[vm1], %[rs], 0 offen sc1\n\t" \
"s_waitcnt vmcnt(1)\n\t" \
"v_mfma_f32_16x16x32_bf16 v[136:139], %[g000], v[128:131], 0\n\t" \
"v_max3_u32 v216, v128, v129, v130\n\t" \
"v_mfma_f32_16x16x32_bf16 v[140:143], %[g010], v[128:131], 0\n\t" \
"v_mfma_f32_16x16x32_bf16 v[144:147], %[g100], v[128:131], 0\n\t" \
"v_mfma_f32_16x16x32_bf16 v[148:151], %[g110], v[128:131], 0\n\t" \
"s_waitcnt vmcnt(0)\n\t" \
"v_mfma_f32_16x16x32_bf16 v[136:139], %[g001], v[132:135], v[136:139]\n\t" \
"v_max3_u32 v217, v131, v132, v133\n\t" \
"v_max3_u32 v216, v216, v134, v135\n\t" \
"v_mfma_f32_16x16x32_bf16 v[140:143], %[g011], v[132:135], v[140:143]\n\t" \
"v_max_u32 v216, v216, v217\n\t" \
"v_cmp_eq_u32 vcc, -1, v216\n\t" \
"v_mfma_f32_16x16x32_bf16 v[144:147], %[g101], v[132:135], v[144:147]\n\t" \
"v_mfma_f32_16x16x32_bf16 v[148:151], %[g111], v[132:135], v[148:151]\n\t" \
"s_cbranch_vccnz Lr%=\n\t" \
"Lok%=:\n\t" \
"s_nop 2\n\t" \
"v_add_f32_dpp v152, v140, v136 row_shl:8 row_mask:0xf bank_mask:0x3\n\t" \
"v_add_f32_dpp v153, v141, v137 row_shl:8 row_mask:0xf bank_mask:0x3\n\t" \
"v_add_f32_dpp v154, v142, v138 row_shl:8 row_mask:0xf bank_mask:0x3\n\t" \
"v_add_f32_dpp v155, v143, v139 row_shl:8 row_mask:0xf bank_mask:0x3\n\t" \
"v_add_f32_dpp v152, v144, v148 row_shr:8 row_mask:0xf bank_mask:0xc\n\t" \
"v_add_f32_dpp v153, v145, v149 row_shr:8 row_mask:0xf bank_mask:0xc\n\t" \
"v_add_f32_dpp v154, v146, v150 row_shr:8 row_mask:0xf bank_mask:0xc\n\t" \
"v_add_f32_dpp v155, v147, v151 row_shr:8 row_mask:0xf bank_mask:0xc\n\t" \
"ds_write_b128 %[lw], v[152:155]\n\t" \
"s_waitcnt lgkmcnt(0)\n\t" \
"s_barrier\n\t" \
"ds_read_b128 v[156:159], %[lr] offset:0\n\t" \
"ds_read_b128 v[160:163], %[lr] offset:1024\n\t" \
"ds_read_b128 v[164:167], %[lr] offset:2048\n\t" \
"ds_read_b128 v[168:171], %[lr] offset:3072\n\t" \
"s_waitcnt lgkmcnt(2)\n\t" \
"v_add_f32 v156, v156, v160\n\t" \
"v_add_f32 v157, v157, v161\n\t" \
"v_add_f32 v158, v158, v162\n\t" \
"v_add_f32 v159, v159, v163\n\t" \
"s_waitcnt lgkmcnt(0)\n\t" \
"v_add_f32 v164, v164, v168\n\t" \
"v_add_f32 v165, v165, v169\n\t" \
"v_add_f32 v166, v166, v170\n\t" \
"v_add_f32 v167, v167, v171\n\t" \
"v_add_f32 v156, v156, v164\n\t" \
"v_add_f32 v157, v157, v165\n\t" \
"v_add_f32 v158, v158, v166\n\t" \
"v_add_f32 v159, v159, v167\n\t" \
"v_max3_f32 v216, v156, v157, v158\n\t" \
"v_max_f32 v216, v216, v159\n\t" \
"s_nop 1\n\t" \
"v_max_f32_dpp v216, v216, v216 row_ror:8 row_mask:0xf bank_mask:0xf\n\t" \
"v_mov_b32 v217, v216\n\t" \
"s_nop 1\n\t" \
"v_permlane16_swap_b32 v216, v217\n\t" \
"v_max_f32 v216, v216, v217\n\t" \
"v_mov_b32 v217, v216\n\t" \
"s_nop 1\n\t" \
"v_permlane32_swap_b32 v216, v217\n\t" \
"v_max_f32 v216, v216, v217\n\t" \
"v_sub_f32 v156, v156, v216\n\t" \
"v_sub_f32 v157, v157, v216\n\t" \
"v_sub_f32 v158, v158, v216\n\t" \
"v_sub_f32 v159, v159, v216\n\t" \
"v_exp_f32 v156, v156\n\t" \
"v_exp_f32 v157, v157\n\t" \
"v_exp_f32 v158, v158\n\t" \
"v_exp_f32 v159, v159\n\t" \
"v_cvt_pk_bf16_f32 v160, v156, v157\n\t" \
"v_cvt_pk_bf16_f32 v161, v158, v159\n\t" \
"v_cvt_pk_bf16_f32 v164, v156, v157\n\t" \
"v_cvt_pk_bf16_f32 v165, v158, v159\n\t" \
"v_cvt_pk_bf16_f32 v166, v156, v157\n\t" \
"v_cvt_pk_bf16_f32 v167, v158, v159\n\t" \
"v_mov_b32_dpp v164, v160 row_ror:8 row_mask:0xf bank_mask:0xc\n\t" \
"v_mov_b32_dpp v165, v161 row_ror:8 row_mask:0xf bank_mask:0xc\n\t" \
"v_mov_b32_dpp v166, v160 row_ror:8 row_mask:0xf bank_mask:0x3\n\t" \
"v_mov_b32_dpp v167, v161 row_ror:8 row_mask:0xf bank_mask:0x3\n\t" \
"s_nop 1\n\t" \
"v_mfma_f32_16x16x32_bf16 v[184:187], v[164:167], %[u0], 0\n\t" \
"v_mfma_f32_16x16x32_bf16 v[188:191], v[164:167], %[u1], 0\n\t" \
"v_mfma_f32_16x16x32_bf16 v[192:195], v[164:167], %[u2], 0\n\t" \
"v_mfma_f32_16x16x32_bf16 v[196:199], v[164:167], %[u3], 0\n\t" \
"v_mfma_f32_16x16x32_bf16 v[200:203], v[164:167], %[u4], 0\n\t" \
"v_mfma_f32_16x16x32_bf16 v[204:207], v[164:167], %[u5], 0\n\t" \
"s_nop 2\n\t" \
"v_cvt_pk_bf16_f32 v168, v184, v185\n\t" \
"v_cvt_pk_bf16_f32 v169, v186, v187\n\t" \
"v_mfma_f32_16x16x32_bf16 v[208:211], v[164:167], %[u6], 0\n\t" \
"v_cvt_pk_bf16_f32 v172, v192, v193\n\t" \
"v_cvt_pk_bf16_f32 v173, v194, v195\n\t" \
"v_cvt_pk_bf16_f32 v170, v188, v189\n\t" \
"v_cvt_pk_bf16_f32 v171, v190, v191\n\t" \
"v_cvt_pk_bf16_f32 v174, v196, v197\n\t" \
"v_cvt_pk_bf16_f32 v175, v198, v199\n\t" \
"v_permlane32_swap_b32 v168, v172\n\t" \
"v_permlane32_swap_b32 v169, v173\n\t" \
"v_permlane32_swap_b32 v170, v174\n\t" \
"v_permlane32_swap_b32 v171, v175\n\t" \
"v_cvt_pk_bf16_f32 v176, v200, v201\n\t" \
"v_cvt_pk_bf16_f32 v177, v202, v203\n\t" \
"v_permlane16_swap_b32 v168, v170\n\t" \
"v_permlane16_swap_b32 v169, v171\n\t" \
"v_min_u32 v128, -2, v168\n\t" \
"v_min_u32 v129, -2, v169\n\t" \
"v_min_u32 v130, -2, v170\n\t" \
"v_min_u32 v131, -2, v171\n\t" \
"buffer_store_dwordx4 v[128:131], %[vo0], %[rs], 0 offen sc1\n\t" \
"v_cvt_pk_bf16_f32 v180, v208, v209\n\t" \
"v_cvt_pk_bf16_f32 v181, v210, v211\n\t" \
"v_cvt_pk_bf16_f32 v178, v204, v205\n\t" \
"v_cvt_pk_bf16_f32 v179, v206, v207\n\t" \
"s_mov_b32 s90, -1\n\t" \
"s_mov_b32 s91, 0xffff\n\t" \
"v_permlane32_swap_b32 v176, v180\n\t" \
"v_permlane32_swap_b32 v177, v181\n\t" \
"s_nop 0\n\t" \
"v_permlane16_swap_b32 v176, v178\n\t" \
"v_permlane16_swap_b32 v177, v179\n\t" \
"v_min_u32 v132, -2, v176\n\t" \
"v_min_u32 v133, -2, v177\n\t" \
"v_min_u32 v134, -2, v178\n\t" \
"v_min_u32 v135, -2, v179\n\t" \
"s_mov_b64 exec, s[90:91]\n\t" \
"buffer_store_dwordx4 v[132:135], %[vo1], %[rs], 0 offen sc1\n\t" \
"s_mov_b64 exec, -1\n\t" \
"s_waitcnt lgkmcnt(0)\n\t" \
"s_mov_b32 %[nret], s92\n\t" \
"s_branch Le%=\n\t" \
"Lr%=:\n\t" \
"s_nop 7\n\t" \
"s_add_u32 s92, s92, 1\n\t" \
"s_cmp_lt_u32 s92, 0x40000\n\t" \
"s_cbranch_scc1 Lp%=\n\t" \
"s_branch Lok%=\n\t" \
"Le%=:\n\t" \
"s_nop 1\n\t"

#define ASM_H1_CLOB "v128", "v129", "v130", "v131", "v132", "v133", "v134", "v135", "v136", "v137", "v138", "v139", "v140", "v141", "v142", "v143", "v144", "v145", "v146", "v147", "v148", "v149", "v150", "v151", "v152", "v153", "v154", "v155", "v156", "v157", "v158", "v159", "v160", "v161", "v162", "v163", "v164", "v165", "v166", "v167", "v168", "v169", "v170", "v171", "v172", "v173", "v174", "v175", "v176", "v177", "v178", "v179", "v180", "v181", "v184", "v185", "v186", "v187", "v188", "v189", "v190", "v191", "v192", "v193", "v194", "v195", "v196", "v197", "v198", "v199", "v200", "v201", "v202", "v203", "v204", "v205", "v206", "v207", "v208", "v209", "v210", "v211", "v216", "v217", "s84", "s85", "s86", "s87", "s88", "s89", "s90", "s91", "s92", "s93", "vcc", "scc", "memory"

#define ASM_LW28_TEXT \
"s_mov_b32 s90, %[soff]\n\t" \
"buffer_load_dwordx4 a[0:3], %[voff], %[rs], s90 offen sc1\n\t" \
"buffer_load_dwordx4 a[4:7], %[voff], %[rs], s90 offen offset:1024 sc1\n\t" \
"s_add_u32 s90, s90, 2048\n\t" \
"buffer_load_dwordx4 a[8:11], %[voff], %[rs], s90 offen sc1\n\t" \
"buffer_load_dwordx4 a[12:15], %[voff], %[rs], s90 offen offset:1024 sc1\n\t" \
"s_add_u32 s90, s90, 2048\n\t" \
"buffer_load_dwordx4 a[16:19], %[voff], %[rs], s90 offen sc1\n\t" \
"buffer_load_dwordx4 a[20:23], %[voff], %[rs], s90 offen offset:1024 sc1\n\t" \
"s_add_u32 s90, s90, 2048\n\t" \
"buffer_load_dwordx4 a[24:27], %[voff], %[rs], s90 offen sc1\n\t" \
"buffer_load_dwordx4 a[28:31], %[voff], %[rs], s90 offen offset:1024 sc1\n\t" \
"s_add_u32 s90, s90, 2048\n\t" \
"buffer_load_dwordx4 a[32:35], %[voff], %[rs], s90 offen sc1\n\t" \
"buffer_load_dwordx4 a[36:39], %[voff], %[rs], s90 offen offset:1024 sc1\n\t" \
"s_add_u32 s90, s90, 2048\n\t" \
"buffer_load_dwordx4 a[40:43], %[voff], %[rs], s90 offen sc1\n\t" \
"buffer_load_dwordx4 a[44:47], %[voff], %[rs], s90 offen offset:1024 sc1\n\t" \
"s_add_u32 s90, s90, 2048\n\t" \
"buffer_load_dwordx4 a[48:51], %[voff], %[rs], s90 offen sc1\n\t" \
"buffer_load_dwordx4 a[52:55], %[voff], %[rs], s90 offen offset:1024 sc1\n\t" \
"s_add_u32 s90, s90, 2048\n\t" \
"buffer_load_dwordx4 a[56:59], %[voff], %[rs], s90 offen sc1\n\t" \
"buffer_load_dwordx4 a[60:63], %[voff], %[rs], s90 offen offset:1024 sc1\n\t" \
"s_add_u32 s90, s90, 2048\n\t" \
"buffer_load_dwordx4 a[64:67], %[voff], %[rs], s90 offen sc1\n\t" \
"buffer_load_dwordx4 a[68:71], %[voff], %[rs], s90 offen offset:1024 sc1\n\t" \
"s_add_u32 s90, s90, 2048\n\t" \
"buffer_load_dwordx4 a[72:75], %[voff], %[rs], s90 offen sc1\n\t" \
"buffer_load_dwordx4 a[76:79], %[voff], %[rs], s90 offen offset:1024 sc1\n\t" \
"s_add_u32 s90, s90, 2048\n\t" \
"buffer_load_dwordx4 a[80:83], %[voff], %[rs], s90 offen sc1\n\t" \
"buffer_load_dwordx4 a[84:87], %[voff], %[rs], s90 offen offset:1024 sc1\n\t" \
"s_add_u32 s90, s90, 2048\n\t" \
"buffer_load_dwordx4 a[88:91], %[voff], %[rs], s90 offen sc1\n\t" \
"buffer_load_dwordx4 a[92:95], %[voff], %[rs], s90 offen offset:1024 sc1\n\t" \
"s_add_u32 s90, s90, 2048\n\t" \
"buffer_load_dwordx4 a[96:99], %[voff], %[rs], s90 offen sc1\n\t" \
"buffer_load_dwordx4 a[100:103], %[voff], %[rs], s90 offen offset:1024 sc1\n\t" \
"s_add_u32 s90, s90, 2048\n\t" \
"buffer_load_dwordx4 a[104:107], %[voff], %[rs], s90 offen sc1\n\t" \
"buffer_load_dwordx4 a[108:111], %[voff], %[rs], s90 offen offset:1024 sc1\n\t" \
"s_add_u32 s90, s90, 2048\n\t" \
"buffer_load_dwordx4 a[112:115], %[voff], %[rs], s90 offen sc1\n\t" \
"buffer_load_dwordx4 a[116:119], %[voff], %[rs], s90 offen offset:1024 sc1\n\t" \
"s_add_u32 s90, s90, 2048\n\t" \
"buffer_load_dwordx4 a[120:123], %[voff], %[rs], s90 offen sc1\n\t" \
"buffer_load_dwordx4 a[124:127], %[voff], %[rs], s90 offen offset:1024 sc1\n\t" \
"s_add_u32 s90, s90, 2048\n\t" \
"buffer_load_dwordx4 a[128:131], %[voff], %[rs], s90 offen sc1\n\t" \
"buffer_load_dwordx4 a[132:135], %[voff], %[rs], s90 offen offset:1024 sc1\n\t" \
"s_add_u32 s90, s90, 2048\n\t" \
"buffer_load_dwordx4 a[136:139], %[voff], %[rs], s90 offen sc1\n\t" \
"buffer_load_dwordx4 a[140:143], %[voff], %[rs], s90 offen offset:1024 sc1\n\t" \
"s_add_u32 s90, s90, 2048\n\t" \
"buffer_load_dwordx4 a[144:147], %[voff], %[rs], s90 offen sc1\n\t" \
"buffer_load_dwordx4 a[148:151], %[voff], %[rs], s90 offen offset:1024 sc1\n\t" \
"s_add_u32 s90, s90, 2048\n\t" \
"buffer_load_dwordx4 a[152:155], %[voff], %[rs], s90 offen sc1\n\t" \
"buffer_load_dwordx4 a[156:159], %[voff], %[rs], s90 offen offset:1024 sc1\n\t" \
"s_add_u32 s90, s90, 2048\n\t" \
"buffer_load_dwordx4 a[160:163], %[voff], %[rs], s90 offen sc1\n\t" \
"buffer_load_dwordx4 a[164:167], %[voff], %[rs], s90 offen offset:1024 sc1\n\t" \
"s_add_u32 s90, s90, 2048\n\t" \
"buffer_load_dwordx4 a[168:171], %[voff], %[rs], s90 offen sc1\n\t" \
"buffer_load_dwordx4 a[172:175], %[voff], %[rs], s90 offen offset:1024 sc1\n\t" \
"s_add_u32 s90, s90, 2048\n\t" \
"buffer_load_dwordx4 a[176:179], %[voff], %[rs], s90 offen sc1\n\t" \
"buffer_load_dwordx4 a[180:183], %[voff], %[rs], s90 offen offset:1024 sc1\n\t" \
"s_add_u32 s90, s90, 2048\n\t" \
"buffer_load_dwordx4 a[184:187], %[voff], %[rs], s90 offen sc1\n\t" \
"buffer_load_dwordx4 a[188:191], %[voff], %[rs], s90 offen offset:1024 sc1\n\t" \
"s_add_u32 s90, s90, 2048\n\t" \
"buffer_load_dwordx4 a[192:195], %[voff], %[rs], s90 offen sc1\n\t" \
"buffer_load_dwordx4 a[196:199], %[voff], %[rs], s90 offen offset:1024 sc1\n\t" \
"s_add_u32 s90, s90, 2048\n\t" \
"buffer_load_dwordx4 a[200:203], %[voff], %[rs], s90 offen sc1\n\t" \
"buffer_load_dwordx4 a[204:207], %[voff], %[rs], s90 offen offset:1024 sc1\n\t" \
"s_add_u32 s90, s90, 2048\n\t" \
"buffer_load_dwordx4 a[208:211], %[voff], %[rs], s90 offen sc1\n\t" \
"buffer_load_dwordx4 a[212:215], %[voff], %[rs], s90 offen offset:1024 sc1\n\t" \
"s_add_u32 s90, s90, 2048\n\t" \
"buffer_load_dwordx4 a[216:219], %[voff], %[rs], s90 offen sc1\n\t" \
"buffer_load_dwordx4 a[220:223], %[voff], %[rs], s90 offen offset:1024 sc1\n\t" \
"s_add_u32 s90, s90, 2048\n\t" \
"s_waitcnt vmcnt(0)\n\t"

#define ASM_LW28_CLOB "a0", "a1", "a2", "a3", "a4", "a5", "a6", "a7", "a8", "a9", "a10", "a11", "a12", "a13", "a14", "a15", "a16", "a17", "a18", "a19", "a20", "a21", "a22", "a23", "a24", "a25", "a26", "a27", "a28", "a29", "a30", "a31", "a32", "a33", "a34", "a35", "a36", "a37", "a38", "a39", "a40", "a41", "a42", "a43", "a44", "a45", "a46", "a47", "a48", "a49", "a50", "a51", "a52", "a53", "a54", "a55", "a56", "a57", "a58", "a59", "a60", "a61", "a62", "a63", "a64", "a65", "a66", "a67", "a68", "a69", "a70", "a71", "a72", "a73", "a74", "a75", "a76", "a77", "a78", "a79", "a80", "a81", "a82", "a83", "a84", "a85", "a86", "a87", "a88", "a89", "a90", "a91", "a92", "a93", "a94", "a95", "a96", "a97", "a98", "a99", "a100", "a101", "a102", "a103", "a104", "a105", "a106", "a107", "a108", "a109", "a110", "a111", "a112", "a113", "a114", "a115", "a116", "a117", "a118", "a119", "a120", "a121", "a122", "a123", "a124", "a125", "a126", "a127", "a128", "a129", "a130", "a131", "a132", "a133", "a134", "a135", "a136", "a137", "a138", "a139", "a140", "a141", "a142", "a143", "a144", "a145", "a146", "a147", "a148", "a149", "a150", "a151", "a152", "a153", "a154", "a155", "a156", "a157", "a158", "a159", "a160", "a161", "a162", "a163", "a164", "a165", "a166", "a167", "a168", "a169", "a170", "a171", "a172", "a173", "a174", "a175", "a176", "a177", "a178", "a179", "a180", "a181", "a182", "a183", "a184", "a185", "a186", "a187", "a188", "a189", "a190", "a191", "a192", "a193", "a194", "a195", "a196", "a197", "a198", "a199", "a200", "a201", "a202", "a203", "a204", "a205", "a206", "a207", "a208", "a209", "a210", "a211", "a212", "a213", "a214", "a215", "a216", "a217", "a218", "a219", "a220", "a221", "a222", "a223", "s84", "s85", "s86", "s87", "s88", "s89", "s90", "s91", "s92", "s93", "vcc", "scc", "memory"

#define ASM_LW14_TEXT \
"s_mov_b32 s90, %[soff]\n\t" \
"buffer_load_dwordx4 a[0:3], %[voff], %[rs], s90 offen sc1\n\t" \
"buffer_load_dwordx4 a[4:7], %[voff], %[rs], s90 offen offset:1024 sc1\n\t" \
"s_add_u32 s90, s90, 2048\n\t" \
"buffer_load_dwordx4 a[8:11], %[voff], %[rs], s90 offen sc1\n\t" \
"buffer_load_dwordx4 a[12:15], %[voff], %[rs], s90 offen offset:1024 sc1\n\t" \
"s_add_u32 s90, s90, 2048\n\t" \
"buffer_load_dwordx4 a[16:19], %[voff], %[rs], s90 offen sc1\n\t" \
"buffer_load_dwordx4 a[20:23], %[voff], %[rs], s90 offen offset:1024 sc1\n\t" \
"s_add_u32 s90, s90, 2048\n\t" \
"buffer_load_dwordx4 a[24:27], %[voff], %[rs], s90 offen sc1\n\t" \
"buffer_load_dwordx4 a[28:31], %[voff], %[rs], s90 offen offset:1024 sc1\n\t" \
"s_add_u32 s90, s90, 2048\n\t" \
"buffer_load_dwordx4 a[32:35], %[voff], %[rs], s90 offen sc1\n\t" \
"buffer_load_dwordx4 a[36:39], %[voff], %[rs], s90 offen offset:1024 sc1\n\t" \
"s_add_u32 s90, s90, 2048\n\t" \
"buffer_load_dwordx4 a[40:43], %[voff], %[rs], s90 offen sc1\n\t" \
"buffer_load_dwordx4 a[44:47], %[voff], %[rs], s90 offen offset:1024 sc1\n\t" \
"s_add_u32 s90, s90, 2048\n\t" \
"buffer_load_dwordx4 a[48:51], %[voff], %[rs], s90 offen sc1\n\t" \
"buffer_load_dwordx4 a[52:55], %[voff], %[rs], s90 offen offset:1024 sc1\n\t" \
"s_add_u32 s90, s90, 2048\n\t" \
"buffer_load_dwordx4 a[56:59], %[voff], %[rs], s90 offen sc1\n\t" \
"buffer_load_dwordx4 a[60:63], %[voff], %[rs], s90 offen offset:1024 sc1\n\t" \
"s_add_u32 s90, s90, 2048\n\t" \
"buffer_load_dwordx4 a[64:67], %[voff], %[rs], s90 offen sc1\n\t" \
"buffer_load_dwordx4 a[68:71], %[voff], %[rs], s90 offen offset:1024 sc1\n\t" \
"s_add_u32 s90, s90, 2048\n\t" \
"buffer_load_dwordx4 a[72:75], %[voff], %[rs], s90 offen sc1\n\t" \
"buffer_load_dwordx4 a[76:79], %[voff], %[rs], s90 offen offset:1024 sc1\n\t" \
"s_add_u32 s90, s90, 2048\n\t" \
"buffer_load_dwordx4 a[80:83], %[voff], %[rs], s90 offen sc1\n\t" \
"buffer_load_dwordx4 a[84:87], %[voff], %[rs], s90 offen offset:1024 sc1\n\t" \
"s_add_u32 s90, s90, 2048\n\t" \
"buffer_load_dwordx4 a[88:91], %[voff], %[rs], s90 offen sc1\n\t" \
"buffer_load_dwordx4 a[92:95], %[voff], %[rs], s90 offen offset:1024 sc1\n\t" \
"s_add_u32 s90, s90, 2048\n\t" \
"buffer_load_dwordx4 a[96:99], %[voff], %[rs], s90 offen sc1\n\t" \
"buffer_load_dwordx4 a[100:103], %[voff], %[rs], s90 offen offset:1024 sc1\n\t" \
"s_add_u32 s90, s90, 2048\n\t" \
"buffer_load_dwordx4 a[104:107], %[voff], %[rs], s90 offen sc1\n\t" \
"buffer_load_dwordx4 a[108:111], %[voff], %[rs], s90 offen offset:1024 sc1\n\t" \
"s_add_u32 s90, s90, 2048\n\t" \
"s_waitcnt vmcnt(0)\n\t"

#define ASM_LW14_CLOB "a0", "a1", "a2", "a3", "a4", "a5", "a6", "a7", "a8", "a9", "a10", "a11", "a12", "a13", "a14", "a15", "a16", "a17", "a18", "a19", "a20", "a21", "a22", "a23", "a24", "a25", "a26", "a27", "a28", "a29", "a30", "a31", "a32", "a33", "a34", "a35", "a36", "a37", "a38", "a39", "a40", "a41", "a42", "a43", "a44", "a45", "a46", "a47", "a48", "a49", "a50", "a51", "a52", "a53", "a54", "a55", "a56", "a57", "a58", "a59", "a60", "a61", "a62", "a63", "a64", "a65", "a66", "a67", "a68", "a69", "a70", "a71", "a72", "a73", "a74", "a75", "a76", "a77", "a78", "a79", "a80", "a81", "a82", "a83", "a84", "a85", "a86", "a87", "a88", "a89", "a90", "a91", "a92", "a93", "a94", "a95", "a96", "a97", "a98", "a99", "a100", "a101", "a102", "a103", "a104", "a105", "a106", "a107", "a108", "a109", "a110", "a111", "s84", "s85", "s86", "s87", "s88", "s89", "s90", "s91", "s92", "s93", "vcc", "scc", "memory"
#define ASM_P3_0W0_TEXT \
"s_mov_b32 s93, m0\n\t" \
"s_add_u32 s89, %[src0], 172032\n\t" \
"s_mov_b32 s87, 86016\n\t" \
"s_waitcnt vmcnt(0)\n\t" \
"s_barrier\n\t" \
"v_mov_b32 v250, 1\n\t" \
"v_mov_b32 v248, %[lr]\n\t" \
"ds_read_b128 v[64:67], v248 offset:0\n\t" \
"ds_read_b128 v[68:71], v248 offset:14336\n\t" \
"ds_read_b128 v[72:75], v248 offset:1024\n\t" \
"ds_read_b128 v[76:79], v248 offset:15360\n\t" \
"ds_read_b128 v[80:83], v248 offset:2048\n\t" \
"ds_read_b128 v[84:87], v248 offset:16384\n\t" \
"ds_read_b128 v[88:91], v248 offset:3072\n\t" \
"ds_read_b128 v[92:95], v248 offset:17408\n\t" \
"ds_read_b128 v[96:99], v248 offset:4096\n\t" \
"ds_read_b128 v[100:103], v248 offset:18432\n\t" \
"ds_read_b128 v[104:107], v248 offset:5120\n\t" \
"ds_read_b128 v[108:111], v248 offset:19456\n\t" \
"ds_read_b128 v[112:115], v248 offset:6144\n\t" \
"ds_read_b128 v[116:119], v248 offset:20480\n\t" \
"s_mov_b32 s92, 0\n\t" \
"s_mov_b32 s91, %[so0]\n\t" \
"s_mov_b32 s94, %[ao0]\n\t" \
"s_waitcnt lgkmcnt(0)\n\t" \
"v_mfma_f32_16x16x32_bf16 v[176:179], a[0:3], v[64:67], 0\n\t" \
"v_mfma_f32_16x16x32_bf16 v[180:183], a[0:3], v[68:71], 0\n\t" \
"ds_read_b128 v[120:123], v248 offset:7168\n\t" \
"v_mfma_f32_16x16x32_bf16 v[184:187], a[56:59], v[64:67], 0\n\t" \
"v_mfma_f32_16x16x32_bf16 v[188:191], a[56:59], v[68:71], 0\n\t" \
"ds_read_b128 v[124:127], v248 offset:21504\n\t" \
"v_mfma_f32_16x16x32_bf16 v[192:195], a[112:115], v[64:67], 0\n\t" \
"v_mfma_f32_16x16x32_bf16 v[196:199], a[112:115], v[68:71], 0\n\t" \
"ds_read_b128 v[128:131], v248 offset:8192\n\t" \
"v_mfma_f32_16x16x32_bf16 v[200:203], a[168:171], v[64:67], 0\n\t" \
"v_mfma_f32_16x16x32_bf16 v[204:207], a[168:171], v[68:71], 0\n\t" \
"ds_read_b128 v[132:135], v248 offset:22528\n\t" \
"v_mfma_f32_16x16x32_bf16 v[176:179], a[4:7], v[72:75], v[176:179]\n\t" \
"v_mfma_f32_16x16x32_bf16 v[180:183], a[4:7], v[76:79], v[180:183]\n\t" \
"ds_read_b128 v[136:139], v248 offset:9216\n\t" \
"v_mfma_f32_16x16x32_bf16 v[184:187], a[60:63], v[72:75], v[184:187]\n\t" \
"v_mfma_f32_16x16x32_bf16 v[188:191], a[60:63], v[76:79], v[188:191]\n\t" \
"ds_read_b128 v[140:143], v248 offset:23552\n\t" \
"v_mfma_f32_16x16x32_bf16 v[192:195], a[116:119], v[72:75], v[192:195]\n\t" \
"v_mfma_f32_16x16x32_bf16 v[196:199], a[116:119], v[76:79], v[196:199]\n\t" \
"ds_read_b128 v[144:147], v248 offset:10240\n\t" \
"v_mfma_f32_16x16x32_bf16 v[200:203], a[172:175], v[72:75], v[200:203]\n\t" \
"v_mfma_f32_16x16x32_bf16 v[204:207], a[172:175], v[76:79], v[204:207]\n\t" \
"ds_read_b128 v[148:151], v248 offset:24576\n\t" \
"v_mfma_f32_16x16x32_bf16 v[176:179], a[8:11], v[80:83], v[176:179]\n\t" \
"v_mfma_f32_16x16x32_bf16 v[180:183], a[8:11], v[84:87], v[180:183]\n\t" \
"ds_read_b128 v[152:155], v248 offset:11264\n\t" \
"v_mfma_f32_16x16x32_bf16 v[184:187], a[64:67], v[80:83], v[184:187]\n\t" \
"v_mfma_f32_16x16x32_bf16 v[188:191], a[64:67], v[84:87], v[188:191]\n\t" \
"ds_read_b128 v[156:159], v248 offset:25600\n\t" \
"s_add_u32 s90, s87, %[lg]\n\t" \
"s_add_u32 s90, s90, 0\n\t" \
"s_mov_b32 m0, s90\n\t" \
"s_add_u32 s95, s89, 0\n\t" \
"buffer_load_dwordx4 %[vg], %[rh], s95 offen offset:2048 lds\n\t" \
"v_mfma_f32_16x16x32_bf16 v[192:195], a[120:123], v[80:83], v[192:195]\n\t" \
"v_mfma_f32_16x16x32_bf16 v[196:199], a[120:123], v[84:87], v[196:199]\n\t" \
"ds_read_b128 v[160:163], v248 offset:12288\n\t" \
"v_mfma_f32_16x16x32_bf16 v[200:203], a[176:179], v[80:83], v[200:203]\n\t" \
"v_mfma_f32_16x16x32_bf16 v[204:207], a[176:179], v[84:87], v[204:207]\n\t" \
"ds_read_b128 v[164:167], v248 offset:26624\n\t" \
"v_mfma_f32_16x16x32_bf16 v[176:179], a[12:15], v[88:91], v[176:179]\n\t" \
"v_mfma_f32_16x16x32_bf16 v[180:183], a[12:15], v[92:95], v[180:183]\n\t" \
"ds_read_b128 v[168:171], v248 offset:13312\n\t" \
"v_mfma_f32_16x16x32_bf16 v[184:187], a[68:71], v[88:91], v[184:187]\n\t" \
"v_mfma_f32_16x16x32_bf16 v[188:191], a[68:71], v[92:95], v[188:191]\n\t" \
"ds_read_b128 v[172:175], v248 offset:27648\n\t" \
"v_mfma_f32_16x16x32_bf16 v[192:195], a[124:127], v[88:91], v[192:195]\n\t" \
"v_mfma_f32_16x16x32_bf16 v[196:199], a[124:127], v[92:95], v[196:199]\n\t" \
"v_mfma_f32_16x16x32_bf16 v[200:203], a[180:183], v[88:91], v[200:203]\n\t" \
"v_mfma_f32_16x16x32_bf16 v[204:207], a[180:183], v[92:95], v[204:207]\n\t" \
"v_mfma_f32_16x16x32_bf16 v[176:179], a[16:19], v[96:99], v[176:179]\n\t" \
"v_mfma_f32_16x16x32_bf16 v[180:183], a[16:19], v[100:103], v[180:183]\n\t" \
"v_mfma_f32_16x16x32_bf16 v[184:187], a[72:75], v[96:99], v[184:187]\n\t" \
"v_mfma_f32_16x16x32_bf16 v[188:191], a[72:75], v[100:103], v[188:191]\n\t" \
"v_mfma_f32_16x16x32_bf16 v[192:195], a[128:131], v[96:99], v[192:195]\n\t" \
"v_mfma_f32_16x16x32_bf16 v[196:199], a[128:131], v[100:103], v[196:199]\n\t" \
"v_mfma_f32_16x16x32_bf16 v[200:203], a[184:187], v[96:99], v[200:203]\n\t" \
"v_mfma_f32_16x16x32_bf16 v[204:207], a[184:187], v[100:103], v[204:207]\n\t" \
"buffer_load_dwordx4 %[vg], %[rh], s95 offen offset:3072 lds\n\t" \
"v_mfma_f32_16x16x32_bf16 v[176:179], a[20:23], v[104:107], v[176:179]\n\t" \
"v_mfma_f32_16x16x32_bf16 v[180:183], a[20:23], v[108:111], v[180:183]\n\t" \
"v_mfma_f32_16x16x32_bf16 v[184:187], a[76:79], v[104:107], v[184:187]\n\t" \
"v_mfma_f32_16x16x32_bf16 v[188:191], a[76:79], v[108:111], v[188:191]\n\t" \
"v_mfma_f32_16x16x32_bf16 v[192:195], a[132:135], v[104:107], v[192:195]\n\t" \
"v_mfma_f32_16x16x32_bf16 v[196:199], a[132:135], v[108:111], v[196:199]\n\t" \
"v_mfma_f32_16x16x32_bf16 v[200:203], a[188:191], v[104:107], v[200:203]\n\t" \
"v_mfma_f32_16x16x32_bf16 v[204:207], a[188:191], v[108:111], v[204:207]\n\t" \
"v_mfma_f32_16x16x32_bf16 v[176:179], a[24:27], v[112:115], v[176:179]\n\t" \
"v_mfma_f32_16x16x32_bf16 v[180:183], a[24:27], v[116:119], v[180:183]\n\t" \
"v_mfma_f32_16x16x32_bf16 v[184:187], a[80:83], v[112:115], v[184:187]\n\t" \
"v_mfma_f32_16x16x32_bf16 v[188:191], a[80:83], v[116:119], v[188:191]\n\t" \
"v_mfma_f32_16x16x32_bf16 v[192:195], a[136:139], v[112:115], v[192:195]\n\t" \
"v_mfma_f32_16x16x32_bf16 v[196:199], a[136:139], v[116:119], v[196:199]\n\t" \
"v_mfma_f32_16x16x32_bf16 v[200:203], a[192:195], v[112:115], v[200:203]\n\t" \
"v_mfma_f32_16x16x32_bf16 v[204:207], a[192:195], v[116:119], v[204:207]\n\t" \
"s_add_u32 s89, s89, 57344\n\t" \
"s_min_u32 s89, s89, 1806336\n\t" \
"s_mov_b32 s87, s92\n\t" \
"s_add_u32 s95, s92, 28672\n\t" \
"s_cmp_eq_u32 s95, 114688\n\t" \
"s_cselect_b32 s95, 0, s95\n\t" \
"v_add_u32 v249, s95, %[lr]\n\t" \
"s_waitcnt vmcnt(63) lgkmcnt(0)\n\t" \
"s_barrier\n\t" \
"v_mfma_f32_16x16x32_bf16 v[176:179], a[28:31], v[120:123], v[176:179]\n\t" \
"ds_read_b128 v[64:67], v249 offset:0\n\t" \
"v_mfma_f32_16x16x32_bf16 v[180:183], a[28:31], v[124:127], v[180:183]\n\t" \
"v_mfma_f32_16x16x32_bf16 v[184:187], a[84:87], v[120:123], v[184:187]\n\t" \
"ds_read_b128 v[68:71], v249 offset:14336\n\t" \
"v_mfma_f32_16x16x32_bf16 v[188:191], a[84:87], v[124:127], v[188:191]\n\t" \
"v_mfma_f32_16x16x32_bf16 v[192:195], a[140:143], v[120:123], v[192:195]\n\t" \
"ds_read_b128 v[72:75], v249 offset:1024\n\t" \
"v_mfma_f32_16x16x32_bf16 v[196:199], a[140:143], v[124:127], v[196:199]\n\t" \
"v_mfma_f32_16x16x32_bf16 v[200:203], a[196:199], v[120:123], v[200:203]\n\t" \
"ds_read_b128 v[76:79], v249 offset:15360\n\t" \
"v_mfma_f32_16x16x32_bf16 v[204:207], a[196:199], v[124:127], v[204:207]\n\t" \
"v_mfma_f32_16x16x32_bf16 v[176:179], a[32:35], v[128:131], v[176:179]\n\t" \
"ds_read_b128 v[80:83], v249 offset:2048\n\t" \
"v_mfma_f32_16x16x32_bf16 v[180:183], a[32:35], v[132:135], v[180:183]\n\t" \
"v_mfma_f32_16x16x32_bf16 v[184:187], a[88:91], v[128:131], v[184:187]\n\t" \
"ds_read_b128 v[84:87], v249 offset:16384\n\t" \
"v_mfma_f32_16x16x32_bf16 v[188:191], a[88:91], v[132:135], v[188:191]\n\t" \
"v_mfma_f32_16x16x32_bf16 v[192:195], a[144:147], v[128:131], v[192:195]\n\t" \
"ds_read_b128 v[88:91], v249 offset:3072\n\t" \
"v_mfma_f32_16x16x32_bf16 v[196:199], a[144:147], v[132:135], v[196:199]\n\t" \
"v_mfma_f32_16x16x32_bf16 v[200:203], a[200:203], v[128:131], v[200:203]\n\t" \
"ds_read_b128 v[92:95], v249 offset:17408\n\t" \
"v_mfma_f32_16x16x32_bf16 v[204:207], a[200:203], v[132:135], v[204:207]\n\t" \
"v_mfma_f32_16x16x32_bf16 v[176:179], a[36:39], v[136:139], v[176:179]\n\t" \
"ds_read_b128 v[96:99], v249 offset:4096\n\t" \
"v_mfma_f32_16x16x32_bf16 v[180:183], a[36:39], v[140:143], v[180:183]\n\t" \
"v_mfma_f32_16x16x32_bf16 v[184:187], a[92:95], v[136:139], v[184:187]\n\t" \
"ds_read_b128 v[100:103], v249 offset:18432\n\t" \
"v_mfma_f32_16x16x32_bf16 v[188:191], a[92:95], v[140:143], v[188:191]\n\t" \
"s_add_u32 s90, s87, %[lg]\n\t" \
"s_add_u32 s90, s90, 0\n\t" \
"s_mov_b32 m0, s90\n\t" \
"s_add_u32 s95, s89, 0\n\t" \
"buffer_load_dwordx4 %[vg], %[rh], s95 offen offset:0 lds\n\t" \
"v_mfma_f32_16x16x32_bf16 v[192:195], a[148:151], v[136:139], v[192:195]\n\t" \
"ds_read_b128 v[104:107], v249 offset:5120\n\t" \
"v_mfma_f32_16x16x32_bf16 v[196:199], a[148:151], v[140:143], v[196:199]\n\t" \
"v_mfma_f32_16x16x32_bf16 v[200:203], a[204:207], v[136:139], v[200:203]\n\t" \
"ds_read_b128 v[108:111], v249 offset:19456\n\t" \
"v_mfma_f32_16x16x32_bf16 v[204:207], a[204:207], v[140:143], v[204:207]\n\t" \
"v_mfma_f32_16x16x32_bf16 v[176:179], a[40:43], v[144:147], v[176:179]\n\t" \
"ds_read_b128 v[112:115], v249 offset:6144\n\t" \
"v_mfma_f32_16x16x32_bf16 v[180:183], a[40:43], v[148:151], v[180:183]\n\t" \
"v_mfma_f32_16x16x32_bf16 v[184:187], a[96:99], v[144:147], v[184:187]\n\t" \
"ds_read_b128 v[116:119], v249 offset:20480\n\t" \
"v_mfma_f32_16x16x32_bf16 v[188:191], a[96:99], v[148:151], v[188:191]\n\t" \
"v_mfma_f32_16x16x32_bf16 v[192:195], a[152:155], v[144:147], v[192:195]\n\t" \
"v_mfma_f32_16x16x32_bf16 v[196:199], a[152:155], v[148:151], v[196:199]\n\t" \
"v_mfma_f32_16x16x32_bf16 v[200:203], a[208:211], v[144:147], v[200:203]\n\t" \
"v_mfma_f32_16x16x32_bf16 v[204:207], a[208:211], v[148:151], v[204:207]\n\t" \
"v_mfma_f32_16x16x32_bf16 v[176:179], a[44:47], v[152:155], v[176:179]\n\t" \
"v_mfma_f32_16x16x32_bf16 v[180:183], a[44:47], v[156:159], v[180:183]\n\t" \
"v_mfma_f32_16x16x32_bf16 v[184:187], a[100:103], v[152:155], v[184:187]\n\t" \
"v_mfma_f32_16x16x32_bf16 v[188:191], a[100:103], v[156:159], v[188:191]\n\t" \
"v_mfma_f32_16x16x32_bf16 v[192:195], a[156:159], v[152:155], v[192:195]\n\t" \
"v_mfma_f32_16x16x32_bf16 v[196:199], a[156:159], v[156:159], v[196:199]\n\t" \
"v_mfma_f32_16x16x32_bf16 v[200:203], a[212:215], v[152:155], v[200:203]\n\t" \
"v_mfma_f32_16x16x32_bf16 v[204:207], a[212:215], v[156:159], v[204:207]\n\t" \
"buffer_load_dwordx4 %[vg], %[rh], s95 offen offset:1024 lds\n\t" \
"v_mfma_f32_16x16x32_bf16 v[176:179], a[48:51], v[160:163], v[176:179]\n\t" \
"v_mfma_f32_16x16x32_bf16 v[180:183], a[48:51], v[164:167], v[180:183]\n\t" \
"v_mfma_f32_16x16x32_bf16 v[184:187], a[104:107], v[160:163], v[184:187]\n\t" \
"v_mfma_f32_16x16x32_bf16 v[188:191], a[104:107], v[164:167], v[188:191]\n\t" \
"v_mfma_f32_16x16x32_bf16 v[192:195], a[160:163], v[160:163], v[192:195]\n\t" \
"v_mfma_f32_16x16x32_bf16 v[196:199], a[160:163], v[164:167], v[196:199]\n\t" \
"v_mfma_f32_16x16x32_bf16 v[200:203], a[216:219], v[160:163], v[200:203]\n\t" \
"v_mfma_f32_16x16x32_bf16 v[204:207], a[216:219], v[164:167], v[204:207]\n\t" \
"v_mfma_f32_16x16x32_bf16 v[176:179], a[52:55], v[168:171], v[176:179]\n\t" \
"v_mfma_f32_16x16x32_bf16 v[180:183], a[52:55], v[172:175], v[180:183]\n\t" \
"v_mfma_f32_16x16x32_bf16 v[184:187], a[108:111], v[168:171], v[184:187]\n\t" \
"v_mfma_f32_16x16x32_bf16 v[188:191], a[108:111], v[172:175], v[188:191]\n\t" \
"v_mfma_f32_16x16x32_bf16 v[192:195], a[164:167], v[168:171], v[192:195]\n\t" \
"v_mfma_f32_16x16x32_bf16 v[196:199], a[164:167], v[172:175], v[196:199]\n\t" \
"v_mfma_f32_16x16x32_bf16 v[200:203], a[220:223], v[168:171], v[200:203]\n\t" \
"v_mfma_f32_16x16x32_bf16 v[204:207], a[220:223], v[172:175], v[204:207]\n\t" \
"s_add_u32 s92, s92, 28672\n\t" \
"s_cmp_eq_u32 s92, 114688\n\t" \
"s_cselect_b32 s92, 0, s92\n\t" \
"v_mov_b32 v248, v249\n\t" \
"s_add_u32 s91, s91, 917504\n\t" \
"v_cvt_pk_bf16_f32 v240, v176, v177\n\t" \
"v_cvt_pk_bf16_f32 v241, v178, v179\n\t" \
"buffer_store_dwordx2 v[240:241], %[vs0], %[ro], s91 offen offset:0 sc1\n\t" \
"v_cvt_pk_bf16_f32 v244, v180, v181\n\t" \
"v_cvt_pk_bf16_f32 v245, v182, v183\n\t" \
"buffer_store_dwordx2 v[244:245], %[vs1], %[ro], s91 offen offset:0 sc1\n\t" \
"v_cvt_pk_bf16_f32 v240, v184, v185\n\t" \
"v_cvt_pk_bf16_f32 v241, v186, v187\n\t" \
"buffer_store_dwordx2 v[240:241], %[vs0], %[ro], s91 offen offset:512 sc1\n\t" \
"v_cvt_pk_bf16_f32 v244, v188, v189\n\t" \
"v_cvt_pk_bf16_f32 v245, v190, v191\n\t" \
"buffer_store_dwordx2 v[244:245], %[vs1], %[ro], s91 offen offset:512 sc1\n\t" \
"v_cvt_pk_bf16_f32 v240, v192, v193\n\t" \
"v_cvt_pk_bf16_f32 v241, v194, v195\n\t" \
"buffer_store_dwordx2 v[240:241], %[vs0], %[ro], s91 offen offset:1024 sc1\n\t" \
"v_cvt_pk_bf16_f32 v244, v196, v197\n\t" \
"v_cvt_pk_bf16_f32 v245, v198, v199\n\t" \
"buffer_store_dwordx2 v[244:245], %[vs1], %[ro], s91 offen offset:1024 sc1\n\t" \
"v_cvt_pk_bf16_f32 v240, v200, v201\n\t" \
"v_cvt_pk_bf16_f32 v241, v202, v203\n\t" \
"buffer_store_dwordx2 v[240:241], %[vs0], %[ro], s91 offen offset:1536 sc1\n\t" \
"v_cvt_pk_bf16_f32 v244, v204, v205\n\t" \
"v_cvt_pk_bf16_f32 v245, v206, v207\n\t" \
"buffer_store_dwordx2 v[244:245], %[vs1], %[ro], s91 offen offset:1536 sc1\n\t" \
"s_waitcnt vmcnt(0)\n\t" \
"s_barrier\n\t" \
"s_mov_b64 exec, 1\n\t" \
"buffer_atomic_add v250, off, %[rc], s94\n\t" \
"s_mov_b64 exec, -1\n\t" \
"s_add_u32 s94, s94, 128\n\t" \
"s_waitcnt lgkmcnt(0)\n\t" \
"v_mfma_f32_16x16x32_bf16 v[208:211], a[0:3], v[64:67], 0\n\t" \
"v_mfma_f32_16x16x32_bf16 v[212:215], a[0:3], v[68:71], 0\n\t" \
"ds_read_b128 v[120:123], v248 offset:7168\n\t" \
"v_mfma_f32_16x16x32_bf16 v[216:219], a[56:59], v[64:67], 0\n\t" \
"v_mfma_f32_16x16x32_bf16 v[220:223], a[56:59], v[68:71], 0\n\t" \
"ds_read_b128 v[124:127], v248 offset:21504\n\t" \
"v_mfma_f32_16x16x32_bf16 v[224:227], a[112:115], v[64:67], 0\n\t" \
"v_mfma_f32_16x16x32_bf16 v[228:231], a[112:115], v[68:71], 0\n\t" \
"ds_read_b128 v[128:131], v248 offset:8192\n\t" \
"v_mfma_f32_16x16x32_bf16 v[232:235], a[168:171], v[64:67], 0\n\t" \
"v_mfma_f32_16x16x32_bf16 v[236:239], a[168:171], v[68:71], 0\n\t" \
"ds_read_b128 v[132:135], v248 offset:22528\n\t" \
"v_mfma_f32_16x16x32_bf16 v[208:211], a[4:7], v[72:75], v[208:211]\n\t" \
"v_mfma_f32_16x16x32_bf16 v[212:215], a[4:7], v[76:79], v[212:215]\n\t" \
"ds_read_b128 v[136:139], v248 offset:9216\n\t" \
"v_mfma_f32_16x16x32_bf16 v[216:219], a[60:63], v[72:75], v[216:219]\n\t" \
"v_mfma_f32_16x16x32_bf16 v[220:223], a[60:63], v[76:79], v[220:223]\n\t" \
"ds_read_b128 v[140:143], v248 offset:23552\n\t" \
"v_mfma_f32_16x16x32_bf16 v[224:227], a[116:119], v[72:75], v[224:227]\n\t" \
"v_mfma_f32_16x16x32_bf16 v[228:231], a[116:119], v[76:79], v[228:231]\n\t" \
"ds_read_b128 v[144:147], v248 offset:10240\n\t" \
"v_mfma_f32_16x16x32_bf16 v[232:235], a[172:175], v[72:75], v[232:235]\n\t" \
"v_mfma_f32_16x16x32_bf16 v[236:239], a[172:175], v[76:79], v[236:239]\n\t" \
"ds_read_b128 v[148:151], v248 offset:24576\n\t" \
"v_mfma_f32_16x16x32_bf16 v[208:211], a[8:11], v[80:83], v[208:211]\n\t" \
"v_mfma_f32_16x16x32_bf16 v[212:215], a[8:11], v[84:87], v[212:215]\n\t" \
"ds_read_b128 v[152:155], v248 offset:11264\n\t" \
"v_mfma_f32_16x16x32_bf16 v[216:219], a[64:67], v[80:83], v[216:219]\n\t" \
"v_mfma_f32_16x16x32_bf16 v[220:223], a[64:67], v[84:87], v[220:223]\n\t" \
"ds_read_b128 v[156:159], v248 offset:25600\n\t" \
"s_add_u32 s90, s87, %[lg]\n\t" \
"s_add_u32 s90, s90, 0\n\t" \
"s_mov_b32 m0, s90\n\t" \
"s_add_u32 s95, s89, 0\n\t" \
"buffer_load_dwordx4 %[vg], %[rh], s95 offen offset:2048 lds\n\t" \
"v_mfma_f32_16x16x32_bf16 v[224:227], a[120:123], v[80:83], v[224:227]\n\t" \
"v_mfma_f32_16x16x32_bf16 v[228:231], a[120:123], v[84:87], v[228:231]\n\t" \
"ds_read_b128 v[160:163], v248 offset:12288\n\t" \
"v_mfma_f32_16x16x32_bf16 v[232:235], a[176:179], v[80:83], v[232:235]\n\t" \
"v_mfma_f32_16x16x32_bf16 v[236:239], a[176:179], v[84:87], v[236:239]\n\t" \
"ds_read_b128 v[164:167], v248 offset:26624\n\t" \
"v_mfma_f32_16x16x32_bf16 v[208:211], a[12:15], v[88:91], v[208:211]\n\t" \
"v_mfma_f32_16x16x32_bf16 v[212:215], a[12:15], v[92:95], v[212:215]\n\t" \
"ds_read_b128 v[168:171], v248 offset:13312\n\t" \
"v_mfma_f32_16x16x32_bf16 v[216:219], a[68:71], v[88:91], v[216:219]\n\t" \
"v_mfma_f32_16x16x32_bf16 v[220:223], a[68:71], v[92:95], v[220:223]\n\t" \
"ds_read_b128 v[172:175], v248 offset:27648\n\t" \
"v_mfma_f32_16x16x32_bf16 v[224:227], a[124:127], v[88:91], v[224:227]\n\t" \
"v_mfma_f32_16x16x32_bf16 v[228:231], a[124:127], v[92:95], v[228:231]\n\t" \
"v_mfma_f32_16x16x32_bf16 v[232:235], a[180:183], v[88:91], v[232:235]\n\t" \
"v_mfma_f32_16x16x32_bf16 v[236:239], a[180:183], v[92:95], v[236:239]\n\t" \
"v_mfma_f32_16x16x32_bf16 v[208:211], a[16:19], v[96:99], v[208:211]\n\t" \
"v_mfma_f32_16x16x32_bf16 v[212:215], a[16:19], v[100:103], v[212:215]\n\t" \
"v_mfma_f32_16x16x32_bf16 v[216:219], a[72:75], v[96:99], v[216:219]\n\t" \
"v_mfma_f32_16x16x32_bf16 v[220:223], a[72:75], v[100:103], v[220:223]\n\t" \
"v_mfma_f32_16x16x32_bf16 v[224:227], a[128:131], v[96:99], v[224:227]\n\t" \
"v_mfma_f32_16x16x32_bf16 v[228:231], a[128:131], v[100:103], v[228:231]\n\t" \
"v_mfma_f32_16x16x32_bf16 v[232:235], a[184:187], v[96:99], v[232:235]\n\t" \
"v_mfma_f32_16x16x32_bf16 v[236:239], a[184:187], v[100:103], v[236:239]\n\t" \
"buffer_load_dwordx4 %[vg], %[rh], s95 offen offset:3072 lds\n\t" \
"v_mfma_f32_16x16x32_bf16 v[208:211], a[20:23], v[104:107], v[208:211]\n\t" \
"v_mfma_f32_16x16x32_bf16 v[212:215], a[20:23], v[108:111], v[212:215]\n\t" \
"v_mfma_f32_16x16x32_bf16 v[216:219], a[76:79], v[104:107], v[216:219]\n\t" \
"v_mfma_f32_16x16x32_bf16 v[220:223], a[76:79], v[108:111], v[220:223]\n\t" \
"v_mfma_f32_16x16x32_bf16 v[224:227], a[132:135], v[104:107], v[224:227]\n\t" \
"v_mfma_f32_16x16x32_bf16 v[228:231], a[132:135], v[108:111], v[228:231]\n\t" \
"v_mfma_f32_16x16x32_bf16 v[232:235], a[188:191], v[104:107], v[232:235]\n\t" \
"v_mfma_f32_16x16x32_bf16 v[236:239], a[188:191], v[108:111], v[236:239]\n\t" \
"v_mfma_f32_16x16x32_bf16 v[208:211], a[24:27], v[112:115], v[208:211]\n\t" \
"v_mfma_f32_16x16x32_bf16 v[212:215], a[24:27], v[116:119], v[212:215]\n\t" \
"v_mfma_f32_16x16x32_bf16 v[216:219], a[80:83], v[112:115], v[216:219]\n\t" \
"v_mfma_f32_16x16x32_bf16 v[220:223], a[80:83], v[116:119], v[220:223]\n\t" \
"v_mfma_f32_16x16x32_bf16 v[224:227], a[136:139], v[112:115], v[224:227]\n\t" \
"v_mfma_f32_16x16x32_bf16 v[228:231], a[136:139], v[116:119], v[228:231]\n\t" \
"v_mfma_f32_16x16x32_bf16 v[232:235], a[192:195], v[112:115], v[232:235]\n\t" \
"v_mfma_f32_16x16x32_bf16 v[236:239], a[192:195], v[116:119], v[236:239]\n\t" \
"s_add_u32 s89, s89, 57344\n\t" \
"s_min_u32 s89, s89, 1806336\n\t" \
"s_mov_b32 s87, s92\n\t" \
"s_add_u32 s95, s92, 28672\n\t" \
"s_cmp_eq_u32 s95, 114688\n\t" \
"s_cselect_b32 s95, 0, s95\n\t" \
"v_add_u32 v249, s95, %[lr]\n\t" \
"s_waitcnt vmcnt(63) lgkmcnt(0)\n\t" \
"s_barrier\n\t" \
"v_mfma_f32_16x16x32_bf16 v[208:211], a[28:31], v[120:123], v[208:211]\n\t" \
"ds_read_b128 v[64:67], v249 offset:0\n\t" \
"v_mfma_f32_16x16x32_bf16 v[212:215], a[28:31], v[124:127], v[212:215]\n\t" \
"v_mfma_f32_16x16x32_bf16 v[216:219], a[84:87], v[120:123], v[216:219]\n\t" \
"ds_read_b128 v[68:71], v249 offset:14336\n\t" \
"v_mfma_f32_16x16x32_bf16 v[220:223], a[84:87], v[124:127], v[220:223]\n\t" \
"v_mfma_f32_16x16x32_bf16 v[224:227], a[140:143], v[120:123], v[224:227]\n\t" \
"ds_read_b128 v[72:75], v249 offset:1024\n\t" \
"v_mfma_f32_16x16x32_bf16 v[228:231], a[140:143], v[124:127], v[228:231]\n\t" \
"v_mfma_f32_16x16x32_bf16 v[232:235], a[196:199], v[120:123], v[232:235]\n\t" \
"ds_read_b128 v[76:79], v249 offset:15360\n\t" \
"v_mfma_f32_16x16x32_bf16 v[236:239], a[196:199], v[124:127], v[236:239]\n\t" \
"v_mfma_f32_16x16x32_bf16 v[208:211], a[32:35], v[128:131], v[208:211]\n\t" \
"ds_read_b128 v[80:83], v249 offset:2048\n\t" \
"v_mfma_f32_16x16x32_bf16 v[212:215], a[32:35], v[132:135], v[212:215]\n\t" \
"v_mfma_f32_16x16x32_bf16 v[216:219], a[88:91], v[128:131], v[216:219]\n\t" \
"ds_read_b128 v[84:87], v249 offset:16384\n\t" \
"v_mfma_f32_16x16x32_bf16 v[220:223], a[88:91], v[132:135], v[220:223]\n\t" \
"v_mfma_f32_16x16x32_bf16 v[224:227], a[144:147], v[128:131], v[224:227]\n\t" \
"ds_read_b128 v[88:91], v249 offset:3072\n\t" \
"v_mfma_f32_16x16x32_bf16 v[228:231], a[144:147], v[132:135], v[228:231]\n\t" \
"v_mfma_f32_16x16x32_bf16 v[232:235], a[200:203], v[128:131], v[232:235]\n\t" \
"ds_read_b128 v[92:95], v249 offset:17408\n\t" \
"v_mfma_f32_16x16x32_bf16 v[236:239], a[200:203], v[132:135], v[236:239]\n\t" \
"v_mfma_f32_16x16x32_bf16 v[208:211], a[36:39], v[136:139], v[208:211]\n\t" \
"ds_read_b128 v[96:99], v249 offset:4096\n\t" \
"v_mfma_f32_16x16x32_bf16 v[212:215], a[36:39], v[140:143], v[212:215]\n\t" \
"v_mfma_f32_16x16x32_bf16 v[216:219], a[92:95], v[136:139], v[216:219]\n\t" \
"ds_read_b128 v[100:103], v249 offset:18432\n\t" \
"v_mfma_f32_16x16x32_bf16 v[220:223], a[92:95], v[140:143], v[220:223]\n\t" \
"s_add_u32 s90, s87, %[lg]\n\t" \
"s_add_u32 s90, s90, 0\n\t" \
"s_mov_b32 m0, s90\n\t" \
"s_add_u32 s95, s89, 0\n\t" \
"buffer_load_dwordx4 %[vg], %[rh], s95 offen offset:0 lds\n\t" \
"v_mfma_f32_16x16x32_bf16 v[224:227], a[148:151], v[136:139], v[224:227]\n\t" \
"ds_read_b128 v[104:107], v249 offset:5120\n\t" \
"v_mfma_f32_16x16x32_bf16 v[228:231], a[148:151], v[140:143], v[228:231]\n\t" \
"v_mfma_f32_16x16x32_bf16 v[232:235], a[204:207], v[136:139], v[232:235]\n\t" \
"ds_read_b128 v[108:111], v249 offset:19456\n\t" \
"v_mfma_f32_16x16x32_bf16 v[236:239], a[204:207], v[140:143], v[236:239]\n\t" \
"v_mfma_f32_16x16x32_bf16 v[208:211], a[40:43], v[144:147], v[208:211]\n\t" \
"ds_read_b128 v[112:115], v249 offset:6144\n\t" \
"v_mfma_f32_16x16x32_bf16 v[212:215], a[40:43], v[148:151], v[212:215]\n\t" \
"v_mfma_f32_16x16x32_bf16 v[216:219], a[96:99], v[144:147], v[216:219]\n\t" \
"ds_read_b128 v[116:119], v249 offset:20480\n\t" \
"v_mfma_f32_16x16x32_bf16 v[220:223], a[96:99], v[148:151], v[220:223]\n\t" \
"v_mfma_f32_16x16x32_bf16 v[224:227], a[152:155], v[144:147], v[224:227]\n\t" \
"v_mfma_f32_16x16x32_bf16 v[228:231], a[152:155], v[148:151], v[228:231]\n\t" \
"v_mfma_f32_16x16x32_bf16 v[232:235], a[208:211], v[144:147], v[232:235]\n\t" \
"v_mfma_f32_16x16x32_bf16 v[236:239], a[208:211], v[148:151], v[236:239]\n\t" \
"v_mfma_f32_16x16x32_bf16 v[208:211], a[44:47], v[152:155], v[208:211]\n\t" \
"v_mfma_f32_16x16x32_bf16 v[212:215], a[44:47], v[156:159], v[212:215]\n\t" \
"v_mfma_f32_16x16x32_bf16 v[216:219], a[100:103], v[152:155], v[216:219]\n\t" \
"v_mfma_f32_16x16x32_bf16 v[220:223], a[100:103], v[156:159], v[220:223]\n\t" \
"v_mfma_f32_16x16x32_bf16 v[224:227], a[156:159], v[152:155], v[224:227]\n\t" \
"v_mfma_f32_16x16x32_bf16 v[228:231], a[156:159], v[156:159], v[228:231]\n\t" \
"v_mfma_f32_16x16x32_bf16 v[232:235], a[212:215], v[152:155], v[232:235]\n\t" \
"v_mfma_f32_16x16x32_bf16 v[236:239], a[212:215], v[156:159], v[236:239]\n\t" \
"buffer_load_dwordx4 %[vg], %[rh], s95 offen offset:1024 lds\n\t" \
"v_mfma_f32_16x16x32_bf16 v[208:211], a[48:51], v[160:163], v[208:211]\n\t" \
"v_mfma_f32_16x16x32_bf16 v[212:215], a[48:51], v[164:167], v[212:215]\n\t" \
"v_mfma_f32_16x16x32_bf16 v[216:219], a[104:107], v[160:163], v[216:219]\n\t" \
"v_mfma_f32_16x16x32_bf16 v[220:223], a[104:107], v[164:167], v[220:223]\n\t" \
"v_mfma_f32_16x16x32_bf16 v[224:227], a[160:163], v[160:163], v[224:227]\n\t" \
"v_mfma_f32_16x16x32_bf16 v[228:231], a[160:163], v[164:167], v[228:231]\n\t" \
"v_mfma_f32_16x16x32_bf16 v[232:235], a[216:219], v[160:163], v[232:235]\n\t" \
"v_mfma_f32_16x16x32_bf16 v[236:239], a[216:219], v[164:167], v[236:239]\n\t" \
"v_mfma_f32_16x16x32_bf16 v[208:211], a[52:55], v[168:171], v[208:211]\n\t" \
"v_mfma_f32_16x16x32_bf16 v[212:215], a[52:55], v[172:175], v[212:215]\n\t" \
"v_mfma_f32_16x16x32_bf16 v[216:219], a[108:111], v[168:171], v[216:219]\n\t" \
"v_mfma_f32_16x16x32_bf16 v[220:223], a[108:111], v[172:175], v[220:223]\n\t" \
"v_mfma_f32_16x16x32_bf16 v[224:227], a[164:167], v[168:171], v[224:227]\n\t" \
"v_mfma_f32_16x16x32_bf16 v[228:231], a[164:167], v[172:175], v[228:231]\n\t" \
"v_mfma_f32_16x16x32_bf16 v[232:235], a[220:223], v[168:171], v[232:235]\n\t" \
"v_mfma_f32_16x16x32_bf16 v[236:239], a[220:223], v[172:175], v[236:239]\n\t" \
"s_add_u32 s92, s92, 28672\n\t" \
"s_cmp_eq_u32 s92, 114688\n\t" \
"s_cselect_b32 s92, 0, s92\n\t" \
"v_mov_b32 v248, v249\n\t" \
"s_add_u32 s91, s91, 917504\n\t" \
"s_waitcnt lgkmcnt(0)\n\t" \
"v_mfma_f32_16x16x32_bf16 v[176:179], a[0:3], v[64:67], 0\n\t" \
"v_mfma_f32_16x16x32_bf16 v[180:183], a[0:3], v[68:71], 0\n\t" \
"ds_read_b128 v[120:123], v248 offset:7168\n\t" \
"v_mfma_f32_16x16x32_bf16 v[184:187], a[56:59], v[64:67], 0\n\t" \
"v_cvt_pk_bf16_f32 v240, v208, v209\n\t" \
"v_mfma_f32_16x16x32_bf16 v[188:191], a[56:59], v[68:71], 0\n\t" \
"v_cvt_pk_bf16_f32 v241, v210, v211\n\t" \
"v_mfma_f32_16x16x32_bf16 v[192:195], a[112:115], v[64:67], 0\n\t" \
"buffer_store_dwordx2 v[240:241], %[vs0], %[ro], s91 offen offset:0 sc1\n\t" \
"v_mfma_f32_16x16x32_bf16 v[196:199], a[112:115], v[68:71], 0\n\t" \
"v_cvt_pk_bf16_f32 v244, v212, v213\n\t" \
"v_mfma_f32_16x16x32_bf16 v[200:203], a[168:171], v[64:67], 0\n\t" \
"v_cvt_pk_bf16_f32 v245, v214, v215\n\t" \
"v_mfma_f32_16x16x32_bf16 v[204:207], a[168:171], v[68:71], 0\n\t" \
"buffer_store_dwordx2 v[244:245], %[vs1], %[ro], s91 offen offset:0 sc1\n\t" \
"v_mfma_f32_16x16x32_bf16 v[176:179], a[4:7], v[72:75], v[176:179]\n\t" \
"v_cvt_pk_bf16_f32 v240, v216, v217\n\t" \
"v_mfma_f32_16x16x32_bf16 v[180:183], a[4:7], v[76:79], v[180:183]\n\t" \
"v_cvt_pk_bf16_f32 v241, v218, v219\n\t" \
"v_mfma_f32_16x16x32_bf16 v[184:187], a[60:63], v[72:75], v[184:187]\n\t" \
"buffer_store_dwordx2 v[240:241], %[vs0], %[ro], s91 offen offset:512 sc1\n\t" \
"v_mfma_f32_16x16x32_bf16 v[188:191], a[60:63], v[76:79], v[188:191]\n\t" \
"v_cvt_pk_bf16_f32 v244, v220, v221\n\t" \
"v_mfma_f32_16x16x32_bf16 v[192:195], a[116:119], v[72:75], v[192:195]\n\t" \
"v_cvt_pk_bf16_f32 v245, v222, v223\n\t" \
"v_mfma_f32_16x16x32_bf16 v[196:199], a[116:119], v[76:79], v[196:199]\n\t" \
"buffer_store_dwordx2 v[244:245], %[vs1], %[ro], s91 offen offset:512 sc1\n\t" \
"v_mfma_f32_16x16x32_bf16 v[200:203], a[172:175], v[72:75], v[200:203]\n\t" \
"v_cvt_pk_bf16_f32 v240, v224, v225\n\t" \
"v_mfma_f32_16x16x32_bf16 v[204:207], a[172:175], v[76:79], v[204:207]\n\t" \
"v_cvt_pk_bf16_f32 v241, v226, v227\n\t" \
"v_mfma_f32_16x16x32_bf16 v[176:179], a[8:11], v[80:83], v[176:179]\n\t" \
"buffer_store_dwordx2 v[240:241], %[vs0], %[ro], s91 offen offset:1024 sc1\n\t" \
"v_mfma_f32_16x16x32_bf16 v[180:183], a[8:11], v[84:87], v[180:183]\n\t" \
"v_cvt_pk_bf16_f32 v244, v228, v229\n\t" \
"v_mfma_f32_16x16x32_bf16 v[184:187], a[64:67], v[80:83], v[184:187]\n\t" \
"v_cvt_pk_bf16_f32 v245, v230, v231\n\t" \
"v_mfma_f32_16x16x32_bf16 v[188:191], a[64:67], v[84:87], v[188:191]\n\t" \
"buffer_store_dwordx2 v[244:245], %[vs1], %[ro], s91 offen offset:1024 sc1\n\t" \
"s_add_u32 s90, s87, %[lg]\n\t" \
"s_add_u32 s90, s90, 0\n\t" \
"s_mov_b32 m0, s90\n\t" \
"s_add_u32 s95, s89, 0\n\t" \
"buffer_load_dwordx4 %[vg], %[rh], s95 offen offset:2048 lds\n\t" \
"v_mfma_f32_16x16x32_bf16 v[192:195], a[120:123], v[80:83], v[192:195]\n\t" \
"v_cvt_pk_bf16_f32 v240, v232, v233\n\t" \
"v_mfma_f32_16x16x32_bf16 v[196:199], a[120:123], v[84:87], v[196:199]\n\t" \
"v_cvt_pk_bf16_f32 v241, v234, v235\n\t" \
"v_mfma_f32_16x16x32_bf16 v[200:203], a[176:179], v[80:83], v[200:203]\n\t" \
"buffer_store_dwordx2 v[240:241], %[vs0], %[ro], s91 offen offset:1536 sc1\n\t" \
"v_mfma_f32_16x16x32_bf16 v[204:207], a[176:179], v[84:87], v[204:207]\n\t" \
"v_cvt_pk_bf16_f32 v244, v236, v237\n\t" \
"v_mfma_f32_16x16x32_bf16 v[176:179], a[12:15], v[88:91], v[176:179]\n\t" \
"v_cvt_pk_bf16_f32 v245, v238, v239\n\t" \
"v_mfma_f32_16x16x32_bf16 v[180:183], a[12:15], v[92:95], v[180:183]\n\t" \
"buffer_store_dwordx2 v[244:245], %[vs1], %[ro], s91 offen offset:1536 sc1\n\t" \
"v_mfma_f32_16x16x32_bf16 v[184:187], a[68:71], v[88:91], v[184:187]\n\t" \
"v_mfma_f32_16x16x32_bf16 v[188:191], a[68:71], v[92:95], v[188:191]\n\t" \
"ds_read_b128 v[124:127], v248 offset:21504\n\t" \
"v_mfma_f32_16x16x32_bf16 v[192:195], a[124:127], v[88:91], v[192:195]\n\t" \
"v_mfma_f32_16x16x32_bf16 v[196:199], a[124:127], v[92:95], v[196:199]\n\t" \
"ds_read_b128 v[128:131], v248 offset:8192\n\t" \
"v_mfma_f32_16x16x32_bf16 v[200:203], a[180:183], v[88:91], v[200:203]\n\t" \
"v_mfma_f32_16x16x32_bf16 v[204:207], a[180:183], v[92:95], v[204:207]\n\t" \
"ds_read_b128 v[132:135], v248 offset:22528\n\t" \
"v_mfma_f32_16x16x32_bf16 v[176:179], a[16:19], v[96:99], v[176:179]\n\t" \
"v_mfma_f32_16x16x32_bf16 v[180:183], a[16:19], v[100:103], v[180:183]\n\t" \
"ds_read_b128 v[136:139], v248 offset:9216\n\t" \
"v_mfma_f32_16x16x32_bf16 v[184:187], a[72:75], v[96:99], v[184:187]\n\t" \
"v_mfma_f32_16x16x32_bf16 v[188:191], a[72:75], v[100:103], v[188:191]\n\t" \
"ds_read_b128 v[140:143], v248 offset:23552\n\t" \
"v_mfma_f32_16x16x32_bf16 v[192:195], a[128:131], v[96:99], v[192:195]\n\t" \
"v_mfma_f32_16x16x32_bf16 v[196:199], a[128:131], v[100:103], v[196:199]\n\t" \
"ds_read_b128 v[144:147], v248 offset:10240\n\t" \
"v_mfma_f32_16x16x32_bf16 v[200:203], a[184:187], v[96:99], v[200:203]\n\t" \
"v_mfma_f32_16x16x32_bf16 v[204:207], a[184:187], v[100:103], v[204:207]\n\t" \
"ds_read_b128 v[148:151], v248 offset:24576\n\t" \
"buffer_load_dwordx4 %[vg], %[rh], s95 offen offset:3072 lds\n\t" \
"v_mfma_f32_16x16x32_bf16 v[176:179], a[20:23], v[104:107], v[176:179]\n\t" \
"v_mfma_f32_16x16x32_bf16 v[180:183], a[20:23], v[108:111], v[180:183]\n\t" \
"ds_read_b128 v[152:155], v248 offset:11264\n\t" \
"v_mfma_f32_16x16x32_bf16 v[184:187], a[76:79], v[104:107], v[184:187]\n\t" \
"v_mfma_f32_16x16x32_bf16 v[188:191], a[76:79], v[108:111], v[188:191]\n\t" \
"ds_read_b128 v[156:159], v248 offset:25600\n\t" \
"v_mfma_f32_16x16x32_bf16 v[192:195], a[132:135], v[104:107], v[192:195]\n\t" \
"v_mfma_f32_16x16x32_bf16 v[196:199], a[132:135], v[108:111], v[196:199]\n\t" \
"ds_read_b128 v[160:163], v248 offset:12288\n\t" \
"v_mfma_f32_16x16x32_bf16 v[200:203], a[188:191], v[104:107], v[200:203]\n\t" \
"v_mfma_f32_16x16x32_bf16 v[204:207], a[188:191], v[108:111], v[204:207]\n\t" \
"ds_read_b128 v[164:167], v248 offset:26624\n\t" \
"v_mfma_f32_16x16x32_bf16 v[176:179], a[24:27], v[112:115], v[176:179]\n\t" \
"v_mfma_f32_16x16x32_bf16 v[180:183], a[24:27], v[116:119], v[180:183]\n\t" \
"ds_read_b128 v[168:171], v248 offset:13312\n\t" \
"v_mfma_f32_16x16x32_bf16 v[184:187], a[80:83], v[112:115], v[184:187]\n\t" \
"v_mfma_f32_16x16x32_bf16 v[188:191], a[80:83], v[116:119], v[188:191]\n\t" \
"ds_read_b128 v[172:175], v248 offset:27648\n\t" \
"v_mfma_f32_16x16x32_bf16 v[192:195], a[136:139], v[112:115], v[192:195]\n\t" \
"v_mfma_f32_16x16x32_bf16 v[196:199], a[136:139], v[116:119], v[196:199]\n\t" \
"v_mfma_f32_16x16x32_bf16 v[200:203], a[192:195], v[112:115], v[200:203]\n\t" \
"v_mfma_f32_16x16x32_bf16 v[204:207], a[192:195], v[116:119], v[204:207]\n\t" \
"s_add_u32 s89, s89, 57344\n\t" \
"s_min_u32 s89, s89, 1806336\n\t" \
"s_mov_b32 s87, s92\n\t" \
"s_add_u32 s95, s92, 28672\n\t" \
"s_cmp_eq_u32 s95, 114688\n\t" \
"s_cselect_b32 s95, 0, s95\n\t" \
"v_add_u32 v249, s95, %[lr]\n\t" \
"s_waitcnt vmcnt(12) lgkmcnt(0)\n\t" \
"s_barrier\n\t" \
"v_mfma_f32_16x16x32_bf16 v[176:179], a[28:31], v[120:123], v[176:179]\n\t" \
"ds_read_b128 v[64:67], v249 offset:0\n\t" \
"v_mfma_f32_16x16x32_bf16 v[180:183], a[28:31], v[124:127], v[180:183]\n\t" \
"v_mfma_f32_16x16x32_bf16 v[184:187], a[84:87], v[120:123], v[184:187]\n\t" \
"ds_read_b128 v[68:71], v249 offset:14336\n\t" \
"v_mfma_f32_16x16x32_bf16 v[188:191], a[84:87], v[124:127], v[188:191]\n\t" \
"v_mfma_f32_16x16x32_bf16 v[192:195], a[140:143], v[120:123], v[192:195]\n\t" \
"ds_read_b128 v[72:75], v249 offset:1024\n\t" \
"v_mfma_f32_16x16x32_bf16 v[196:199], a[140:143], v[124:127], v[196:199]\n\t" \
"v_mfma_f32_16x16x32_bf16 v[200:203], a[196:199], v[120:123], v[200:203]\n\t" \
"ds_read_b128 v[76:79], v249 offset:15360\n\t" \
"v_mfma_f32_16x16x32_bf16 v[204:207], a[196:199], v[124:127], v[204:207]\n\t" \
"v_mfma_f32_16x16x32_bf16 v[176:179], a[32:35], v[128:131], v[176:179]\n\t" \
"ds_read_b128 v[80:83], v249 offset:2048\n\t" \
"v_mfma_f32_16x16x32_bf16 v[180:183], a[32:35], v[132:135], v[180:183]\n\t" \
"v_mfma_f32_16x16x32_bf16 v[184:187], a[88:91], v[128:131], v[184:187]\n\t" \
"ds_read_b128 v[84:87], v249 offset:16384\n\t" \
"v_mfma_f32_16x16x32_bf16 v[188:191], a[88:91], v[132:135], v[188:191]\n\t" \
"v_mfma_f32_16x16x32_bf16 v[192:195], a[144:147], v[128:131], v[192:195]\n\t" \
"ds_read_b128 v[88:91], v249 offset:3072\n\t" \
"v_mfma_f32_16x16x32_bf16 v[196:199], a[144:147], v[132:135], v[196:199]\n\t" \
"v_mfma_f32_16x16x32_bf16 v[200:203], a[200:203], v[128:131], v[200:203]\n\t" \
"ds_read_b128 v[92:95], v249 offset:17408\n\t" \
"v_mfma_f32_16x16x32_bf16 v[204:207], a[200:203], v[132:135], v[204:207]\n\t" \
"v_mfma_f32_16x16x32_bf16 v[176:179], a[36:39], v[136:139], v[176:179]\n\t" \
"ds_read_b128 v[96:99], v249 offset:4096\n\t" \
"v_mfma_f32_16x16x32_bf16 v[180:183], a[36:39], v[140:143], v[180:183]\n\t" \
"v_mfma_f32_16x16x32_bf16 v[184:187], a[92:95], v[136:139], v[184:187]\n\t" \
"ds_read_b128 v[100:103], v249 offset:18432\n\t" \
"v_mfma_f32_16x16x32_bf16 v[188:191], a[92:95], v[140:143], v[188:191]\n\t" \
"s_add_u32 s90, s87, %[lg]\n\t" \
"s_add_u32 s90, s90, 0\n\t" \
"s_mov_b32 m0, s90\n\t" \
"s_add_u32 s95, s89, 0\n\t" \
"buffer_load_dwordx4 %[vg], %[rh], s95 offen offset:0 lds\n\t" \
"v_mfma_f32_16x16x32_bf16 v[192:195], a[148:151], v[136:139], v[192:195]\n\t" \
"ds_read_b128 v[104:107], v249 offset:5120\n\t" \
"v_mfma_f32_16x16x32_bf16 v[196:199], a[148:151], v[140:143], v[196:199]\n\t" \
"v_mfma_f32_16x16x32_bf16 v[200:203], a[204:207], v[136:139], v[200:203]\n\t" \
"ds_read_b128 v[108:111], v249 offset:19456\n\t" \
"v_mfma_f32_16x16x32_bf16 v[204:207], a[204:207], v[140:143], v[204:207]\n\t" \
"v_mfma_f32_16x16x32_bf16 v[176:179], a[40:43], v[144:147], v[176:179]\n\t" \
"ds_read_b128 v[112:115], v249 offset:6144\n\t" \
"v_mfma_f32_16x16x32_bf16 v[180:183], a[40:43], v[148:151], v[180:183]\n\t" \
"v_mfma_f32_16x16x32_bf16 v[184:187], a[96:99], v[144:147], v[184:187]\n\t" \
"ds_read_b128 v[116:119], v249 offset:20480\n\t" \
"v_mfma_f32_16x16x32_bf16 v[188:191], a[96:99], v[148:151], v[188:191]\n\t" \
"v_mfma_f32_16x16x32_bf16 v[192:195], a[152:155], v[144:147], v[192:195]\n\t" \
"v_mfma_f32_16x16x32_bf16 v[196:199], a[152:155], v[148:151], v[196:199]\n\t" \
"v_mfma_f32_16x16x32_bf16 v[200:203], a[208:211], v[144:147], v[200:203]\n\t" \
"v_mfma_f32_16x16x32_bf16 v[204:207], a[208:211], v[148:151], v[204:207]\n\t" \
"v_mfma_f32_16x16x32_bf16 v[176:179], a[44:47], v[152:155], v[176:179]\n\t" \
"v_mfma_f32_16x16x32_bf16 v[180:183], a[44:47], v[156:159], v[180:183]\n\t" \
"v_mfma_f32_16x16x32_bf16 v[184:187], a[100:103], v[152:155], v[184:187]\n\t" \
"v_mfma_f32_16x16x32_bf16 v[188:191], a[100:103], v[156:159], v[188:191]\n\t" \
"v_mfma_f32_16x16x32_bf16 v[192:195], a[156:159], v[152:155], v[192:195]\n\t" \
"v_mfma_f32_16x16x32_bf16 v[196:199], a[156:159], v[156:159], v[196:199]\n\t" \
"v_mfma_f32_16x16x32_bf16 v[200:203], a[212:215], v[152:155], v[200:203]\n\t" \
"v_mfma_f32_16x16x32_bf16 v[204:207], a[212:215], v[156:159], v[204:207]\n\t" \
"buffer_load_dwordx4 %[vg], %[rh], s95 offen offset:1024 lds\n\t" \
"v_mfma_f32_16x16x32_bf16 v[176:179], a[48:51], v[160:163], v[176:179]\n\t" \
"v_mfma_f32_16x16x32_bf16 v[180:183], a[48:51], v[164:167], v[180:183]\n\t" \
"v_mfma_f32_16x16x32_bf16 v[184:187], a[104:107], v[160:163], v[184:187]\n\t" \
"v_mfma_f32_16x16x32_bf16 v[188:191], a[104:107], v[164:167], v[188:191]\n\t" \
"v_mfma_f32_16x16x32_bf16 v[192:195], a[160:163], v[160:163], v[192:195]\n\t" \
"v_mfma_f32_16x16x32_bf16 v[196:199], a[160:163], v[164:167], v[196:199]\n\t" \
"v_mfma_f32_16x16x32_bf16 v[200:203], a[216:219], v[160:163], v[200:203]\n\t" \
"v_mfma_f32_16x16x32_bf16 v[204:207], a[216:219], v[164:167], v[204:207]\n\t" \
"v_mfma_f32_16x16x32_bf16 v[176:179], a[52:55], v[168:171], v[176:179]\n\t" \
"v_mfma_f32_16x16x32_bf16 v[180:183], a[52:55], v[172:175], v[180:183]\n\t" \
"v_mfma_f32_16x16x32_bf16 v[184:187], a[108:111], v[168:171], v[184:187]\n\t" \
"v_mfma_f32_16x16x32_bf16 v[188:191], a[108:111], v[172:175], v[188:191]\n\t" \
"v_mfma_f32_16x16x32_bf16 v[192:195], a[164:167], v[168:171], v[192:195]\n\t" \
"v_mfma_f32_16x16x32_bf16 v[196:199], a[164:167], v[172:175], v[196:199]\n\t" \
"v_mfma_f32_16x16x32_bf16 v[200:203], a[220:223], v[168:171], v[200:203]\n\t" \
"v_mfma_f32_16x16x32_bf16 v[204:207], a[220:223], v[172:175], v[204:207]\n\t" \
"s_add_u32 s92, s92, 28672\n\t" \
"s_cmp_eq_u32 s92, 114688\n\t" \
"s_cselect_b32 s92, 0, s92\n\t" \
"v_mov_b32 v248, v249\n\t" \
"s_add_u32 s91, s91, 917504\n\t" \
"s_waitcnt lgkmcnt(0)\n\t" \
"v_mfma_f32_16x16x32_bf16 v[208:211], a[0:3], v[64:67], 0\n\t" \
"v_mfma_f32_16x16x32_bf16 v[212:215], a[0:3], v[68:71], 0\n\t" \
"ds_read_b128 v[120:123], v248 offset:7168\n\t" \
"v_mfma_f32_16x16x32_bf16 v[216:219], a[56:59], v[64:67], 0\n\t" \
"v_cvt_pk_bf16_f32 v240, v176, v177\n\t" \
"v_mfma_f32_16x16x32_bf16 v[220:223], a[56:59], v[68:71], 0\n\t" \
"v_cvt_pk_bf16_f32 v241, v178, v179\n\t" \
"v_mfma_f32_16x16x32_bf16 v[224:227], a[112:115], v[64:67], 0\n\t" \
"buffer_store_dwordx2 v[240:241], %[vs0], %[ro], s91 offen offset:0 sc1\n\t" \
"v_mfma_f32_16x16x32_bf16 v[228:231], a[112:115], v[68:71], 0\n\t" \
"v_cvt_pk_bf16_f32 v244, v180, v181\n\t" \
"v_mfma_f32_16x16x32_bf16 v[232:235], a[168:171], v[64:67], 0\n\t" \
"v_cvt_pk_bf16_f32 v245, v182, v183\n\t" \
"v_mfma_f32_16x16x32_bf16 v[236:239], a[168:171], v[68:71], 0\n\t" \
"buffer_store_dwordx2 v[244:245], %[vs1], %[ro], s91 offen offset:0 sc1\n\t" \
"v_mfma_f32_16x16x32_bf16 v[208:211], a[4:7], v[72:75], v[208:211]\n\t" \
"v_cvt_pk_bf16_f32 v240, v184, v185\n\t" \
"v_mfma_f32_16x16x32_bf16 v[212:215], a[4:7], v[76:79], v[212:215]\n\t" \
"v_cvt_pk_bf16_f32 v241, v186, v187\n\t" \
"v_mfma_f32_16x16x32_bf16 v[216:219], a[60:63], v[72:75], v[216:219]\n\t" \
"buffer_store_dwordx2 v[240:241], %[vs0], %[ro], s91 offen offset:512 sc1\n\t" \
"v_mfma_f32_16x16x32_bf16 v[220:223], a[60:63], v[76:79], v[220:223]\n\t" \
"v_cvt_pk_bf16_f32 v244, v188, v189\n\t" \
"v_mfma_f32_16x16x32_bf16 v[224:227], a[116:119], v[72:75], v[224:227]\n\t" \
"v_cvt_pk_bf16_f32 v245, v190, v191\n\t" \
"v_mfma_f32_16x16x32_bf16 v[228:231], a[116:119], v[76:79], v[228:231]\n\t" \
"buffer_store_dwordx2 v[244:245], %[vs1], %[ro], s91 offen offset:512 sc1\n\t" \
"v_mfma_f32_16x16x32_bf16 v[232:235], a[172:175], v[72:75], v[232:235]\n\t" \
"v_cvt_pk_bf16_f32 v240, v192, v193\n\t" \
"v_mfma_f32_16x16x32_bf16 v[236:239], a[172:175], v[76:79], v[236:239]\n\t" \
"v_cvt_pk_bf16_f32 v241, v194, v195\n\t" \
"v_mfma_f32_16x16x32_bf16 v[208:211], a[8:11], v[80:83], v[208:211]\n\t" \
"buffer_store_dwordx2 v[240:241], %[vs0], %[ro], s91 offen offset:1024 sc1\n\t" \
"v_mfma_f32_16x16x32_bf16 v[212:215], a[8:11], v[84:87], v[212:215]\n\t" \
"v_cvt_pk_bf16_f32 v244, v196, v197\n\t" \
"v_mfma_f32_16x16x32_bf16 v[216:219], a[64:67], v[80:83], v[216:219]\n\t" \
"v_cvt_pk_bf16_f32 v245, v198, v199\n\t" \
"v_mfma_f32_16x16x32_bf16 v[220:223], a[64:67], v[84:87], v[220:223]\n\t" \
"buffer_store_dwordx2 v[244:245], %[vs1], %[ro], s91 offen offset:1024 sc1\n\t" \
"s_add_u32 s90, s87, %[lg]\n\t" \
"s_add_u32 s90, s90, 0\n\t" \
"s_mov_b32 m0, s90\n\t" \
"s_add_u32 s95, s89, 0\n\t" \
"buffer_load_dwordx4 %[vg], %[rh], s95 offen offset:2048 lds\n\t" \
"v_mfma_f32_16x16x32_bf16 v[224:227], a[120:123], v[80:83], v[224:227]\n\t" \
"v_cvt_pk_bf16_f32 v240, v200, v201\n\t" \
"v_mfma_f32_16x16x32_bf16 v[228:231], a[120:123], v[84:87], v[228:231]\n\t" \
"v_cvt_pk_bf16_f32 v241, v202, v203\n\t" \
"v_mfma_f32_16x16x32_bf16 v[232:235], a[176:179], v[80:83], v[232:235]\n\t" \
"buffer_store_dwordx2 v[240:241], %[vs0], %[ro], s91 offen offset:1536 sc1\n\t" \
"v_mfma_f32_16x16x32_bf16 v[236:239], a[176:179], v[84:87], v[236:239]\n\t" \
"v_cvt_pk_bf16_f32 v244, v204, v205\n\t" \
"v_mfma_f32_16x16x32_bf16 v[208:211], a[12:15], v[88:91], v[208:211]\n\t" \
"v_cvt_pk_bf16_f32 v245, v206, v207\n\t" \
"v_mfma_f32_16x16x32_bf16 v[212:215], a[12:15], v[92:95], v[212:215]\n\t" \
"buffer_store_dwordx2 v[244:245], %[vs1], %[ro], s91 offen offset:1536 sc1\n\t" \
"v_mfma_f32_16x16x32_bf16 v[216:219], a[68:71], v[88:91], v[216:219]\n\t" \
"v_mfma_f32_16x16x32_bf16 v[220:223], a[68:71], v[92:95], v[220:223]\n\t" \
"ds_read_b128 v[124:127], v248 offset:21504\n\t" \
"v_mfma_f32_16x16x32_bf16 v[224:227], a[124:127], v[88:91], v[224:227]\n\t" \
"v_mfma_f32_16x16x32_bf16 v[228:231], a[124:127], v[92:95], v[228:231]\n\t" \
"ds_read_b128 v[128:131], v248 offset:8192\n\t" \
"v_mfma_f32_16x16x32_bf16 v[232:235], a[180:183], v[88:91], v[232:235]\n\t" \
"v_mfma_f32_16x16x32_bf16 v[236:239], a[180:183], v[92:95], v[236:239]\n\t" \
"ds_read_b128 v[132:135], v248 offset:22528\n\t" \
"v_mfma_f32_16x16x32_bf16 v[208:211], a[16:19], v[96:99], v[208:211]\n\t" \
"v_mfma_f32_16x16x32_bf16 v[212:215], a[16:19], v[100:103], v[212:215]\n\t" \
"ds_read_b128 v[136:139], v248 offset:9216\n\t" \
"v_mfma_f32_16x16x32_bf16 v[216:219], a[72:75], v[96:99], v[216:219]\n\t" \
"v_mfma_f32_16x16x32_bf16 v[220:223], a[72:75], v[100:103], v[220:223]\n\t" \
"ds_read_b128 v[140:143], v248 offset:23552\n\t" \
"v_mfma_f32_16x16x32_bf16 v[224:227], a[128:131], v[96:99], v[224:227]\n\t" \
"v_mfma_f32_16x16x32_bf16 v[228:231], a[128:131], v[100:103], v[228:231]\n\t" \
"ds_read_b128 v[144:147], v248 offset:10240\n\t" \
"v_mfma_f32_16x16x32_bf16 v[232:235], a[184:187], v[96:99], v[232:235]\n\t" \
"v_mfma_f32_16x16x32_bf16 v[236:239], a[184:187], v[100:103], v[236:239]\n\t" \
"ds_read_b128 v[148:151], v248 offset:24576\n\t" \
"buffer_load_dwordx4 %[vg], %[rh], s95 offen offset:3072 lds\n\t" \
"v_mfma_f32_16x16x32_bf16 v[208:211], a[20:23], v[104:107], v[208:211]\n\t" \
"v_mfma_f32_16x16x32_bf16 v[212:215], a[20:23], v[108:111], v[212:215]\n\t" \
"ds_read_b128 v[152:155], v248 offset:11264\n\t" \
"v_mfma_f32_16x16x32_bf16 v[216:219], a[76:79], v[104:107], v[216:219]\n\t" \
"v_mfma_f32_16x16x32_bf16 v[220:223], a[76:79], v[108:111], v[220:223]\n\t" \
"ds_read_b128 v[156:159], v248 offset:25600\n\t" \
"v_mfma_f32_16x16x32_bf16 v[224:227], a[132:135], v[104:107], v[224:227]\n\t" \
"v_mfma_f32_16x16x32_bf16 v[228:231], a[132:135], v[108:111], v[228:231]\n\t" \
"ds_read_b128 v[160:163], v248 offset:12288\n\t" \
"v_mfma_f32_16x16x32_bf16 v[232:235], a[188:191], v[104:107], v[232:235]\n\t" \
"v_mfma_f32_16x16x32_bf16 v[236:239], a[188:191], v[108:111], v[236:239]\n\t" \
"ds_read_b128 v[164:167], v248 offset:26624\n\t" \
"v_mfma_f32_16x16x32_bf16 v[208:211], a[24:27], v[112:115], v[208:211]\n\t" \
"v_mfma_f32_16x16x32_bf16 v[212:215], a[24:27], v[116:119], v[212:215]\n\t" \
"ds_read_b128 v[168:171], v248 offset:13312\n\t" \
"v_mfma_f32_16x16x32_bf16 v[216:219], a[80:83], v[112:115], v[216:219]\n\t" \
"v_mfma_f32_16x16x32_bf16 v[220:223], a[80:83], v[116:119], v[220:223]\n\t" \
"ds_read_b128 v[172:175], v248 offset:27648\n\t" \
"v_mfma_f32_16x16x32_bf16 v[224:227], a[136:139], v[112:115], v[224:227]\n\t" \
"v_mfma_f32_16x16x32_bf16 v[228:231], a[136:139], v[116:119], v[228:231]\n\t" \
"v_mfma_f32_16x16x32_bf16 v[232:235], a[192:195], v[112:115], v[232:235]\n\t" \
"v_mfma_f32_16x16x32_bf16 v[236:239], a[192:195], v[116:119], v[236:239]\n\t" \
"s_add_u32 s89, s89, 57344\n\t" \
"s_min_u32 s89, s89, 1806336\n\t" \
"s_mov_b32 s87, s92\n\t" \
"s_add_u32 s95, s92, 28672\n\t" \
"s_cmp_eq_u32 s95, 114688\n\t" \
"s_cselect_b32 s95, 0, s95\n\t" \
"v_add_u32 v249, s95, %[lr]\n\t" \
"s_waitcnt vmcnt(12) lgkmcnt(0)\n\t" \
"s_barrier\n\t" \
"s_mov_b64 exec, 1\n\t" \
"buffer_atomic_add v250, off, %[rc], s94\n\t" \
"s_mov_b64 exec, -1\n\t" \
"s_add_u32 s94, s94, 128\n\t" \
"v_mfma_f32_16x16x32_bf16 v[208:211], a[28:31], v[120:123], v[208:211]\n\t" \
"ds_read_b128 v[64:67], v249 offset:0\n\t" \
"v_mfma_f32_16x16x32_bf16 v[212:215], a[28:31], v[124:127], v[212:215]\n\t" \
"v_mfma_f32_16x16x32_bf16 v[216:219], a[84:87], v[120:123], v[216:219]\n\t" \
"ds_read_b128 v[68:71], v249 offset:14336\n\t" \
"v_mfma_f32_16x16x32_bf16 v[220:223], a[84:87], v[124:127], v[220:223]\n\t" \
"v_mfma_f32_16x16x32_bf16 v[224:227], a[140:143], v[120:123], v[224:227]\n\t" \
"ds_read_b128 v[72:75], v249 offset:1024\n\t" \
"v_mfma_f32_16x16x32_bf16 v[228:231], a[140:143], v[124:127], v[228:231]\n\t" \
"v_mfma_f32_16x16x32_bf16 v[232:235], a[196:199], v[120:123], v[232:235]\n\t" \
"ds_read_b128 v[76:79], v249 offset:15360\n\t" \
"v_mfma_f32_16x16x32_bf16 v[236:239], a[196:199], v[124:127], v[236:239]\n\t" \
"v_mfma_f32_16x16x32_bf16 v[208:211], a[32:35], v[128:131], v[208:211]\n\t" \
"ds_read_b128 v[80:83], v249 offset:2048\n\t" \
"v_mfma_f32_16x16x32_bf16 v[212:215], a[32:35], v[132:135], v[212:215]\n\t" \
"v_mfma_f32_16x16x32_bf16 v[216:219], a[88:91], v[128:131], v[216:219]\n\t" \
"ds_read_b128 v[84:87], v249 offset:16384\n\t" \
"v_mfma_f32_16x16x32_bf16 v[220:223], a[88:91], v[132:135], v[220:223]\n\t" \
"v_mfma_f32_16x16x32_bf16 v[224:227], a[144:147], v[128:131], v[224:227]\n\t" \
"ds_read_b128 v[88:91], v249 offset:3072\n\t" \
"v_mfma_f32_16x16x32_bf16 v[228:231], a[144:147], v[132:135], v[228:231]\n\t" \
"v_mfma_f32_16x16x32_bf16 v[232:235], a[200:203], v[128:131], v[232:235]\n\t" \
"ds_read_b128 v[92:95], v249 offset:17408\n\t" \
"v_mfma_f32_16x16x32_bf16 v[236:239], a[200:203], v[132:135], v[236:239]\n\t" \
"v_mfma_f32_16x16x32_bf16 v[208:211], a[36:39], v[136:139], v[208:211]\n\t" \
"ds_read_b128 v[96:99], v249 offset:4096\n\t" \
"v_mfma_f32_16x16x32_bf16 v[212:215], a[36:39], v[140:143], v[212:215]\n\t" \
"v_mfma_f32_16x16x32_bf16 v[216:219], a[92:95], v[136:139], v[216:219]\n\t" \
"ds_read_b128 v[100:103], v249 offset:18432\n\t" \
"v_mfma_f32_16x16x32_bf16 v[220:223], a[92:95], v[140:143], v[220:223]\n\t" \
"s_add_u32 s90, s87, %[lg]\n\t" \
"s_add_u32 s90, s90, 0\n\t" \
"s_mov_b32 m0, s90\n\t" \
"s_add_u32 s95, s89, 0\n\t" \
"buffer_load_dwordx4 %[vg], %[rh], s95 offen offset:0 lds\n\t" \
"v_mfma_f32_16x16x32_bf16 v[224:227], a[148:151], v[136:139], v[224:227]\n\t" \
"ds_read_b128 v[104:107], v249 offset:5120\n\t" \
"v_mfma_f32_16x16x32_bf16 v[228:231], a[148:151], v[140:143], v[228:231]\n\t" \
"v_mfma_f32_16x16x32_bf16 v[232:235], a[204:207], v[136:139], v[232:235]\n\t" \
"ds_read_b128 v[108:111], v249 offset:19456\n\t" \
"v_mfma_f32_16x16x32_bf16 v[236:239], a[204:207], v[140:143], v[236:239]\n\t" \
"v_mfma_f32_16x16x32_bf16 v[208:211], a[40:43], v[144:147], v[208:211]\n\t" \
"ds_read_b128 v[112:115], v249 offset:6144\n\t" \
"v_mfma_f32_16x16x32_bf16 v[212:215], a[40:43], v[148:151], v[212:215]\n\t" \
"v_mfma_f32_16x16x32_bf16 v[216:219], a[96:99], v[144:147], v[216:219]\n\t" \
"ds_read_b128 v[116:119], v249 offset:20480\n\t" \
"v_mfma_f32_16x16x32_bf16 v[220:223], a[96:99], v[148:151], v[220:223]\n\t" \
"v_mfma_f32_16x16x32_bf16 v[224:227], a[152:155], v[144:147], v[224:227]\n\t" \
"v_mfma_f32_16x16x32_bf16 v[228:231], a[152:155], v[148:151], v[228:231]\n\t" \
"v_mfma_f32_16x16x32_bf16 v[232:235], a[208:211], v[144:147], v[232:235]\n\t" \
"v_mfma_f32_16x16x32_bf16 v[236:239], a[208:211], v[148:151], v[236:239]\n\t" \
"v_mfma_f32_16x16x32_bf16 v[208:211], a[44:47], v[152:155], v[208:211]\n\t" \
"v_mfma_f32_16x16x32_bf16 v[212:215], a[44:47], v[156:159], v[212:215]\n\t" \
"v_mfma_f32_16x16x32_bf16 v[216:219], a[100:103], v[152:155], v[216:219]\n\t" \
"v_mfma_f32_16x16x32_bf16 v[220:223], a[100:103], v[156:159], v[220:223]\n\t" \
"v_mfma_f32_16x16x32_bf16 v[224:227], a[156:159], v[152:155], v[224:227]\n\t" \
"v_mfma_f32_16x16x32_bf16 v[228:231], a[156:159], v[156:159], v[228:231]\n\t" \
"v_mfma_f32_16x16x32_bf16 v[232:235], a[212:215], v[152:155], v[232:235]\n\t" \
"v_mfma_f32_16x16x32_bf16 v[236:239], a[212:215], v[156:159], v[236:239]\n\t" \
"buffer_load_dwordx4 %[vg], %[rh], s95 offen offset:1024 lds\n\t" \
"v_mfma_f32_16x16x32_bf16 v[208:211], a[48:51], v[160:163], v[208:211]\n\t" \
"v_mfma_f32_16x16x32_bf16 v[212:215], a[48:51], v[164:167], v[212:215]\n\t" \
"v_mfma_f32_16x16x32_bf16 v[216:219], a[104:107], v[160:163], v[216:219]\n\t" \
"v_mfma_f32_16x16x32_bf16 v[220:223], a[104:107], v[164:167], v[220:223]\n\t" \
"v_mfma_f32_16x16x32_bf16 v[224:227], a[160:163], v[160:163], v[224:227]\n\t" \
"v_mfma_f32_16x16x32_bf16 v[228:231], a[160:163], v[164:167], v[228:231]\n\t" \
"v_mfma_f32_16x16x32_bf16 v[232:235], a[216:219], v[160:163], v[232:235]\n\t" \
"v_mfma_f32_16x16x32_bf16 v[236:239], a[216:219], v[164:167], v[236:239]\n\t" \
"v_mfma_f32_16x16x32_bf16 v[208:211], a[52:55], v[168:171], v[208:211]\n\t" \
"v_mfma_f32_16x16x32_bf16 v[212:215], a[52:55], v[172:175], v[212:215]\n\t" \
"v_mfma_f32_16x16x32_bf16 v[216:219], a[108:111], v[168:171], v[216:219]\n\t" \
"v_mfma_f32_16x16x32_bf16 v[220:223], a[108:111], v[172:175], v[220:223]\n\t" \
"v_mfma_f32_16x16x32_bf16 v[224:227], a[164:167], v[168:171], v[224:227]\n\t" \
"v_mfma_f32_16x16x32_bf16 v[228:231], a[164:167], v[172:175], v[228:231]\n\t" \
"v_mfma_f32_16x16x32_bf16 v[232:235], a[220:223], v[168:171], v[232:235]\n\t" \
"v_mfma_f32_16x16x32_bf16 v[236:239], a[220:223], v[172:175], v[236:239]\n\t" \
"s_add_u32 s92, s92, 28672\n\t" \
"s_cmp_eq_u32 s92, 114688\n\t" \
"s_cselect_b32 s92, 0, s92\n\t" \
"v_mov_b32 v248, v249\n\t" \
"s_add_u32 s91, s91, 917504\n\t" \
"s_mov_b32 s88, 4\n\t" \
"s_nop 1\n\t" \
"Lp3%=:\n\t" \
"s_waitcnt lgkmcnt(0)\n\t" \
"v_mfma_f32_16x16x32_bf16 v[176:179], a[0:3], v[64:67], 0\n\t" \
"v_mfma_f32_16x16x32_bf16 v[180:183], a[0:3], v[68:71], 0\n\t" \
"ds_read_b128 v[120:123], v248 offset:7168\n\t" \
"v_mfma_f32_16x16x32_bf16 v[184:187], a[56:59], v[64:67], 0\n\t" \
"v_cvt_pk_bf16_f32 v240, v208, v209\n\t" \
"v_mfma_f32_16x16x32_bf16 v[188:191], a[56:59], v[68:71], 0\n\t" \
"v_cvt_pk_bf16_f32 v241, v210, v211\n\t" \
"v_mfma_f32_16x16x32_bf16 v[192:195], a[112:115], v[64:67], 0\n\t" \
"buffer_store_dwordx2 v[240:241], %[vs0], %[ro], s91 offen offset:0 sc1\n\t" \
"v_mfma_f32_16x16x32_bf16 v[196:199], a[112:115], v[68:71], 0\n\t" \
"v_cvt_pk_bf16_f32 v244, v212, v213\n\t" \
"v_mfma_f32_16x16x32_bf16 v[200:203], a[168:171], v[64:67], 0\n\t" \
"v_cvt_pk_bf16_f32 v245, v214, v215\n\t" \
"v_mfma_f32_16x16x32_bf16 v[204:207], a[168:171], v[68:71], 0\n\t" \
"buffer_store_dwordx2 v[244:245], %[vs1], %[ro], s91 offen offset:0 sc1\n\t" \
"v_mfma_f32_16x16x32_bf16 v[176:179], a[4:7], v[72:75], v[176:179]\n\t" \
"v_cvt_pk_bf16_f32 v240, v216, v217\n\t" \
"v_mfma_f32_16x16x32_bf16 v[180:183], a[4:7], v[76:79], v[180:183]\n\t" \
"v_cvt_pk_bf16_f32 v241, v218, v219\n\t" \
"v_mfma_f32_16x16x32_bf16 v[184:187], a[60:63], v[72:75], v[184:187]\n\t" \
"buffer_store_dwordx2 v[240:241], %[vs0], %[ro], s91 offen offset:512 sc1\n\t" \
"v_mfma_f32_16x16x32_bf16 v[188:191], a[60:63], v[76:79], v[188:191]\n\t" \
"v_cvt_pk_bf16_f32 v244, v220, v221\n\t" \
"v_mfma_f32_16x16x32_bf16 v[192:195], a[116:119], v[72:75], v[192:195]\n\t" \
"v_cvt_pk_bf16_f32 v245, v222, v223\n\t" \
"v_mfma_f32_16x16x32_bf16 v[196:199], a[116:119], v[76:79], v[196:199]\n\t" \
"buffer_store_dwordx2 v[244:245], %[vs1], %[ro], s91 offen offset:512 sc1\n\t" \
"v_mfma_f32_16x16x32_bf16 v[200:203], a[172:175], v[72:75], v[200:203]\n\t" \
"v_cvt_pk_bf16_f32 v240, v224, v225\n\t" \
"v_mfma_f32_16x16x32_bf16 v[204:207], a[172:175], v[76:79], v[204:207]\n\t" \
"v_cvt_pk_bf16_f32 v241, v226, v227\n\t" \
"v_mfma_f32_16x16x32_bf16 v[176:179], a[8:11], v[80:83], v[176:179]\n\t" \
"buffer_store_dwordx2 v[240:241], %[vs0], %[ro], s91 offen offset:1024 sc1\n\t" \
"v_mfma_f32_16x16x32_bf16 v[180:183], a[8:11], v[84:87], v[180:183]\n\t" \
"v_cvt_pk_bf16_f32 v244, v228, v229\n\t" \
"v_mfma_f32_16x16x32_bf16 v[184:187], a[64:67], v[80:83], v[184:187]\n\t" \
"v_cvt_pk_bf16_f32 v245, v230, v231\n\t" \
"v_mfma_f32_16x16x32_bf16 v[188:191], a[64:67], v[84:87], v[188:191]\n\t" \
"buffer_store_dwordx2 v[244:245], %[vs1], %[ro], s91 offen offset:1024 sc1\n\t" \
"s_add_u32 s90, s87, %[lg]\n\t" \
"s_add_u32 s90, s90, 0\n\t" \
"s_mov_b32 m0, s90\n\t" \
"s_add_u32 s95, s89, 0\n\t" \
"buffer_load_dwordx4 %[vg], %[rh], s95 offen offset:2048 lds\n\t" \
"v_mfma_f32_16x16x32_bf16 v[192:195], a[120:123], v[80:83], v[192:195]\n\t" \
"v_cvt_pk_bf16_f32 v240, v232, v233\n\t" \
"v_mfma_f32_16x16x32_bf16 v[196:199], a[120:123], v[84:87], v[196:199]\n\t" \
"v_cvt_pk_bf16_f32 v241, v234, v235\n\t" \
"v_mfma_f32_16x16x32_bf16 v[200:203], a[176:179], v[80:83], v[200:203]\n\t" \
"buffer_store_dwordx2 v[240:241], %[vs0], %[ro], s91 offen offset:1536 sc1\n\t" \
"v_mfma_f32_16x16x32_bf16 v[204:207], a[176:179], v[84:87], v[204:207]\n\t" \
"v_cvt_pk_bf16_f32 v244, v236, v237\n\t" \
"v_mfma_f32_16x16x32_bf16 v[176:179], a[12:15], v[88:91], v[176:179]\n\t" \
"v_cvt_pk_bf16_f32 v245, v238, v239\n\t" \
"v_mfma_f32_16x16x32_bf16 v[180:183], a[12:15], v[92:95], v[180:183]\n\t" \
"buffer_store_dwordx2 v[244:245], %[vs1], %[ro], s91 offen offset:1536 sc1\n\t" \
"v_mfma_f32_16x16x32_bf16 v[184:187], a[68:71], v[88:91], v[184:187]\n\t" \
"v_mfma_f32_16x16x32_bf16 v[188:191], a[68:71], v[92:95], v[188:191]\n\t" \
"ds_read_b128 v[124:127], v248 offset:21504\n\t" \
"v_mfma_f32_16x16x32_bf16 v[192:195], a[124:127], v[88:91], v[192:195]\n\t" \
"v_mfma_f32_16x16x32_bf16 v[196:199], a[124:127], v[92:95], v[196:199]\n\t" \
"ds_read_b128 v[128:131], v248 offset:8192\n\t" \
"v_mfma_f32_16x16x32_bf16 v[200:203], a[180:183], v[88:91], v[200:203]\n\t" \
"v_mfma_f32_16x16x32_bf16 v[204:207], a[180:183], v[92:95], v[204:207]\n\t" \
"ds_read_b128 v[132:135], v248 offset:22528\n\t" \
"v_mfma_f32_16x16x32_bf16 v[176:179], a[16:19], v[96:99], v[176:179]\n\t" \
"v_mfma_f32_16x16x32_bf16 v[180:183], a[16:19], v[100:103], v[180:183]\n\t" \
"ds_read_b128 v[136:139], v248 offset:9216\n\t" \
"v_mfma_f32_16x16x32_bf16 v[184:187], a[72:75], v[96:99], v[184:187]\n\t" \
"v_mfma_f32_16x16x32_bf16 v[188:191], a[72:75], v[100:103], v[188:191]\n\t" \
"ds_read_b128 v[140:143], v248 offset:23552\n\t" \
"v_mfma_f32_16x16x32_bf16 v[192:195], a[128:131], v[96:99], v[192:195]\n\t" \
"v_mfma_f32_16x16x32_bf16 v[196:199], a[128:131], v[100:103], v[196:199]\n\t" \
"ds_read_b128 v[144:147], v248 offset:10240\n\t" \
"v_mfma_f32_16x16x32_bf16 v[200:203], a[184:187], v[96:99], v[200:203]\n\t" \
"v_mfma_f32_16x16x32_bf16 v[204:207], a[184:187], v[100:103], v[204:207]\n\t" \
"ds_read_b128 v[148:151], v248 offset:24576\n\t" \
"buffer_load_dwordx4 %[vg], %[rh], s95 offen offset:3072 lds\n\t" \
"v_mfma_f32_16x16x32_bf16 v[176:179], a[20:23], v[104:107], v[176:179]\n\t" \
"v_mfma_f32_16x16x32_bf16 v[180:183], a[20:23], v[108:111], v[180:183]\n\t" \
"ds_read_b128 v[152:155], v248 offset:11264\n\t" \
"v_mfma_f32_16x16x32_bf16 v[184:187], a[76:79], v[104:107], v[184:187]\n\t" \
"v_mfma_f32_16x16x32_bf16 v[188:191], a[76:79], v[108:111], v[188:191]\n\t" \
"ds_read_b128 v[156:159], v248 offset:25600\n\t" \
"v_mfma_f32_16x16x32_bf16 v[192:195], a[132:135], v[104:107], v[192:195]\n\t" \
"v_mfma_f32_16x16x32_bf16 v[196:199], a[132:135], v[108:111], v[196:199]\n\t" \
"ds_read_b128 v[160:163], v248 offset:12288\n\t" \
"v_mfma_f32_16x16x32_bf16 v[200:203], a[188:191], v[104:107], v[200:203]\n\t" \
"v_mfma_f32_16x16x32_bf16 v[204:207], a[188:191], v[108:111], v[204:207]\n\t" \
"ds_read_b128 v[164:167], v248 offset:26624\n\t" \
"v_mfma_f32_16x16x32_bf16 v[176:179], a[24:27], v[112:115], v[176:179]\n\t" \
"v_mfma_f32_16x16x32_bf16 v[180:183], a[24:27], v[116:119], v[180:183]\n\t" \
"ds_read_b128 v[168:171], v248 offset:13312\n\t" \
"v_mfma_f32_16x16x32_bf16 v[184:187], a[80:83], v[112:115], v[184:187]\n\t" \
"v_mfma_f32_16x16x32_bf16 v[188:191], a[80:83], v[116:119], v[188:191]\n\t" \
"ds_read_b128 v[172:175], v248 offset:27648\n\t" \
"v_mfma_f32_16x16x32_bf16 v[192:195], a[136:139], v[112:115], v[192:195]\n\t" \
"v_mfma_f32_16x16x32_bf16 v[196:199], a[136:139], v[116:119], v[196:199]\n\t" \
"v_mfma_f32_16x16x32_bf16 v[200:203], a[192:195], v[112:115], v[200:203]\n\t" \
"v_mfma_f32_16x16x32_bf16 v[204:207], a[192:195], v[116:119], v[204:207]\n\t" \
"s_add_u32 s89, s89, 57344\n\t" \
"s_min_u32 s89, s89, 1806336\n\t" \
"s_mov_b32 s87, s92\n\t" \
"s_add_u32 s95, s92, 28672\n\t" \
"s_cmp_eq_u32 s95, 114688\n\t" \
"s_cselect_b32 s95, 0, s95\n\t" \
"v_add_u32 v249, s95, %[lr]\n\t" \
"s_waitcnt vmcnt(12) lgkmcnt(0)\n\t" \
"s_barrier\n\t" \
"s_mov_b64 exec, 1\n\t" \
"buffer_atomic_add v250, off, %[rc], s94\n\t" \
"s_mov_b64 exec, -1\n\t" \
"s_add_u32 s94, s94, 128\n\t" \
"v_mfma_f32_16x16x32_bf16 v[176:179], a[28:31], v[120:123], v[176:179]\n\t" \
"ds_read_b128 v[64:67], v249 offset:0\n\t" \
"v_mfma_f32_16x16x32_bf16 v[180:183], a[28:31], v[124:127], v[180:183]\n\t" \
"v_mfma_f32_16x16x32_bf16 v[184:187], a[84:87], v[120:123], v[184:187]\n\t" \
"ds_read_b128 v[68:71], v249 offset:14336\n\t" \
"v_mfma_f32_16x16x32_bf16 v[188:191], a[84:87], v[124:127], v[188:191]\n\t" \
"v_mfma_f32_16x16x32_bf16 v[192:195], a[140:143], v[120:123], v[192:195]\n\t" \
"ds_read_b128 v[72:75], v249 offset:1024\n\t" \
"v_mfma_f32_16x16x32_bf16 v[196:199], a[140:143], v[124:127], v[196:199]\n\t" \
"v_mfma_f32_16x16x32_bf16 v[200:203], a[196:199], v[120:123], v[200:203]\n\t" \
"ds_read_b128 v[76:79], v249 offset:15360\n\t" \
"v_mfma_f32_16x16x32_bf16 v[204:207], a[196:199], v[124:127], v[204:207]\n\t" \
"v_mfma_f32_16x16x32_bf16 v[176:179], a[32:35], v[128:131], v[176:179]\n\t" \
"ds_read_b128 v[80:83], v249 offset:2048\n\t" \
"v_mfma_f32_16x16x32_bf16 v[180:183], a[32:35], v[132:135], v[180:183]\n\t" \
"v_mfma_f32_16x16x32_bf16 v[184:187], a[88:91], v[128:131], v[184:187]\n\t" \
"ds_read_b128 v[84:87], v249 offset:16384\n\t" \
"v_mfma_f32_16x16x32_bf16 v[188:191], a[88:91], v[132:135], v[188:191]\n\t" \
"v_mfma_f32_16x16x32_bf16 v[192:195], a[144:147], v[128:131], v[192:195]\n\t" \
"ds_read_b128 v[88:91], v249 offset:3072\n\t" \
"v_mfma_f32_16x16x32_bf16 v[196:199], a[144:147], v[132:135], v[196:199]\n\t" \
"v_mfma_f32_16x16x32_bf16 v[200:203], a[200:203], v[128:131], v[200:203]\n\t" \
"ds_read_b128 v[92:95], v249 offset:17408\n\t" \
"v_mfma_f32_16x16x32_bf16 v[204:207], a[200:203], v[132:135], v[204:207]\n\t" \
"v_mfma_f32_16x16x32_bf16 v[176:179], a[36:39], v[136:139], v[176:179]\n\t" \
"ds_read_b128 v[96:99], v249 offset:4096\n\t" \
"v_mfma_f32_16x16x32_bf16 v[180:183], a[36:39], v[140:143], v[180:183]\n\t" \
"v_mfma_f32_16x16x32_bf16 v[184:187], a[92:95], v[136:139], v[184:187]\n\t" \
"ds_read_b128 v[100:103], v249 offset:18432\n\t" \
"v_mfma_f32_16x16x32_bf16 v[188:191], a[92:95], v[140:143], v[188:191]\n\t" \
"s_add_u32 s90, s87, %[lg]\n\t" \
"s_add_u32 s90, s90, 0\n\t" \
"s_mov_b32 m0, s90\n\t" \
"s_add_u32 s95, s89, 0\n\t" \
"buffer_load_dwordx4 %[vg], %[rh], s95 offen offset:0 lds\n\t" \
"v_mfma_f32_16x16x32_bf16 v[192:195], a[148:151], v[136:139], v[192:195]\n\t" \
"ds_read_b128 v[104:107], v249 offset:5120\n\t" \
"v_mfma_f32_16x16x32_bf16 v[196:199], a[148:151], v[140:143], v[196:199]\n\t" \
"v_mfma_f32_16x16x32_bf16 v[200:203], a[204:207], v[136:139], v[200:203]\n\t" \
"ds_read_b128 v[108:111], v249 offset:19456\n\t" \
"v_mfma_f32_16x16x32_bf16 v[204:207], a[204:207], v[140:143], v[204:207]\n\t" \
"v_mfma_f32_16x16x32_bf16 v[176:179], a[40:43], v[144:147], v[176:179]\n\t" \
"ds_read_b128 v[112:115], v249 offset:6144\n\t" \
"v_mfma_f32_16x16x32_bf16 v[180:183], a[40:43], v[148:151], v[180:183]\n\t" \
"v_mfma_f32_16x16x32_bf16 v[184:187], a[96:99], v[144:147], v[184:187]\n\t" \
"ds_read_b128 v[116:119], v249 offset:20480\n\t" \
"v_mfma_f32_16x16x32_bf16 v[188:191], a[96:99], v[148:151], v[188:191]\n\t" \
"v_mfma_f32_16x16x32_bf16 v[192:195], a[152:155], v[144:147], v[192:195]\n\t" \
"v_mfma_f32_16x16x32_bf16 v[196:199], a[152:155], v[148:151], v[196:199]\n\t" \
"v_mfma_f32_16x16x32_bf16 v[200:203], a[208:211], v[144:147], v[200:203]\n\t" \
"v_mfma_f32_16x16x32_bf16 v[204:207], a[208:211], v[148:151], v[204:207]\n\t" \
"v_mfma_f32_16x16x32_bf16 v[176:179], a[44:47], v[152:155], v[176:179]\n\t" \
"v_mfma_f32_16x16x32_bf16 v[180:183], a[44:47], v[156:159], v[180:183]\n\t" \
"v_mfma_f32_16x16x32_bf16 v[184:187], a[100:103], v[152:155], v[184:187]\n\t" \
"v_mfma_f32_16x16x32_bf16 v[188:191], a[100:103], v[156:159], v[188:191]\n\t" \
"v_mfma_f32_16x16x32_bf16 v[192:195], a[156:159], v[152:155], v[192:195]\n\t" \
"v_mfma_f32_16x16x32_bf16 v[196:199], a[156:159], v[156:159], v[196:199]\n\t" \
"v_mfma_f32_16x16x32_bf16 v[200:203], a[212:215], v[152:155], v[200:203]\n\t" \
"v_mfma_f32_16x16x32_bf16 v[204:207], a[212:215], v[156:159], v[204:207]\n\t" \
"buffer_load_dwordx4 %[vg], %[rh], s95 offen offset:1024 lds\n\t" \
"v_mfma_f32_16x16x32_bf16 v[176:179], a[48:51], v[160:163], v[176:179]\n\t" \
"v_mfma_f32_16x16x32_bf16 v[180:183], a[48:51], v[164:167], v[180:183]\n\t" \
"v_mfma_f32_16x16x32_bf16 v[184:187], a[104:107], v[160:163], v[184:187]\n\t" \
"v_mfma_f32_16x16x32_bf16 v[188:191], a[104:107], v[164:167], v[188:191]\n\t" \
"v_mfma_f32_16x16x32_bf16 v[192:195], a[160:163], v[160:163], v[192:195]\n\t" \
"v_mfma_f32_16x16x32_bf16 v[196:199], a[160:163], v[164:167], v[196:199]\n\t" \
"v_mfma_f32_16x16x32_bf16 v[200:203], a[216:219], v[160:163], v[200:203]\n\t" \
"v_mfma_f32_16x16x32_bf16 v[204:207], a[216:219], v[164:167], v[204:207]\n\t" \
"v_mfma_f32_16x16x32_bf16 v[176:179], a[52:55], v[168:171], v[176:179]\n\t" \
"v_mfma_f32_16x16x32_bf16 v[180:183], a[52:55], v[172:175], v[180:183]\n\t" \
"v_mfma_f32_16x16x32_bf16 v[184:187], a[108:111], v[168:171], v[184:187]\n\t" \
"v_mfma_f32_16x16x32_bf16 v[188:191], a[108:111], v[172:175], v[188:191]\n\t" \
"v_mfma_f32_16x16x32_bf16 v[192:195], a[164:167], v[168:171], v[192:195]\n\t" \
"v_mfma_f32_16x16x32_bf16 v[196:199], a[164:167], v[172:175], v[196:199]\n\t" \
"v_mfma_f32_16x16x32_bf16 v[200:203], a[220:223], v[168:171], v[200:203]\n\t" \
"v_mfma_f32_16x16x32_bf16 v[204:207], a[220:223], v[172:175], v[204:207]\n\t" \
"s_add_u32 s92, s92, 28672\n\t" \
"s_cmp_eq_u32 s92, 114688\n\t" \
"s_cselect_b32 s92, 0, s92\n\t" \
"v_mov_b32 v248, v249\n\t" \
"s_add_u32 s91, s91, 917504\n\t" \
"s_waitcnt lgkmcnt(0)\n\t" \
"v_mfma_f32_16x16x32_bf16 v[208:211], a[0:3], v[64:67], 0\n\t" \
"v_mfma_f32_16x16x32_bf16 v[212:215], a[0:3], v[68:71], 0\n\t" \
"ds_read_b128 v[120:123], v248 offset:7168\n\t" \
"v_mfma_f32_16x16x32_bf16 v[216:219], a[56:59], v[64:67], 0\n\t" \
"v_cvt_pk_bf16_f32 v240, v176, v177\n\t" \
"v_mfma_f32_16x16x32_bf16 v[220:223], a[56:59], v[68:71], 0\n\t" \
"v_cvt_pk_bf16_f32 v241, v178, v179\n\t" \
"v_mfma_f32_16x16x32_bf16 v[224:227], a[112:115], v[64:67], 0\n\t" \
"buffer_store_dwordx2 v[240:241], %[vs0], %[ro], s91 offen offset:0 sc1\n\t" \
"v_mfma_f32_16x16x32_bf16 v[228:231], a[112:115], v[68:71], 0\n\t" \
"v_cvt_pk_bf16_f32 v244, v180, v181\n\t" \
"v_mfma_f32_16x16x32_bf16 v[232:235], a[168:171], v[64:67], 0\n\t" \
"v_cvt_pk_bf16_f32 v245, v182, v183\n\t" \
"v_mfma_f32_16x16x32_bf16 v[236:239], a[168:171], v[68:71], 0\n\t" \
"buffer_store_dwordx2 v[244:245], %[vs1], %[ro], s91 offen offset:0 sc1\n\t" \
"v_mfma_f32_16x16x32_bf16 v[208:211], a[4:7], v[72:75], v[208:211]\n\t" \
"v_cvt_pk_bf16_f32 v240, v184, v185\n\t" \
"v_mfma_f32_16x16x32_bf16 v[212:215], a[4:7], v[76:79], v[212:215]\n\t" \
"v_cvt_pk_bf16_f32 v241, v186, v187\n\t" \
"v_mfma_f32_16x16x32_bf16 v[216:219], a[60:63], v[72:75], v[216:219]\n\t" \
"buffer_store_dwordx2 v[240:241], %[vs0], %[ro], s91 offen offset:512 sc1\n\t" \
"v_mfma_f32_16x16x32_bf16 v[220:223], a[60:63], v[76:79], v[220:223]\n\t" \
"v_cvt_pk_bf16_f32 v244, v188, v189\n\t" \
"v_mfma_f32_16x16x32_bf16 v[224:227], a[116:119], v[72:75], v[224:227]\n\t" \
"v_cvt_pk_bf16_f32 v245, v190, v191\n\t" \
"v_mfma_f32_16x16x32_bf16 v[228:231], a[116:119], v[76:79], v[228:231]\n\t" \
"buffer_store_dwordx2 v[244:245], %[vs1], %[ro], s91 offen offset:512 sc1\n\t" \
"v_mfma_f32_16x16x32_bf16 v[232:235], a[172:175], v[72:75], v[232:235]\n\t" \
"v_cvt_pk_bf16_f32 v240, v192, v193\n\t" \
"v_mfma_f32_16x16x32_bf16 v[236:239], a[172:175], v[76:79], v[236:239]\n\t" \
"v_cvt_pk_bf16_f32 v241, v194, v195\n\t" \
"v_mfma_f32_16x16x32_bf16 v[208:211], a[8:11], v[80:83], v[208:211]\n\t" \
"buffer_store_dwordx2 v[240:241], %[vs0], %[ro], s91 offen offset:1024 sc1\n\t" \
"v_mfma_f32_16x16x32_bf16 v[212:215], a[8:11], v[84:87], v[212:215]\n\t" \
"v_cvt_pk_bf16_f32 v244, v196, v197\n\t" \
"v_mfma_f32_16x16x32_bf16 v[216:219], a[64:67], v[80:83], v[216:219]\n\t" \
"v_cvt_pk_bf16_f32 v245, v198, v199\n\t" \
"v_mfma_f32_16x16x32_bf16 v[220:223], a[64:67], v[84:87], v[220:223]\n\t" \
"buffer_store_dwordx2 v[244:245], %[vs1], %[ro], s91 offen offset:1024 sc1\n\t" \
"s_add_u32 s90, s87, %[lg]\n\t" \
"s_add_u32 s90, s90, 0\n\t" \
"s_mov_b32 m0, s90\n\t" \
"s_add_u32 s95, s89, 0\n\t" \
"buffer_load_dwordx4 %[vg], %[rh], s95 offen offset:2048 lds\n\t" \
"v_mfma_f32_16x16x32_bf16 v[224:227], a[120:123], v[80:83], v[224:227]\n\t" \
"v_cvt_pk_bf16_f32 v240, v200, v201\n\t" \
"v_mfma_f32_16x16x32_bf16 v[228:231], a[120:123], v[84:87], v[228:231]\n\t" \
"v_cvt_pk_bf16_f32 v241, v202, v203\n\t" \
"v_mfma_f32_16x16x32_bf16 v[232:235], a[176:179], v[80:83], v[232:235]\n\t" \
"buffer_store_dwordx2 v[240:241], %[vs0], %[ro], s91 offen offset:1536 sc1\n\t" \
"v_mfma_f32_16x16x32_bf16 v[236:239], a[176:179], v[84:87], v[236:239]\n\t" \
"v_cvt_pk_bf16_f32 v244, v204, v205\n\t" \
"v_mfma_f32_16x16x32_bf16 v[208:211], a[12:15], v[88:91], v[208:211]\n\t" \
"v_cvt_pk_bf16_f32 v245, v206, v207\n\t" \
"v_mfma_f32_16x16x32_bf16 v[212:215], a[12:15], v[92:95], v[212:215]\n\t" \
"buffer_store_dwordx2 v[244:245], %[vs1], %[ro], s91 offen offset:1536 sc1\n\t" \
"v_mfma_f32_16x16x32_bf16 v[216:219], a[68:71], v[88:91], v[216:219]\n\t" \
"v_mfma_f32_16x16x32_bf16 v[220:223], a[68:71], v[92:95], v[220:223]\n\t" \
"ds_read_b128 v[124:127], v248 offset:21504\n\t" \
"v_mfma_f32_16x16x32_bf16 v[224:227], a[124:127], v[88:91], v[224:227]\n\t" \
"v_mfma_f32_16x16x32_bf16 v[228:231], a[124:127], v[92:95], v[228:231]\n\t" \
"ds_read_b128 v[128:131], v248 offset:8192\n\t" \
"v_mfma_f32_16x16x32_bf16 v[232:235], a[180:183], v[88:91], v[232:235]\n\t" \
"v_mfma_f32_16x16x32_bf16 v[236:239], a[180:183], v[92:95], v[236:239]\n\t" \
"ds_read_b128 v[132:135], v248 offset:22528\n\t" \
"v_mfma_f32_16x16x32_bf16 v[208:211], a[16:19], v[96:99], v[208:211]\n\t" \
"v_mfma_f32_16x16x32_bf16 v[212:215], a[16:19], v[100:103], v[212:215]\n\t" \
"ds_read_b128 v[136:139], v248 offset:9216\n\t" \
"v_mfma_f32_16x16x32_bf16 v[216:219], a[72:75], v[96:99], v[216:219]\n\t" \
"v_mfma_f32_16x16x32_bf16 v[220:223], a[72:75], v[100:103], v[220:223]\n\t" \
"ds_read_b128 v[140:143], v248 offset:23552\n\t" \
"v_mfma_f32_16x16x32_bf16 v[224:227], a[128:131], v[96:99], v[224:227]\n\t" \
"v_mfma_f32_16x16x32_bf16 v[228:231], a[128:131], v[100:103], v[228:231]\n\t" \
"ds_read_b128 v[144:147], v248 offset:10240\n\t" \
"v_mfma_f32_16x16x32_bf16 v[232:235], a[184:187], v[96:99], v[232:235]\n\t" \
"v_mfma_f32_16x16x32_bf16 v[236:239], a[184:187], v[100:103], v[236:239]\n\t" \
"ds_read_b128 v[148:151], v248 offset:24576\n\t" \
"buffer_load_dwordx4 %[vg], %[rh], s95 offen offset:3072 lds\n\t" \
"v_mfma_f32_16x16x32_bf16 v[208:211], a[20:23], v[104:107], v[208:211]\n\t" \
"v_mfma_f32_16x16x32_bf16 v[212:215], a[20:23], v[108:111], v[212:215]\n\t" \
"ds_read_b128 v[152:155], v248 offset:11264\n\t" \
"v_mfma_f32_16x16x32_bf16 v[216:219], a[76:79], v[104:107], v[216:219]\n\t" \
"v_mfma_f32_16x16x32_bf16 v[220:223], a[76:79], v[108:111], v[220:223]\n\t" \
"ds_read_b128 v[156:159], v248 offset:25600\n\t" \
"v_mfma_f32_16x16x32_bf16 v[224:227], a[132:135], v[104:107], v[224:227]\n\t" \
"v_mfma_f32_16x16x32_bf16 v[228:231], a[132:135], v[108:111], v[228:231]\n\t" \
"ds_read_b128 v[160:163], v248 offset:12288\n\t" \
"v_mfma_f32_16x16x32_bf16 v[232:235], a[188:191], v[104:107], v[232:235]\n\t" \
"v_mfma_f32_16x16x32_bf16 v[236:239], a[188:191], v[108:111], v[236:239]\n\t" \
"ds_read_b128 v[164:167], v248 offset:26624\n\t" \
"v_mfma_f32_16x16x32_bf16 v[208:211], a[24:27], v[112:115], v[208:211]\n\t" \
"v_mfma_f32_16x16x32_bf16 v[212:215], a[24:27], v[116:119], v[212:215]\n\t" \
"ds_read_b128 v[168:171], v248 offset:13312\n\t" \
"v_mfma_f32_16x16x32_bf16 v[216:219], a[80:83], v[112:115], v[216:219]\n\t" \
"v_mfma_f32_16x16x32_bf16 v[220:223], a[80:83], v[116:119], v[220:223]\n\t" \
"ds_read_b128 v[172:175], v248 offset:27648\n\t" \
"v_mfma_f32_16x16x32_bf16 v[224:227], a[136:139], v[112:115], v[224:227]\n\t" \
"v_mfma_f32_16x16x32_bf16 v[228:231], a[136:139], v[116:119], v[228:231]\n\t" \
"v_mfma_f32_16x16x32_bf16 v[232:235], a[192:195], v[112:115], v[232:235]\n\t" \
"v_mfma_f32_16x16x32_bf16 v[236:239], a[192:195], v[116:119], v[236:239]\n\t" \
"s_add_u32 s89, s89, 57344\n\t" \
"s_min_u32 s89, s89, 1806336\n\t" \
"s_mov_b32 s87, s92\n\t" \
"s_add_u32 s95, s92, 28672\n\t" \
"s_cmp_eq_u32 s95, 114688\n\t" \
"s_cselect_b32 s95, 0, s95\n\t" \
"v_add_u32 v249, s95, %[lr]\n\t" \
"s_waitcnt vmcnt(12) lgkmcnt(0)\n\t" \
"s_barrier\n\t" \
"s_mov_b64 exec, 1\n\t" \
"buffer_atomic_add v250, off, %[rc], s94\n\t" \
"s_mov_b64 exec, -1\n\t" \
"s_add_u32 s94, s94, 128\n\t" \
"v_mfma_f32_16x16x32_bf16 v[208:211], a[28:31], v[120:123], v[208:211]\n\t" \
"ds_read_b128 v[64:67], v249 offset:0\n\t" \
"v_mfma_f32_16x16x32_bf16 v[212:215], a[28:31], v[124:127], v[212:215]\n\t" \
"v_mfma_f32_16x16x32_bf16 v[216:219], a[84:87], v[120:123], v[216:219]\n\t" \
"ds_read_b128 v[68:71], v249 offset:14336\n\t" \
"v_mfma_f32_16x16x32_bf16 v[220:223], a[84:87], v[124:127], v[220:223]\n\t" \
"v_mfma_f32_16x16x32_bf16 v[224:227], a[140:143], v[120:123], v[224:227]\n\t" \
"ds_read_b128 v[72:75], v249 offset:1024\n\t" \
"v_mfma_f32_16x16x32_bf16 v[228:231], a[140:143], v[124:127], v[228:231]\n\t" \
"v_mfma_f32_16x16x32_bf16 v[232:235], a[196:199], v[120:123], v[232:235]\n\t" \
"ds_read_b128 v[76:79], v249 offset:15360\n\t" \
"v_mfma_f32_16x16x32_bf16 v[236:239], a[196:199], v[124:127], v[236:239]\n\t" \
"v_mfma_f32_16x16x32_bf16 v[208:211], a[32:35], v[128:131], v[208:211]\n\t" \
"ds_read_b128 v[80:83], v249 offset:2048\n\t" \
"v_mfma_f32_16x16x32_bf16 v[212:215], a[32:35], v[132:135], v[212:215]\n\t" \
"v_mfma_f32_16x16x32_bf16 v[216:219], a[88:91], v[128:131], v[216:219]\n\t" \
"ds_read_b128 v[84:87], v249 offset:16384\n\t" \
"v_mfma_f32_16x16x32_bf16 v[220:223], a[88:91], v[132:135], v[220:223]\n\t" \
"v_mfma_f32_16x16x32_bf16 v[224:227], a[144:147], v[128:131], v[224:227]\n\t" \
"ds_read_b128 v[88:91], v249 offset:3072\n\t" \
"v_mfma_f32_16x16x32_bf16 v[228:231], a[144:147], v[132:135], v[228:231]\n\t" \
"v_mfma_f32_16x16x32_bf16 v[232:235], a[200:203], v[128:131], v[232:235]\n\t" \
"ds_read_b128 v[92:95], v249 offset:17408\n\t" \
"v_mfma_f32_16x16x32_bf16 v[236:239], a[200:203], v[132:135], v[236:239]\n\t" \
"v_mfma_f32_16x16x32_bf16 v[208:211], a[36:39], v[136:139], v[208:211]\n\t" \
"ds_read_b128 v[96:99], v249 offset:4096\n\t" \
"v_mfma_f32_16x16x32_bf16 v[212:215], a[36:39], v[140:143], v[212:215]\n\t" \
"v_mfma_f32_16x16x32_bf16 v[216:219], a[92:95], v[136:139], v[216:219]\n\t" \
"ds_read_b128 v[100:103], v249 offset:18432\n\t" \
"v_mfma_f32_16x16x32_bf16 v[220:223], a[92:95], v[140:143], v[220:223]\n\t" \
"s_add_u32 s90, s87, %[lg]\n\t" \
"s_add_u32 s90, s90, 0\n\t" \
"s_mov_b32 m0, s90\n\t" \
"s_add_u32 s95, s89, 0\n\t" \
"buffer_load_dwordx4 %[vg], %[rh], s95 offen offset:0 lds\n\t" \
"v_mfma_f32_16x16x32_bf16 v[224:227], a[148:151], v[136:139], v[224:227]\n\t" \
"ds_read_b128 v[104:107], v249 offset:5120\n\t" \
"v_mfma_f32_16x16x32_bf16 v[228:231], a[148:151], v[140:143], v[228:231]\n\t" \
"v_mfma_f32_16x16x32_bf16 v[232:235], a[204:207], v[136:139], v[232:235]\n\t" \
"ds_read_b128 v[108:111], v249 offset:19456\n\t" \
"v_mfma_f32_16x16x32_bf16 v[236:239], a[204:207], v[140:143], v[236:239]\n\t" \
"v_mfma_f32_16x16x32_bf16 v[208:211], a[40:43], v[144:147], v[208:211]\n\t" \
"ds_read_b128 v[112:115], v249 offset:6144\n\t" \
"v_mfma_f32_16x16x32_bf16 v[212:215], a[40:43], v[148:151], v[212:215]\n\t" \
"v_mfma_f32_16x16x32_bf16 v[216:219], a[96:99], v[144:147], v[216:219]\n\t" \
"ds_read_b128 v[116:119], v249 offset:20480\n\t" \
"v_mfma_f32_16x16x32_bf16 v[220:223], a[96:99], v[148:151], v[220:223]\n\t" \
"v_mfma_f32_16x16x32_bf16 v[224:227], a[152:155], v[144:147], v[224:227]\n\t" \
"v_mfma_f32_16x16x32_bf16 v[228:231], a[152:155], v[148:151], v[228:231]\n\t" \
"v_mfma_f32_16x16x32_bf16 v[232:235], a[208:211], v[144:147], v[232:235]\n\t" \
"v_mfma_f32_16x16x32_bf16 v[236:239], a[208:211], v[148:151], v[236:239]\n\t" \
"v_mfma_f32_16x16x32_bf16 v[208:211], a[44:47], v[152:155], v[208:211]\n\t" \
"v_mfma_f32_16x16x32_bf16 v[212:215], a[44:47], v[156:159], v[212:215]\n\t" \
"v_mfma_f32_16x16x32_bf16 v[216:219], a[100:103], v[152:155], v[216:219]\n\t" \
"v_mfma_f32_16x16x32_bf16 v[220:223], a[100:103], v[156:159], v[220:223]\n\t" \
"v_mfma_f32_16x16x32_bf16 v[224:227], a[156:159], v[152:155], v[224:227]\n\t" \
"v_mfma_f32_16x16x32_bf16 v[228:231], a[156:159], v[156:159], v[228:231]\n\t" \
"v_mfma_f32_16x16x32_bf16 v[232:235], a[212:215], v[152:155], v[232:235]\n\t" \
"v_mfma_f32_16x16x32_bf16 v[236:239], a[212:215], v[156:159], v[236:239]\n\t" \
"buffer_load_dwordx4 %[vg], %[rh], s95 offen offset:1024 lds\n\t" \
"v_mfma_f32_16x16x32_bf16 v[208:211], a[48:51], v[160:163], v[208:211]\n\t" \
"v_mfma_f32_16x16x32_bf16 v[212:215], a[48:51], v[164:167], v[212:215]\n\t" \
"v_mfma_f32_16x16x32_bf16 v[216:219], a[104:107], v[160:163], v[216:219]\n\t" \
"v_mfma_f32_16x16x32_bf16 v[220:223], a[104:107], v[164:167], v[220:223]\n\t" \
"v_mfma_f32_16x16x32_bf16 v[224:227], a[160:163], v[160:163], v[224:227]\n\t" \
"v_mfma_f32_16x16x32_bf16 v[228:231], a[160:163], v[164:167], v[228:231]\n\t" \
"v_mfma_f32_16x16x32_bf16 v[232:235], a[216:219], v[160:163], v[232:235]\n\t" \
"v_mfma_f32_16x16x32_bf16 v[236:239], a[216:219], v[164:167], v[236:239]\n\t" \
"v_mfma_f32_16x16x32_bf16 v[208:211], a[52:55], v[168:171], v[208:211]\n\t" \
"v_mfma_f32_16x16x32_bf16 v[212:215], a[52:55], v[172:175], v[212:215]\n\t" \
"v_mfma_f32_16x16x32_bf16 v[216:219], a[108:111], v[168:171], v[216:219]\n\t" \
"v_mfma_f32_16x16x32_bf16 v[220:223], a[108:111], v[172:175], v[220:223]\n\t" \
"v_mfma_f32_16x16x32_bf16 v[224:227], a[164:167], v[168:171], v[224:227]\n\t" \
"v_mfma_f32_16x16x32_bf16 v[228:231], a[164:167], v[172:175], v[228:231]\n\t" \
"v_mfma_f32_16x16x32_bf16 v[232:235], a[220:223], v[168:171], v[232:235]\n\t" \
"v_mfma_f32_16x16x32_bf16 v[236:239], a[220:223], v[172:175], v[236:239]\n\t" \
"s_add_u32 s92, s92, 28672\n\t" \
"s_cmp_eq_u32 s92, 114688\n\t" \
"s_cselect_b32 s92, 0, s92\n\t" \
"v_mov_b32 v248, v249\n\t" \
"s_add_u32 s91, s91, 917504\n\t" \
"s_add_u32 s88, s88, 2\n\t" \
"s_cmp_lt_u32 s88, 32\n\t" \
"s_nop 0\n\t" \
"s_cbranch_scc1 Lp3%=\n\t" \
"v_cvt_pk_bf16_f32 v240, v208, v209\n\t" \
"v_cvt_pk_bf16_f32 v241, v210, v211\n\t" \
"buffer_store_dwordx2 v[240:241], %[vs0], %[ro], s91 offen offset:0 sc1\n\t" \
"v_cvt_pk_bf16_f32 v244, v212, v213\n\t" \
"v_cvt_pk_bf16_f32 v245, v214, v215\n\t" \
"buffer_store_dwordx2 v[244:245], %[vs1], %[ro], s91 offen offset:0 sc1\n\t" \
"v_cvt_pk_bf16_f32 v240, v216, v217\n\t" \
"v_cvt_pk_bf16_f32 v241, v218, v219\n\t" \
"buffer_store_dwordx2 v[240:241], %[vs0], %[ro], s91 offen offset:512 sc1\n\t" \
"v_cvt_pk_bf16_f32 v244, v220, v221\n\t" \
"v_cvt_pk_bf16_f32 v245, v222, v223\n\t" \
"buffer_store_dwordx2 v[244:245], %[vs1], %[ro], s91 offen offset:512 sc1\n\t" \
"v_cvt_pk_bf16_f32 v240, v224, v225\n\t" \
"v_cvt_pk_bf16_f32 v241, v226, v227\n\t" \
"buffer_store_dwordx2 v[240:241], %[vs0], %[ro], s91 offen offset:1024 sc1\n\t" \
"v_cvt_pk_bf16_f32 v244, v228, v229\n\t" \
"v_cvt_pk_bf16_f32 v245, v230, v231\n\t" \
"buffer_store_dwordx2 v[244:245], %[vs1], %[ro], s91 offen offset:1024 sc1\n\t" \
"v_cvt_pk_bf16_f32 v240, v232, v233\n\t" \
"v_cvt_pk_bf16_f32 v241, v234, v235\n\t" \
"buffer_store_dwordx2 v[240:241], %[vs0], %[ro], s91 offen offset:1536 sc1\n\t" \
"v_cvt_pk_bf16_f32 v244, v236, v237\n\t" \
"v_cvt_pk_bf16_f32 v245, v238, v239\n\t" \
"buffer_store_dwordx2 v[244:245], %[vs1], %[ro], s91 offen offset:1536 sc1\n\t" \
"s_waitcnt vmcnt(0) lgkmcnt(0)\n\t" \
"s_barrier\n\t" \
"s_mov_b64 exec, 1\n\t" \
"buffer_atomic_add v250, off, %[rc], s94\n\t" \
"s_mov_b64 exec, -1\n\t" \
"s_add_u32 s94, s94, 128\n\t" \
"s_mov_b64 exec, 1\n\t" \
"buffer_atomic_add v250, off, %[rc], s94\n\t" \
"s_mov_b64 exec, -1\n\t" \
"s_add_u32 s94, s94, 128\n\t" \
"s_mov_b32 m0, s93\n\t" \
"s_nop 1\n\t"

#define ASM_P3_0W0_CLOB "v64", "v65", "v66", "v67", "v68", "v69", "v70", "v71", "v72", "v73", "v74", "v75", "v76", "v77", "v78", "v79", "v80", "v81", "v82", "v83", "v84", "v85", "v86", "v87", "v88", "v89", "v90", "v91", "v92", "v93", "v94", "v95", "v96", "v97", "v98", "v99", "v100", "v101", "v102", "v103", "v104", "v105", "v106", "v107", "v108", "v109", "v110", "v111", "v112", "v113", "v114", "v115", "v116", "v117", "v118", "v119", "v120", "v121", "v122", "v123", "v124", "v125", "v126", "v127", "v128", "v129", "v130", "v131", "v132", "v133", "v134", "v135", "v136", "v137", "v138", "v139", "v140", "v141", "v142", "v143", "v144", "v145", "v146", "v147", "v148", "v149", "v150", "v151", "v152", "v153", "v154", "v155", "v156", "v157", "v158", "v159", "v160", "v161", "v162", "v163", "v164", "v165", "v166", "v167", "v168", "v169", "v170", "v171", "v172", "v173", "v174", "v175", "v176", "v177", "v178", "v179", "v180", "v181", "v182", "v183", "v184", "v185", "v186", "v187", "v188", "v189", "v190", "v191", "v192", "v193", "v194", "v195", "v196", "v197", "v198", "v199", "v200", "v201", "v202", "v203", "v204", "v205", "v206", "v207", "v208", "v209", "v210", "v211", "v212", "v213", "v214", "v215", "v216", "v217", "v218", "v219", "v220", "v221", "v222", "v223", "v224", "v225", "v226", "v227", "v228", "v229", "v230", "v231", "v232", "v233", "v234", "v235", "v236", "v237", "v238", "v239", "v240", "v241", "v244", "v245", "v248", "v249", "v250", "a0", "a1", "a2", "a3", "a4", "a5", "a6", "a7", "a8", "a9", "a10", "a11", "a12", "a13", "a14", "a15", "a16", "a17", "a18", "a19", "a20", "a21", "a22", "a23", "a24", "a25", "a26", "a27", "a28", "a29", "a30", "a31", "a32", "a33", "a34", "a35", "a36", "a37", "a38", "a39", "a40", "a41", "a42", "a43", "a44", "a45", "a46", "a47", "a48", "a49", "a50", "a51", "a52", "a53", "a54", "a55", "a56", "a57", "a58", "a59", "a60", "a61", "a62", "a63", "a64", "a65", "a66", "a67", "a68", "a69", "a70", "a71", "a72", "a73", "a74", "a75", "a76", "a77", "a78", "a79", "a80", "a81", "a82", "a83", "a84", "a85", "a86", "a87", "a88", "a89", "a90", "a91", "a92", "a93", "a94", "a95", "a96", "a97", "a98", "a99", "a100", "a101", "a102", "a103", "a104", "a105", "a106", "a107", "a108", "a109", "a110", "a111", "a112", "a113", "a114", "a115", "a116", "a117", "a118", "a119", "a120", "a121", "a122", "a123", "a124", "a125", "a126", "a127", "a128", "a129", "a130", "a131", "a132", "a133", "a134", "a135", "a136", "a137", "a138", "a139", "a140", "a141", "a142", "a143", "a144", "a145", "a146", "a147", "a148", "a149", "a150", "a151", "a152", "a153", "a154", "a155", "a156", "a157", "a158", "a159", "a160", "a161", "a162", "a163", "a164", "a165", "a166", "a167", "a168", "a169", "a170", "a171", "a172", "a173", "a174", "a175", "a176", "a177", "a178", "a179", "a180", "a181", "a182", "a183", "a184", "a185", "a186", "a187", "a188", "a189", "a190", "a191", "a192", "a193", "a194", "a195", "a196", "a197", "a198", "a199", "a200", "a201", "a202", "a203", "a204", "a205", "a206", "a207", "a208", "a209", "a210", "a211", "a212", "a213", "a214", "a215", "a216", "a217", "a218", "a219", "a220", "a221", "a222", "a223", "s86", "s87", "s88", "s89", "s90", "s91", "s92", "s93", "s94", "s95", "vcc", "scc", "memory"

#define ASM_P3_0W1_TEXT \
"s_mov_b32 s93, m0\n\t" \
"s_add_u32 s89, %[src0], 172032\n\t" \
"s_mov_b32 s87, 86016\n\t" \
"s_waitcnt vmcnt(0)\n\t" \
"s_barrier\n\t" \
"v_mov_b32 v250, 1\n\t" \
"v_mov_b32 v248, %[lr]\n\t" \
"ds_read_b128 v[64:67], v248 offset:0\n\t" \
"ds_read_b128 v[68:71], v248 offset:14336\n\t" \
"ds_read_b128 v[72:75], v248 offset:1024\n\t" \
"ds_read_b128 v[76:79], v248 offset:15360\n\t" \
"ds_read_b128 v[80:83], v248 offset:2048\n\t" \
"ds_read_b128 v[84:87], v248 offset:16384\n\t" \
"ds_read_b128 v[88:91], v248 offset:3072\n\t" \
"ds_read_b128 v[92:95], v248 offset:17408\n\t" \
"ds_read_b128 v[96:99], v248 offset:4096\n\t" \
"ds_read_b128 v[100:103], v248 offset:18432\n\t" \
"ds_read_b128 v[104:107], v248 offset:5120\n\t" \
"ds_read_b128 v[108:111], v248 offset:19456\n\t" \
"ds_read_b128 v[112:115], v248 offset:6144\n\t" \
"ds_read_b128 v[116:119], v248 offset:20480\n\t" \
"s_mov_b32 s92, 0\n\t" \
"s_mov_b32 s91, %[so0]\n\t" \
"s_mov_b32 s94, %[ao0]\n\t" \
"s_waitcnt lgkmcnt(0)\n\t" \
"v_mfma_f32_16x16x32_bf16 v[176:179], a[0:3], v[64:67], 0\n\t" \
"v_mfma_f32_16x16x32_bf16 v[180:183], a[0:3], v[68:71], 0\n\t" \
"ds_read_b128 v[120:123], v248 offset:7168\n\t" \
"v_mfma_f32_16x16x32_bf16 v[184:187], a[56:59], v[64:67], 0\n\t" \
"v_mfma_f32_16x16x32_bf16 v[188:191], a[56:59], v[68:71], 0\n\t" \
"ds_read_b128 v[124:127], v248 offset:21504\n\t" \
"v_mfma_f32_16x16x32_bf16 v[192:195], a[112:115], v[64:67], 0\n\t" \
"v_mfma_f32_16x16x32_bf16 v[196:199], a[112:115], v[68:71], 0\n\t" \
"ds_read_b128 v[128:131], v248 offset:8192\n\t" \
"v_mfma_f32_16x16x32_bf16 v[200:203], a[168:171], v[64:67], 0\n\t" \
"v_mfma_f32_16x16x32_bf16 v[204:207], a[168:171], v[68:71], 0\n\t" \
"ds_read_b128 v[132:135], v248 offset:22528\n\t" \
"v_mfma_f32_16x16x32_bf16 v[176:179], a[4:7], v[72:75], v[176:179]\n\t" \
"v_mfma_f32_16x16x32_bf16 v[180:183], a[4:7], v[76:79], v[180:183]\n\t" \
"ds_read_b128 v[136:139], v248 offset:9216\n\t" \
"v_mfma_f32_16x16x32_bf16 v[184:187], a[60:63], v[72:75], v[184:187]\n\t" \
"v_mfma_f32_16x16x32_bf16 v[188:191], a[60:63], v[76:79], v[188:191]\n\t" \
"ds_read_b128 v[140:143], v248 offset:23552\n\t" \
"v_mfma_f32_16x16x32_bf16 v[192:195], a[116:119], v[72:75], v[192:195]\n\t" \
"v_mfma_f32_16x16x32_bf16 v[196:199], a[116:119], v[76:79], v[196:199]\n\t" \
"ds_read_b128 v[144:147], v248 offset:10240\n\t" \
"v_mfma_f32_16x16x32_bf16 v[200:203], a[172:175], v[72:75], v[200:203]\n\t" \
"v_mfma_f32_16x16x32_bf16 v[204:207], a[172:175], v[76:79], v[204:207]\n\t" \
"ds_read_b128 v[148:151], v248 offset:24576\n\t" \
"v_mfma_f32_16x16x32_bf16 v[176:179], a[8:11], v[80:83], v[176:179]\n\t" \
"v_mfma_f32_16x16x32_bf16 v[180:183], a[8:11], v[84:87], v[180:183]\n\t" \
"ds_read_b128 v[152:155], v248 offset:11264\n\t" \
"v_mfma_f32_16x16x32_bf16 v[184:187], a[64:67], v[80:83], v[184:187]\n\t" \
"v_mfma_f32_16x16x32_bf16 v[188:191], a[64:67], v[84:87], v[188:191]\n\t" \
"ds_read_b128 v[156:159], v248 offset:25600\n\t" \
"s_add_u32 s90, s87, %[lg]\n\t" \
"s_add_u32 s90, s90, 0\n\t" \
"s_mov_b32 m0, s90\n\t" \
"s_add_u32 s95, s89, 0\n\t" \
"buffer_load_dwordx4 %[vg], %[rh], s95 offen offset:2048 lds\n\t" \
"v_mfma_f32_16x16x32_bf16 v[192:195], a[120:123], v[80:83], v[192:195]\n\t" \
"v_mfma_f32_16x16x32_bf16 v[196:199], a[120:123], v[84:87], v[196:199]\n\t" \
"ds_read_b128 v[160:163], v248 offset:12288\n\t" \
"v_mfma_f32_16x16x32_bf16 v[200:203], a[176:179], v[80:83], v[200:203]\n\t" \
"v_mfma_f32_16x16x32_bf16 v[204:207], a[176:179], v[84:87], v[204:207]\n\t" \
"ds_read_b128 v[164:167], v248 offset:26624\n\t" \
"v_mfma_f32_16x16x32_bf16 v[176:179], a[12:15], v[88:91], v[176:179]\n\t" \
"v_mfma_f32_16x16x32_bf16 v[180:183], a[12:15], v[92:95], v[180:183]\n\t" \
"ds_read_b128 v[168:171], v248 offset:13312\n\t" \
"v_mfma_f32_16x16x32_bf16 v[184:187], a[68:71], v[88:91], v[184:187]\n\t" \
"v_mfma_f32_16x16x32_bf16 v[188:191], a[68:71], v[92:95], v[188:191]\n\t" \
"ds_read_b128 v[172:175], v248 offset:27648\n\t" \
"v_mfma_f32_16x16x32_bf16 v[192:195], a[124:127], v[88:91], v[192:195]\n\t" \
"v_mfma_f32_16x16x32_bf16 v[196:199], a[124:127], v[92:95], v[196:199]\n\t" \
"v_mfma_f32_16x16x32_bf16 v[200:203], a[180:183], v[88:91], v[200:203]\n\t" \
"v_mfma_f32_16x16x32_bf16 v[204:207], a[180:183], v[92:95], v[204:207]\n\t" \
"v_mfma_f32_16x16x32_bf16 v[176:179], a[16:19], v[96:99], v[176:179]\n\t" \
"v_mfma_f32_16x16x32_bf16 v[180:183], a[16:19], v[100:103], v[180:183]\n\t" \
"v_mfma_f32_16x16x32_bf16 v[184:187], a[72:75], v[96:99], v[184:187]\n\t" \
"v_mfma_f32_16x16x32_bf16 v[188:191], a[72:75], v[100:103], v[188:191]\n\t" \
"v_mfma_f32_16x16x32_bf16 v[192:195], a[128:131], v[96:99], v[192:195]\n\t" \
"v_mfma_f32_16x16x32_bf16 v[196:199], a[128:131], v[100:103], v[196:199]\n\t" \
"v_mfma_f32_16x16x32_bf16 v[200:203], a[184:187], v[96:99], v[200:203]\n\t" \
"v_mfma_f32_16x16x32_bf16 v[204:207], a[184:187], v[100:103], v[204:207]\n\t" \
"buffer_load_dwordx4 %[vg], %[rh], s95 offen offset:3072 lds\n\t" \
"v_mfma_f32_16x16x32_bf16 v[176:179], a[20:23], v[104:107], v[176:179]\n\t" \
"v_mfma_f32_16x16x32_bf16 v[180:183], a[20:23], v[108:111], v[180:183]\n\t" \
"v_mfma_f32_16x16x32_bf16 v[184:187], a[76:79], v[104:107], v[184:187]\n\t" \
"v_mfma_f32_16x16x32_bf16 v[188:191], a[76:79], v[108:111], v[188:191]\n\t" \
"v_mfma_f32_16x16x32_bf16 v[192:195], a[132:135], v[104:107], v[192:195]\n\t" \
"v_mfma_f32_16x16x32_bf16 v[196:199], a[132:135], v[108:111], v[196:199]\n\t" \
"v_mfma_f32_16x16x32_bf16 v[200:203], a[188:191], v[104:107], v[200:203]\n\t" \
"v_mfma_f32_16x16x32_bf16 v[204:207], a[188:191], v[108:111], v[204:207]\n\t" \
"v_mfma_f32_16x16x32_bf16 v[176:179], a[24:27], v[112:115], v[176:179]\n\t" \
"v_mfma_f32_16x16x32_bf16 v[180:183], a[24:27], v[116:119], v[180:183]\n\t" \
"v_mfma_f32_16x16x32_bf16 v[184:187], a[80:83], v[112:115], v[184:187]\n\t" \
"v_mfma_f32_16x16x32_bf16 v[188:191], a[80:83], v[116:119], v[188:191]\n\t" \
"v_mfma_f32_16x16x32_bf16 v[192:195], a[136:139], v[112:115], v[192:195]\n\t" \
"v_mfma_f32_16x16x32_bf16 v[196:199], a[136:139], v[116:119], v[196:199]\n\t" \
"v_mfma_f32_16x16x32_bf16 v[200:203], a[192:195], v[112:115], v[200:203]\n\t" \
"v_mfma_f32_16x16x32_bf16 v[204:207], a[192:195], v[116:119], v[204:207]\n\t" \
"s_add_u32 s89, s89, 57344\n\t" \
"s_min_u32 s89, s89, 1806336\n\t" \
"s_mov_b32 s87, s92\n\t" \
"s_add_u32 s95, s92, 28672\n\t" \
"s_cmp_eq_u32 s95, 114688\n\t" \
"s_cselect_b32 s95, 0, s95\n\t" \
"v_add_u32 v249, s95, %[lr]\n\t" \
"s_waitcnt vmcnt(63) lgkmcnt(0)\n\t" \
"s_barrier\n\t" \
"v_mfma_f32_16x16x32_bf16 v[176:179], a[28:31], v[120:123], v[176:179]\n\t" \
"ds_read_b128 v[64:67], v249 offset:0\n\t" \
"v_mfma_f32_16x16x32_bf16 v[180:183], a[28:31], v[124:127], v[180:183]\n\t" \
"v_mfma_f32_16x16x32_bf16 v[184:187], a[84:87], v[120:123], v[184:187]\n\t" \
"ds_read_b128 v[68:71], v249 offset:14336\n\t" \
"v_mfma_f32_16x16x32_bf16 v[188:191], a[84:87], v[124:127], v[188:191]\n\t" \
"v_mfma_f32_16x16x32_bf16 v[192:195], a[140:143], v[120:123], v[192:195]\n\t" \
"ds_read_b128 v[72:75], v249 offset:1024\n\t" \
"v_mfma_f32_16x16x32_bf16 v[196:199], a[140:143], v[124:127], v[196:199]\n\t" \
"v_mfma_f32_16x16x32_bf16 v[200:203], a[196:199], v[120:123], v[200:203]\n\t" \
"ds_read_b128 v[76:79], v249 offset:15360\n\t" \
"v_mfma_f32_16x16x32_bf16 v[204:207], a[196:199], v[124:127], v[204:207]\n\t" \
"v_mfma_f32_16x16x32_bf16 v[176:179], a[32:35], v[128:131], v[176:179]\n\t" \
"ds_read_b128 v[80:83], v249 offset:2048\n\t" \
"v_mfma_f32_16x16x32_bf16 v[180:183], a[32:35], v[132:135], v[180:183]\n\t" \
"v_mfma_f32_16x16x32_bf16 v[184:187], a[88:91], v[128:131], v[184:187]\n\t" \
"ds_read_b128 v[84:87], v249 offset:16384\n\t" \
"v_mfma_f32_16x16x32_bf16 v[188:191], a[88:91], v[132:135], v[188:191]\n\t" \
"v_mfma_f32_16x16x32_bf16 v[192:195], a[144:147], v[128:131], v[192:195]\n\t" \
"ds_read_b128 v[88:91], v249 offset:3072\n\t" \
"v_mfma_f32_16x16x32_bf16 v[196:199], a[144:147], v[132:135], v[196:199]\n\t" \
"v_mfma_f32_16x16x32_bf16 v[200:203], a[200:203], v[128:131], v[200:203]\n\t" \
"ds_read_b128 v[92:95], v249 offset:17408\n\t" \
"v_mfma_f32_16x16x32_bf16 v[204:207], a[200:203], v[132:135], v[204:207]\n\t" \
"v_mfma_f32_16x16x32_bf16 v[176:179], a[36:39], v[136:139], v[176:179]\n\t" \
"ds_read_b128 v[96:99], v249 offset:4096\n\t" \
"v_mfma_f32_16x16x32_bf16 v[180:183], a[36:39], v[140:143], v[180:183]\n\t" \
"v_mfma_f32_16x16x32_bf16 v[184:187], a[92:95], v[136:139], v[184:187]\n\t" \
"ds_read_b128 v[100:103], v249 offset:18432\n\t" \
"v_mfma_f32_16x16x32_bf16 v[188:191], a[92:95], v[140:143], v[188:191]\n\t" \
"s_add_u32 s90, s87, %[lg]\n\t" \
"s_add_u32 s90, s90, 0\n\t" \
"s_mov_b32 m0, s90\n\t" \
"s_add_u32 s95, s89, 0\n\t" \
"buffer_load_dwordx4 %[vg], %[rh], s95 offen offset:0 lds\n\t" \
"v_mfma_f32_16x16x32_bf16 v[192:195], a[148:151], v[136:139], v[192:195]\n\t" \
"ds_read_b128 v[104:107], v249 offset:5120\n\t" \
"v_mfma_f32_16x16x32_bf16 v[196:199], a[148:151], v[140:143], v[196:199]\n\t" \
"v_mfma_f32_16x16x32_bf16 v[200:203], a[204:207], v[136:139], v[200:203]\n\t" \
"ds_read_b128 v[108:111], v249 offset:19456\n\t" \
"v_mfma_f32_16x16x32_bf16 v[204:207], a[204:207], v[140:143], v[204:207]\n\t" \
"v_mfma_f32_16x16x32_bf16 v[176:179], a[40:43], v[144:147], v[176:179]\n\t" \
"ds_read_b128 v[112:115], v249 offset:6144\n\t" \
"v_mfma_f32_16x16x32_bf16 v[180:183], a[40:43], v[148:151], v[180:183]\n\t" \
"v_mfma_f32_16x16x32_bf16 v[184:187], a[96:99], v[144:147], v[184:187]\n\t" \
"ds_read_b128 v[116:119], v249 offset:20480\n\t" \
"v_mfma_f32_16x16x32_bf16 v[188:191], a[96:99], v[148:151], v[188:191]\n\t" \
"v_mfma_f32_16x16x32_bf16 v[192:195], a[152:155], v[144:147], v[192:195]\n\t" \
"v_mfma_f32_16x16x32_bf16 v[196:199], a[152:155], v[148:151], v[196:199]\n\t" \
"v_mfma_f32_16x16x32_bf16 v[200:203], a[208:211], v[144:147], v[200:203]\n\t" \
"v_mfma_f32_16x16x32_bf16 v[204:207], a[208:211], v[148:151], v[204:207]\n\t" \
"v_mfma_f32_16x16x32_bf16 v[176:179], a[44:47], v[152:155], v[176:179]\n\t" \
"v_mfma_f32_16x16x32_bf16 v[180:183], a[44:47], v[156:159], v[180:183]\n\t" \
"v_mfma_f32_16x16x32_bf16 v[184:187], a[100:103], v[152:155], v[184:187]\n\t" \
"v_mfma_f32_16x16x32_bf16 v[188:191], a[100:103], v[156:159], v[188:191]\n\t" \
"v_mfma_f32_16x16x32_bf16 v[192:195], a[156:159], v[152:155], v[192:195]\n\t" \
"v_mfma_f32_16x16x32_bf16 v[196:199], a[156:159], v[156:159], v[196:199]\n\t" \
"v_mfma_f32_16x16x32_bf16 v[200:203], a[212:215], v[152:155], v[200:203]\n\t" \
"v_mfma_f32_16x16x32_bf16 v[204:207], a[212:215], v[156:159], v[204:207]\n\t" \
"buffer_load_dwordx4 %[vg], %[rh], s95 offen offset:1024 lds\n\t" \
"v_mfma_f32_16x16x32_bf16 v[176:179], a[48:51], v[160:163], v[176:179]\n\t" \
"v_mfma_f32_16x16x32_bf16 v[180:183], a[48:51], v[164:167], v[180:183]\n\t" \
"v_mfma_f32_16x16x32_bf16 v[184:187], a[104:107], v[160:163], v[184:187]\n\t" \
"v_mfma_f32_16x16x32_bf16 v[188:191], a[104:107], v[164:167], v[188:191]\n\t" \
"v_mfma_f32_16x16x32_bf16 v[192:195], a[160:163], v[160:163], v[192:195]\n\t" \
"v_mfma_f32_16x16x32_bf16 v[196:199], a[160:163], v[164:167], v[196:199]\n\t" \
"v_mfma_f32_16x16x32_bf16 v[200:203], a[216:219], v[160:163], v[200:203]\n\t" \
"v_mfma_f32_16x16x32_bf16 v[204:207], a[216:219], v[164:167], v[204:207]\n\t" \
"v_mfma_f32_16x16x32_bf16 v[176:179], a[52:55], v[168:171], v[176:179]\n\t" \
"v_mfma_f32_16x16x32_bf16 v[180:183], a[52:55], v[172:175], v[180:183]\n\t" \
"v_mfma_f32_16x16x32_bf16 v[184:187], a[108:111], v[168:171], v[184:187]\n\t" \
"v_mfma_f32_16x16x32_bf16 v[188:191], a[108:111], v[172:175], v[188:191]\n\t" \
"v_mfma_f32_16x16x32_bf16 v[192:195], a[164:167], v[168:171], v[192:195]\n\t" \
"v_mfma_f32_16x16x32_bf16 v[196:199], a[164:167], v[172:175], v[196:199]\n\t" \
"v_mfma_f32_16x16x32_bf16 v[200:203], a[220:223], v[168:171], v[200:203]\n\t" \
"v_mfma_f32_16x16x32_bf16 v[204:207], a[220:223], v[172:175], v[204:207]\n\t" \
"s_add_u32 s92, s92, 28672\n\t" \
"s_cmp_eq_u32 s92, 114688\n\t" \
"s_cselect_b32 s92, 0, s92\n\t" \
"v_mov_b32 v248, v249\n\t" \
"s_add_u32 s91, s91, 917504\n\t" \
"v_cvt_pk_bf16_f32 v240, v176, v177\n\t" \
"v_cvt_pk_bf16_f32 v241, v178, v179\n\t" \
"buffer_store_dwordx2 v[240:241], %[vs0], %[ro], s91 offen offset:0 sc1\n\t" \
"v_cvt_pk_bf16_f32 v244, v180, v181\n\t" \
"v_cvt_pk_bf16_f32 v245, v182, v183\n\t" \
"buffer_store_dwordx2 v[244:245], %[vs1], %[ro], s91 offen offset:0 sc1\n\t" \
"v_cvt_pk_bf16_f32 v240, v184, v185\n\t" \
"v_cvt_pk_bf16_f32 v241, v186, v187\n\t" \
"buffer_store_dwordx2 v[240:241], %[vs0], %[ro], s91 offen offset:512 sc1\n\t" \
"v_cvt_pk_bf16_f32 v244, v188, v189\n\t" \
"v_cvt_pk_bf16_f32 v245, v190, v191\n\t" \
"buffer_store_dwordx2 v[244:245], %[vs1], %[ro], s91 offen offset:512 sc1\n\t" \
"v_cvt_pk_bf16_f32 v240, v192, v193\n\t" \
"v_cvt_pk_bf16_f32 v241, v194, v195\n\t" \
"buffer_store_dwordx2 v[240:241], %[vs0], %[ro], s91 offen offset:1024 sc1\n\t" \
"v_cvt_pk_bf16_f32 v244, v196, v197\n\t" \
"v_cvt_pk_bf16_f32 v245, v198, v199\n\t" \
"buffer_store_dwordx2 v[244:245], %[vs1], %[ro], s91 offen offset:1024 sc1\n\t" \
"v_cvt_pk_bf16_f32 v240, v200, v201\n\t" \
"v_cvt_pk_bf16_f32 v241, v202, v203\n\t" \
"buffer_store_dwordx2 v[240:241], %[vs0], %[ro], s91 offen offset:1536 sc1\n\t" \
"v_cvt_pk_bf16_f32 v244, v204, v205\n\t" \
"v_cvt_pk_bf16_f32 v245, v206, v207\n\t" \
"buffer_store_dwordx2 v[244:245], %[vs1], %[ro], s91 offen offset:1536 sc1\n\t" \
"s_waitcnt vmcnt(0)\n\t" \
"s_barrier\n\t" \
"s_add_u32 s94, s94, 128\n\t" \
"s_waitcnt lgkmcnt(0)\n\t" \
"v_mfma_f32_16x16x32_bf16 v[208:211], a[0:3], v[64:67], 0\n\t" \
"v_mfma_f32_16x16x32_bf16 v[212:215], a[0:3], v[68:71], 0\n\t" \
"ds_read_b128 v[120:123], v248 offset:7168\n\t" \
"v_mfma_f32_16x16x32_bf16 v[216:219], a[56:59], v[64:67], 0\n\t" \
"v_mfma_f32_16x16x32_bf16 v[220:223], a[56:59], v[68:71], 0\n\t" \
"ds_read_b128 v[124:127], v248 offset:21504\n\t" \
"v_mfma_f32_16x16x32_bf16 v[224:227], a[112:115], v[64:67], 0\n\t" \
"v_mfma_f32_16x16x32_bf16 v[228:231], a[112:115], v[68:71], 0\n\t" \
"ds_read_b128 v[128:131], v248 offset:8192\n\t" \
"v_mfma_f32_16x16x32_bf16 v[232:235], a[168:171], v[64:67], 0\n\t" \
"v_mfma_f32_16x16x32_bf16 v[236:239], a[168:171], v[68:71], 0\n\t" \
"ds_read_b128 v[132:135], v248 offset:22528\n\t" \
"v_mfma_f32_16x16x32_bf16 v[208:211], a[4:7], v[72:75], v[208:211]\n\t" \
"v_mfma_f32_16x16x32_bf16 v[212:215], a[4:7], v[76:79], v[212:215]\n\t" \
"ds_read_b128 v[136:139], v248 offset:9216\n\t" \
"v_mfma_f32_16x16x32_bf16 v[216:219], a[60:63], v[72:75], v[216:219]\n\t" \
"v_mfma_f32_16x16x32_bf16 v[220:223], a[60:63], v[76:79], v[220:223]\n\t" \
"ds_read_b128 v[140:143], v248 offset:23552\n\t" \
"v_mfma_f32_16x16x32_bf16 v[224:227], a[116:119], v[72:75], v[224:227]\n\t" \
"v_mfma_f32_16x16x32_bf16 v[228:231], a[116:119], v[76:79], v[228:231]\n\t" \
"ds_read_b128 v[144:147], v248 offset:10240\n\t" \
"v_mfma_f32_16x16x32_bf16 v[232:235], a[172:175], v[72:75], v[232:235]\n\t" \
"v_mfma_f32_16x16x32_bf16 v[236:239], a[172:175], v[76:79], v[236:239]\n\t" \
"ds_read_b128 v[148:151], v248 offset:24576\n\t" \
"v_mfma_f32_16x16x32_bf16 v[208:211], a[8:11], v[80:83], v[208:211]\n\t" \
"v_mfma_f32_16x16x32_bf16 v[212:215], a[8:11], v[84:87], v[212:215]\n\t" \
"ds_read_b128 v[152:155], v248 offset:11264\n\t" \
"v_mfma_f32_16x16x32_bf16 v[216:219], a[64:67], v[80:83], v[216:219]\n\t" \
"v_mfma_f32_16x16x32_bf16 v[220:223], a[64:67], v[84:87], v[220:223]\n\t" \
"ds_read_b128 v[156:159], v248 offset:25600\n\t" \
"s_add_u32 s90, s87, %[lg]\n\t" \
"s_add_u32 s90, s90, 0\n\t" \
"s_mov_b32 m0, s90\n\t" \
"s_add_u32 s95, s89, 0\n\t" \
"buffer_load_dwordx4 %[vg], %[rh], s95 offen offset:2048 lds\n\t" \
"v_mfma_f32_16x16x32_bf16 v[224:227], a[120:123], v[80:83], v[224:227]\n\t" \
"v_mfma_f32_16x16x32_bf16 v[228:231], a[120:123], v[84:87], v[228:231]\n\t" \
"ds_read_b128 v[160:163], v248 offset:12288\n\t" \
"v_mfma_f32_16x16x32_bf16 v[232:235], a[176:179], v[80:83], v[232:235]\n\t" \
"v_mfma_f32_16x16x32_bf16 v[236:239], a[176:179], v[84:87], v[236:239]\n\t" \
"ds_read_b128 v[164:167], v248 offset:26624\n\t" \
"v_mfma_f32_16x16x32_bf16 v[208:211], a[12:15], v[88:91], v[208:211]\n\t" \
"v_mfma_f32_16x16x32_bf16 v[212:215], a[12:15], v[92:95], v[212:215]\n\t" \
"ds_read_b128 v[168:171], v248 offset:13312\n\t" \
"v_mfma_f32_16x16x32_bf16 v[216:219], a[68:71], v[88:91], v[216:219]\n\t" \
"v_mfma_f32_16x16x32_bf16 v[220:223], a[68:71], v[92:95], v[220:223]\n\t" \
"ds_read_b128 v[172:175], v248 offset:27648\n\t" \
"v_mfma_f32_16x16x32_bf16 v[224:227], a[124:127], v[88:91], v[224:227]\n\t" \
"v_mfma_f32_16x16x32_bf16 v[228:231], a[124:127], v[92:95], v[228:231]\n\t" \
"v_mfma_f32_16x16x32_bf16 v[232:235], a[180:183], v[88:91], v[232:235]\n\t" \
"v_mfma_f32_16x16x32_bf16 v[236:239], a[180:183], v[92:95], v[236:239]\n\t" \
"v_mfma_f32_16x16x32_bf16 v[208:211], a[16:19], v[96:99], v[208:211]\n\t" \
"v_mfma_f32_16x16x32_bf16 v[212:215], a[16:19], v[100:103], v[212:215]\n\t" \
"v_mfma_f32_16x16x32_bf16 v[216:219], a[72:75], v[96:99], v[216:219]\n\t" \
"v_mfma_f32_16x16x32_bf16 v[220:223], a[72:75], v[100:103], v[220:223]\n\t" \
"v_mfma_f32_16x16x32_bf16 v[224:227], a[128:131], v[96:99], v[224:227]\n\t" \
"v_mfma_f32_16x16x32_bf16 v[228:231], a[128:131], v[100:103], v[228:231]\n\t" \
"v_mfma_f32_16x16x32_bf16 v[232:235], a[184:187], v[96:99], v[232:235]\n\t" \
"v_mfma_f32_16x16x32_bf16 v[236:239], a[184:187], v[100:103], v[236:239]\n\t" \
"buffer_load_dwordx4 %[vg], %[rh], s95 offen offset:3072 lds\n\t" \
"v_mfma_f32_16x16x32_bf16 v[208:211], a[20:23], v[104:107], v[208:211]\n\t" \
"v_mfma_f32_16x16x32_bf16 v[212:215], a[20:23], v[108:111], v[212:215]\n\t" \
"v_mfma_f32_16x16x32_bf16 v[216:219], a[76:79], v[104:107], v[216:219]\n\t" \
"v_mfma_f32_16x16x32_bf16 v[220:223], a[76:79], v[108:111], v[220:223]\n\t" \
"v_mfma_f32_16x16x32_bf16 v[224:227], a[132:135], v[104:107], v[224:227]\n\t" \
"v_mfma_f32_16x16x32_bf16 v[228:231], a[132:135], v[108:111], v[228:231]\n\t" \
"v_mfma_f32_16x16x32_bf16 v[232:235], a[188:191], v[104:107], v[232:235]\n\t" \
"v_mfma_f32_16x16x32_bf16 v[236:239], a[188:191], v[108:111], v[236:239]\n\t" \
"v_mfma_f32_16x16x32_bf16 v[208:211], a[24:27], v[112:115], v[208:211]\n\t" \
"v_mfma_f32_16x16x32_bf16 v[212:215], a[24:27], v[116:119], v[212:215]\n\t" \
"v_mfma_f32_16x16x32_bf16 v[216:219], a[80:83], v[112:115], v[216:219]\n\t" \
"v_mfma_f32_16x16x32_bf16 v[220:223], a[80:83], v[116:119], v[220:223]\n\t" \
"v_mfma_f32_16x16x32_bf16 v[224:227], a[136:139], v[112:115], v[224:227]\n\t" \
"v_mfma_f32_16x16x32_bf16 v[228:231], a[136:139], v[116:119], v[228:231]\n\t" \
"v_mfma_f32_16x16x32_bf16 v[232:235], a[192:195], v[112:115], v[232:235]\n\t" \
"v_mfma_f32_16x16x32_bf16 v[236:239], a[192:195], v[116:119], v[236:239]\n\t" \
"s_add_u32 s89, s89, 57344\n\t" \
"s_min_u32 s89, s89, 1806336\n\t" \
"s_mov_b32 s87, s92\n\t" \
"s_add_u32 s95, s92, 28672\n\t" \
"s_cmp_eq_u32 s95, 114688\n\t" \
"s_cselect_b32 s95, 0, s95\n\t" \
"v_add_u32 v249, s95, %[lr]\n\t" \
"s_waitcnt vmcnt(63) lgkmcnt(0)\n\t" \
"s_barrier\n\t" \
"v_mfma_f32_16x16x32_bf16 v[208:211], a[28:31], v[120:123], v[208:211]\n\t" \
"ds_read_b128 v[64:67], v249 offset:0\n\t" \
"v_mfma_f32_16x16x32_bf16 v[212:215], a[28:31], v[124:127], v[212:215]\n\t" \
"v_mfma_f32_16x16x32_bf16 v[216:219], a[84:87], v[120:123], v[216:219]\n\t" \
"ds_read_b128 v[68:71], v249 offset:14336\n\t" \
"v_mfma_f32_16x16x32_bf16 v[220:223], a[84:87], v[124:127], v[220:223]\n\t" \
"v_mfma_f32_16x16x32_bf16 v[224:227], a[140:143], v[120:123], v[224:227]\n\t" \
"ds_read_b128 v[72:75], v249 offset:1024\n\t" \
"v_mfma_f32_16x16x32_bf16 v[228:231], a[140:143], v[124:127], v[228:231]\n\t" \
"v_mfma_f32_16x16x32_bf16 v[232:235], a[196:199], v[120:123], v[232:235]\n\t" \
"ds_read_b128 v[76:79], v249 offset:15360\n\t" \
"v_mfma_f32_16x16x32_bf16 v[236:239], a[196:199], v[124:127], v[236:239]\n\t" \
"v_mfma_f32_16x16x32_bf16 v[208:211], a[32:35], v[128:131], v[208:211]\n\t" \
"ds_read_b128 v[80:83], v249 offset:2048\n\t" \
"v_mfma_f32_16x16x32_bf16 v[212:215], a[32:35], v[132:135], v[212:215]\n\t" \
"v_mfma_f32_16x16x32_bf16 v[216:219], a[88:91], v[128:131], v[216:219]\n\t" \
"ds_read_b128 v[84:87], v249 offset:16384\n\t" \
"v_mfma_f32_16x16x32_bf16 v[220:223], a[88:91], v[132:135], v[220:223]\n\t" \
"v_mfma_f32_16x16x32_bf16 v[224:227], a[144:147], v[128:131], v[224:227]\n\t" \
"ds_read_b128 v[88:91], v249 offset:3072\n\t" \
"v_mfma_f32_16x16x32_bf16 v[228:231], a[144:147], v[132:135], v[228:231]\n\t" \
"v_mfma_f32_16x16x32_bf16 v[232:235], a[200:203], v[128:131], v[232:235]\n\t" \
"ds_read_b128 v[92:95], v249 offset:17408\n\t" \
"v_mfma_f32_16x16x32_bf16 v[236:239], a[200:203], v[132:135], v[236:239]\n\t" \
"v_mfma_f32_16x16x32_bf16 v[208:211], a[36:39], v[136:139], v[208:211]\n\t" \
"ds_read_b128 v[96:99], v249 offset:4096\n\t" \
"v_mfma_f32_16x16x32_bf16 v[212:215], a[36:39], v[140:143], v[212:215]\n\t" \
"v_mfma_f32_16x16x32_bf16 v[216:219], a[92:95], v[136:139], v[216:219]\n\t" \
"ds_read_b128 v[100:103], v249 offset:18432\n\t" \
"v_mfma_f32_16x16x32_bf16 v[220:223], a[92:95], v[140:143], v[220:223]\n\t" \
"s_add_u32 s90, s87, %[lg]\n\t" \
"s_add_u32 s90, s90, 0\n\t" \
"s_mov_b32 m0, s90\n\t" \
"s_add_u32 s95, s89, 0\n\t" \
"buffer_load_dwordx4 %[vg], %[rh], s95 offen offset:0 lds\n\t" \
"v_mfma_f32_16x16x32_bf16 v[224:227], a[148:151], v[136:139], v[224:227]\n\t" \
"ds_read_b128 v[104:107], v249 offset:5120\n\t" \
"v_mfma_f32_16x16x32_bf16 v[228:231], a[148:151], v[140:143], v[228:231]\n\t" \
"v_mfma_f32_16x16x32_bf16 v[232:235], a[204:207], v[136:139], v[232:235]\n\t" \
"ds_read_b128 v[108:111], v249 offset:19456\n\t" \
"v_mfma_f32_16x16x32_bf16 v[236:239], a[204:207], v[140:143], v[236:239]\n\t" \
"v_mfma_f32_16x16x32_bf16 v[208:211], a[40:43], v[144:147], v[208:211]\n\t" \
"ds_read_b128 v[112:115], v249 offset:6144\n\t" \
"v_mfma_f32_16x16x32_bf16 v[212:215], a[40:43], v[148:151], v[212:215]\n\t" \
"v_mfma_f32_16x16x32_bf16 v[216:219], a[96:99], v[144:147], v[216:219]\n\t" \
"ds_read_b128 v[116:119], v249 offset:20480\n\t" \
"v_mfma_f32_16x16x32_bf16 v[220:223], a[96:99], v[148:151], v[220:223]\n\t" \
"v_mfma_f32_16x16x32_bf16 v[224:227], a[152:155], v[144:147], v[224:227]\n\t" \
"v_mfma_f32_16x16x32_bf16 v[228:231], a[152:155], v[148:151], v[228:231]\n\t" \
"v_mfma_f32_16x16x32_bf16 v[232:235], a[208:211], v[144:147], v[232:235]\n\t" \
"v_mfma_f32_16x16x32_bf16 v[236:239], a[208:211], v[148:151], v[236:239]\n\t" \
"v_mfma_f32_16x16x32_bf16 v[208:211], a[44:47], v[152:155], v[208:211]\n\t" \
"v_mfma_f32_16x16x32_bf16 v[212:215], a[44:47], v[156:159], v[212:215]\n\t" \
"v_mfma_f32_16x16x32_bf16 v[216:219], a[100:103], v[152:155], v[216:219]\n\t" \
"v_mfma_f32_16x16x32_bf16 v[220:223], a[100:103], v[156:159], v[220:223]\n\t" \
"v_mfma_f32_16x16x32_bf16 v[224:227], a[156:159], v[152:155], v[224:227]\n\t" \
"v_mfma_f32_16x16x32_bf16 v[228:231], a[156:159], v[156:159], v[228:231]\n\t" \
"v_mfma_f32_16x16x32_bf16 v[232:235], a[212:215], v[152:155], v[232:235]\n\t" \
"v_mfma_f32_16x16x32_bf16 v[236:239], a[212:215], v[156:159], v[236:239]\n\t" \
"buffer_load_dwordx4 %[vg], %[rh], s95 offen offset:1024 lds\n\t" \
"v_mfma_f32_16x16x32_bf16 v[208:211], a[48:51], v[160:163], v[208:211]\n\t" \
"v_mfma_f32_16x16x32_bf16 v[212:215], a[48:51], v[164:167], v[212:215]\n\t" \
"v_mfma_f32_16x16x32_bf16 v[216:219], a[104:107], v[160:163], v[216:219]\n\t" \
"v_mfma_f32_16x16x32_bf16 v[220:223], a[104:107], v[164:167], v[220:223]\n\t" \
"v_mfma_f32_16x16x32_bf16 v[224:227], a[160:163], v[160:163], v[224:227]\n\t" \
"v_mfma_f32_16x16x32_bf16 v[228:231], a[160:163], v[164:167], v[228:231]\n\t" \
"v_mfma_f32_16x16x32_bf16 v[232:235], a[216:219], v[160:163], v[232:235]\n\t" \
"v_mfma_f32_16x16x32_bf16 v[236:239], a[216:219], v[164:167], v[236:239]\n\t" \
"v_mfma_f32_16x16x32_bf16 v[208:211], a[52:55], v[168:171], v[208:211]\n\t" \
"v_mfma_f32_16x16x32_bf16 v[212:215], a[52:55], v[172:175], v[212:215]\n\t" \
"v_mfma_f32_16x16x32_bf16 v[216:219], a[108:111], v[168:171], v[216:219]\n\t" \
"v_mfma_f32_16x16x32_bf16 v[220:223], a[108:111], v[172:175], v[220:223]\n\t" \
"v_mfma_f32_16x16x32_bf16 v[224:227], a[164:167], v[168:171], v[224:227]\n\t" \
"v_mfma_f32_16x16x32_bf16 v[228:231], a[164:167], v[172:175], v[228:231]\n\t" \
"v_mfma_f32_16x16x32_bf16 v[232:235], a[220:223], v[168:171], v[232:235]\n\t" \
"v_mfma_f32_16x16x32_bf16 v[236:239], a[220:223], v[172:175], v[236:239]\n\t" \
"s_add_u32 s92, s92, 28672\n\t" \
"s_cmp_eq_u32 s92, 114688\n\t" \
"s_cselect_b32 s92, 0, s92\n\t" \
"v_mov_b32 v248, v249\n\t" \
"s_add_u32 s91, s91, 917504\n\t" \
"s_waitcnt lgkmcnt(0)\n\t" \
"v_mfma_f32_16x16x32_bf16 v[176:179], a[0:3], v[64:67], 0\n\t" \
"v_mfma_f32_16x16x32_bf16 v[180:183], a[0:3], v[68:71], 0\n\t" \
"ds_read_b128 v[120:123], v248 offset:7168\n\t" \
"v_mfma_f32_16x16x32_bf16 v[184:187], a[56:59], v[64:67], 0\n\t" \
"v_cvt_pk_bf16_f32 v240, v208, v209\n\t" \
"v_mfma_f32_16x16x32_bf16 v[188:191], a[56:59], v[68:71], 0\n\t" \
"v_cvt_pk_bf16_f32 v241, v210, v211\n\t" \
"v_mfma_f32_16x16x32_bf16 v[192:195], a[112:115], v[64:67], 0\n\t" \
"buffer_store_dwordx2 v[240:241], %[vs0], %[ro], s91 offen offset:0 sc1\n\t" \
"v_mfma_f32_16x16x32_bf16 v[196:199], a[112:115], v[68:71], 0\n\t" \
"v_cvt_pk_bf16_f32 v244, v212, v213\n\t" \
"v_mfma_f32_16x16x32_bf16 v[200:203], a[168:171], v[64:67], 0\n\t" \
"v_cvt_pk_bf16_f32 v245, v214, v215\n\t" \
"v_mfma_f32_16x16x32_bf16 v[204:207], a[168:171], v[68:71], 0\n\t" \
"buffer_store_dwordx2 v[244:245], %[vs1], %[ro], s91 offen offset:0 sc1\n\t" \
"v_mfma_f32_16x16x32_bf16 v[176:179], a[4:7], v[72:75], v[176:179]\n\t" \
"v_cvt_pk_bf16_f32 v240, v216, v217\n\t" \
"v_mfma_f32_16x16x32_bf16 v[180:183], a[4:7], v[76:79], v[180:183]\n\t" \
"v_cvt_pk_bf16_f32 v241, v218, v219\n\t" \
"v_mfma_f32_16x16x32_bf16 v[184:187], a[60:63], v[72:75], v[184:187]\n\t" \
"buffer_store_dwordx2 v[240:241], %[vs0], %[ro], s91 offen offset:512 sc1\n\t" \
"v_mfma_f32_16x16x32_bf16 v[188:191], a[60:63], v[76:79], v[188:191]\n\t" \
"v_cvt_pk_bf16_f32 v244, v220, v221\n\t" \
"v_mfma_f32_16x16x32_bf16 v[192:195], a[116:119], v[72:75], v[192:195]\n\t" \
"v_cvt_pk_bf16_f32 v245, v222, v223\n\t" \
"v_mfma_f32_16x16x32_bf16 v[196:199], a[116:119], v[76:79], v[196:199]\n\t" \
"buffer_store_dwordx2 v[244:245], %[vs1], %[ro], s91 offen offset:512 sc1\n\t" \
"v_mfma_f32_16x16x32_bf16 v[200:203], a[172:175], v[72:75], v[200:203]\n\t" \
"v_cvt_pk_bf16_f32 v240, v224, v225\n\t" \
"v_mfma_f32_16x16x32_bf16 v[204:207], a[172:175], v[76:79], v[204:207]\n\t" \
"v_cvt_pk_bf16_f32 v241, v226, v227\n\t" \
"v_mfma_f32_16x16x32_bf16 v[176:179], a[8:11], v[80:83], v[176:179]\n\t" \
"buffer_store_dwordx2 v[240:241], %[vs0], %[ro], s91 offen offset:1024 sc1\n\t" \
"v_mfma_f32_16x16x32_bf16 v[180:183], a[8:11], v[84:87], v[180:183]\n\t" \
"v_cvt_pk_bf16_f32 v244, v228, v229\n\t" \
"v_mfma_f32_16x16x32_bf16 v[184:187], a[64:67], v[80:83], v[184:187]\n\t" \
"v_cvt_pk_bf16_f32 v245, v230, v231\n\t" \
"v_mfma_f32_16x16x32_bf16 v[188:191], a[64:67], v[84:87], v[188:191]\n\t" \
"buffer_store_dwordx2 v[244:245], %[vs1], %[ro], s91 offen offset:1024 sc1\n\t" \
"s_add_u32 s90, s87, %[lg]\n\t" \
"s_add_u32 s90, s90, 0\n\t" \
"s_mov_b32 m0, s90\n\t" \
"s_add_u32 s95, s89, 0\n\t" \
"buffer_load_dwordx4 %[vg], %[rh], s95 offen offset:2048 lds\n\t" \
"v_mfma_f32_16x16x32_bf16 v[192:195], a[120:123], v[80:83], v[192:195]\n\t" \
"v_cvt_pk_bf16_f32 v240, v232, v233\n\t" \
"v_mfma_f32_16x16x32_bf16 v[196:199], a[120:123], v[84:87], v[196:199]\n\t" \
"v_cvt_pk_bf16_f32 v241, v234, v235\n\t" \
"v_mfma_f32_16x16x32_bf16 v[200:203], a[176:179], v[80:83], v[200:203]\n\t" \
"buffer_store_dwordx2 v[240:241], %[vs0], %[ro], s91 offen offset:1536 sc1\n\t" \
"v_mfma_f32_16x16x32_bf16 v[204:207], a[176:179], v[84:87], v[204:207]\n\t" \
"v_cvt_pk_bf16_f32 v244, v236, v237\n\t" \
"v_mfma_f32_16x16x32_bf16 v[176:179], a[12:15], v[88:91], v[176:179]\n\t" \
"v_cvt_pk_bf16_f32 v245, v238, v239\n\t" \
"v_mfma_f32_16x16x32_bf16 v[180:183], a[12:15], v[92:95], v[180:183]\n\t" \
"buffer_store_dwordx2 v[244:245], %[vs1], %[ro], s91 offen offset:1536 sc1\n\t" \
"v_mfma_f32_16x16x32_bf16 v[184:187], a[68:71], v[88:91], v[184:187]\n\t" \
"v_mfma_f32_16x16x32_bf16 v[188:191], a[68:71], v[92:95], v[188:191]\n\t" \
"ds_read_b128 v[124:127], v248 offset:21504\n\t" \
"v_mfma_f32_16x16x32_bf16 v[192:195], a[124:127], v[88:91], v[192:195]\n\t" \
"v_mfma_f32_16x16x32_bf16 v[196:199], a[124:127], v[92:95], v[196:199]\n\t" \
"ds_read_b128 v[128:131], v248 offset:8192\n\t" \
"v_mfma_f32_16x16x32_bf16 v[200:203], a[180:183], v[88:91], v[200:203]\n\t" \
"v_mfma_f32_16x16x32_bf16 v[204:207], a[180:183], v[92:95], v[204:207]\n\t" \
"ds_read_b128 v[132:135], v248 offset:22528\n\t" \
"v_mfma_f32_16x16x32_bf16 v[176:179], a[16:19], v[96:99], v[176:179]\n\t" \
"v_mfma_f32_16x16x32_bf16 v[180:183], a[16:19], v[100:103], v[180:183]\n\t" \
"ds_read_b128 v[136:139], v248 offset:9216\n\t" \
"v_mfma_f32_16x16x32_bf16 v[184:187], a[72:75], v[96:99], v[184:187]\n\t" \
"v_mfma_f32_16x16x32_bf16 v[188:191], a[72:75], v[100:103], v[188:191]\n\t" \
"ds_read_b128 v[140:143], v248 offset:23552\n\t" \
"v_mfma_f32_16x16x32_bf16 v[192:195], a[128:131], v[96:99], v[192:195]\n\t" \
"v_mfma_f32_16x16x32_bf16 v[196:199], a[128:131], v[100:103], v[196:199]\n\t" \
"ds_read_b128 v[144:147], v248 offset:10240\n\t" \
"v_mfma_f32_16x16x32_bf16 v[200:203], a[184:187], v[96:99], v[200:203]\n\t" \
"v_mfma_f32_16x16x32_bf16 v[204:207], a[184:187], v[100:103], v[204:207]\n\t" \
"ds_read_b128 v[148:151], v248 offset:24576\n\t" \
"buffer_load_dwordx4 %[vg], %[rh], s95 offen offset:3072 lds\n\t" \
"v_mfma_f32_16x16x32_bf16 v[176:179], a[20:23], v[104:107], v[176:179]\n\t" \
"v_mfma_f32_16x16x32_bf16 v[180:183], a[20:23], v[108:111], v[180:183]\n\t" \
"ds_read_b128 v[152:155], v248 offset:11264\n\t" \
"v_mfma_f32_16x16x32_bf16 v[184:187], a[76:79], v[104:107], v[184:187]\n\t" \
"v_mfma_f32_16x16x32_bf16 v[188:191], a[76:79], v[108:111], v[188:191]\n\t" \
"ds_read_b128 v[156:159], v248 offset:25600\n\t" \
"v_mfma_f32_16x16x32_bf16 v[192:195], a[132:135], v[104:107], v[192:195]\n\t" \
"v_mfma_f32_16x16x32_bf16 v[196:199], a[132:135], v[108:111], v[196:199]\n\t" \
"ds_read_b128 v[160:163], v248 offset:12288\n\t" \
"v_mfma_f32_16x16x32_bf16 v[200:203], a[188:191], v[104:107], v[200:203]\n\t" \
"v_mfma_f32_16x16x32_bf16 v[204:207], a[188:191], v[108:111], v[204:207]\n\t" \
"ds_read_b128 v[164:167], v248 offset:26624\n\t" \
"v_mfma_f32_16x16x32_bf16 v[176:179], a[24:27], v[112:115], v[176:179]\n\t" \
"v_mfma_f32_16x16x32_bf16 v[180:183], a[24:27], v[116:119], v[180:183]\n\t" \
"ds_read_b128 v[168:171], v248 offset:13312\n\t" \
"v_mfma_f32_16x16x32_bf16 v[184:187], a[80:83], v[112:115], v[184:187]\n\t" \
"v_mfma_f32_16x16x32_bf16 v[188:191], a[80:83], v[116:119], v[188:191]\n\t" \
"ds_read_b128 v[172:175], v248 offset:27648\n\t" \
"v_mfma_f32_16x16x32_bf16 v[192:195], a[136:139], v[112:115], v[192:195]\n\t" \
"v_mfma_f32_16x16x32_bf16 v[196:199], a[136:139], v[116:119], v[196:199]\n\t" \
"v_mfma_f32_16x16x32_bf16 v[200:203], a[192:195], v[112:115], v[200:203]\n\t" \
"v_mfma_f32_16x16x32_bf16 v[204:207], a[192:195], v[116:119], v[204:207]\n\t" \
"s_add_u32 s89, s89, 57344\n\t" \
"s_min_u32 s89, s89, 1806336\n\t" \
"s_mov_b32 s87, s92\n\t" \
"s_add_u32 s95, s92, 28672\n\t" \
"s_cmp_eq_u32 s95, 114688\n\t" \
"s_cselect_b32 s95, 0, s95\n\t" \
"v_add_u32 v249, s95, %[lr]\n\t" \
"s_waitcnt vmcnt(12) lgkmcnt(0)\n\t" \
"s_barrier\n\t" \
"v_mfma_f32_16x16x32_bf16 v[176:179], a[28:31], v[120:123], v[176:179]\n\t" \
"ds_read_b128 v[64:67], v249 offset:0\n\t" \
"v_mfma_f32_16x16x32_bf16 v[180:183], a[28:31], v[124:127], v[180:183]\n\t" \
"v_mfma_f32_16x16x32_bf16 v[184:187], a[84:87], v[120:123], v[184:187]\n\t" \
"ds_read_b128 v[68:71], v249 offset:14336\n\t" \
"v_mfma_f32_16x16x32_bf16 v[188:191], a[84:87], v[124:127], v[188:191]\n\t" \
"v_mfma_f32_16x16x32_bf16 v[192:195], a[140:143], v[120:123], v[192:195]\n\t" \
"ds_read_b128 v[72:75], v249 offset:1024\n\t" \
"v_mfma_f32_16x16x32_bf16 v[196:199], a[140:143], v[124:127], v[196:199]\n\t" \
"v_mfma_f32_16x16x32_bf16 v[200:203], a[196:199], v[120:123], v[200:203]\n\t" \
"ds_read_b128 v[76:79], v249 offset:15360\n\t" \
"v_mfma_f32_16x16x32_bf16 v[204:207], a[196:199], v[124:127], v[204:207]\n\t" \
"v_mfma_f32_16x16x32_bf16 v[176:179], a[32:35], v[128:131], v[176:179]\n\t" \
"ds_read_b128 v[80:83], v249 offset:2048\n\t" \
"v_mfma_f32_16x16x32_bf16 v[180:183], a[32:35], v[132:135], v[180:183]\n\t" \
"v_mfma_f32_16x16x32_bf16 v[184:187], a[88:91], v[128:131], v[184:187]\n\t" \
"ds_read_b128 v[84:87], v249 offset:16384\n\t" \
"v_mfma_f32_16x16x32_bf16 v[188:191], a[88:91], v[132:135], v[188:191]\n\t" \
"v_mfma_f32_16x16x32_bf16 v[192:195], a[144:147], v[128:131], v[192:195]\n\t" \
"ds_read_b128 v[88:91], v249 offset:3072\n\t" \
"v_mfma_f32_16x16x32_bf16 v[196:199], a[144:147], v[132:135], v[196:199]\n\t" \
"v_mfma_f32_16x16x32_bf16 v[200:203], a[200:203], v[128:131], v[200:203]\n\t" \
"ds_read_b128 v[92:95], v249 offset:17408\n\t" \
"v_mfma_f32_16x16x32_bf16 v[204:207], a[200:203], v[132:135], v[204:207]\n\t" \
"v_mfma_f32_16x16x32_bf16 v[176:179], a[36:39], v[136:139], v[176:179]\n\t" \
"ds_read_b128 v[96:99], v249 offset:4096\n\t" \
"v_mfma_f32_16x16x32_bf16 v[180:183], a[36:39], v[140:143], v[180:183]\n\t" \
"v_mfma_f32_16x16x32_bf16 v[184:187], a[92:95], v[136:139], v[184:187]\n\t" \
"ds_read_b128 v[100:103], v249 offset:18432\n\t" \
"v_mfma_f32_16x16x32_bf16 v[188:191], a[92:95], v[140:143], v[188:191]\n\t" \
"s_add_u32 s90, s87, %[lg]\n\t" \
"s_add_u32 s90, s90, 0\n\t" \
"s_mov_b32 m0, s90\n\t" \
"s_add_u32 s95, s89, 0\n\t" \
"buffer_load_dwordx4 %[vg], %[rh], s95 offen offset:0 lds\n\t" \
"v_mfma_f32_16x16x32_bf16 v[192:195], a[148:151], v[136:139], v[192:195]\n\t" \
"ds_read_b128 v[104:107], v249 offset:5120\n\t" \
"v_mfma_f32_16x16x32_bf16 v[196:199], a[148:151], v[140:143], v[196:199]\n\t" \
"v_mfma_f32_16x16x32_bf16 v[200:203], a[204:207], v[136:139], v[200:203]\n\t" \
"ds_read_b128 v[108:111], v249 offset:19456\n\t" \
"v_mfma_f32_16x16x32_bf16 v[204:207], a[204:207], v[140:143], v[204:207]\n\t" \
"v_mfma_f32_16x16x32_bf16 v[176:179], a[40:43], v[144:147], v[176:179]\n\t" \
"ds_read_b128 v[112:115], v249 offset:6144\n\t" \
"v_mfma_f32_16x16x32_bf16 v[180:183], a[40:43], v[148:151], v[180:183]\n\t" \
"v_mfma_f32_16x16x32_bf16 v[184:187], a[96:99], v[144:147], v[184:187]\n\t" \
"ds_read_b128 v[116:119], v249 offset:20480\n\t" \
"v_mfma_f32_16x16x32_bf16 v[188:191], a[96:99], v[148:151], v[188:191]\n\t" \
"v_mfma_f32_16x16x32_bf16 v[192:195], a[152:155], v[144:147], v[192:195]\n\t" \
"v_mfma_f32_16x16x32_bf16 v[196:199], a[152:155], v[148:151], v[196:199]\n\t" \
"v_mfma_f32_16x16x32_bf16 v[200:203], a[208:211], v[144:147], v[200:203]\n\t" \
"v_mfma_f32_16x16x32_bf16 v[204:207], a[208:211], v[148:151], v[204:207]\n\t" \
"v_mfma_f32_16x16x32_bf16 v[176:179], a[44:47], v[152:155], v[176:179]\n\t" \
"v_mfma_f32_16x16x32_bf16 v[180:183], a[44:47], v[156:159], v[180:183]\n\t" \
"v_mfma_f32_16x16x32_bf16 v[184:187], a[100:103], v[152:155], v[184:187]\n\t" \
"v_mfma_f32_16x16x32_bf16 v[188:191], a[100:103], v[156:159], v[188:191]\n\t" \
"v_mfma_f32_16x16x32_bf16 v[192:195], a[156:159], v[152:155], v[192:195]\n\t" \
"v_mfma_f32_16x16x32_bf16 v[196:199], a[156:159], v[156:159], v[196:199]\n\t" \
"v_mfma_f32_16x16x32_bf16 v[200:203], a[212:215], v[152:155], v[200:203]\n\t" \
"v_mfma_f32_16x16x32_bf16 v[204:207], a[212:215], v[156:159], v[204:207]\n\t" \
"buffer_load_dwordx4 %[vg], %[rh], s95 offen offset:1024 lds\n\t" \
"v_mfma_f32_16x16x32_bf16 v[176:179], a[48:51], v[160:163], v[176:179]\n\t" \
"v_mfma_f32_16x16x32_bf16 v[180:183], a[48:51], v[164:167], v[180:183]\n\t" \
"v_mfma_f32_16x16x32_bf16 v[184:187], a[104:107], v[160:163], v[184:187]\n\t" \
"v_mfma_f32_16x16x32_bf16 v[188:191], a[104:107], v[164:167], v[188:191]\n\t" \
"v_mfma_f32_16x16x32_bf16 v[192:195], a[160:163], v[160:163], v[192:195]\n\t" \
"v_mfma_f32_16x16x32_bf16 v[196:199], a[160:163], v[164:167], v[196:199]\n\t" \
"v_mfma_f32_16x16x32_bf16 v[200:203], a[216:219], v[160:163], v[200:203]\n\t" \
"v_mfma_f32_16x16x32_bf16 v[204:207], a[216:219], v[164:167], v[204:207]\n\t" \
"v_mfma_f32_16x16x32_bf16 v[176:179], a[52:55], v[168:171], v[176:179]\n\t" \
"v_mfma_f32_16x16x32_bf16 v[180:183], a[52:55], v[172:175], v[180:183]\n\t" \
"v_mfma_f32_16x16x32_bf16 v[184:187], a[108:111], v[168:171], v[184:187]\n\t" \
"v_mfma_f32_16x16x32_bf16 v[188:191], a[108:111], v[172:175], v[188:191]\n\t" \
"v_mfma_f32_16x16x32_bf16 v[192:195], a[164:167], v[168:171], v[192:195]\n\t" \
"v_mfma_f32_16x16x32_bf16 v[196:199], a[164:167], v[172:175], v[196:199]\n\t" \
"v_mfma_f32_16x16x32_bf16 v[200:203], a[220:223], v[168:171], v[200:203]\n\t" \
"v_mfma_f32_16x16x32_bf16 v[204:207], a[220:223], v[172:175], v[204:207]\n\t" \
"s_add_u32 s92, s92, 28672\n\t" \
"s_cmp_eq_u32 s92, 114688\n\t" \
"s_cselect_b32 s92, 0, s92\n\t" \
"v_mov_b32 v248, v249\n\t" \
"s_add_u32 s91, s91, 917504\n\t" \
"s_waitcnt lgkmcnt(0)\n\t" \
"v_mfma_f32_16x16x32_bf16 v[208:211], a[0:3], v[64:67], 0\n\t" \
"v_mfma_f32_16x16x32_bf16 v[212:215], a[0:3], v[68:71], 0\n\t" \
"ds_read_b128 v[120:123], v248 offset:7168\n\t" \
"v_mfma_f32_16x16x32_bf16 v[216:219], a[56:59], v[64:67], 0\n\t" \
"v_cvt_pk_bf16_f32 v240, v176, v177\n\t" \
"v_mfma_f32_16x16x32_bf16 v[220:223], a[56:59], v[68:71], 0\n\t" \
"v_cvt_pk_bf16_f32 v241, v178, v179\n\t" \
"v_mfma_f32_16x16x32_bf16 v[224:227], a[112:115], v[64:67], 0\n\t" \
"buffer_store_dwordx2 v[240:241], %[vs0], %[ro], s91 offen offset:0 sc1\n\t" \
"v_mfma_f32_16x16x32_bf16 v[228:231], a[112:115], v[68:71], 0\n\t" \
"v_cvt_pk_bf16_f32 v244, v180, v181\n\t" \
"v_mfma_f32_16x16x32_bf16 v[232:235], a[168:171], v[64:67], 0\n\t" \
"v_cvt_pk_bf16_f32 v245, v182, v183\n\t" \
"v_mfma_f32_16x16x32_bf16 v[236:239], a[168:171], v[68:71], 0\n\t" \
"buffer_store_dwordx2 v[244:245], %[vs1], %[ro], s91 offen offset:0 sc1\n\t" \
"v_mfma_f32_16x16x32_bf16 v[208:211], a[4:7], v[72:75], v[208:211]\n\t" \
"v_cvt_pk_bf16_f32 v240, v184, v185\n\t" \
"v_mfma_f32_16x16x32_bf16 v[212:215], a[4:7], v[76:79], v[212:215]\n\t" \
"v_cvt_pk_bf16_f32 v241, v186, v187\n\t" \
"v_mfma_f32_16x16x32_bf16 v[216:219], a[60:63], v[72:75], v[216:219]\n\t" \
"buffer_store_dwordx2 v[240:241], %[vs0], %[ro], s91 offen offset:512 sc1\n\t" \
"v_mfma_f32_16x16x32_bf16 v[220:223], a[60:63], v[76:79], v[220:223]\n\t" \
"v_cvt_pk_bf16_f32 v244, v188, v189\n\t" \
"v_mfma_f32_16x16x32_bf16 v[224:227], a[116:119], v[72:75], v[224:227]\n\t" \
"v_cvt_pk_bf16_f32 v245, v190, v191\n\t" \
"v_mfma_f32_16x16x32_bf16 v[228:231], a[116:119], v[76:79], v[228:231]\n\t" \
"buffer_store_dwordx2 v[244:245], %[vs1], %[ro], s91 offen offset:512 sc1\n\t" \
"v_mfma_f32_16x16x32_bf16 v[232:235], a[172:175], v[72:75], v[232:235]\n\t" \
"v_cvt_pk_bf16_f32 v240, v192, v193\n\t" \
"v_mfma_f32_16x16x32_bf16 v[236:239], a[172:175], v[76:79], v[236:239]\n\t" \
"v_cvt_pk_bf16_f32 v241, v194, v195\n\t" \
"v_mfma_f32_16x16x32_bf16 v[208:211], a[8:11], v[80:83], v[208:211]\n\t" \
"buffer_store_dwordx2 v[240:241], %[vs0], %[ro], s91 offen offset:1024 sc1\n\t" \
"v_mfma_f32_16x16x32_bf16 v[212:215], a[8:11], v[84:87], v[212:215]\n\t" \
"v_cvt_pk_bf16_f32 v244, v196, v197\n\t" \
"v_mfma_f32_16x16x32_bf16 v[216:219], a[64:67], v[80:83], v[216:219]\n\t" \
"v_cvt_pk_bf16_f32 v245, v198, v199\n\t" \
"v_mfma_f32_16x16x32_bf16 v[220:223], a[64:67], v[84:87], v[220:223]\n\t" \
"buffer_store_dwordx2 v[244:245], %[vs1], %[ro], s91 offen offset:1024 sc1\n\t" \
"s_add_u32 s90, s87, %[lg]\n\t" \
"s_add_u32 s90, s90, 0\n\t" \
"s_mov_b32 m0, s90\n\t" \
"s_add_u32 s95, s89, 0\n\t" \
"buffer_load_dwordx4 %[vg], %[rh], s95 offen offset:2048 lds\n\t" \
"v_mfma_f32_16x16x32_bf16 v[224:227], a[120:123], v[80:83], v[224:227]\n\t" \
"v_cvt_pk_bf16_f32 v240, v200, v201\n\t" \
"v_mfma_f32_16x16x32_bf16 v[228:231], a[120:123], v[84:87], v[228:231]\n\t" \
"v_cvt_pk_bf16_f32 v241, v202, v203\n\t" \
"v_mfma_f32_16x16x32_bf16 v[232:235], a[176:179], v[80:83], v[232:235]\n\t" \
"buffer_store_dwordx2 v[240:241], %[vs0], %[ro], s91 offen offset:1536 sc1\n\t" \
"v_mfma_f32_16x16x32_bf16 v[236:239], a[176:179], v[84:87], v[236:239]\n\t" \
"v_cvt_pk_bf16_f32 v244, v204, v205\n\t" \
"v_mfma_f32_16x16x32_bf16 v[208:211], a[12:15], v[88:91], v[208:211]\n\t" \
"v_cvt_pk_bf16_f32 v245, v206, v207\n\t" \
"v_mfma_f32_16x16x32_bf16 v[212:215], a[12:15], v[92:95], v[212:215]\n\t" \
"buffer_store_dwordx2 v[244:245], %[vs1], %[ro], s91 offen offset:1536 sc1\n\t" \
"v_mfma_f32_16x16x32_bf16 v[216:219], a[68:71], v[88:91], v[216:219]\n\t" \
"v_mfma_f32_16x16x32_bf16 v[220:223], a[68:71], v[92:95], v[220:223]\n\t" \
"ds_read_b128 v[124:127], v248 offset:21504\n\t" \
"v_mfma_f32_16x16x32_bf16 v[224:227], a[124:127], v[88:91], v[224:227]\n\t" \
"v_mfma_f32_16x16x32_bf16 v[228:231], a[124:127], v[92:95], v[228:231]\n\t" \
"ds_read_b128 v[128:131], v248 offset:8192\n\t" \
"v_mfma_f32_16x16x32_bf16 v[232:235], a[180:183], v[88:91], v[232:235]\n\t" \
"v_mfma_f32_16x16x32_bf16 v[236:239], a[180:183], v[92:95], v[236:239]\n\t" \
"ds_read_b128 v[132:135], v248 offset:22528\n\t" \
"v_mfma_f32_16x16x32_bf16 v[208:211], a[16:19], v[96:99], v[208:211]\n\t" \
"v_mfma_f32_16x16x32_bf16 v[212:215], a[16:19], v[100:103], v[212:215]\n\t" \
"ds_read_b128 v[136:139], v248 offset:9216\n\t" \
"v_mfma_f32_16x16x32_bf16 v[216:219], a[72:75], v[96:99], v[216:219]\n\t" \
"v_mfma_f32_16x16x32_bf16 v[220:223], a[72:75], v[100:103], v[220:223]\n\t" \
"ds_read_b128 v[140:143], v248 offset:23552\n\t" \
"v_mfma_f32_16x16x32_bf16 v[224:227], a[128:131], v[96:99], v[224:227]\n\t" \
"v_mfma_f32_16x16x32_bf16 v[228:231], a[128:131], v[100:103], v[228:231]\n\t" \
"ds_read_b128 v[144:147], v248 offset:10240\n\t" \
"v_mfma_f32_16x16x32_bf16 v[232:235], a[184:187], v[96:99], v[232:235]\n\t" \
"v_mfma_f32_16x16x32_bf16 v[236:239], a[184:187], v[100:103], v[236:239]\n\t" \
"ds_read_b128 v[148:151], v248 offset:24576\n\t" \
"buffer_load_dwordx4 %[vg], %[rh], s95 offen offset:3072 lds\n\t" \
"v_mfma_f32_16x16x32_bf16 v[208:211], a[20:23], v[104:107], v[208:211]\n\t" \
"v_mfma_f32_16x16x32_bf16 v[212:215], a[20:23], v[108:111], v[212:215]\n\t" \
"ds_read_b128 v[152:155], v248 offset:11264\n\t" \
"v_mfma_f32_16x16x32_bf16 v[216:219], a[76:79], v[104:107], v[216:219]\n\t" \
"v_mfma_f32_16x16x32_bf16 v[220:223], a[76:79], v[108:111], v[220:223]\n\t" \
"ds_read_b128 v[156:159], v248 offset:25600\n\t" \
"v_mfma_f32_16x16x32_bf16 v[224:227], a[132:135], v[104:107], v[224:227]\n\t" \
"v_mfma_f32_16x16x32_bf16 v[228:231], a[132:135], v[108:111], v[228:231]\n\t" \
"ds_read_b128 v[160:163], v248 offset:12288\n\t" \
"v_mfma_f32_16x16x32_bf16 v[232:235], a[188:191], v[104:107], v[232:235]\n\t" \
"v_mfma_f32_16x16x32_bf16 v[236:239], a[188:191], v[108:111], v[236:239]\n\t" \
"ds_read_b128 v[164:167], v248 offset:26624\n\t" \
"v_mfma_f32_16x16x32_bf16 v[208:211], a[24:27], v[112:115], v[208:211]\n\t" \
"v_mfma_f32_16x16x32_bf16 v[212:215], a[24:27], v[116:119], v[212:215]\n\t" \
"ds_read_b128 v[168:171], v248 offset:13312\n\t" \
"v_mfma_f32_16x16x32_bf16 v[216:219], a[80:83], v[112:115], v[216:219]\n\t" \
"v_mfma_f32_16x16x32_bf16 v[220:223], a[80:83], v[116:119], v[220:223]\n\t" \
"ds_read_b128 v[172:175], v248 offset:27648\n\t" \
"v_mfma_f32_16x16x32_bf16 v[224:227], a[136:139], v[112:115], v[224:227]\n\t" \
"v_mfma_f32_16x16x32_bf16 v[228:231], a[136:139], v[116:119], v[228:231]\n\t" \
"v_mfma_f32_16x16x32_bf16 v[232:235], a[192:195], v[112:115], v[232:235]\n\t" \
"v_mfma_f32_16x16x32_bf16 v[236:239], a[192:195], v[116:119], v[236:239]\n\t" \
"s_add_u32 s89, s89, 57344\n\t" \
"s_min_u32 s89, s89, 1806336\n\t" \
"s_mov_b32 s87, s92\n\t" \
"s_add_u32 s95, s92, 28672\n\t" \
"s_cmp_eq_u32 s95, 114688\n\t" \
"s_cselect_b32 s95, 0, s95\n\t" \
"v_add_u32 v249, s95, %[lr]\n\t" \
"s_waitcnt vmcnt(12) lgkmcnt(0)\n\t" \
"s_barrier\n\t" \
"s_add_u32 s94, s94, 128\n\t" \
"v_mfma_f32_16x16x32_bf16 v[208:211], a[28:31], v[120:123], v[208:211]\n\t" \
"ds_read_b128 v[64:67], v249 offset:0\n\t" \
"v_mfma_f32_16x16x32_bf16 v[212:215], a[28:31], v[124:127], v[212:215]\n\t" \
"v_mfma_f32_16x16x32_bf16 v[216:219], a[84:87], v[120:123], v[216:219]\n\t" \
"ds_read_b128 v[68:71], v249 offset:14336\n\t" \
"v_mfma_f32_16x16x32_bf16 v[220:223], a[84:87], v[124:127], v[220:223]\n\t" \
"v_mfma_f32_16x16x32_bf16 v[224:227], a[140:143], v[120:123], v[224:227]\n\t" \
"ds_read_b128 v[72:75], v249 offset:1024\n\t" \
"v_mfma_f32_16x16x32_bf16 v[228:231], a[140:143], v[124:127], v[228:231]\n\t" \
"v_mfma_f32_16x16x32_bf16 v[232:235], a[196:199], v[120:123], v[232:235]\n\t" \
"ds_read_b128 v[76:79], v249 offset:15360\n\t" \
"v_mfma_f32_16x16x32_bf16 v[236:239], a[196:199], v[124:127], v[236:239]\n\t" \
"v_mfma_f32_16x16x32_bf16 v[208:211], a[32:35], v[128:131], v[208:211]\n\t" \
"ds_read_b128 v[80:83], v249 offset:2048\n\t" \
"v_mfma_f32_16x16x32_bf16 v[212:215], a[32:35], v[132:135], v[212:215]\n\t" \
"v_mfma_f32_16x16x32_bf16 v[216:219], a[88:91], v[128:131], v[216:219]\n\t" \
"ds_read_b128 v[84:87], v249 offset:16384\n\t" \
"v_mfma_f32_16x16x32_bf16 v[220:223], a[88:91], v[132:135], v[220:223]\n\t" \
"v_mfma_f32_16x16x32_bf16 v[224:227], a[144:147], v[128:131], v[224:227]\n\t" \
"ds_read_b128 v[88:91], v249 offset:3072\n\t" \
"v_mfma_f32_16x16x32_bf16 v[228:231], a[144:147], v[132:135], v[228:231]\n\t" \
"v_mfma_f32_16x16x32_bf16 v[232:235], a[200:203], v[128:131], v[232:235]\n\t" \
"ds_read_b128 v[92:95], v249 offset:17408\n\t" \
"v_mfma_f32_16x16x32_bf16 v[236:239], a[200:203], v[132:135], v[236:239]\n\t" \
"v_mfma_f32_16x16x32_bf16 v[208:211], a[36:39], v[136:139], v[208:211]\n\t" \
"ds_read_b128 v[96:99], v249 offset:4096\n\t" \
"v_mfma_f32_16x16x32_bf16 v[212:215], a[36:39], v[140:143], v[212:215]\n\t" \
"v_mfma_f32_16x16x32_bf16 v[216:219], a[92:95], v[136:139], v[216:219]\n\t" \
"ds_read_b128 v[100:103], v249 offset:18432\n\t" \
"v_mfma_f32_16x16x32_bf16 v[220:223], a[92:95], v[140:143], v[220:223]\n\t" \
"s_add_u32 s90, s87, %[lg]\n\t" \
"s_add_u32 s90, s90, 0\n\t" \
"s_mov_b32 m0, s90\n\t" \
"s_add_u32 s95, s89, 0\n\t" \
"buffer_load_dwordx4 %[vg], %[rh], s95 offen offset:0 lds\n\t" \
"v_mfma_f32_16x16x32_bf16 v[224:227], a[148:151], v[136:139], v[224:227]\n\t" \
"ds_read_b128 v[104:107], v249 offset:5120\n\t" \
"v_mfma_f32_16x16x32_bf16 v[228:231], a[148:151], v[140:143], v[228:231]\n\t" \
"v_mfma_f32_16x16x32_bf16 v[232:235], a[204:207], v[136:139], v[232:235]\n\t" \
"ds_read_b128 v[108:111], v249 offset:19456\n\t" \
"v_mfma_f32_16x16x32_bf16 v[236:239], a[204:207], v[140:143], v[236:239]\n\t" \
"v_mfma_f32_16x16x32_bf16 v[208:211], a[40:43], v[144:147], v[208:211]\n\t" \
"ds_read_b128 v[112:115], v249 offset:6144\n\t" \
"v_mfma_f32_16x16x32_bf16 v[212:215], a[40:43], v[148:151], v[212:215]\n\t" \
"v_mfma_f32_16x16x32_bf16 v[216:219], a[96:99], v[144:147], v[216:219]\n\t" \
"ds_read_b128 v[116:119], v249 offset:20480\n\t" \
"v_mfma_f32_16x16x32_bf16 v[220:223], a[96:99], v[148:151], v[220:223]\n\t" \
"v_mfma_f32_16x16x32_bf16 v[224:227], a[152:155], v[144:147], v[224:227]\n\t" \
"v_mfma_f32_16x16x32_bf16 v[228:231], a[152:155], v[148:151], v[228:231]\n\t" \
"v_mfma_f32_16x16x32_bf16 v[232:235], a[208:211], v[144:147], v[232:235]\n\t" \
"v_mfma_f32_16x16x32_bf16 v[236:239], a[208:211], v[148:151], v[236:239]\n\t" \
"v_mfma_f32_16x16x32_bf16 v[208:211], a[44:47], v[152:155], v[208:211]\n\t" \
"v_mfma_f32_16x16x32_bf16 v[212:215], a[44:47], v[156:159], v[212:215]\n\t" \
"v_mfma_f32_16x16x32_bf16 v[216:219], a[100:103], v[152:155], v[216:219]\n\t" \
"v_mfma_f32_16x16x32_bf16 v[220:223], a[100:103], v[156:159], v[220:223]\n\t" \
"v_mfma_f32_16x16x32_bf16 v[224:227], a[156:159], v[152:155], v[224:227]\n\t" \
"v_mfma_f32_16x16x32_bf16 v[228:231], a[156:159], v[156:159], v[228:231]\n\t" \
"v_mfma_f32_16x16x32_bf16 v[232:235], a[212:215], v[152:155], v[232:235]\n\t" \
"v_mfma_f32_16x16x32_bf16 v[236:239], a[212:215], v[156:159], v[236:239]\n\t" \
"buffer_load_dwordx4 %[vg], %[rh], s95 offen offset:1024 lds\n\t" \
"v_mfma_f32_16x16x32_bf16 v[208:211], a[48:51], v[160:163], v[208:211]\n\t" \
"v_mfma_f32_16x16x32_bf16 v[212:215], a[48:51], v[164:167], v[212:215]\n\t" \
"v_mfma_f32_16x16x32_bf16 v[216:219], a[104:107], v[160:163], v[216:219]\n\t" \
"v_mfma_f32_16x16x32_bf16 v[220:223], a[104:107], v[164:167], v[220:223]\n\t" \
"v_mfma_f32_16x16x32_bf16 v[224:227], a[160:163], v[160:163], v[224:227]\n\t" \
"v_mfma_f32_16x16x32_bf16 v[228:231], a[160:163], v[164:167], v[228:231]\n\t" \
"v_mfma_f32_16x16x32_bf16 v[232:235], a[216:219], v[160:163], v[232:235]\n\t" \
"v_mfma_f32_16x16x32_bf16 v[236:239], a[216:219], v[164:167], v[236:239]\n\t" \
"v_mfma_f32_16x16x32_bf16 v[208:211], a[52:55], v[168:171], v[208:211]\n\t" \
"v_mfma_f32_16x16x32_bf16 v[212:215], a[52:55], v[172:175], v[212:215]\n\t" \
"v_mfma_f32_16x16x32_bf16 v[216:219], a[108:111], v[168:171], v[216:219]\n\t" \
"v_mfma_f32_16x16x32_bf16 v[220:223], a[108:111], v[172:175], v[220:223]\n\t" \
"v_mfma_f32_16x16x32_bf16 v[224:227], a[164:167], v[168:171], v[224:227]\n\t" \
"v_mfma_f32_16x16x32_bf16 v[228:231], a[164:167], v[172:175], v[228:231]\n\t" \
"v_mfma_f32_16x16x32_bf16 v[232:235], a[220:223], v[168:171], v[232:235]\n\t" \
"v_mfma_f32_16x16x32_bf16 v[236:239], a[220:223], v[172:175], v[236:239]\n\t" \
"s_add_u32 s92, s92, 28672\n\t" \
"s_cmp_eq_u32 s92, 114688\n\t" \
"s_cselect_b32 s92, 0, s92\n\t" \
"v_mov_b32 v248, v249\n\t" \
"s_add_u32 s91, s91, 917504\n\t" \
"s_mov_b32 s88, 4\n\t" \
"s_nop 1\n\t" \
"Lp3%=:\n\t" \
"s_waitcnt lgkmcnt(0)\n\t" \
"v_mfma_f32_16x16x32_bf16 v[176:179], a[0:3], v[64:67], 0\n\t" \
"v_mfma_f32_16x16x32_bf16 v[180:183], a[0:3], v[68:71], 0\n\t" \
"ds_read_b128 v[120:123], v248 offset:7168\n\t" \
"v_mfma_f32_16x16x32_bf16 v[184:187], a[56:59], v[64:67], 0\n\t" \
"v_cvt_pk_bf16_f32 v240, v208, v209\n\t" \
"v_mfma_f32_16x16x32_bf16 v[188:191], a[56:59], v[68:71], 0\n\t" \
"v_cvt_pk_bf16_f32 v241, v210, v211\n\t" \
"v_mfma_f32_16x16x32_bf16 v[192:195], a[112:115], v[64:67], 0\n\t" \
"buffer_store_dwordx2 v[240:241], %[vs0], %[ro], s91 offen offset:0 sc1\n\t" \
"v_mfma_f32_16x16x32_bf16 v[196:199], a[112:115], v[68:71], 0\n\t" \
"v_cvt_pk_bf16_f32 v244, v212, v213\n\t" \
"v_mfma_f32_16x16x32_bf16 v[200:203], a[168:171], v[64:67], 0\n\t" \
"v_cvt_pk_bf16_f32 v245, v214, v215\n\t" \
"v_mfma_f32_16x16x32_bf16 v[204:207], a[168:171], v[68:71], 0\n\t" \
"buffer_store_dwordx2 v[244:245], %[vs1], %[ro], s91 offen offset:0 sc1\n\t" \
"v_mfma_f32_16x16x32_bf16 v[176:179], a[4:7], v[72:75], v[176:179]\n\t" \
"v_cvt_pk_bf16_f32 v240, v216, v217\n\t" \
"v_mfma_f32_16x16x32_bf16 v[180:183], a[4:7], v[76:79], v[180:183]\n\t" \
"v_cvt_pk_bf16_f32 v241, v218, v219\n\t" \
"v_mfma_f32_16x16x32_bf16 v[184:187], a[60:63], v[72:75], v[184:187]\n\t" \
"buffer_store_dwordx2 v[240:241], %[vs0], %[ro], s91 offen offset:512 sc1\n\t" \
"v_mfma_f32_16x16x32_bf16 v[188:191], a[60:63], v[76:79], v[188:191]\n\t" \
"v_cvt_pk_bf16_f32 v244, v220, v221\n\t" \
"v_mfma_f32_16x16x32_bf16 v[192:195], a[116:119], v[72:75], v[192:195]\n\t" \
"v_cvt_pk_bf16_f32 v245, v222, v223\n\t" \
"v_mfma_f32_16x16x32_bf16 v[196:199], a[116:119], v[76:79], v[196:199]\n\t" \
"buffer_store_dwordx2 v[244:245], %[vs1], %[ro], s91 offen offset:512 sc1\n\t" \
"v_mfma_f32_16x16x32_bf16 v[200:203], a[172:175], v[72:75], v[200:203]\n\t" \
"v_cvt_pk_bf16_f32 v240, v224, v225\n\t" \
"v_mfma_f32_16x16x32_bf16 v[204:207], a[172:175], v[76:79], v[204:207]\n\t" \
"v_cvt_pk_bf16_f32 v241, v226, v227\n\t" \
"v_mfma_f32_16x16x32_bf16 v[176:179], a[8:11], v[80:83], v[176:179]\n\t" \
"buffer_store_dwordx2 v[240:241], %[vs0], %[ro], s91 offen offset:1024 sc1\n\t" \
"v_mfma_f32_16x16x32_bf16 v[180:183], a[8:11], v[84:87], v[180:183]\n\t" \
"v_cvt_pk_bf16_f32 v244, v228, v229\n\t" \
"v_mfma_f32_16x16x32_bf16 v[184:187], a[64:67], v[80:83], v[184:187]\n\t" \
"v_cvt_pk_bf16_f32 v245, v230, v231\n\t" \
"v_mfma_f32_16x16x32_bf16 v[188:191], a[64:67], v[84:87], v[188:191]\n\t" \
"buffer_store_dwordx2 v[244:245], %[vs1], %[ro], s91 offen offset:1024 sc1\n\t" \
"s_add_u32 s90, s87, %[lg]\n\t" \
"s_add_u32 s90, s90, 0\n\t" \
"s_mov_b32 m0, s90\n\t" \
"s_add_u32 s95, s89, 0\n\t" \
"buffer_load_dwordx4 %[vg], %[rh], s95 offen offset:2048 lds\n\t" \
"v_mfma_f32_16x16x32_bf16 v[192:195], a[120:123], v[80:83], v[192:195]\n\t" \
"v_cvt_pk_bf16_f32 v240, v232, v233\n\t" \
"v_mfma_f32_16x16x32_bf16 v[196:199], a[120:123], v[84:87], v[196:199]\n\t" \
"v_cvt_pk_bf16_f32 v241, v234, v235\n\t" \
"v_mfma_f32_16x16x32_bf16 v[200:203], a[176:179], v[80:83], v[200:203]\n\t" \
"buffer_store_dwordx2 v[240:241], %[vs0], %[ro], s91 offen offset:1536 sc1\n\t" \
"v_mfma_f32_16x16x32_bf16 v[204:207], a[176:179], v[84:87], v[204:207]\n\t" \
"v_cvt_pk_bf16_f32 v244, v236, v237\n\t" \
"v_mfma_f32_16x16x32_bf16 v[176:179], a[12:15], v[88:91], v[176:179]\n\t" \
"v_cvt_pk_bf16_f32 v245, v238, v239\n\t" \
"v_mfma_f32_16x16x32_bf16 v[180:183], a[12:15], v[92:95], v[180:183]\n\t" \
"buffer_store_dwordx2 v[244:245], %[vs1], %[ro], s91 offen offset:1536 sc1\n\t" \
"v_mfma_f32_16x16x32_bf16 v[184:187], a[68:71], v[88:91], v[184:187]\n\t" \
"v_mfma_f32_16x16x32_bf16 v[188:191], a[68:71], v[92:95], v[188:191]\n\t" \
"ds_read_b128 v[124:127], v248 offset:21504\n\t" \
"v_mfma_f32_16x16x32_bf16 v[192:195], a[124:127], v[88:91], v[192:195]\n\t" \
"v_mfma_f32_16x16x32_bf16 v[196:199], a[124:127], v[92:95], v[196:199]\n\t" \
"ds_read_b128 v[128:131], v248 offset:8192\n\t" \
"v_mfma_f32_16x16x32_bf16 v[200:203], a[180:183], v[88:91], v[200:203]\n\t" \
"v_mfma_f32_16x16x32_bf16 v[204:207], a[180:183], v[92:95], v[204:207]\n\t" \
"ds_read_b128 v[132:135], v248 offset:22528\n\t" \
"v_mfma_f32_16x16x32_bf16 v[176:179], a[16:19], v[96:99], v[176:179]\n\t" \
"v_mfma_f32_16x16x32_bf16 v[180:183], a[16:19], v[100:103], v[180:183]\n\t" \
"ds_read_b128 v[136:139], v248 offset:9216\n\t" \
"v_mfma_f32_16x16x32_bf16 v[184:187], a[72:75], v[96:99], v[184:187]\n\t" \
"v_mfma_f32_16x16x32_bf16 v[188:191], a[72:75], v[100:103], v[188:191]\n\t" \
"ds_read_b128 v[140:143], v248 offset:23552\n\t" \
"v_mfma_f32_16x16x32_bf16 v[192:195], a[128:131], v[96:99], v[192:195]\n\t" \
"v_mfma_f32_16x16x32_bf16 v[196:199], a[128:131], v[100:103], v[196:199]\n\t" \
"ds_read_b128 v[144:147], v248 offset:10240\n\t" \
"v_mfma_f32_16x16x32_bf16 v[200:203], a[184:187], v[96:99], v[200:203]\n\t" \
"v_mfma_f32_16x16x32_bf16 v[204:207], a[184:187], v[100:103], v[204:207]\n\t" \
"ds_read_b128 v[148:151], v248 offset:24576\n\t" \
"buffer_load_dwordx4 %[vg], %[rh], s95 offen offset:3072 lds\n\t" \
"v_mfma_f32_16x16x32_bf16 v[176:179], a[20:23], v[104:107], v[176:179]\n\t" \
"v_mfma_f32_16x16x32_bf16 v[180:183], a[20:23], v[108:111], v[180:183]\n\t" \
"ds_read_b128 v[152:155], v248 offset:11264\n\t" \
"v_mfma_f32_16x16x32_bf16 v[184:187], a[76:79], v[104:107], v[184:187]\n\t" \
"v_mfma_f32_16x16x32_bf16 v[188:191], a[76:79], v[108:111], v[188:191]\n\t" \
"ds_read_b128 v[156:159], v248 offset:25600\n\t" \
"v_mfma_f32_16x16x32_bf16 v[192:195], a[132:135], v[104:107], v[192:195]\n\t" \
"v_mfma_f32_16x16x32_bf16 v[196:199], a[132:135], v[108:111], v[196:199]\n\t" \
"ds_read_b128 v[160:163], v248 offset:12288\n\t" \
"v_mfma_f32_16x16x32_bf16 v[200:203], a[188:191], v[104:107], v[200:203]\n\t" \
"v_mfma_f32_16x16x32_bf16 v[204:207], a[188:191], v[108:111], v[204:207]\n\t" \
"ds_read_b128 v[164:167], v248 offset:26624\n\t" \
"v_mfma_f32_16x16x32_bf16 v[176:179], a[24:27], v[112:115], v[176:179]\n\t" \
"v_mfma_f32_16x16x32_bf16 v[180:183], a[24:27], v[116:119], v[180:183]\n\t" \
"ds_read_b128 v[168:171], v248 offset:13312\n\t" \
"v_mfma_f32_16x16x32_bf16 v[184:187], a[80:83], v[112:115], v[184:187]\n\t" \
"v_mfma_f32_16x16x32_bf16 v[188:191], a[80:83], v[116:119], v[188:191]\n\t" \
"ds_read_b128 v[172:175], v248 offset:27648\n\t" \
"v_mfma_f32_16x16x32_bf16 v[192:195], a[136:139], v[112:115], v[192:195]\n\t" \
"v_mfma_f32_16x16x32_bf16 v[196:199], a[136:139], v[116:119], v[196:199]\n\t" \
"v_mfma_f32_16x16x32_bf16 v[200:203], a[192:195], v[112:115], v[200:203]\n\t" \
"v_mfma_f32_16x16x32_bf16 v[204:207], a[192:195], v[116:119], v[204:207]\n\t" \
"s_add_u32 s89, s89, 57344\n\t" \
"s_min_u32 s89, s89, 1806336\n\t" \
"s_mov_b32 s87, s92\n\t" \
"s_add_u32 s95, s92, 28672\n\t" \
"s_cmp_eq_u32 s95, 114688\n\t" \
"s_cselect_b32 s95, 0, s95\n\t" \
"v_add_u32 v249, s95, %[lr]\n\t" \
"s_waitcnt vmcnt(12) lgkmcnt(0)\n\t" \
"s_barrier\n\t" \
"s_add_u32 s94, s94, 128\n\t" \
"v_mfma_f32_16x16x32_bf16 v[176:179], a[28:31], v[120:123], v[176:179]\n\t" \
"ds_read_b128 v[64:67], v249 offset:0\n\t" \
"v_mfma_f32_16x16x32_bf16 v[180:183], a[28:31], v[124:127], v[180:183]\n\t" \
"v_mfma_f32_16x16x32_bf16 v[184:187], a[84:87], v[120:123], v[184:187]\n\t" \
"ds_read_b128 v[68:71], v249 offset:14336\n\t" \
"v_mfma_f32_16x16x32_bf16 v[188:191], a[84:87], v[124:127], v[188:191]\n\t" \
"v_mfma_f32_16x16x32_bf16 v[192:195], a[140:143], v[120:123], v[192:195]\n\t" \
"ds_read_b128 v[72:75], v249 offset:1024\n\t" \
"v_mfma_f32_16x16x32_bf16 v[196:199], a[140:143], v[124:127], v[196:199]\n\t" \
"v_mfma_f32_16x16x32_bf16 v[200:203], a[196:199], v[120:123], v[200:203]\n\t" \
"ds_read_b128 v[76:79], v249 offset:15360\n\t" \
"v_mfma_f32_16x16x32_bf16 v[204:207], a[196:199], v[124:127], v[204:207]\n\t" \
"v_mfma_f32_16x16x32_bf16 v[176:179], a[32:35], v[128:131], v[176:179]\n\t" \
"ds_read_b128 v[80:83], v249 offset:2048\n\t" \
"v_mfma_f32_16x16x32_bf16 v[180:183], a[32:35], v[132:135], v[180:183]\n\t" \
"v_mfma_f32_16x16x32_bf16 v[184:187], a[88:91], v[128:131], v[184:187]\n\t" \
"ds_read_b128 v[84:87], v249 offset:16384\n\t" \
"v_mfma_f32_16x16x32_bf16 v[188:191], a[88:91], v[132:135], v[188:191]\n\t" \
"v_mfma_f32_16x16x32_bf16 v[192:195], a[144:147], v[128:131], v[192:195]\n\t" \
"ds_read_b128 v[88:91], v249 offset:3072\n\t" \
"v_mfma_f32_16x16x32_bf16 v[196:199], a[144:147], v[132:135], v[196:199]\n\t" \
"v_mfma_f32_16x16x32_bf16 v[200:203], a[200:203], v[128:131], v[200:203]\n\t" \
"ds_read_b128 v[92:95], v249 offset:17408\n\t" \
"v_mfma_f32_16x16x32_bf16 v[204:207], a[200:203], v[132:135], v[204:207]\n\t" \
"v_mfma_f32_16x16x32_bf16 v[176:179], a[36:39], v[136:139], v[176:179]\n\t" \
"ds_read_b128 v[96:99], v249 offset:4096\n\t" \
"v_mfma_f32_16x16x32_bf16 v[180:183], a[36:39], v[140:143], v[180:183]\n\t" \
"v_mfma_f32_16x16x32_bf16 v[184:187], a[92:95], v[136:139], v[184:187]\n\t" \
"ds_read_b128 v[100:103], v249 offset:18432\n\t" \
"v_mfma_f32_16x16x32_bf16 v[188:191], a[92:95], v[140:143], v[188:191]\n\t" \
"s_add_u32 s90, s87, %[lg]\n\t" \
"s_add_u32 s90, s90, 0\n\t" \
"s_mov_b32 m0, s90\n\t" \
"s_add_u32 s95, s89, 0\n\t" \
"buffer_load_dwordx4 %[vg], %[rh], s95 offen offset:0 lds\n\t" \
"v_mfma_f32_16x16x32_bf16 v[192:195], a[148:151], v[136:139], v[192:195]\n\t" \
"ds_read_b128 v[104:107], v249 offset:5120\n\t" \
"v_mfma_f32_16x16x32_bf16 v[196:199], a[148:151], v[140:143], v[196:199]\n\t" \
"v_mfma_f32_16x16x32_bf16 v[200:203], a[204:207], v[136:139], v[200:203]\n\t" \
"ds_read_b128 v[108:111], v249 offset:19456\n\t" \
"v_mfma_f32_16x16x32_bf16 v[204:207], a[204:207], v[140:143], v[204:207]\n\t" \
"v_mfma_f32_16x16x32_bf16 v[176:179], a[40:43], v[144:147], v[176:179]\n\t" \
"ds_read_b128 v[112:115], v249 offset:6144\n\t" \
"v_mfma_f32_16x16x32_bf16 v[180:183], a[40:43], v[148:151], v[180:183]\n\t" \
"v_mfma_f32_16x16x32_bf16 v[184:187], a[96:99], v[144:147], v[184:187]\n\t" \
"ds_read_b128 v[116:119], v249 offset:20480\n\t" \
"v_mfma_f32_16x16x32_bf16 v[188:191], a[96:99], v[148:151], v[188:191]\n\t" \
"v_mfma_f32_16x16x32_bf16 v[192:195], a[152:155], v[144:147], v[192:195]\n\t" \
"v_mfma_f32_16x16x32_bf16 v[196:199], a[152:155], v[148:151], v[196:199]\n\t" \
"v_mfma_f32_16x16x32_bf16 v[200:203], a[208:211], v[144:147], v[200:203]\n\t" \
"v_mfma_f32_16x16x32_bf16 v[204:207], a[208:211], v[148:151], v[204:207]\n\t" \
"v_mfma_f32_16x16x32_bf16 v[176:179], a[44:47], v[152:155], v[176:179]\n\t" \
"v_mfma_f32_16x16x32_bf16 v[180:183], a[44:47], v[156:159], v[180:183]\n\t" \
"v_mfma_f32_16x16x32_bf16 v[184:187], a[100:103], v[152:155], v[184:187]\n\t" \
"v_mfma_f32_16x16x32_bf16 v[188:191], a[100:103], v[156:159], v[188:191]\n\t" \
"v_mfma_f32_16x16x32_bf16 v[192:195], a[156:159], v[152:155], v[192:195]\n\t" \
"v_mfma_f32_16x16x32_bf16 v[196:199], a[156:159], v[156:159], v[196:199]\n\t" \
"v_mfma_f32_16x16x32_bf16 v[200:203], a[212:215], v[152:155], v[200:203]\n\t" \
"v_mfma_f32_16x16x32_bf16 v[204:207], a[212:215], v[156:159], v[204:207]\n\t" \
"buffer_load_dwordx4 %[vg], %[rh], s95 offen offset:1024 lds\n\t" \
"v_mfma_f32_16x16x32_bf16 v[176:179], a[48:51], v[160:163], v[176:179]\n\t" \
"v_mfma_f32_16x16x32_bf16 v[180:183], a[48:51], v[164:167], v[180:183]\n\t" \
"v_mfma_f32_16x16x32_bf16 v[184:187], a[104:107], v[160:163], v[184:187]\n\t" \
"v_mfma_f32_16x16x32_bf16 v[188:191], a[104:107], v[164:167], v[188:191]\n\t" \
"v_mfma_f32_16x16x32_bf16 v[192:195], a[160:163], v[160:163], v[192:195]\n\t" \
"v_mfma_f32_16x16x32_bf16 v[196:199], a[160:163], v[164:167], v[196:199]\n\t" \
"v_mfma_f32_16x16x32_bf16 v[200:203], a[216:219], v[160:163], v[200:203]\n\t" \
"v_mfma_f32_16x16x32_bf16 v[204:207], a[216:219], v[164:167], v[204:207]\n\t" \
"v_mfma_f32_16x16x32_bf16 v[176:179], a[52:55], v[168:171], v[176:179]\n\t" \
"v_mfma_f32_16x16x32_bf16 v[180:183], a[52:55], v[172:175], v[180:183]\n\t" \
"v_mfma_f32_16x16x32_bf16 v[184:187], a[108:111], v[168:171], v[184:187]\n\t" \
"v_mfma_f32_16x16x32_bf16 v[188:191], a[108:111], v[172:175], v[188:191]\n\t" \
"v_mfma_f32_16x16x32_bf16 v[192:195], a[164:167], v[168:171], v[192:195]\n\t" \
"v_mfma_f32_16x16x32_bf16 v[196:199], a[164:167], v[172:175], v[196:199]\n\t" \
"v_mfma_f32_16x16x32_bf16 v[200:203], a[220:223], v[168:171], v[200:203]\n\t" \
"v_mfma_f32_16x16x32_bf16 v[204:207], a[220:223], v[172:175], v[204:207]\n\t" \
"s_add_u32 s92, s92, 28672\n\t" \
"s_cmp_eq_u32 s92, 114688\n\t" \
"s_cselect_b32 s92, 0, s92\n\t" \
"v_mov_b32 v248, v249\n\t" \
"s_add_u32 s91, s91, 917504\n\t" \
"s_waitcnt lgkmcnt(0)\n\t" \
"v_mfma_f32_16x16x32_bf16 v[208:211], a[0:3], v[64:67], 0\n\t" \
"v_mfma_f32_16x16x32_bf16 v[212:215], a[0:3], v[68:71], 0\n\t" \
"ds_read_b128 v[120:123], v248 offset:7168\n\t" \
"v_mfma_f32_16x16x32_bf16 v[216:219], a[56:59], v[64:67], 0\n\t" \
"v_cvt_pk_bf16_f32 v240, v176, v177\n\t" \
"v_mfma_f32_16x16x32_bf16 v[220:223], a[56:59], v[68:71], 0\n\t" \
"v_cvt_pk_bf16_f32 v241, v178, v179\n\t" \
"v_mfma_f32_16x16x32_bf16 v[224:227], a[112:115], v[64:67], 0\n\t" \
"buffer_store_dwordx2 v[240:241], %[vs0], %[ro], s91 offen offset:0 sc1\n\t" \
"v_mfma_f32_16x16x32_bf16 v[228:231], a[112:115], v[68:71], 0\n\t" \
"v_cvt_pk_bf16_f32 v244, v180, v181\n\t" \
"v_mfma_f32_16x16x32_bf16 v[232:235], a[168:171], v[64:67], 0\n\t" \
"v_cvt_pk_bf16_f32 v245, v182, v183\n\t" \
"v_mfma_f32_16x16x32_bf16 v[236:239], a[168:171], v[68:71], 0\n\t" \
"buffer_store_dwordx2 v[244:245], %[vs1], %[ro], s91 offen offset:0 sc1\n\t" \
"v_mfma_f32_16x16x32_bf16 v[208:211], a[4:7], v[72:75], v[208:211]\n\t" \
"v_cvt_pk_bf16_f32 v240, v184, v185\n\t" \
"v_mfma_f32_16x16x32_bf16 v[212:215], a[4:7], v[76:79], v[212:215]\n\t" \
"v_cvt_pk_bf16_f32 v241, v186, v187\n\t" \
"v_mfma_f32_16x16x32_bf16 v[216:219], a[60:63], v[72:75], v[216:219]\n\t" \
"buffer_store_dwordx2 v[240:241], %[vs0], %[ro], s91 offen offset:512 sc1\n\t" \
"v_mfma_f32_16x16x32_bf16 v[220:223], a[60:63], v[76:79], v[220:223]\n\t" \
"v_cvt_pk_bf16_f32 v244, v188, v189\n\t" \
"v_mfma_f32_16x16x32_bf16 v[224:227], a[116:119], v[72:75], v[224:227]\n\t" \
"v_cvt_pk_bf16_f32 v245, v190, v191\n\t" \
"v_mfma_f32_16x16x32_bf16 v[228:231], a[116:119], v[76:79], v[228:231]\n\t" \
"buffer_store_dwordx2 v[244:245], %[vs1], %[ro], s91 offen offset:512 sc1\n\t" \
"v_mfma_f32_16x16x32_bf16 v[232:235], a[172:175], v[72:75], v[232:235]\n\t" \
"v_cvt_pk_bf16_f32 v240, v192, v193\n\t" \
"v_mfma_f32_16x16x32_bf16 v[236:239], a[172:175], v[76:79], v[236:239]\n\t" \
"v_cvt_pk_bf16_f32 v241, v194, v195\n\t" \
"v_mfma_f32_16x16x32_bf16 v[208:211], a[8:11], v[80:83], v[208:211]\n\t" \
"buffer_store_dwordx2 v[240:241], %[vs0], %[ro], s91 offen offset:1024 sc1\n\t" \
"v_mfma_f32_16x16x32_bf16 v[212:215], a[8:11], v[84:87], v[212:215]\n\t" \
"v_cvt_pk_bf16_f32 v244, v196, v197\n\t" \
"v_mfma_f32_16x16x32_bf16 v[216:219], a[64:67], v[80:83], v[216:219]\n\t" \
"v_cvt_pk_bf16_f32 v245, v198, v199\n\t" \
"v_mfma_f32_16x16x32_bf16 v[220:223], a[64:67], v[84:87], v[220:223]\n\t" \
"buffer_store_dwordx2 v[244:245], %[vs1], %[ro], s91 offen offset:1024 sc1\n\t" \
"s_add_u32 s90, s87, %[lg]\n\t" \
"s_add_u32 s90, s90, 0\n\t" \
"s_mov_b32 m0, s90\n\t" \
"s_add_u32 s95, s89, 0\n\t" \
"buffer_load_dwordx4 %[vg], %[rh], s95 offen offset:2048 lds\n\t" \
"v_mfma_f32_16x16x32_bf16 v[224:227], a[120:123], v[80:83], v[224:227]\n\t" \
"v_cvt_pk_bf16_f32 v240, v200, v201\n\t" \
"v_mfma_f32_16x16x32_bf16 v[228:231], a[120:123], v[84:87], v[228:231]\n\t" \
"v_cvt_pk_bf16_f32 v241, v202, v203\n\t" \
"v_mfma_f32_16x16x32_bf16 v[232:235], a[176:179], v[80:83], v[232:235]\n\t" \
"buffer_store_dwordx2 v[240:241], %[vs0], %[ro], s91 offen offset:1536 sc1\n\t" \
"v_mfma_f32_16x16x32_bf16 v[236:239], a[176:179], v[84:87], v[236:239]\n\t" \
"v_cvt_pk_bf16_f32 v244, v204, v205\n\t" \
"v_mfma_f32_16x16x32_bf16 v[208:211], a[12:15], v[88:91], v[208:211]\n\t" \
"v_cvt_pk_bf16_f32 v245, v206, v207\n\t" \
"v_mfma_f32_16x16x32_bf16 v[212:215], a[12:15], v[92:95], v[212:215]\n\t" \
"buffer_store_dwordx2 v[244:245], %[vs1], %[ro], s91 offen offset:1536 sc1\n\t" \
"v_mfma_f32_16x16x32_bf16 v[216:219], a[68:71], v[88:91], v[216:219]\n\t" \
"v_mfma_f32_16x16x32_bf16 v[220:223], a[68:71], v[92:95], v[220:223]\n\t" \
"ds_read_b128 v[124:127], v248 offset:21504\n\t" \
"v_mfma_f32_16x16x32_bf16 v[224:227], a[124:127], v[88:91], v[224:227]\n\t" \
"v_mfma_f32_16x16x32_bf16 v[228:231], a[124:127], v[92:95], v[228:231]\n\t" \
"ds_read_b128 v[128:131], v248 offset:8192\n\t" \
"v_mfma_f32_16x16x32_bf16 v[232:235], a[180:183], v[88:91], v[232:235]\n\t" \
"v_mfma_f32_16x16x32_bf16 v[236:239], a[180:183], v[92:95], v[236:239]\n\t" \
"ds_read_b128 v[132:135], v248 offset:22528\n\t" \
"v_mfma_f32_16x16x32_bf16 v[208:211], a[16:19], v[96:99], v[208:211]\n\t" \
"v_mfma_f32_16x16x32_bf16 v[212:215], a[16:19], v[100:103], v[212:215]\n\t" \
"ds_read_b128 v[136:139], v248 offset:9216\n\t" \
"v_mfma_f32_16x16x32_bf16 v[216:219], a[72:75], v[96:99], v[216:219]\n\t" \
"v_mfma_f32_16x16x32_bf16 v[220:223], a[72:75], v[100:103], v[220:223]\n\t" \
"ds_read_b128 v[140:143], v248 offset:23552\n\t" \
"v_mfma_f32_16x16x32_bf16 v[224:227], a[128:131], v[96:99], v[224:227]\n\t" \
"v_mfma_f32_16x16x32_bf16 v[228:231], a[128:131], v[100:103], v[228:231]\n\t" \
"ds_read_b128 v[144:147], v248 offset:10240\n\t" \
"v_mfma_f32_16x16x32_bf16 v[232:235], a[184:187], v[96:99], v[232:235]\n\t" \
"v_mfma_f32_16x16x32_bf16 v[236:239], a[184:187], v[100:103], v[236:239]\n\t" \
"ds_read_b128 v[148:151], v248 offset:24576\n\t" \
"buffer_load_dwordx4 %[vg], %[rh], s95 offen offset:3072 lds\n\t" \
"v_mfma_f32_16x16x32_bf16 v[208:211], a[20:23], v[104:107], v[208:211]\n\t" \
"v_mfma_f32_16x16x32_bf16 v[212:215], a[20:23], v[108:111], v[212:215]\n\t" \
"ds_read_b128 v[152:155], v248 offset:11264\n\t" \
"v_mfma_f32_16x16x32_bf16 v[216:219], a[76:79], v[104:107], v[216:219]\n\t" \
"v_mfma_f32_16x16x32_bf16 v[220:223], a[76:79], v[108:111], v[220:223]\n\t" \
"ds_read_b128 v[156:159], v248 offset:25600\n\t" \
"v_mfma_f32_16x16x32_bf16 v[224:227], a[132:135], v[104:107], v[224:227]\n\t" \
"v_mfma_f32_16x16x32_bf16 v[228:231], a[132:135], v[108:111], v[228:231]\n\t" \
"ds_read_b128 v[160:163], v248 offset:12288\n\t" \
"v_mfma_f32_16x16x32_bf16 v[232:235], a[188:191], v[104:107], v[232:235]\n\t" \
"v_mfma_f32_16x16x32_bf16 v[236:239], a[188:191], v[108:111], v[236:239]\n\t" \
"ds_read_b128 v[164:167], v248 offset:26624\n\t" \
"v_mfma_f32_16x16x32_bf16 v[208:211], a[24:27], v[112:115], v[208:211]\n\t" \
"v_mfma_f32_16x16x32_bf16 v[212:215], a[24:27], v[116:119], v[212:215]\n\t" \
"ds_read_b128 v[168:171], v248 offset:13312\n\t" \
"v_mfma_f32_16x16x32_bf16 v[216:219], a[80:83], v[112:115], v[216:219]\n\t" \
"v_mfma_f32_16x16x32_bf16 v[220:223], a[80:83], v[116:119], v[220:223]\n\t" \
"ds_read_b128 v[172:175], v248 offset:27648\n\t" \
"v_mfma_f32_16x16x32_bf16 v[224:227], a[136:139], v[112:115], v[224:227]\n\t" \
"v_mfma_f32_16x16x32_bf16 v[228:231], a[136:139], v[116:119], v[228:231]\n\t" \
"v_mfma_f32_16x16x32_bf16 v[232:235], a[192:195], v[112:115], v[232:235]\n\t" \
"v_mfma_f32_16x16x32_bf16 v[236:239], a[192:195], v[116:119], v[236:239]\n\t" \
"s_add_u32 s89, s89, 57344\n\t" \
"s_min_u32 s89, s89, 1806336\n\t" \
"s_mov_b32 s87, s92\n\t" \
"s_add_u32 s95, s92, 28672\n\t" \
"s_cmp_eq_u32 s95, 114688\n\t" \
"s_cselect_b32 s95, 0, s95\n\t" \
"v_add_u32 v249, s95, %[lr]\n\t" \
"s_waitcnt vmcnt(12) lgkmcnt(0)\n\t" \
"s_barrier\n\t" \
"s_add_u32 s94, s94, 128\n\t" \
"v_mfma_f32_16x16x32_bf16 v[208:211], a[28:31], v[120:123], v[208:211]\n\t" \
"ds_read_b128 v[64:67], v249 offset:0\n\t" \
"v_mfma_f32_16x16x32_bf16 v[212:215], a[28:31], v[124:127], v[212:215]\n\t" \
"v_mfma_f32_16x16x32_bf16 v[216:219], a[84:87], v[120:123], v[216:219]\n\t" \
"ds_read_b128 v[68:71], v249 offset:14336\n\t" \
"v_mfma_f32_16x16x32_bf16 v[220:223], a[84:87], v[124:127], v[220:223]\n\t" \
"v_mfma_f32_16x16x32_bf16 v[224:227], a[140:143], v[120:123], v[224:227]\n\t" \
"ds_read_b128 v[72:75], v249 offset:1024\n\t" \
"v_mfma_f32_16x16x32_bf16 v[228:231], a[140:143], v[124:127], v[228:231]\n\t" \
"v_mfma_f32_16x16x32_bf16 v[232:235], a[196:199], v[120:123], v[232:235]\n\t" \
"ds_read_b128 v[76:79], v249 offset:15360\n\t" \
"v_mfma_f32_16x16x32_bf16 v[236:239], a[196:199], v[124:127], v[236:239]\n\t" \
"v_mfma_f32_16x16x32_bf16 v[208:211], a[32:35], v[128:131], v[208:211]\n\t" \
"ds_read_b128 v[80:83], v249 offset:2048\n\t" \
"v_mfma_f32_16x16x32_bf16 v[212:215], a[32:35], v[132:135], v[212:215]\n\t" \
"v_mfma_f32_16x16x32_bf16 v[216:219], a[88:91], v[128:131], v[216:219]\n\t" \
"ds_read_b128 v[84:87], v249 offset:16384\n\t" \
"v_mfma_f32_16x16x32_bf16 v[220:223], a[88:91], v[132:135], v[220:223]\n\t" \
"v_mfma_f32_16x16x32_bf16 v[224:227], a[144:147], v[128:131], v[224:227]\n\t" \
"ds_read_b128 v[88:91], v249 offset:3072\n\t" \
"v_mfma_f32_16x16x32_bf16 v[228:231], a[144:147], v[132:135], v[228:231]\n\t" \
"v_mfma_f32_16x16x32_bf16 v[232:235], a[200:203], v[128:131], v[232:235]\n\t" \
"ds_read_b128 v[92:95], v249 offset:17408\n\t" \
"v_mfma_f32_16x16x32_bf16 v[236:239], a[200:203], v[132:135], v[236:239]\n\t" \
"v_mfma_f32_16x16x32_bf16 v[208:211], a[36:39], v[136:139], v[208:211]\n\t" \
"ds_read_b128 v[96:99], v249 offset:4096\n\t" \
"v_mfma_f32_16x16x32_bf16 v[212:215], a[36:39], v[140:143], v[212:215]\n\t" \
"v_mfma_f32_16x16x32_bf16 v[216:219], a[92:95], v[136:139], v[216:219]\n\t" \
"ds_read_b128 v[100:103], v249 offset:18432\n\t" \
"v_mfma_f32_16x16x32_bf16 v[220:223], a[92:95], v[140:143], v[220:223]\n\t" \
"s_add_u32 s90, s87, %[lg]\n\t" \
"s_add_u32 s90, s90, 0\n\t" \
"s_mov_b32 m0, s90\n\t" \
"s_add_u32 s95, s89, 0\n\t" \
"buffer_load_dwordx4 %[vg], %[rh], s95 offen offset:0 lds\n\t" \
"v_mfma_f32_16x16x32_bf16 v[224:227], a[148:151], v[136:139], v[224:227]\n\t" \
"ds_read_b128 v[104:107], v249 offset:5120\n\t" \
"v_mfma_f32_16x16x32_bf16 v[228:231], a[148:151], v[140:143], v[228:231]\n\t" \
"v_mfma_f32_16x16x32_bf16 v[232:235], a[204:207], v[136:139], v[232:235]\n\t" \
"ds_read_b128 v[108:111], v249 offset:19456\n\t" \
"v_mfma_f32_16x16x32_bf16 v[236:239], a[204:207], v[140:143], v[236:239]\n\t" \
"v_mfma_f32_16x16x32_bf16 v[208:211], a[40:43], v[144:147], v[208:211]\n\t" \
"ds_read_b128 v[112:115], v249 offset:6144\n\t" \
"v_mfma_f32_16x16x32_bf16 v[212:215], a[40:43], v[148:151], v[212:215]\n\t" \
"v_mfma_f32_16x16x32_bf16 v[216:219], a[96:99], v[144:147], v[216:219]\n\t" \
"ds_read_b128 v[116:119], v249 offset:20480\n\t" \
"v_mfma_f32_16x16x32_bf16 v[220:223], a[96:99], v[148:151], v[220:223]\n\t" \
"v_mfma_f32_16x16x32_bf16 v[224:227], a[152:155], v[144:147], v[224:227]\n\t" \
"v_mfma_f32_16x16x32_bf16 v[228:231], a[152:155], v[148:151], v[228:231]\n\t" \
"v_mfma_f32_16x16x32_bf16 v[232:235], a[208:211], v[144:147], v[232:235]\n\t" \
"v_mfma_f32_16x16x32_bf16 v[236:239], a[208:211], v[148:151], v[236:239]\n\t" \
"v_mfma_f32_16x16x32_bf16 v[208:211], a[44:47], v[152:155], v[208:211]\n\t" \
"v_mfma_f32_16x16x32_bf16 v[212:215], a[44:47], v[156:159], v[212:215]\n\t" \
"v_mfma_f32_16x16x32_bf16 v[216:219], a[100:103], v[152:155], v[216:219]\n\t" \
"v_mfma_f32_16x16x32_bf16 v[220:223], a[100:103], v[156:159], v[220:223]\n\t" \
"v_mfma_f32_16x16x32_bf16 v[224:227], a[156:159], v[152:155], v[224:227]\n\t" \
"v_mfma_f32_16x16x32_bf16 v[228:231], a[156:159], v[156:159], v[228:231]\n\t" \
"v_mfma_f32_16x16x32_bf16 v[232:235], a[212:215], v[152:155], v[232:235]\n\t" \
"v_mfma_f32_16x16x32_bf16 v[236:239], a[212:215], v[156:159], v[236:239]\n\t" \
"buffer_load_dwordx4 %[vg], %[rh], s95 offen offset:1024 lds\n\t" \
"v_mfma_f32_16x16x32_bf16 v[208:211], a[48:51], v[160:163], v[208:211]\n\t" \
"v_mfma_f32_16x16x32_bf16 v[212:215], a[48:51], v[164:167], v[212:215]\n\t" \
"v_mfma_f32_16x16x32_bf16 v[216:219], a[104:107], v[160:163], v[216:219]\n\t" \
"v_mfma_f32_16x16x32_bf16 v[220:223], a[104:107], v[164:167], v[220:223]\n\t" \
"v_mfma_f32_16x16x32_bf16 v[224:227], a[160:163], v[160:163], v[224:227]\n\t" \
"v_mfma_f32_16x16x32_bf16 v[228:231], a[160:163], v[164:167], v[228:231]\n\t" \
"v_mfma_f32_16x16x32_bf16 v[232:235], a[216:219], v[160:163], v[232:235]\n\t" \
"v_mfma_f32_16x16x32_bf16 v[236:239], a[216:219], v[164:167], v[236:239]\n\t" \
"v_mfma_f32_16x16x32_bf16 v[208:211], a[52:55], v[168:171], v[208:211]\n\t" \
"v_mfma_f32_16x16x32_bf16 v[212:215], a[52:55], v[172:175], v[212:215]\n\t" \
"v_mfma_f32_16x16x32_bf16 v[216:219], a[108:111], v[168:171], v[216:219]\n\t" \
"v_mfma_f32_16x16x32_bf16 v[220:223], a[108:111], v[172:175], v[220:223]\n\t" \
"v_mfma_f32_16x16x32_bf16 v[224:227], a[164:167], v[168:171], v[224:227]\n\t" \
"v_mfma_f32_16x16x32_bf16 v[228:231], a[164:167], v[172:175], v[228:231]\n\t" \
"v_mfma_f32_16x16x32_bf16 v[232:235], a[220:223], v[168:171], v[232:235]\n\t" \
"v_mfma_f32_16x16x32_bf16 v[236:239], a[220:223], v[172:175], v[236:239]\n\t" \
"s_add_u32 s92, s92, 28672\n\t" \
"s_cmp_eq_u32 s92, 114688\n\t" \
"s_cselect_b32 s92, 0, s92\n\t" \
"v_mov_b32 v248, v249\n\t" \
"s_add_u32 s91, s91, 917504\n\t" \
"s_add_u32 s88, s88, 2\n\t" \
"s_cmp_lt_u32 s88, 32\n\t" \
"s_nop 0\n\t" \
"s_cbranch_scc1 Lp3%=\n\t" \
"v_cvt_pk_bf16_f32 v240, v208, v209\n\t" \
"v_cvt_pk_bf16_f32 v241, v210, v211\n\t" \
"buffer_store_dwordx2 v[240:241], %[vs0], %[ro], s91 offen offset:0 sc1\n\t" \
"v_cvt_pk_bf16_f32 v244, v212, v213\n\t" \
"v_cvt_pk_bf16_f32 v245, v214, v215\n\t" \
"buffer_store_dwordx2 v[244:245], %[vs1], %[ro], s91 offen offset:0 sc1\n\t" \
"v_cvt_pk_bf16_f32 v240, v216, v217\n\t" \
"v_cvt_pk_bf16_f32 v241, v218, v219\n\t" \
"buffer_store_dwordx2 v[240:241], %[vs0], %[ro], s91 offen offset:512 sc1\n\t" \
"v_cvt_pk_bf16_f32 v244, v220, v221\n\t" \
"v_cvt_pk_bf16_f32 v245, v222, v223\n\t" \
"buffer_store_dwordx2 v[244:245], %[vs1], %[ro], s91 offen offset:512 sc1\n\t" \
"v_cvt_pk_bf16_f32 v240, v224, v225\n\t" \
"v_cvt_pk_bf16_f32 v241, v226, v227\n\t" \
"buffer_store_dwordx2 v[240:241], %[vs0], %[ro], s91 offen offset:1024 sc1\n\t" \
"v_cvt_pk_bf16_f32 v244, v228, v229\n\t" \
"v_cvt_pk_bf16_f32 v245, v230, v231\n\t" \
"buffer_store_dwordx2 v[244:245], %[vs1], %[ro], s91 offen offset:1024 sc1\n\t" \
"v_cvt_pk_bf16_f32 v240, v232, v233\n\t" \
"v_cvt_pk_bf16_f32 v241, v234, v235\n\t" \
"buffer_store_dwordx2 v[240:241], %[vs0], %[ro], s91 offen offset:1536 sc1\n\t" \
"v_cvt_pk_bf16_f32 v244, v236, v237\n\t" \
"v_cvt_pk_bf16_f32 v245, v238, v239\n\t" \
"buffer_store_dwordx2 v[244:245], %[vs1], %[ro], s91 offen offset:1536 sc1\n\t" \
"s_waitcnt vmcnt(0) lgkmcnt(0)\n\t" \
"s_barrier\n\t" \
"s_add_u32 s94, s94, 128\n\t" \
"s_add_u32 s94, s94, 128\n\t" \
"s_mov_b32 m0, s93\n\t" \
"s_nop 1\n\t"

#define ASM_P3_0W1_CLOB "v64", "v65", "v66", "v67", "v68", "v69", "v70", "v71", "v72", "v73", "v74", "v75", "v76", "v77", "v78", "v79", "v80", "v81", "v82", "v83", "v84", "v85", "v86", "v87", "v88", "v89", "v90", "v91", "v92", "v93", "v94", "v95", "v96", "v97", "v98", "v99", "v100", "v101", "v102", "v103", "v104", "v105", "v106", "v107", "v108", "v109", "v110", "v111", "v112", "v113", "v114", "v115", "v116", "v117", "v118", "v119", "v120", "v121", "v122", "v123", "v124", "v125", "v126", "v127", "v128", "v129", "v130", "v131", "v132", "v133", "v134", "v135", "v136", "v137", "v138", "v139", "v140", "v141", "v142", "v143", "v144", "v145", "v146", "v147", "v148", "v149", "v150", "v151", "v152", "v153", "v154", "v155", "v156", "v157", "v158", "v159", "v160", "v161", "v162", "v163", "v164", "v165", "v166", "v167", "v168", "v169", "v170", "v171", "v172", "v173", "v174", "v175", "v176", "v177", "v178", "v179", "v180", "v181", "v182", "v183", "v184", "v185", "v186", "v187", "v188", "v189", "v190", "v191", "v192", "v193", "v194", "v195", "v196", "v197", "v198", "v199", "v200", "v201", "v202", "v203", "v204", "v205", "v206", "v207", "v208", "v209", "v210", "v211", "v212", "v213", "v214", "v215", "v216", "v217", "v218", "v219", "v220", "v221", "v222", "v223", "v224", "v225", "v226", "v227", "v228", "v229", "v230", "v231", "v232", "v233", "v234", "v235", "v236", "v237", "v238", "v239", "v240", "v241", "v244", "v245", "v248", "v249", "v250", "a0", "a1", "a2", "a3", "a4", "a5", "a6", "a7", "a8", "a9", "a10", "a11", "a12", "a13", "a14", "a15", "a16", "a17", "a18", "a19", "a20", "a21", "a22", "a23", "a24", "a25", "a26", "a27", "a28", "a29", "a30", "a31", "a32", "a33", "a34", "a35", "a36", "a37", "a38", "a39", "a40", "a41", "a42", "a43", "a44", "a45", "a46", "a47", "a48", "a49", "a50", "a51", "a52", "a53", "a54", "a55", "a56", "a57", "a58", "a59", "a60", "a61", "a62", "a63", "a64", "a65", "a66", "a67", "a68", "a69", "a70", "a71", "a72", "a73", "a74", "a75", "a76", "a77", "a78", "a79", "a80", "a81", "a82", "a83", "a84", "a85", "a86", "a87", "a88", "a89", "a90", "a91", "a92", "a93", "a94", "a95", "a96", "a97", "a98", "a99", "a100", "a101", "a102", "a103", "a104", "a105", "a106", "a107", "a108", "a109", "a110", "a111", "a112", "a113", "a114", "a115", "a116", "a117", "a118", "a119", "a120", "a121", "a122", "a123", "a124", "a125", "a126", "a127", "a128", "a129", "a130", "a131", "a132", "a133", "a134", "a135", "a136", "a137", "a138", "a139", "a140", "a141", "a142", "a143", "a144", "a145", "a146", "a147", "a148", "a149", "a150", "a151", "a152", "a153", "a154", "a155", "a156", "a157", "a158", "a159", "a160", "a161", "a162", "a163", "a164", "a165", "a166", "a167", "a168", "a169", "a170", "a171", "a172", "a173", "a174", "a175", "a176", "a177", "a178", "a179", "a180", "a181", "a182", "a183", "a184", "a185", "a186", "a187", "a188", "a189", "a190", "a191", "a192", "a193", "a194", "a195", "a196", "a197", "a198", "a199", "a200", "a201", "a202", "a203", "a204", "a205", "a206", "a207", "a208", "a209", "a210", "a211", "a212", "a213", "a214", "a215", "a216", "a217", "a218", "a219", "a220", "a221", "a222", "a223", "s86", "s87", "s88", "s89", "s90", "s91", "s92", "s93", "s94", "s95", "vcc", "scc", "memory"

#define ASM_P3_0W2_TEXT \
"s_mov_b32 s93, m0\n\t" \
"s_add_u32 s89, %[src0], 172032\n\t" \
"s_mov_b32 s87, 86016\n\t" \
"s_waitcnt vmcnt(0)\n\t" \
"s_barrier\n\t" \
"v_mov_b32 v250, 1\n\t" \
"v_mov_b32 v248, %[lr]\n\t" \
"ds_read_b128 v[64:67], v248 offset:0\n\t" \
"ds_read_b128 v[68:71], v248 offset:14336\n\t" \
"ds_read_b128 v[72:75], v248 offset:1024\n\t" \
"ds_read_b128 v[76:79], v248 offset:15360\n\t" \
"ds_read_b128 v[80:83], v248 offset:2048\n\t" \
"ds_read_b128 v[84:87], v248 offset:16384\n\t" \
"ds_read_b128 v[88:91], v248 offset:3072\n\t" \
"ds_read_b128 v[92:95], v248 offset:17408\n\t" \
"ds_read_b128 v[96:99], v248 offset:4096\n\t" \
"ds_read_b128 v[100:103], v248 offset:18432\n\t" \
"ds_read_b128 v[104:107], v248 offset:5120\n\t" \
"ds_read_b128 v[108:111], v248 offset:19456\n\t" \
"ds_read_b128 v[112:115], v248 offset:6144\n\t" \
"ds_read_b128 v[116:119], v248 offset:20480\n\t" \
"s_mov_b32 s92, 0\n\t" \
"s_mov_b32 s91, %[so0]\n\t" \
"s_mov_b32 s94, %[ao0]\n\t" \
"s_waitcnt lgkmcnt(0)\n\t" \
"v_mfma_f32_16x16x32_bf16 v[176:179], a[0:3], v[64:67], 0\n\t" \
"v_mfma_f32_16x16x32_bf16 v[180:183], a[0:3], v[68:71], 0\n\t" \
"ds_read_b128 v[120:123], v248 offset:7168\n\t" \
"v_mfma_f32_16x16x32_bf16 v[184:187], a[56:59], v[64:67], 0\n\t" \
"v_mfma_f32_16x16x32_bf16 v[188:191], a[56:59], v[68:71], 0\n\t" \
"ds_read_b128 v[124:127], v248 offset:21504\n\t" \
"v_mfma_f32_16x16x32_bf16 v[192:195], a[112:115], v[64:67], 0\n\t" \
"v_mfma_f32_16x16x32_bf16 v[196:199], a[112:115], v[68:71], 0\n\t" \
"ds_read_b128 v[128:131], v248 offset:8192\n\t" \
"v_mfma_f32_16x16x32_bf16 v[176:179], a[4:7], v[72:75], v[176:179]\n\t" \
"v_mfma_f32_16x16x32_bf16 v[180:183], a[4:7], v[76:79], v[180:183]\n\t" \
"ds_read_b128 v[132:135], v248 offset:22528\n\t" \
"s_add_u32 s90, s87, %[lg]\n\t" \
"s_add_u32 s90, s90, 4096\n\t" \
"s_mov_b32 m0, s90\n\t" \
"s_add_u32 s95, s89, 4096\n\t" \
"buffer_load_dwordx4 %[vg], %[rh], s95 offen offset:1024 lds\n\t" \
"v_mfma_f32_16x16x32_bf16 v[184:187], a[60:63], v[72:75], v[184:187]\n\t" \
"v_mfma_f32_16x16x32_bf16 v[188:191], a[60:63], v[76:79], v[188:191]\n\t" \
"ds_read_b128 v[136:139], v248 offset:9216\n\t" \
"v_mfma_f32_16x16x32_bf16 v[192:195], a[116:119], v[72:75], v[192:195]\n\t" \
"v_mfma_f32_16x16x32_bf16 v[196:199], a[116:119], v[76:79], v[196:199]\n\t" \
"ds_read_b128 v[140:143], v248 offset:23552\n\t" \
"v_mfma_f32_16x16x32_bf16 v[176:179], a[8:11], v[80:83], v[176:179]\n\t" \
"v_mfma_f32_16x16x32_bf16 v[180:183], a[8:11], v[84:87], v[180:183]\n\t" \
"ds_read_b128 v[144:147], v248 offset:10240\n\t" \
"v_mfma_f32_16x16x32_bf16 v[184:187], a[64:67], v[80:83], v[184:187]\n\t" \
"v_mfma_f32_16x16x32_bf16 v[188:191], a[64:67], v[84:87], v[188:191]\n\t" \
"ds_read_b128 v[148:151], v248 offset:24576\n\t" \
"buffer_load_dwordx4 %[vg], %[rh], s95 offen offset:2048 lds\n\t" \
"v_mfma_f32_16x16x32_bf16 v[192:195], a[120:123], v[80:83], v[192:195]\n\t" \
"v_mfma_f32_16x16x32_bf16 v[196:199], a[120:123], v[84:87], v[196:199]\n\t" \
"ds_read_b128 v[152:155], v248 offset:11264\n\t" \
"v_mfma_f32_16x16x32_bf16 v[176:179], a[12:15], v[88:91], v[176:179]\n\t" \
"v_mfma_f32_16x16x32_bf16 v[180:183], a[12:15], v[92:95], v[180:183]\n\t" \
"ds_read_b128 v[156:159], v248 offset:25600\n\t" \
"v_mfma_f32_16x16x32_bf16 v[184:187], a[68:71], v[88:91], v[184:187]\n\t" \
"v_mfma_f32_16x16x32_bf16 v[188:191], a[68:71], v[92:95], v[188:191]\n\t" \
"ds_read_b128 v[160:163], v248 offset:12288\n\t" \
"v_mfma_f32_16x16x32_bf16 v[192:195], a[124:127], v[88:91], v[192:195]\n\t" \
"v_mfma_f32_16x16x32_bf16 v[196:199], a[124:127], v[92:95], v[196:199]\n\t" \
"ds_read_b128 v[164:167], v248 offset:26624\n\t" \
"buffer_load_dwordx4 %[vg], %[rh], s95 offen offset:3072 lds\n\t" \
"v_mfma_f32_16x16x32_bf16 v[176:179], a[16:19], v[96:99], v[176:179]\n\t" \
"v_mfma_f32_16x16x32_bf16 v[180:183], a[16:19], v[100:103], v[180:183]\n\t" \
"ds_read_b128 v[168:171], v248 offset:13312\n\t" \
"v_mfma_f32_16x16x32_bf16 v[184:187], a[72:75], v[96:99], v[184:187]\n\t" \
"v_mfma_f32_16x16x32_bf16 v[188:191], a[72:75], v[100:103], v[188:191]\n\t" \
"ds_read_b128 v[172:175], v248 offset:27648\n\t" \
"v_mfma_f32_16x16x32_bf16 v[192:195], a[128:131], v[96:99], v[192:195]\n\t" \
"v_mfma_f32_16x16x32_bf16 v[196:199], a[128:131], v[100:103], v[196:199]\n\t" \
"v_mfma_f32_16x16x32_bf16 v[176:179], a[20:23], v[104:107], v[176:179]\n\t" \
"v_mfma_f32_16x16x32_bf16 v[180:183], a[20:23], v[108:111], v[180:183]\n\t" \
"s_add_u32 s90, s87, %[lg]\n\t" \
"s_add_u32 s90, s90, 8192\n\t" \
"s_mov_b32 m0, s90\n\t" \
"s_add_u32 s95, s89, 8192\n\t" \
"buffer_load_dwordx4 %[vg], %[rh], s95 offen offset:0 lds\n\t" \
"v_mfma_f32_16x16x32_bf16 v[184:187], a[76:79], v[104:107], v[184:187]\n\t" \
"v_mfma_f32_16x16x32_bf16 v[188:191], a[76:79], v[108:111], v[188:191]\n\t" \
"v_mfma_f32_16x16x32_bf16 v[192:195], a[132:135], v[104:107], v[192:195]\n\t" \
"v_mfma_f32_16x16x32_bf16 v[196:199], a[132:135], v[108:111], v[196:199]\n\t" \
"v_mfma_f32_16x16x32_bf16 v[176:179], a[24:27], v[112:115], v[176:179]\n\t" \
"v_mfma_f32_16x16x32_bf16 v[180:183], a[24:27], v[116:119], v[180:183]\n\t" \
"v_mfma_f32_16x16x32_bf16 v[184:187], a[80:83], v[112:115], v[184:187]\n\t" \
"v_mfma_f32_16x16x32_bf16 v[188:191], a[80:83], v[116:119], v[188:191]\n\t" \
"buffer_load_dwordx4 %[vg], %[rh], s95 offen offset:1024 lds\n\t" \
"v_mfma_f32_16x16x32_bf16 v[192:195], a[136:139], v[112:115], v[192:195]\n\t" \
"v_mfma_f32_16x16x32_bf16 v[196:199], a[136:139], v[116:119], v[196:199]\n\t" \
"s_add_u32 s89, s89, 57344\n\t" \
"s_min_u32 s89, s89, 1806336\n\t" \
"s_mov_b32 s87, s92\n\t" \
"s_add_u32 s95, s92, 28672\n\t" \
"s_cmp_eq_u32 s95, 114688\n\t" \
"s_cselect_b32 s95, 0, s95\n\t" \
"v_add_u32 v249, s95, %[lr]\n\t" \
"s_waitcnt vmcnt(63) lgkmcnt(0)\n\t" \
"s_barrier\n\t" \
"v_mfma_f32_16x16x32_bf16 v[176:179], a[28:31], v[120:123], v[176:179]\n\t" \
"ds_read_b128 v[64:67], v249 offset:0\n\t" \
"v_mfma_f32_16x16x32_bf16 v[180:183], a[28:31], v[124:127], v[180:183]\n\t" \
"v_mfma_f32_16x16x32_bf16 v[184:187], a[84:87], v[120:123], v[184:187]\n\t" \
"ds_read_b128 v[68:71], v249 offset:14336\n\t" \
"v_mfma_f32_16x16x32_bf16 v[188:191], a[84:87], v[124:127], v[188:191]\n\t" \
"v_mfma_f32_16x16x32_bf16 v[192:195], a[140:143], v[120:123], v[192:195]\n\t" \
"ds_read_b128 v[72:75], v249 offset:1024\n\t" \
"v_mfma_f32_16x16x32_bf16 v[196:199], a[140:143], v[124:127], v[196:199]\n\t" \
"v_mfma_f32_16x16x32_bf16 v[176:179], a[32:35], v[128:131], v[176:179]\n\t" \
"ds_read_b128 v[76:79], v249 offset:15360\n\t" \
"v_mfma_f32_16x16x32_bf16 v[180:183], a[32:35], v[132:135], v[180:183]\n\t" \
"s_add_u32 s90, s87, %[lg]\n\t" \
"s_add_u32 s90, s90, 0\n\t" \
"s_mov_b32 m0, s90\n\t" \
"s_add_u32 s95, s89, 0\n\t" \
"buffer_load_dwordx4 %[vg], %[rh], s95 offen offset:0 lds\n\t" \
"v_mfma_f32_16x16x32_bf16 v[184:187], a[88:91], v[128:131], v[184:187]\n\t" \
"ds_read_b128 v[80:83], v249 offset:2048\n\t" \
"v_mfma_f32_16x16x32_bf16 v[188:191], a[88:91], v[132:135], v[188:191]\n\t" \
"v_mfma_f32_16x16x32_bf16 v[192:195], a[144:147], v[128:131], v[192:195]\n\t" \
"ds_read_b128 v[84:87], v249 offset:16384\n\t" \
"v_mfma_f32_16x16x32_bf16 v[196:199], a[144:147], v[132:135], v[196:199]\n\t" \
"v_mfma_f32_16x16x32_bf16 v[176:179], a[36:39], v[136:139], v[176:179]\n\t" \
"ds_read_b128 v[88:91], v249 offset:3072\n\t" \
"v_mfma_f32_16x16x32_bf16 v[180:183], a[36:39], v[140:143], v[180:183]\n\t" \
"v_mfma_f32_16x16x32_bf16 v[184:187], a[92:95], v[136:139], v[184:187]\n\t" \
"ds_read_b128 v[92:95], v249 offset:17408\n\t" \
"v_mfma_f32_16x16x32_bf16 v[188:191], a[92:95], v[140:143], v[188:191]\n\t" \
"buffer_load_dwordx4 %[vg], %[rh], s95 offen offset:1024 lds\n\t" \
"v_mfma_f32_16x16x32_bf16 v[192:195], a[148:151], v[136:139], v[192:195]\n\t" \
"ds_read_b128 v[96:99], v249 offset:4096\n\t" \
"v_mfma_f32_16x16x32_bf16 v[196:199], a[148:151], v[140:143], v[196:199]\n\t" \
"v_mfma_f32_16x16x32_bf16 v[176:179], a[40:43], v[144:147], v[176:179]\n\t" \
"ds_read_b128 v[100:103], v249 offset:18432\n\t" \
"v_mfma_f32_16x16x32_bf16 v[180:183], a[40:43], v[148:151], v[180:183]\n\t" \
"v_mfma_f32_16x16x32_bf16 v[184:187], a[96:99], v[144:147], v[184:187]\n\t" \
"ds_read_b128 v[104:107], v249 offset:5120\n\t" \
"v_mfma_f32_16x16x32_bf16 v[188:191], a[96:99], v[148:151], v[188:191]\n\t" \
"v_mfma_f32_16x16x32_bf16 v[192:195], a[152:155], v[144:147], v[192:195]\n\t" \
"ds_read_b128 v[108:111], v249 offset:19456\n\t" \
"v_mfma_f32_16x16x32_bf16 v[196:199], a[152:155], v[148:151], v[196:199]\n\t" \
"buffer_load_dwordx4 %[vg], %[rh], s95 offen offset:2048 lds\n\t" \
"v_mfma_f32_16x16x32_bf16 v[176:179], a[44:47], v[152:155], v[176:179]\n\t" \
"ds_read_b128 v[112:115], v249 offset:6144\n\t" \
"v_mfma_f32_16x16x32_bf16 v[180:183], a[44:47], v[156:159], v[180:183]\n\t" \
"v_mfma_f32_16x16x32_bf16 v[184:187], a[100:103], v[152:155], v[184:187]\n\t" \
"ds_read_b128 v[116:119], v249 offset:20480\n\t" \
"v_mfma_f32_16x16x32_bf16 v[188:191], a[100:103], v[156:159], v[188:191]\n\t" \
"v_mfma_f32_16x16x32_bf16 v[192:195], a[156:159], v[152:155], v[192:195]\n\t" \
"v_mfma_f32_16x16x32_bf16 v[196:199], a[156:159], v[156:159], v[196:199]\n\t" \
"v_mfma_f32_16x16x32_bf16 v[176:179], a[48:51], v[160:163], v[176:179]\n\t" \
"v_mfma_f32_16x16x32_bf16 v[180:183], a[48:51], v[164:167], v[180:183]\n\t" \
"buffer_load_dwordx4 %[vg], %[rh], s95 offen offset:3072 lds\n\t" \
"v_mfma_f32_16x16x32_bf16 v[184:187], a[104:107], v[160:163], v[184:187]\n\t" \
"v_mfma_f32_16x16x32_bf16 v[188:191], a[104:107], v[164:167], v[188:191]\n\t" \
"v_mfma_f32_16x16x32_bf16 v[192:195], a[160:163], v[160:163], v[192:195]\n\t" \
"v_mfma_f32_16x16x32_bf16 v[196:199], a[160:163], v[164:167], v[196:199]\n\t" \
"v_mfma_f32_16x16x32_bf16 v[176:179], a[52:55], v[168:171], v[176:179]\n\t" \
"v_mfma_f32_16x16x32_bf16 v[180:183], a[52:55], v[172:175], v[180:183]\n\t" \
"v_mfma_f32_16x16x32_bf16 v[184:187], a[108:111], v[168:171], v[184:187]\n\t" \
"v_mfma_f32_16x16x32_bf16 v[188:191], a[108:111], v[172:175], v[188:191]\n\t" \
"s_add_u32 s90, s87, %[lg]\n\t" \
"s_add_u32 s90, s90, 4096\n\t" \
"s_mov_b32 m0, s90\n\t" \
"s_add_u32 s95, s89, 4096\n\t" \
"buffer_load_dwordx4 %[vg], %[rh], s95 offen offset:0 lds\n\t" \
"v_mfma_f32_16x16x32_bf16 v[192:195], a[164:167], v[168:171], v[192:195]\n\t" \
"v_mfma_f32_16x16x32_bf16 v[196:199], a[164:167], v[172:175], v[196:199]\n\t" \
"s_add_u32 s92, s92, 28672\n\t" \
"s_cmp_eq_u32 s92, 114688\n\t" \
"s_cselect_b32 s92, 0, s92\n\t" \
"v_mov_b32 v248, v249\n\t" \
"s_add_u32 s91, s91, 917504\n\t" \
"v_cvt_pk_bf16_f32 v240, v176, v177\n\t" \
"v_cvt_pk_bf16_f32 v241, v178, v179\n\t" \
"buffer_store_dwordx2 v[240:241], %[vs0], %[ro], s91 offen offset:0 sc1\n\t" \
"v_cvt_pk_bf16_f32 v244, v180, v181\n\t" \
"v_cvt_pk_bf16_f32 v245, v182, v183\n\t" \
"buffer_store_dwordx2 v[244:245], %[vs1], %[ro], s91 offen offset:0 sc1\n\t" \
"v_cvt_pk_bf16_f32 v240, v184, v185\n\t" \
"v_cvt_pk_bf16_f32 v241, v186, v187\n\t" \
"buffer_store_dwordx2 v[240:241], %[vs0], %[ro], s91 offen offset:512 sc1\n\t" \
"v_cvt_pk_bf16_f32 v244, v188, v189\n\t" \
"v_cvt_pk_bf16_f32 v245, v190, v191\n\t" \
"buffer_store_dwordx2 v[244:245], %[vs1], %[ro], s91 offen offset:512 sc1\n\t" \
"v_cvt_pk_bf16_f32 v240, v192, v193\n\t" \
"v_cvt_pk_bf16_f32 v241, v194, v195\n\t" \
"buffer_store_dwordx2 v[240:241], %[vs0], %[ro], s91 offen offset:1024 sc1\n\t" \
"v_cvt_pk_bf16_f32 v244, v196, v197\n\t" \
"v_cvt_pk_bf16_f32 v245, v198, v199\n\t" \
"buffer_store_dwordx2 v[244:245], %[vs1], %[ro], s91 offen offset:1024 sc1\n\t" \
"s_waitcnt vmcnt(0)\n\t" \
"s_barrier\n\t" \
"s_add_u32 s94, s94, 128\n\t" \
"s_waitcnt lgkmcnt(0)\n\t" \
"v_mfma_f32_16x16x32_bf16 v[208:211], a[0:3], v[64:67], 0\n\t" \
"v_mfma_f32_16x16x32_bf16 v[212:215], a[0:3], v[68:71], 0\n\t" \
"ds_read_b128 v[120:123], v248 offset:7168\n\t" \
"v_mfma_f32_16x16x32_bf16 v[216:219], a[56:59], v[64:67], 0\n\t" \
"v_mfma_f32_16x16x32_bf16 v[220:223], a[56:59], v[68:71], 0\n\t" \
"ds_read_b128 v[124:127], v248 offset:21504\n\t" \
"v_mfma_f32_16x16x32_bf16 v[224:227], a[112:115], v[64:67], 0\n\t" \
"v_mfma_f32_16x16x32_bf16 v[228:231], a[112:115], v[68:71], 0\n\t" \
"ds_read_b128 v[128:131], v248 offset:8192\n\t" \
"v_mfma_f32_16x16x32_bf16 v[208:211], a[4:7], v[72:75], v[208:211]\n\t" \
"v_mfma_f32_16x16x32_bf16 v[212:215], a[4:7], v[76:79], v[212:215]\n\t" \
"ds_read_b128 v[132:135], v248 offset:22528\n\t" \
"s_add_u32 s90, s87, %[lg]\n\t" \
"s_add_u32 s90, s90, 4096\n\t" \
"s_mov_b32 m0, s90\n\t" \
"s_add_u32 s95, s89, 4096\n\t" \
"buffer_load_dwordx4 %[vg], %[rh], s95 offen offset:1024 lds\n\t" \
"v_mfma_f32_16x16x32_bf16 v[216:219], a[60:63], v[72:75], v[216:219]\n\t" \
"v_mfma_f32_16x16x32_bf16 v[220:223], a[60:63], v[76:79], v[220:223]\n\t" \
"ds_read_b128 v[136:139], v248 offset:9216\n\t" \
"v_mfma_f32_16x16x32_bf16 v[224:227], a[116:119], v[72:75], v[224:227]\n\t" \
"v_mfma_f32_16x16x32_bf16 v[228:231], a[116:119], v[76:79], v[228:231]\n\t" \
"ds_read_b128 v[140:143], v248 offset:23552\n\t" \
"v_mfma_f32_16x16x32_bf16 v[208:211], a[8:11], v[80:83], v[208:211]\n\t" \
"v_mfma_f32_16x16x32_bf16 v[212:215], a[8:11], v[84:87], v[212:215]\n\t" \
"ds_read_b128 v[144:147], v248 offset:10240\n\t" \
"v_mfma_f32_16x16x32_bf16 v[216:219], a[64:67], v[80:83], v[216:219]\n\t" \
"v_mfma_f32_16x16x32_bf16 v[220:223], a[64:67], v[84:87], v[220:223]\n\t" \
"ds_read_b128 v[148:151], v248 offset:24576\n\t" \
"buffer_load_dwordx4 %[vg], %[rh], s95 offen offset:2048 lds\n\t" \
"v_mfma_f32_16x16x32_bf16 v[224:227], a[120:123], v[80:83], v[224:227]\n\t" \
"v_mfma_f32_16x16x32_bf16 v[228:231], a[120:123], v[84:87], v[228:231]\n\t" \
"ds_read_b128 v[152:155], v248 offset:11264\n\t" \
"v_mfma_f32_16x16x32_bf16 v[208:211], a[12:15], v[88:91], v[208:211]\n\t" \
"v_mfma_f32_16x16x32_bf16 v[212:215], a[12:15], v[92:95], v[212:215]\n\t" \
"ds_read_b128 v[156:159], v248 offset:25600\n\t" \
"v_mfma_f32_16x16x32_bf16 v[216:219], a[68:71], v[88:91], v[216:219]\n\t" \
"v_mfma_f32_16x16x32_bf16 v[220:223], a[68:71], v[92:95], v[220:223]\n\t" \
"ds_read_b128 v[160:163], v248 offset:12288\n\t" \
"v_mfma_f32_16x16x32_bf16 v[224:227], a[124:127], v[88:91], v[224:227]\n\t" \
"v_mfma_f32_16x16x32_bf16 v[228:231], a[124:127], v[92:95], v[228:231]\n\t" \
"ds_read_b128 v[164:167], v248 offset:26624\n\t" \
"buffer_load_dwordx4 %[vg], %[rh], s95 offen offset:3072 lds\n\t" \
"v_mfma_f32_16x16x32_bf16 v[208:211], a[16:19], v[96:99], v[208:211]\n\t" \
"v_mfma_f32_16x16x32_bf16 v[212:215], a[16:19], v[100:103], v[212:215]\n\t" \
"ds_read_b128 v[168:171], v248 offset:13312\n\t" \
"v_mfma_f32_16x16x32_bf16 v[216:219], a[72:75], v[96:99], v[216:219]\n\t" \
"v_mfma_f32_16x16x32_bf16 v[220:223], a[72:75], v[100:103], v[220:223]\n\t" \
"ds_read_b128 v[172:175], v248 offset:27648\n\t" \
"v_mfma_f32_16x16x32_bf16 v[224:227], a[128:131], v[96:99], v[224:227]\n\t" \
"v_mfma_f32_16x16x32_bf16 v[228:231], a[128:131], v[100:103], v[228:231]\n\t" \
"v_mfma_f32_16x16x32_bf16 v[208:211], a[20:23], v[104:107], v[208:211]\n\t" \
"v_mfma_f32_16x16x32_bf16 v[212:215], a[20:23], v[108:111], v[212:215]\n\t" \
"s_add_u32 s90, s87, %[lg]\n\t" \
"s_add_u32 s90, s90, 8192\n\t" \
"s_mov_b32 m0, s90\n\t" \
"s_add_u32 s95, s89, 8192\n\t" \
"buffer_load_dwordx4 %[vg], %[rh], s95 offen offset:0 lds\n\t" \
"v_mfma_f32_16x16x32_bf16 v[216:219], a[76:79], v[104:107], v[216:219]\n\t" \
"v_mfma_f32_16x16x32_bf16 v[220:223], a[76:79], v[108:111], v[220:223]\n\t" \
"v_mfma_f32_16x16x32_bf16 v[224:227], a[132:135], v[104:107], v[224:227]\n\t" \
"v_mfma_f32_16x16x32_bf16 v[228:231], a[132:135], v[108:111], v[228:231]\n\t" \
"v_mfma_f32_16x16x32_bf16 v[208:211], a[24:27], v[112:115], v[208:211]\n\t" \
"v_mfma_f32_16x16x32_bf16 v[212:215], a[24:27], v[116:119], v[212:215]\n\t" \
"v_mfma_f32_16x16x32_bf16 v[216:219], a[80:83], v[112:115], v[216:219]\n\t" \
"v_mfma_f32_16x16x32_bf16 v[220:223], a[80:83], v[116:119], v[220:223]\n\t" \
"buffer_load_dwordx4 %[vg], %[rh], s95 offen offset:1024 lds\n\t" \
"v_mfma_f32_16x16x32_bf16 v[224:227], a[136:139], v[112:115], v[224:227]\n\t" \
"v_mfma_f32_16x16x32_bf16 v[228:231], a[136:139], v[116:119], v[228:231]\n\t" \
"s_add_u32 s89, s89, 57344\n\t" \
"s_min_u32 s89, s89, 1806336\n\t" \
"s_mov_b32 s87, s92\n\t" \
"s_add_u32 s95, s92, 28672\n\t" \
"s_cmp_eq_u32 s95, 114688\n\t" \
"s_cselect_b32 s95, 0, s95\n\t" \
"v_add_u32 v249, s95, %[lr]\n\t" \
"s_waitcnt vmcnt(63) lgkmcnt(0)\n\t" \
"s_barrier\n\t" \
"v_mfma_f32_16x16x32_bf16 v[208:211], a[28:31], v[120:123], v[208:211]\n\t" \
"ds_read_b128 v[64:67], v249 offset:0\n\t" \
"v_mfma_f32_16x16x32_bf16 v[212:215], a[28:31], v[124:127], v[212:215]\n\t" \
"v_mfma_f32_16x16x32_bf16 v[216:219], a[84:87], v[120:123], v[216:219]\n\t" \
"ds_read_b128 v[68:71], v249 offset:14336\n\t" \
"v_mfma_f32_16x16x32_bf16 v[220:223], a[84:87], v[124:127], v[220:223]\n\t" \
"v_mfma_f32_16x16x32_bf16 v[224:227], a[140:143], v[120:123], v[224:227]\n\t" \
"ds_read_b128 v[72:75], v249 offset:1024\n\t" \
"v_mfma_f32_16x16x32_bf16 v[228:231], a[140:143], v[124:127], v[228:231]\n\t" \
"v_mfma_f32_16x16x32_bf16 v[208:211], a[32:35], v[128:131], v[208:211]\n\t" \
"ds_read_b128 v[76:79], v249 offset:15360\n\t" \
"v_mfma_f32_16x16x32_bf16 v[212:215], a[32:35], v[132:135], v[212:215]\n\t" \
"s_add_u32 s90, s87, %[lg]\n\t" \
"s_add_u32 s90, s90, 0\n\t" \
"s_mov_b32 m0, s90\n\t" \
"s_add_u32 s95, s89, 0\n\t" \
"buffer_load_dwordx4 %[vg], %[rh], s95 offen offset:0 lds\n\t" \
"v_mfma_f32_16x16x32_bf16 v[216:219], a[88:91], v[128:131], v[216:219]\n\t" \
"ds_read_b128 v[80:83], v249 offset:2048\n\t" \
"v_mfma_f32_16x16x32_bf16 v[220:223], a[88:91], v[132:135], v[220:223]\n\t" \
"v_mfma_f32_16x16x32_bf16 v[224:227], a[144:147], v[128:131], v[224:227]\n\t" \
"ds_read_b128 v[84:87], v249 offset:16384\n\t" \
"v_mfma_f32_16x16x32_bf16 v[228:231], a[144:147], v[132:135], v[228:231]\n\t" \
"v_mfma_f32_16x16x32_bf16 v[208:211], a[36:39], v[136:139], v[208:211]\n\t" \
"ds_read_b128 v[88:91], v249 offset:3072\n\t" \
"v_mfma_f32_16x16x32_bf16 v[212:215], a[36:39], v[140:143], v[212:215]\n\t" \
"v_mfma_f32_16x16x32_bf16 v[216:219], a[92:95], v[136:139], v[216:219]\n\t" \
"ds_read_b128 v[92:95], v249 offset:17408\n\t" \
"v_mfma_f32_16x16x32_bf16 v[220:223], a[92:95], v[140:143], v[220:223]\n\t" \
"buffer_load_dwordx4 %[vg], %[rh], s95 offen offset:1024 lds\n\t" \
"v_mfma_f32_16x16x32_bf16 v[224:227], a[148:151], v[136:139], v[224:227]\n\t" \
"ds_read_b128 v[96:99], v249 offset:4096\n\t" \
"v_mfma_f32_16x16x32_bf16 v[228:231], a[148:151], v[140:143], v[228:231]\n\t" \
"v_mfma_f32_16x16x32_bf16 v[208:211], a[40:43], v[144:147], v[208:211]\n\t" \
"ds_read_b128 v[100:103], v249 offset:18432\n\t" \
"v_mfma_f32_16x16x32_bf16 v[212:215], a[40:43], v[148:151], v[212:215]\n\t" \
"v_mfma_f32_16x16x32_bf16 v[216:219], a[96:99], v[144:147], v[216:219]\n\t" \
"ds_read_b128 v[104:107], v249 offset:5120\n\t" \
"v_mfma_f32_16x16x32_bf16 v[220:223], a[96:99], v[148:151], v[220:223]\n\t" \
"v_mfma_f32_16x16x32_bf16 v[224:227], a[152:155], v[144:147], v[224:227]\n\t" \
"ds_read_b128 v[108:111], v249 offset:19456\n\t" \
"v_mfma_f32_16x16x32_bf16 v[228:231], a[152:155], v[148:151], v[228:231]\n\t" \
"buffer_load_dwordx4 %[vg], %[rh], s95 offen offset:2048 lds\n\t" \
"v_mfma_f32_16x16x32_bf16 v[208:211], a[44:47], v[152:155], v[208:211]\n\t" \
"ds_read_b128 v[112:115], v249 offset:6144\n\t" \
"v_mfma_f32_16x16x32_bf16 v[212:215], a[44:47], v[156:159], v[212:215]\n\t" \
"v_mfma_f32_16x16x32_bf16 v[216:219], a[100:103], v[152:155], v[216:219]\n\t" \
"ds_read_b128 v[116:119], v249 offset:20480\n\t" \
"v_mfma_f32_16x16x32_bf16 v[220:223], a[100:103], v[156:159], v[220:223]\n\t" \
"v_mfma_f32_16x16x32_bf16 v[224:227], a[156:159], v[152:155], v[224:227]\n\t" \
"v_mfma_f32_16x16x32_bf16 v[228:231], a[156:159], v[156:159], v[228:231]\n\t" \
"v_mfma_f32_16x16x32_bf16 v[208:211], a[48:51], v[160:163], v[208:211]\n\t" \
"v_mfma_f32_16x16x32_bf16 v[212:215], a[48:51], v[164:167], v[212:215]\n\t" \
"buffer_load_dwordx4 %[vg], %[rh], s95 offen offset:3072 lds\n\t" \
"v_mfma_f32_16x16x32_bf16 v[216:219], a[104:107], v[160:163], v[216:219]\n\t" \
"v_mfma_f32_16x16x32_bf16 v[220:223], a[104:107], v[164:167], v[220:223]\n\t" \
"v_mfma_f32_16x16x32_bf16 v[224:227], a[160:163], v[160:163], v[224:227]\n\t" \
"v_mfma_f32_16x16x32_bf16 v[228:231], a[160:163], v[164:167], v[228:231]\n\t" \
"v_mfma_f32_16x16x32_bf16 v[208:211], a[52:55], v[168:171], v[208:211]\n\t" \
"v_mfma_f32_16x16x32_bf16 v[212:215], a[52:55], v[172:175], v[212:215]\n\t" \
"v_mfma_f32_16x16x32_bf16 v[216:219], a[108:111], v[168:171], v[216:219]\n\t" \
"v_mfma_f32_16x16x32_bf16 v[220:223], a[108:111], v[172:175], v[220:223]\n\t" \
"s_add_u32 s90, s87, %[lg]\n\t" \
"s_add_u32 s90, s90, 4096\n\t" \
"s_mov_b32 m0, s90\n\t" \
"s_add_u32 s95, s89, 4096\n\t" \
"buffer_load_dwordx4 %[vg], %[rh], s95 offen offset:0 lds\n\t" \
"v_mfma_f32_16x16x32_bf16 v[224:227], a[164:167], v[168:171], v[224:227]\n\t" \
"v_mfma_f32_16x16x32_bf16 v[228:231], a[164:167], v[172:175], v[228:231]\n\t" \
"s_add_u32 s92, s92, 28672\n\t" \
"s_cmp_eq_u32 s92, 114688\n\t" \
"s_cselect_b32 s92, 0, s92\n\t" \
"v_mov_b32 v248, v249\n\t" \
"s_add_u32 s91, s91, 917504\n\t" \
"s_waitcnt lgkmcnt(0)\n\t" \
"v_mfma_f32_16x16x32_bf16 v[176:179], a[0:3], v[64:67], 0\n\t" \
"v_mfma_f32_16x16x32_bf16 v[180:183], a[0:3], v[68:71], 0\n\t" \
"ds_read_b128 v[120:123], v248 offset:7168\n\t" \
"v_mfma_f32_16x16x32_bf16 v[184:187], a[56:59], v[64:67], 0\n\t" \
"v_cvt_pk_bf16_f32 v240, v208, v209\n\t" \
"v_mfma_f32_16x16x32_bf16 v[188:191], a[56:59], v[68:71], 0\n\t" \
"v_cvt_pk_bf16_f32 v241, v210, v211\n\t" \
"v_mfma_f32_16x16x32_bf16 v[192:195], a[112:115], v[64:67], 0\n\t" \
"buffer_store_dwordx2 v[240:241], %[vs0], %[ro], s91 offen offset:0 sc1\n\t" \
"v_mfma_f32_16x16x32_bf16 v[196:199], a[112:115], v[68:71], 0\n\t" \
"v_cvt_pk_bf16_f32 v244, v212, v213\n\t" \
"v_mfma_f32_16x16x32_bf16 v[176:179], a[4:7], v[72:75], v[176:179]\n\t" \
"v_cvt_pk_bf16_f32 v245, v214, v215\n\t" \
"v_mfma_f32_16x16x32_bf16 v[180:183], a[4:7], v[76:79], v[180:183]\n\t" \
"buffer_store_dwordx2 v[244:245], %[vs1], %[ro], s91 offen offset:0 sc1\n\t" \
"s_add_u32 s90, s87, %[lg]\n\t" \
"s_add_u32 s90, s90, 4096\n\t" \
"s_mov_b32 m0, s90\n\t" \
"s_add_u32 s95, s89, 4096\n\t" \
"buffer_load_dwordx4 %[vg], %[rh], s95 offen offset:1024 lds\n\t" \
"v_mfma_f32_16x16x32_bf16 v[184:187], a[60:63], v[72:75], v[184:187]\n\t" \
"v_cvt_pk_bf16_f32 v240, v216, v217\n\t" \
"v_mfma_f32_16x16x32_bf16 v[188:191], a[60:63], v[76:79], v[188:191]\n\t" \
"v_cvt_pk_bf16_f32 v241, v218, v219\n\t" \
"v_mfma_f32_16x16x32_bf16 v[192:195], a[116:119], v[72:75], v[192:195]\n\t" \
"buffer_store_dwordx2 v[240:241], %[vs0], %[ro], s91 offen offset:512 sc1\n\t" \
"v_mfma_f32_16x16x32_bf16 v[196:199], a[116:119], v[76:79], v[196:199]\n\t" \
"v_cvt_pk_bf16_f32 v244, v220, v221\n\t" \
"v_mfma_f32_16x16x32_bf16 v[176:179], a[8:11], v[80:83], v[176:179]\n\t" \
"v_cvt_pk_bf16_f32 v245, v222, v223\n\t" \
"v_mfma_f32_16x16x32_bf16 v[180:183], a[8:11], v[84:87], v[180:183]\n\t" \
"buffer_store_dwordx2 v[244:245], %[vs1], %[ro], s91 offen offset:512 sc1\n\t" \
"v_mfma_f32_16x16x32_bf16 v[184:187], a[64:67], v[80:83], v[184:187]\n\t" \
"v_cvt_pk_bf16_f32 v240, v224, v225\n\t" \
"v_mfma_f32_16x16x32_bf16 v[188:191], a[64:67], v[84:87], v[188:191]\n\t" \
"v_cvt_pk_bf16_f32 v241, v226, v227\n\t" \
"buffer_load_dwordx4 %[vg], %[rh], s95 offen offset:2048 lds\n\t" \
"v_mfma_f32_16x16x32_bf16 v[192:195], a[120:123], v[80:83], v[192:195]\n\t" \
"buffer_store_dwordx2 v[240:241], %[vs0], %[ro], s91 offen offset:1024 sc1\n\t" \
"v_mfma_f32_16x16x32_bf16 v[196:199], a[120:123], v[84:87], v[196:199]\n\t" \
"v_cvt_pk_bf16_f32 v244, v228, v229\n\t" \
"v_mfma_f32_16x16x32_bf16 v[176:179], a[12:15], v[88:91], v[176:179]\n\t" \
"v_cvt_pk_bf16_f32 v245, v230, v231\n\t" \
"v_mfma_f32_16x16x32_bf16 v[180:183], a[12:15], v[92:95], v[180:183]\n\t" \
"buffer_store_dwordx2 v[244:245], %[vs1], %[ro], s91 offen offset:1024 sc1\n\t" \
"v_mfma_f32_16x16x32_bf16 v[184:187], a[68:71], v[88:91], v[184:187]\n\t" \
"v_mfma_f32_16x16x32_bf16 v[188:191], a[68:71], v[92:95], v[188:191]\n\t" \
"ds_read_b128 v[124:127], v248 offset:21504\n\t" \
"v_mfma_f32_16x16x32_bf16 v[192:195], a[124:127], v[88:91], v[192:195]\n\t" \
"v_mfma_f32_16x16x32_bf16 v[196:199], a[124:127], v[92:95], v[196:199]\n\t" \
"ds_read_b128 v[128:131], v248 offset:8192\n\t" \
"buffer_load_dwordx4 %[vg], %[rh], s95 offen offset:3072 lds\n\t" \
"v_mfma_f32_16x16x32_bf16 v[176:179], a[16:19], v[96:99], v[176:179]\n\t" \
"v_mfma_f32_16x16x32_bf16 v[180:183], a[16:19], v[100:103], v[180:183]\n\t" \
"ds_read_b128 v[132:135], v248 offset:22528\n\t" \
"v_mfma_f32_16x16x32_bf16 v[184:187], a[72:75], v[96:99], v[184:187]\n\t" \
"v_mfma_f32_16x16x32_bf16 v[188:191], a[72:75], v[100:103], v[188:191]\n\t" \
"ds_read_b128 v[136:139], v248 offset:9216\n\t" \
"v_mfma_f32_16x16x32_bf16 v[192:195], a[128:131], v[96:99], v[192:195]\n\t" \
"v_mfma_f32_16x16x32_bf16 v[196:199], a[128:131], v[100:103], v[196:199]\n\t" \
"ds_read_b128 v[140:143], v248 offset:23552\n\t" \
"v_mfma_f32_16x16x32_bf16 v[176:179], a[20:23], v[104:107], v[176:179]\n\t" \
"v_mfma_f32_16x16x32_bf16 v[180:183], a[20:23], v[108:111], v[180:183]\n\t" \
"ds_read_b128 v[144:147], v248 offset:10240\n\t" \
"s_add_u32 s90, s87, %[lg]\n\t" \
"s_add_u32 s90, s90, 8192\n\t" \
"s_mov_b32 m0, s90\n\t" \
"s_add_u32 s95, s89, 8192\n\t" \
"buffer_load_dwordx4 %[vg], %[rh], s95 offen offset:0 lds\n\t" \
"v_mfma_f32_16x16x32_bf16 v[184:187], a[76:79], v[104:107], v[184:187]\n\t" \
"v_mfma_f32_16x16x32_bf16 v[188:191], a[76:79], v[108:111], v[188:191]\n\t" \
"ds_read_b128 v[148:151], v248 offset:24576\n\t" \
"v_mfma_f32_16x16x32_bf16 v[192:195], a[132:135], v[104:107], v[192:195]\n\t" \
"v_mfma_f32_16x16x32_bf16 v[196:199], a[132:135], v[108:111], v[196:199]\n\t" \
"ds_read_b128 v[152:155], v248 offset:11264\n\t" \
"v_mfma_f32_16x16x32_bf16 v[176:179], a[24:27], v[112:115], v[176:179]\n\t" \
"v_mfma_f32_16x16x32_bf16 v[180:183], a[24:27], v[116:119], v[180:183]\n\t" \
"ds_read_b128 v[156:159], v248 offset:25600\n\t" \
"v_mfma_f32_16x16x32_bf16 v[184:187], a[80:83], v[112:115], v[184:187]\n\t" \
"v_mfma_f32_16x16x32_bf16 v[188:191], a[80:83], v[116:119], v[188:191]\n\t" \
"ds_read_b128 v[160:163], v248 offset:12288\n\t" \
"buffer_load_dwordx4 %[vg], %[rh], s95 offen offset:1024 lds\n\t" \
"v_mfma_f32_16x16x32_bf16 v[192:195], a[136:139], v[112:115], v[192:195]\n\t" \
"v_mfma_f32_16x16x32_bf16 v[196:199], a[136:139], v[116:119], v[196:199]\n\t" \
"ds_read_b128 v[164:167], v248 offset:26624\n\t" \
"ds_read_b128 v[168:171], v248 offset:13312\n\t" \
"ds_read_b128 v[172:175], v248 offset:27648\n\t" \
"s_add_u32 s89, s89, 57344\n\t" \
"s_min_u32 s89, s89, 1806336\n\t" \
"s_mov_b32 s87, s92\n\t" \
"s_add_u32 s95, s92, 28672\n\t" \
"s_cmp_eq_u32 s95, 114688\n\t" \
"s_cselect_b32 s95, 0, s95\n\t" \
"v_add_u32 v249, s95, %[lr]\n\t" \
"s_waitcnt vmcnt(16) lgkmcnt(0)\n\t" \
"s_barrier\n\t" \
"v_mfma_f32_16x16x32_bf16 v[176:179], a[28:31], v[120:123], v[176:179]\n\t" \
"ds_read_b128 v[64:67], v249 offset:0\n\t" \
"v_mfma_f32_16x16x32_bf16 v[180:183], a[28:31], v[124:127], v[180:183]\n\t" \
"v_mfma_f32_16x16x32_bf16 v[184:187], a[84:87], v[120:123], v[184:187]\n\t" \
"ds_read_b128 v[68:71], v249 offset:14336\n\t" \
"v_mfma_f32_16x16x32_bf16 v[188:191], a[84:87], v[124:127], v[188:191]\n\t" \
"v_mfma_f32_16x16x32_bf16 v[192:195], a[140:143], v[120:123], v[192:195]\n\t" \
"ds_read_b128 v[72:75], v249 offset:1024\n\t" \
"v_mfma_f32_16x16x32_bf16 v[196:199], a[140:143], v[124:127], v[196:199]\n\t" \
"v_mfma_f32_16x16x32_bf16 v[176:179], a[32:35], v[128:131], v[176:179]\n\t" \
"ds_read_b128 v[76:79], v249 offset:15360\n\t" \
"v_mfma_f32_16x16x32_bf16 v[180:183], a[32:35], v[132:135], v[180:183]\n\t" \
"s_add_u32 s90, s87, %[lg]\n\t" \
"s_add_u32 s90, s90, 0\n\t" \
"s_mov_b32 m0, s90\n\t" \
"s_add_u32 s95, s89, 0\n\t" \
"buffer_load_dwordx4 %[vg], %[rh], s95 offen offset:0 lds\n\t" \
"v_mfma_f32_16x16x32_bf16 v[184:187], a[88:91], v[128:131], v[184:187]\n\t" \
"ds_read_b128 v[80:83], v249 offset:2048\n\t" \
"v_mfma_f32_16x16x32_bf16 v[188:191], a[88:91], v[132:135], v[188:191]\n\t" \
"v_mfma_f32_16x16x32_bf16 v[192:195], a[144:147], v[128:131], v[192:195]\n\t" \
"ds_read_b128 v[84:87], v249 offset:16384\n\t" \
"v_mfma_f32_16x16x32_bf16 v[196:199], a[144:147], v[132:135], v[196:199]\n\t" \
"v_mfma_f32_16x16x32_bf16 v[176:179], a[36:39], v[136:139], v[176:179]\n\t" \
"ds_read_b128 v[88:91], v249 offset:3072\n\t" \
"v_mfma_f32_16x16x32_bf16 v[180:183], a[36:39], v[140:143], v[180:183]\n\t" \
"v_mfma_f32_16x16x32_bf16 v[184:187], a[92:95], v[136:139], v[184:187]\n\t" \
"ds_read_b128 v[92:95], v249 offset:17408\n\t" \
"v_mfma_f32_16x16x32_bf16 v[188:191], a[92:95], v[140:143], v[188:191]\n\t" \
"buffer_load_dwordx4 %[vg], %[rh], s95 offen offset:1024 lds\n\t" \
"v_mfma_f32_16x16x32_bf16 v[192:195], a[148:151], v[136:139], v[192:195]\n\t" \
"ds_read_b128 v[96:99], v249 offset:4096\n\t" \
"v_mfma_f32_16x16x32_bf16 v[196:199], a[148:151], v[140:143], v[196:199]\n\t" \
"v_mfma_f32_16x16x32_bf16 v[176:179], a[40:43], v[144:147], v[176:179]\n\t" \
"ds_read_b128 v[100:103], v249 offset:18432\n\t" \
"v_mfma_f32_16x16x32_bf16 v[180:183], a[40:43], v[148:151], v[180:183]\n\t" \
"v_mfma_f32_16x16x32_bf16 v[184:187], a[96:99], v[144:147], v[184:187]\n\t" \
"ds_read_b128 v[104:107], v249 offset:5120\n\t" \
"v_mfma_f32_16x16x32_bf16 v[188:191], a[96:99], v[148:151], v[188:191]\n\t" \
"v_mfma_f32_16x16x32_bf16 v[192:195], a[152:155], v[144:147], v[192:195]\n\t" \
"ds_read_b128 v[108:111], v249 offset:19456\n\t" \
"v_mfma_f32_16x16x32_bf16 v[196:199], a[152:155], v[148:151], v[196:199]\n\t" \
"buffer_load_dwordx4 %[vg], %[rh], s95 offen offset:2048 lds\n\t" \
"v_mfma_f32_16x16x32_bf16 v[176:179], a[44:47], v[152:155], v[176:179]\n\t" \
"ds_read_b128 v[112:115], v249 offset:6144\n\t" \
"v_mfma_f32_16x16x32_bf16 v[180:183], a[44:47], v[156:159], v[180:183]\n\t" \
"v_mfma_f32_16x16x32_bf16 v[184:187], a[100:103], v[152:155], v[184:187]\n\t" \
"ds_read_b128 v[116:119], v249 offset:20480\n\t" \
"v_mfma_f32_16x16x32_bf16 v[188:191], a[100:103], v[156:159], v[188:191]\n\t" \
"v_mfma_f32_16x16x32_bf16 v[192:195], a[156:159], v[152:155], v[192:195]\n\t" \
"v_mfma_f32_16x16x32_bf16 v[196:199], a[156:159], v[156:159], v[196:199]\n\t" \
"v_mfma_f32_16x16x32_bf16 v[176:179], a[48:51], v[160:163], v[176:179]\n\t" \
"v_mfma_f32_16x16x32_bf16 v[180:183], a[48:51], v[164:167], v[180:183]\n\t" \
"buffer_load_dwordx4 %[vg], %[rh], s95 offen offset:3072 lds\n\t" \
"v_mfma_f32_16x16x32_bf16 v[184:187], a[104:107], v[160:163], v[184:187]\n\t" \
"v_mfma_f32_16x16x32_bf16 v[188:191], a[104:107], v[164:167], v[188:191]\n\t" \
"v_mfma_f32_16x16x32_bf16 v[192:195], a[160:163], v[160:163], v[192:195]\n\t" \
"v_mfma_f32_16x16x32_bf16 v[196:199], a[160:163], v[164:167], v[196:199]\n\t" \
"v_mfma_f32_16x16x32_bf16 v[176:179], a[52:55], v[168:171], v[176:179]\n\t" \
"v_mfma_f32_16x16x32_bf16 v[180:183], a[52:55], v[172:175], v[180:183]\n\t" \
"v_mfma_f32_16x16x32_bf16 v[184:187], a[108:111], v[168:171], v[184:187]\n\t" \
"v_mfma_f32_16x16x32_bf16 v[188:191], a[108:111], v[172:175], v[188:191]\n\t" \
"s_add_u32 s90, s87, %[lg]\n\t" \
"s_add_u32 s90, s90, 4096\n\t" \
"s_mov_b32 m0, s90\n\t" \
"s_add_u32 s95, s89, 4096\n\t" \
"buffer_load_dwordx4 %[vg], %[rh], s95 offen offset:0 lds\n\t" \
"v_mfma_f32_16x16x32_bf16 v[192:195], a[164:167], v[168:171], v[192:195]\n\t" \
"v_mfma_f32_16x16x32_bf16 v[196:199], a[164:167], v[172:175], v[196:199]\n\t" \
"s_add_u32 s92, s92, 28672\n\t" \
"s_cmp_eq_u32 s92, 114688\n\t" \
"s_cselect_b32 s92, 0, s92\n\t" \
"v_mov_b32 v248, v249\n\t" \
"s_add_u32 s91, s91, 917504\n\t" \
"s_waitcnt lgkmcnt(0)\n\t" \
"v_mfma_f32_16x16x32_bf16 v[208:211], a[0:3], v[64:67], 0\n\t" \
"v_mfma_f32_16x16x32_bf16 v[212:215], a[0:3], v[68:71], 0\n\t" \
"ds_read_b128 v[120:123], v248 offset:7168\n\t" \
"v_mfma_f32_16x16x32_bf16 v[216:219], a[56:59], v[64:67], 0\n\t" \
"v_cvt_pk_bf16_f32 v240, v176, v177\n\t" \
"v_mfma_f32_16x16x32_bf16 v[220:223], a[56:59], v[68:71], 0\n\t" \
"v_cvt_pk_bf16_f32 v241, v178, v179\n\t" \
"v_mfma_f32_16x16x32_bf16 v[224:227], a[112:115], v[64:67], 0\n\t" \
"buffer_store_dwordx2 v[240:241], %[vs0], %[ro], s91 offen offset:0 sc1\n\t" \
"v_mfma_f32_16x16x32_bf16 v[228:231], a[112:115], v[68:71], 0\n\t" \
"v_cvt_pk_bf16_f32 v244, v180, v181\n\t" \
"v_mfma_f32_16x16x32_bf16 v[208:211], a[4:7], v[72:75], v[208:211]\n\t" \
"v_cvt_pk_bf16_f32 v245, v182, v183\n\t" \
"v_mfma_f32_16x16x32_bf16 v[212:215], a[4:7], v[76:79], v[212:215]\n\t" \
"buffer_store_dwordx2 v[244:245], %[vs1], %[ro], s91 offen offset:0 sc1\n\t" \
"s_add_u32 s90, s87, %[lg]\n\t" \
"s_add_u32 s90, s90, 4096\n\t" \
"s_mov_b32 m0, s90\n\t" \
"s_add_u32 s95, s89, 4096\n\t" \
"buffer_load_dwordx4 %[vg], %[rh], s95 offen offset:1024 lds\n\t" \
"v_mfma_f32_16x16x32_bf16 v[216:219], a[60:63], v[72:75], v[216:219]\n\t" \
"v_cvt_pk_bf16_f32 v240, v184, v185\n\t" \
"v_mfma_f32_16x16x32_bf16 v[220:223], a[60:63], v[76:79], v[220:223]\n\t" \
"v_cvt_pk_bf16_f32 v241, v186, v187\n\t" \
"v_mfma_f32_16x16x32_bf16 v[224:227], a[116:119], v[72:75], v[224:227]\n\t" \
"buffer_store_dwordx2 v[240:241], %[vs0], %[ro], s91 offen offset:512 sc1\n\t" \
"v_mfma_f32_16x16x32_bf16 v[228:231], a[116:119], v[76:79], v[228:231]\n\t" \
"v_cvt_pk_bf16_f32 v244, v188, v189\n\t" \
"v_mfma_f32_16x16x32_bf16 v[208:211], a[8:11], v[80:83], v[208:211]\n\t" \
"v_cvt_pk_bf16_f32 v245, v190, v191\n\t" \
"v_mfma_f32_16x16x32_bf16 v[212:215], a[8:11], v[84:87], v[212:215]\n\t" \
"buffer_store_dwordx2 v[244:245], %[vs1], %[ro], s91 offen offset:512 sc1\n\t" \
"v_mfma_f32_16x16x32_bf16 v[216:219], a[64:67], v[80:83], v[216:219]\n\t" \
"v_cvt_pk_bf16_f32 v240, v192, v193\n\t" \
"v_mfma_f32_16x16x32_bf16 v[220:223], a[64:67], v[84:87], v[220:223]\n\t" \
"v_cvt_pk_bf16_f32 v241, v194, v195\n\t" \
"buffer_load_dwordx4 %[vg], %[rh], s95 offen offset:2048 lds\n\t" \
"v_mfma_f32_16x16x32_bf16 v[224:227], a[120:123], v[80:83], v[224:227]\n\t" \
"buffer_store_dwordx2 v[240:241], %[vs0], %[ro], s91 offen offset:1024 sc1\n\t" \
"v_mfma_f32_16x16x32_bf16 v[228:231], a[120:123], v[84:87], v[228:231]\n\t" \
"v_cvt_pk_bf16_f32 v244, v196, v197\n\t" \
"v_mfma_f32_16x16x32_bf16 v[208:211], a[12:15], v[88:91], v[208:211]\n\t" \
"v_cvt_pk_bf16_f32 v245, v198, v199\n\t" \
"v_mfma_f32_16x16x32_bf16 v[212:215], a[12:15], v[92:95], v[212:215]\n\t" \
"buffer_store_dwordx2 v[244:245], %[vs1], %[ro], s91 offen offset:1024 sc1\n\t" \
"v_mfma_f32_16x16x32_bf16 v[216:219], a[68:71], v[88:91], v[216:219]\n\t" \
"v_mfma_f32_16x16x32_bf16 v[220:223], a[68:71], v[92:95], v[220:223]\n\t" \
"ds_read_b128 v[124:127], v248 offset:21504\n\t" \
"v_mfma_f32_16x16x32_bf16 v[224:227], a[124:127], v[88:91], v[224:227]\n\t" \
"v_mfma_f32_16x16x32_bf16 v[228:231], a[124:127], v[92:95], v[228:231]\n\t" \
"ds_read_b128 v[128:131], v248 offset:8192\n\t" \
"buffer_load_dwordx4 %[vg], %[rh], s95 offen offset:3072 lds\n\t" \
"v_mfma_f32_16x16x32_bf16 v[208:211], a[16:19], v[96:99], v[208:211]\n\t" \
"v_mfma_f32_16x16x32_bf16 v[212:215], a[16:19], v[100:103], v[212:215]\n\t" \
"ds_read_b128 v[132:135], v248 offset:22528\n\t" \
"v_mfma_f32_16x16x32_bf16 v[216:219], a[72:75], v[96:99], v[216:219]\n\t" \
"v_mfma_f32_16x16x32_bf16 v[220:223], a[72:75], v[100:103], v[220:223]\n\t" \
"ds_read_b128 v[136:139], v248 offset:9216\n\t" \
"v_mfma_f32_16x16x32_bf16 v[224:227], a[128:131], v[96:99], v[224:227]\n\t" \
"v_mfma_f32_16x16x32_bf16 v[228:231], a[128:131], v[100:103], v[228:231]\n\t" \
"ds_read_b128 v[140:143], v248 offset:23552\n\t" \
"v_mfma_f32_16x16x32_bf16 v[208:211], a[20:23], v[104:107], v[208:211]\n\t" \
"v_mfma_f32_16x16x32_bf16 v[212:215], a[20:23], v[108:111], v[212:215]\n\t" \
"ds_read_b128 v[144:147], v248 offset:10240\n\t" \
"s_add_u32 s90, s87, %[lg]\n\t" \
"s_add_u32 s90, s90, 8192\n\t" \
"s_mov_b32 m0, s90\n\t" \
"s_add_u32 s95, s89, 8192\n\t" \
"buffer_load_dwordx4 %[vg], %[rh], s95 offen offset:0 lds\n\t" \
"v_mfma_f32_16x16x32_bf16 v[216:219], a[76:79], v[104:107], v[216:219]\n\t" \
"v_mfma_f32_16x16x32_bf16 v[220:223], a[76:79], v[108:111], v[220:223]\n\t" \
"ds_read_b128 v[148:151], v248 offset:24576\n\t" \
"v_mfma_f32_16x16x32_bf16 v[224:227], a[132:135], v[104:107], v[224:227]\n\t" \
"v_mfma_f32_16x16x32_bf16 v[228:231], a[132:135], v[108:111], v[228:231]\n\t" \
"ds_read_b128 v[152:155], v248 offset:11264\n\t" \
"v_mfma_f32_16x16x32_bf16 v[208:211], a[24:27], v[112:115], v[208:211]\n\t" \
"v_mfma_f32_16x16x32_bf16 v[212:215], a[24:27], v[116:119], v[212:215]\n\t" \
"ds_read_b128 v[156:159], v248 offset:25600\n\t" \
"v_mfma_f32_16x16x32_bf16 v[216:219], a[80:83], v[112:115], v[216:219]\n\t" \
"v_mfma_f32_16x16x32_bf16 v[220:223], a[80:83], v[116:119], v[220:223]\n\t" \
"ds_read_b128 v[160:163], v248 offset:12288\n\t" \
"buffer_load_dwordx4 %[vg], %[rh], s95 offen offset:1024 lds\n\t" \
"v_mfma_f32_16x16x32_bf16 v[224:227], a[136:139], v[112:115], v[224:227]\n\t" \
"v_mfma_f32_16x16x32_bf16 v[228:231], a[136:139], v[116:119], v[228:231]\n\t" \
"ds_read_b128 v[164:167], v248 offset:26624\n\t" \
"ds_read_b128 v[168:171], v248 offset:13312\n\t" \
"ds_read_b128 v[172:175], v248 offset:27648\n\t" \
"s_add_u32 s89, s89, 57344\n\t" \
"s_min_u32 s89, s89, 1806336\n\t" \
"s_mov_b32 s87, s92\n\t" \
"s_add_u32 s95, s92, 28672\n\t" \
"s_cmp_eq_u32 s95, 114688\n\t" \
"s_cselect_b32 s95, 0, s95\n\t" \
"v_add_u32 v249, s95, %[lr]\n\t" \
"s_waitcnt vmcnt(16) lgkmcnt(0)\n\t" \
"s_barrier\n\t" \
"s_add_u32 s94, s94, 128\n\t" \
"v_mfma_f32_16x16x32_bf16 v[208:211], a[28:31], v[120:123], v[208:211]\n\t" \
"ds_read_b128 v[64:67], v249 offset:0\n\t" \
"v_mfma_f32_16x16x32_bf16 v[212:215], a[28:31], v[124:127], v[212:215]\n\t" \
"v_mfma_f32_16x16x32_bf16 v[216:219], a[84:87], v[120:123], v[216:219]\n\t" \
"ds_read_b128 v[68:71], v249 offset:14336\n\t" \
"v_mfma_f32_16x16x32_bf16 v[220:223], a[84:87], v[124:127], v[220:223]\n\t" \
"v_mfma_f32_16x16x32_bf16 v[224:227], a[140:143], v[120:123], v[224:227]\n\t" \
"ds_read_b128 v[72:75], v249 offset:1024\n\t" \
"v_mfma_f32_16x16x32_bf16 v[228:231], a[140:143], v[124:127], v[228:231]\n\t" \
"v_mfma_f32_16x16x32_bf16 v[208:211], a[32:35], v[128:131], v[208:211]\n\t" \
"ds_read_b128 v[76:79], v249 offset:15360\n\t" \
"v_mfma_f32_16x16x32_bf16 v[212:215], a[32:35], v[132:135], v[212:215]\n\t" \
"s_add_u32 s90, s87, %[lg]\n\t" \
"s_add_u32 s90, s90, 0\n\t" \
"s_mov_b32 m0, s90\n\t" \
"s_add_u32 s95, s89, 0\n\t" \
"buffer_load_dwordx4 %[vg], %[rh], s95 offen offset:0 lds\n\t" \
"v_mfma_f32_16x16x32_bf16 v[216:219], a[88:91], v[128:131], v[216:219]\n\t" \
"ds_read_b128 v[80:83], v249 offset:2048\n\t" \
"v_mfma_f32_16x16x32_bf16 v[220:223], a[88:91], v[132:135], v[220:223]\n\t" \
"v_mfma_f32_16x16x32_bf16 v[224:227], a[144:147], v[128:131], v[224:227]\n\t" \
"ds_read_b128 v[84:87], v249 offset:16384\n\t" \
"v_mfma_f32_16x16x32_bf16 v[228:231], a[144:147], v[132:135], v[228:231]\n\t" \
"v_mfma_f32_16x16x32_bf16 v[208:211], a[36:39], v[136:139], v[208:211]\n\t" \
"ds_read_b128 v[88:91], v249 offset:3072\n\t" \
"v_mfma_f32_16x16x32_bf16 v[212:215], a[36:39], v[140:143], v[212:215]\n\t" \
"v_mfma_f32_16x16x32_bf16 v[216:219], a[92:95], v[136:139], v[216:219]\n\t" \
"ds_read_b128 v[92:95], v249 offset:17408\n\t" \
"v_mfma_f32_16x16x32_bf16 v[220:223], a[92:95], v[140:143], v[220:223]\n\t" \
"buffer_load_dwordx4 %[vg], %[rh], s95 offen offset:1024 lds\n\t" \
"v_mfma_f32_16x16x32_bf16 v[224:227], a[148:151], v[136:139], v[224:227]\n\t" \
"ds_read_b128 v[96:99], v249 offset:4096\n\t" \
"v_mfma_f32_16x16x32_bf16 v[228:231], a[148:151], v[140:143], v[228:231]\n\t" \
"v_mfma_f32_16x16x32_bf16 v[208:211], a[40:43], v[144:147], v[208:211]\n\t" \
"ds_read_b128 v[100:103], v249 offset:18432\n\t" \
"v_mfma_f32_16x16x32_bf16 v[212:215], a[40:43], v[148:151], v[212:215]\n\t" \
"v_mfma_f32_16x16x32_bf16 v[216:219], a[96:99], v[144:147], v[216:219]\n\t" \
"ds_read_b128 v[104:107], v249 offset:5120\n\t" \
"v_mfma_f32_16x16x32_bf16 v[220:223], a[96:99], v[148:151], v[220:223]\n\t" \
"v_mfma_f32_16x16x32_bf16 v[224:227], a[152:155], v[144:147], v[224:227]\n\t" \
"ds_read_b128 v[108:111], v249 offset:19456\n\t" \
"v_mfma_f32_16x16x32_bf16 v[228:231], a[152:155], v[148:151], v[228:231]\n\t" \
"buffer_load_dwordx4 %[vg], %[rh], s95 offen offset:2048 lds\n\t" \
"v_mfma_f32_16x16x32_bf16 v[208:211], a[44:47], v[152:155], v[208:211]\n\t" \
"ds_read_b128 v[112:115], v249 offset:6144\n\t" \
"v_mfma_f32_16x16x32_bf16 v[212:215], a[44:47], v[156:159], v[212:215]\n\t" \
"v_mfma_f32_16x16x32_bf16 v[216:219], a[100:103], v[152:155], v[216:219]\n\t" \
"ds_read_b128 v[116:119], v249 offset:20480\n\t" \
"v_mfma_f32_16x16x32_bf16 v[220:223], a[100:103], v[156:159], v[220:223]\n\t" \
"v_mfma_f32_16x16x32_bf16 v[224:227], a[156:159], v[152:155], v[224:227]\n\t" \
"v_mfma_f32_16x16x32_bf16 v[228:231], a[156:159], v[156:159], v[228:231]\n\t" \
"v_mfma_f32_16x16x32_bf16 v[208:211], a[48:51], v[160:163], v[208:211]\n\t" \
"v_mfma_f32_16x16x32_bf16 v[212:215], a[48:51], v[164:167], v[212:215]\n\t" \
"buffer_load_dwordx4 %[vg], %[rh], s95 offen offset:3072 lds\n\t" \
"v_mfma_f32_16x16x32_bf16 v[216:219], a[104:107], v[160:163], v[216:219]\n\t" \
"v_mfma_f32_16x16x32_bf16 v[220:223], a[104:107], v[164:167], v[220:223]\n\t" \
"v_mfma_f32_16x16x32_bf16 v[224:227], a[160:163], v[160:163], v[224:227]\n\t" \
"v_mfma_f32_16x16x32_bf16 v[228:231], a[160:163], v[164:167], v[228:231]\n\t" \
"v_mfma_f32_16x16x32_bf16 v[208:211], a[52:55], v[168:171], v[208:211]\n\t" \
"v_mfma_f32_16x16x32_bf16 v[212:215], a[52:55], v[172:175], v[212:215]\n\t" \
"v_mfma_f32_16x16x32_bf16 v[216:219], a[108:111], v[168:171], v[216:219]\n\t" \
"v_mfma_f32_16x16x32_bf16 v[220:223], a[108:111], v[172:175], v[220:223]\n\t" \
"s_add_u32 s90, s87, %[lg]\n\t" \
"s_add_u32 s90, s90, 4096\n\t" \
"s_mov_b32 m0, s90\n\t" \
"s_add_u32 s95, s89, 4096\n\t" \
"buffer_load_dwordx4 %[vg], %[rh], s95 offen offset:0 lds\n\t" \
"v_mfma_f32_16x16x32_bf16 v[224:227], a[164:167], v[168:171], v[224:227]\n\t" \
"v_mfma_f32_16x16x32_bf16 v[228:231], a[164:167], v[172:175], v[228:231]\n\t" \
"s_add_u32 s92, s92, 28672\n\t" \
"s_cmp_eq_u32 s92, 114688\n\t" \
"s_cselect_b32 s92, 0, s92\n\t" \
"v_mov_b32 v248, v249\n\t" \
"s_add_u32 s91, s91, 917504\n\t" \
"s_mov_b32 s88, 4\n\t" \
"s_nop 1\n\t" \
"Lp3%=:\n\t" \
"s_waitcnt lgkmcnt(0)\n\t" \
"v_mfma_f32_16x16x32_bf16 v[176:179], a[0:3], v[64:67], 0\n\t" \
"v_mfma_f32_16x16x32_bf16 v[180:183], a[0:3], v[68:71], 0\n\t" \
"ds_read_b128 v[120:123], v248 offset:7168\n\t" \
"v_mfma_f32_16x16x32_bf16 v[184:187], a[56:59], v[64:67], 0\n\t" \
"v_cvt_pk_bf16_f32 v240, v208, v209\n\t" \
"v_mfma_f32_16x16x32_bf16 v[188:191], a[56:59], v[68:71], 0\n\t" \
"v_cvt_pk_bf16_f32 v241, v210, v211\n\t" \
"v_mfma_f32_16x16x32_bf16 v[192:195], a[112:115], v[64:67], 0\n\t" \
"buffer_store_dwordx2 v[240:241], %[vs0], %[ro], s91 offen offset:0 sc1\n\t" \
"v_mfma_f32_16x16x32_bf16 v[196:199], a[112:115], v[68:71], 0\n\t" \
"v_cvt_pk_bf16_f32 v244, v212, v213\n\t" \
"v_mfma_f32_16x16x32_bf16 v[176:179], a[4:7], v[72:75], v[176:179]\n\t" \
"v_cvt_pk_bf16_f32 v245, v214, v215\n\t" \
"v_mfma_f32_16x16x32_bf16 v[180:183], a[4:7], v[76:79], v[180:183]\n\t" \
"buffer_store_dwordx2 v[244:245], %[vs1], %[ro], s91 offen offset:0 sc1\n\t" \
"s_add_u32 s90, s87, %[lg]\n\t" \
"s_add_u32 s90, s90, 4096\n\t" \
"s_mov_b32 m0, s90\n\t" \
"s_add_u32 s95, s89, 4096\n\t" \
"buffer_load_dwordx4 %[vg], %[rh], s95 offen offset:1024 lds\n\t" \
"v_mfma_f32_16x16x32_bf16 v[184:187], a[60:63], v[72:75], v[184:187]\n\t" \
"v_cvt_pk_bf16_f32 v240, v216, v217\n\t" \
"v_mfma_f32_16x16x32_bf16 v[188:191], a[60:63], v[76:79], v[188:191]\n\t" \
"v_cvt_pk_bf16_f32 v241, v218, v219\n\t" \
"v_mfma_f32_16x16x32_bf16 v[192:195], a[116:119], v[72:75], v[192:195]\n\t" \
"buffer_store_dwordx2 v[240:241], %[vs0], %[ro], s91 offen offset:512 sc1\n\t" \
"v_mfma_f32_16x16x32_bf16 v[196:199], a[116:119], v[76:79], v[196:199]\n\t" \
"v_cvt_pk_bf16_f32 v244, v220, v221\n\t" \
"v_mfma_f32_16x16x32_bf16 v[176:179], a[8:11], v[80:83], v[176:179]\n\t" \
"v_cvt_pk_bf16_f32 v245, v222, v223\n\t" \
"v_mfma_f32_16x16x32_bf16 v[180:183], a[8:11], v[84:87], v[180:183]\n\t" \
"buffer_store_dwordx2 v[244:245], %[vs1], %[ro], s91 offen offset:512 sc1\n\t" \
"v_mfma_f32_16x16x32_bf16 v[184:187], a[64:67], v[80:83], v[184:187]\n\t" \
"v_cvt_pk_bf16_f32 v240, v224, v225\n\t" \
"v_mfma_f32_16x16x32_bf16 v[188:191], a[64:67], v[84:87], v[188:191]\n\t" \
"v_cvt_pk_bf16_f32 v241, v226, v227\n\t" \
"buffer_load_dwordx4 %[vg], %[rh], s95 offen offset:2048 lds\n\t" \
"v_mfma_f32_16x16x32_bf16 v[192:195], a[120:123], v[80:83], v[192:195]\n\t" \
"buffer_store_dwordx2 v[240:241], %[vs0], %[ro], s91 offen offset:1024 sc1\n\t" \
"v_mfma_f32_16x16x32_bf16 v[196:199], a[120:123], v[84:87], v[196:199]\n\t" \
"v_cvt_pk_bf16_f32 v244, v228, v229\n\t" \
"v_mfma_f32_16x16x32_bf16 v[176:179], a[12:15], v[88:91], v[176:179]\n\t" \
"v_cvt_pk_bf16_f32 v245, v230, v231\n\t" \
"v_mfma_f32_16x16x32_bf16 v[180:183], a[12:15], v[92:95], v[180:183]\n\t" \
"buffer_store_dwordx2 v[244:245], %[vs1], %[ro], s91 offen offset:1024 sc1\n\t" \
"v_mfma_f32_16x16x32_bf16 v[184:187], a[68:71], v[88:91], v[184:187]\n\t" \
"v_mfma_f32_16x16x32_bf16 v[188:191], a[68:71], v[92:95], v[188:191]\n\t" \
"ds_read_b128 v[124:127], v248 offset:21504\n\t" \
"v_mfma_f32_16x16x32_bf16 v[192:195], a[124:127], v[88:91], v[192:195]\n\t" \
"v_mfma_f32_16x16x32_bf16 v[196:199], a[124:127], v[92:95], v[196:199]\n\t" \
"ds_read_b128 v[128:131], v248 offset:8192\n\t" \
"buffer_load_dwordx4 %[vg], %[rh], s95 offen offset:3072 lds\n\t" \
"v_mfma_f32_16x16x32_bf16 v[176:179], a[16:19], v[96:99], v[176:179]\n\t" \
"v_mfma_f32_16x16x32_bf16 v[180:183], a[16:19], v[100:103], v[180:183]\n\t" \
"ds_read_b128 v[132:135], v248 offset:22528\n\t" \
"v_mfma_f32_16x16x32_bf16 v[184:187], a[72:75], v[96:99], v[184:187]\n\t" \
"v_mfma_f32_16x16x32_bf16 v[188:191], a[72:75], v[100:103], v[188:191]\n\t" \
"ds_read_b128 v[136:139], v248 offset:9216\n\t" \
"v_mfma_f32_16x16x32_bf16 v[192:195], a[128:131], v[96:99], v[192:195]\n\t" \
"v_mfma_f32_16x16x32_bf16 v[196:199], a[128:131], v[100:103], v[196:199]\n\t" \
"ds_read_b128 v[140:143], v248 offset:23552\n\t" \
"v_mfma_f32_16x16x32_bf16 v[176:179], a[20:23], v[104:107], v[176:179]\n\t" \
"v_mfma_f32_16x16x32_bf16 v[180:183], a[20:23], v[108:111], v[180:183]\n\t" \
"ds_read_b128 v[144:147], v248 offset:10240\n\t" \
"s_add_u32 s90, s87, %[lg]\n\t" \
"s_add_u32 s90, s90, 8192\n\t" \
"s_mov_b32 m0, s90\n\t" \
"s_add_u32 s95, s89, 8192\n\t" \
"buffer_load_dwordx4 %[vg], %[rh], s95 offen offset:0 lds\n\t" \
"v_mfma_f32_16x16x32_bf16 v[184:187], a[76:79], v[104:107], v[184:187]\n\t" \
"v_mfma_f32_16x16x32_bf16 v[188:191], a[76:79], v[108:111], v[188:191]\n\t" \
"ds_read_b128 v[148:151], v248 offset:24576\n\t" \
"v_mfma_f32_16x16x32_bf16 v[192:195], a[132:135], v[104:107], v[192:195]\n\t" \
"v_mfma_f32_16x16x32_bf16 v[196:199], a[132:135], v[108:111], v[196:199]\n\t" \
"ds_read_b128 v[152:155], v248 offset:11264\n\t" \
"v_mfma_f32_16x16x32_bf16 v[176:179], a[24:27], v[112:115], v[176:179]\n\t" \
"v_mfma_f32_16x16x32_bf16 v[180:183], a[24:27], v[116:119], v[180:183]\n\t" \
"ds_read_b128 v[156:159], v248 offset:25600\n\t" \
"v_mfma_f32_16x16x32_bf16 v[184:187], a[80:83], v[112:115], v[184:187]\n\t" \
"v_mfma_f32_16x16x32_bf16 v[188:191], a[80:83], v[116:119], v[188:191]\n\t" \
"ds_read_b128 v[160:163], v248 offset:12288\n\t" \
"buffer_load_dwordx4 %[vg], %[rh], s95 offen offset:1024 lds\n\t" \
"v_mfma_f32_16x16x32_bf16 v[192:195], a[136:139], v[112:115], v[192:195]\n\t" \
"v_mfma_f32_16x16x32_bf16 v[196:199], a[136:139], v[116:119], v[196:199]\n\t" \
"ds_read_b128 v[164:167], v248 offset:26624\n\t" \
"ds_read_b128 v[168:171], v248 offset:13312\n\t" \
"ds_read_b128 v[172:175], v248 offset:27648\n\t" \
"s_add_u32 s89, s89, 57344\n\t" \
"s_min_u32 s89, s89, 1806336\n\t" \
"s_mov_b32 s87, s92\n\t" \
"s_add_u32 s95, s92, 28672\n\t" \
"s_cmp_eq_u32 s95, 114688\n\t" \
"s_cselect_b32 s95, 0, s95\n\t" \
"v_add_u32 v249, s95, %[lr]\n\t" \
"s_waitcnt vmcnt(16) lgkmcnt(0)\n\t" \
"s_barrier\n\t" \
"s_add_u32 s94, s94, 128\n\t" \
"v_mfma_f32_16x16x32_bf16 v[176:179], a[28:31], v[120:123], v[176:179]\n\t" \
"ds_read_b128 v[64:67], v249 offset:0\n\t" \
"v_mfma_f32_16x16x32_bf16 v[180:183], a[28:31], v[124:127], v[180:183]\n\t" \
"v_mfma_f32_16x16x32_bf16 v[184:187], a[84:87], v[120:123], v[184:187]\n\t" \
"ds_read_b128 v[68:71], v249 offset:14336\n\t" \
"v_mfma_f32_16x16x32_bf16 v[188:191], a[84:87], v[124:127], v[188:191]\n\t" \
"v_mfma_f32_16x16x32_bf16 v[192:195], a[140:143], v[120:123], v[192:195]\n\t" \
"ds_read_b128 v[72:75], v249 offset:1024\n\t" \
"v_mfma_f32_16x16x32_bf16 v[196:199], a[140:143], v[124:127], v[196:199]\n\t" \
"v_mfma_f32_16x16x32_bf16 v[176:179], a[32:35], v[128:131], v[176:179]\n\t" \
"ds_read_b128 v[76:79], v249 offset:15360\n\t" \
"v_mfma_f32_16x16x32_bf16 v[180:183], a[32:35], v[132:135], v[180:183]\n\t" \
"s_add_u32 s90, s87, %[lg]\n\t" \
"s_add_u32 s90, s90, 0\n\t" \
"s_mov_b32 m0, s90\n\t" \
"s_add_u32 s95, s89, 0\n\t" \
"buffer_load_dwordx4 %[vg], %[rh], s95 offen offset:0 lds\n\t" \
"v_mfma_f32_16x16x32_bf16 v[184:187], a[88:91], v[128:131], v[184:187]\n\t" \
"ds_read_b128 v[80:83], v249 offset:2048\n\t" \
"v_mfma_f32_16x16x32_bf16 v[188:191], a[88:91], v[132:135], v[188:191]\n\t" \
"v_mfma_f32_16x16x32_bf16 v[192:195], a[144:147], v[128:131], v[192:195]\n\t" \
"ds_read_b128 v[84:87], v249 offset:16384\n\t" \
"v_mfma_f32_16x16x32_bf16 v[196:199], a[144:147], v[132:135], v[196:199]\n\t" \
"v_mfma_f32_16x16x32_bf16 v[176:179], a[36:39], v[136:139], v[176:179]\n\t" \
"ds_read_b128 v[88:91], v249 offset:3072\n\t" \
"v_mfma_f32_16x16x32_bf16 v[180:183], a[36:39], v[140:143], v[180:183]\n\t" \
"v_mfma_f32_16x16x32_bf16 v[184:187], a[92:95], v[136:139], v[184:187]\n\t" \
"ds_read_b128 v[92:95], v249 offset:17408\n\t" \
"v_mfma_f32_16x16x32_bf16 v[188:191], a[92:95], v[140:143], v[188:191]\n\t" \
"buffer_load_dwordx4 %[vg], %[rh], s95 offen offset:1024 lds\n\t" \
"v_mfma_f32_16x16x32_bf16 v[192:195], a[148:151], v[136:139], v[192:195]\n\t" \
"ds_read_b128 v[96:99], v249 offset:4096\n\t" \
"v_mfma_f32_16x16x32_bf16 v[196:199], a[148:151], v[140:143], v[196:199]\n\t" \
"v_mfma_f32_16x16x32_bf16 v[176:179], a[40:43], v[144:147], v[176:179]\n\t" \
"ds_read_b128 v[100:103], v249 offset:18432\n\t" \
"v_mfma_f32_16x16x32_bf16 v[180:183], a[40:43], v[148:151], v[180:183]\n\t" \
"v_mfma_f32_16x16x32_bf16 v[184:187], a[96:99], v[144:147], v[184:187]\n\t" \
"ds_read_b128 v[104:107], v249 offset:5120\n\t" \
"v_mfma_f32_16x16x32_bf16 v[188:191], a[96:99], v[148:151], v[188:191]\n\t" \
"v_mfma_f32_16x16x32_bf16 v[192:195], a[152:155], v[144:147], v[192:195]\n\t" \
"ds_read_b128 v[108:111], v249 offset:19456\n\t" \
"v_mfma_f32_16x16x32_bf16 v[196:199], a[152:155], v[148:151], v[196:199]\n\t" \
"buffer_load_dwordx4 %[vg], %[rh], s95 offen offset:2048 lds\n\t" \
"v_mfma_f32_16x16x32_bf16 v[176:179], a[44:47], v[152:155], v[176:179]\n\t" \
"ds_read_b128 v[112:115], v249 offset:6144\n\t" \
"v_mfma_f32_16x16x32_bf16 v[180:183], a[44:47], v[156:159], v[180:183]\n\t" \
"v_mfma_f32_16x16x32_bf16 v[184:187], a[100:103], v[152:155], v[184:187]\n\t" \
"ds_read_b128 v[116:119], v249 offset:20480\n\t" \
"v_mfma_f32_16x16x32_bf16 v[188:191], a[100:103], v[156:159], v[188:191]\n\t" \
"v_mfma_f32_16x16x32_bf16 v[192:195], a[156:159], v[152:155], v[192:195]\n\t" \
"v_mfma_f32_16x16x32_bf16 v[196:199], a[156:159], v[156:159], v[196:199]\n\t" \
"v_mfma_f32_16x16x32_bf16 v[176:179], a[48:51], v[160:163], v[176:179]\n\t" \
"v_mfma_f32_16x16x32_bf16 v[180:183], a[48:51], v[164:167], v[180:183]\n\t" \
"buffer_load_dwordx4 %[vg], %[rh], s95 offen offset:3072 lds\n\t" \
"v_mfma_f32_16x16x32_bf16 v[184:187], a[104:107], v[160:163], v[184:187]\n\t" \
"v_mfma_f32_16x16x32_bf16 v[188:191], a[104:107], v[164:167], v[188:191]\n\t" \
"v_mfma_f32_16x16x32_bf16 v[192:195], a[160:163], v[160:163], v[192:195]\n\t" \
"v_mfma_f32_16x16x32_bf16 v[196:199], a[160:163], v[164:167], v[196:199]\n\t" \
"v_mfma_f32_16x16x32_bf16 v[176:179], a[52:55], v[168:171], v[176:179]\n\t" \
"v_mfma_f32_16x16x32_bf16 v[180:183], a[52:55], v[172:175], v[180:183]\n\t" \
"v_mfma_f32_16x16x32_bf16 v[184:187], a[108:111], v[168:171], v[184:187]\n\t" \
"v_mfma_f32_16x16x32_bf16 v[188:191], a[108:111], v[172:175], v[188:191]\n\t" \
"s_add_u32 s90, s87, %[lg]\n\t" \
"s_add_u32 s90, s90, 4096\n\t" \
"s_mov_b32 m0, s90\n\t" \
"s_add_u32 s95, s89, 4096\n\t" \
"buffer_load_dwordx4 %[vg], %[rh], s95 offen offset:0 lds\n\t" \
"v_mfma_f32_16x16x32_bf16 v[192:195], a[164:167], v[168:171], v[192:195]\n\t" \
"v_mfma_f32_16x16x32_bf16 v[196:199], a[164:167], v[172:175], v[196:199]\n\t" \
"s_add_u32 s92, s92, 28672\n\t" \
"s_cmp_eq_u32 s92, 114688\n\t" \
"s_cselect_b32 s92, 0, s92\n\t" \
"v_mov_b32 v248, v249\n\t" \
"s_add_u32 s91, s91, 917504\n\t" \
"s_waitcnt lgkmcnt(0)\n\t" \
"v_mfma_f32_16x16x32_bf16 v[208:211], a[0:3], v[64:67], 0\n\t" \
"v_mfma_f32_16x16x32_bf16 v[212:215], a[0:3], v[68:71], 0\n\t" \
"ds_read_b128 v[120:123], v248 offset:7168\n\t" \
"v_mfma_f32_16x16x32_bf16 v[216:219], a[56:59], v[64:67], 0\n\t" \
"v_cvt_pk_bf16_f32 v240, v176, v177\n\t" \
"v_mfma_f32_16x16x32_bf16 v[220:223], a[56:59], v[68:71], 0\n\t" \
"v_cvt_pk_bf16_f32 v241, v178, v179\n\t" \
"v_mfma_f32_16x16x32_bf16 v[224:227], a[112:115], v[64:67], 0\n\t" \
"buffer_store_dwordx2 v[240:241], %[vs0], %[ro], s91 offen offset:0 sc1\n\t" \
"v_mfma_f32_16x16x32_bf16 v[228:231], a[112:115], v[68:71], 0\n\t" \
"v_cvt_pk_bf16_f32 v244, v180, v181\n\t" \
"v_mfma_f32_16x16x32_bf16 v[208:211], a[4:7], v[72:75], v[208:211]\n\t" \
"v_cvt_pk_bf16_f32 v245, v182, v183\n\t" \
"v_mfma_f32_16x16x32_bf16 v[212:215], a[4:7], v[76:79], v[212:215]\n\t" \
"buffer_store_dwordx2 v[244:245], %[vs1], %[ro], s91 offen offset:0 sc1\n\t" \
"s_add_u32 s90, s87, %[lg]\n\t" \
"s_add_u32 s90, s90, 4096\n\t" \
"s_mov_b32 m0, s90\n\t" \
"s_add_u32 s95, s89, 4096\n\t" \
"buffer_load_dwordx4 %[vg], %[rh], s95 offen offset:1024 lds\n\t" \
"v_mfma_f32_16x16x32_bf16 v[216:219], a[60:63], v[72:75], v[216:219]\n\t" \
"v_cvt_pk_bf16_f32 v240, v184, v185\n\t" \
"v_mfma_f32_16x16x32_bf16 v[220:223], a[60:63], v[76:79], v[220:223]\n\t" \
"v_cvt_pk_bf16_f32 v241, v186, v187\n\t" \
"v_mfma_f32_16x16x32_bf16 v[224:227], a[116:119], v[72:75], v[224:227]\n\t" \
"buffer_store_dwordx2 v[240:241], %[vs0], %[ro], s91 offen offset:512 sc1\n\t" \
"v_mfma_f32_16x16x32_bf16 v[228:231], a[116:119], v[76:79], v[228:231]\n\t" \
"v_cvt_pk_bf16_f32 v244, v188, v189\n\t" \
"v_mfma_f32_16x16x32_bf16 v[208:211], a[8:11], v[80:83], v[208:211]\n\t" \
"v_cvt_pk_bf16_f32 v245, v190, v191\n\t" \
"v_mfma_f32_16x16x32_bf16 v[212:215], a[8:11], v[84:87], v[212:215]\n\t" \
"buffer_store_dwordx2 v[244:245], %[vs1], %[ro], s91 offen offset:512 sc1\n\t" \
"v_mfma_f32_16x16x32_bf16 v[216:219], a[64:67], v[80:83], v[216:219]\n\t" \
"v_cvt_pk_bf16_f32 v240, v192, v193\n\t" \
"v_mfma_f32_16x16x32_bf16 v[220:223], a[64:67], v[84:87], v[220:223]\n\t" \
"v_cvt_pk_bf16_f32 v241, v194, v195\n\t" \
"buffer_load_dwordx4 %[vg], %[rh], s95 offen offset:2048 lds\n\t" \
"v_mfma_f32_16x16x32_bf16 v[224:227], a[120:123], v[80:83], v[224:227]\n\t" \
"buffer_store_dwordx2 v[240:241], %[vs0], %[ro], s91 offen offset:1024 sc1\n\t" \
"v_mfma_f32_16x16x32_bf16 v[228:231], a[120:123], v[84:87], v[228:231]\n\t" \
"v_cvt_pk_bf16_f32 v244, v196, v197\n\t" \
"v_mfma_f32_16x16x32_bf16 v[208:211], a[12:15], v[88:91], v[208:211]\n\t" \
"v_cvt_pk_bf16_f32 v245, v198, v199\n\t" \
"v_mfma_f32_16x16x32_bf16 v[212:215], a[12:15], v[92:95], v[212:215]\n\t" \
"buffer_store_dwordx2 v[244:245], %[vs1], %[ro], s91 offen offset:1024 sc1\n\t" \
"v_mfma_f32_16x16x32_bf16 v[216:219], a[68:71], v[88:91], v[216:219]\n\t" \
"v_mfma_f32_16x16x32_bf16 v[220:223], a[68:71], v[92:95], v[220:223]\n\t" \
"ds_read_b128 v[124:127], v248 offset:21504\n\t" \
"v_mfma_f32_16x16x32_bf16 v[224:227], a[124:127], v[88:91], v[224:227]\n\t" \
"v_mfma_f32_16x16x32_bf16 v[228:231], a[124:127], v[92:95], v[228:231]\n\t" \
"ds_read_b128 v[128:131], v248 offset:8192\n\t" \
"buffer_load_dwordx4 %[vg], %[rh], s95 offen offset:3072 lds\n\t" \
"v_mfma_f32_16x16x32_bf16 v[208:211], a[16:19], v[96:99], v[208:211]\n\t" \
"v_mfma_f32_16x16x32_bf16 v[212:215], a[16:19], v[100:103], v[212:215]\n\t" \
"ds_read_b128 v[132:135], v248 offset:22528\n\t" \
"v_mfma_f32_16x16x32_bf16 v[216:219], a[72:75], v[96:99], v[216:219]\n\t" \
"v_mfma_f32_16x16x32_bf16 v[220:223], a[72:75], v[100:103], v[220:223]\n\t" \
"ds_read_b128 v[136:139], v248 offset:9216\n\t" \
"v_mfma_f32_16x16x32_bf16 v[224:227], a[128:131], v[96:99], v[224:227]\n\t" \
"v_mfma_f32_16x16x32_bf16 v[228:231], a[128:131], v[100:103], v[228:231]\n\t" \
"ds_read_b128 v[140:143], v248 offset:23552\n\t" \
"v_mfma_f32_16x16x32_bf16 v[208:211], a[20:23], v[104:107], v[208:211]\n\t" \
"v_mfma_f32_16x16x32_bf16 v[212:215], a[20:23], v[108:111], v[212:215]\n\t" \
"ds_read_b128 v[144:147], v248 offset:10240\n\t" \
"s_add_u32 s90, s87, %[lg]\n\t" \
"s_add_u32 s90, s90, 8192\n\t" \
"s_mov_b32 m0, s90\n\t" \
"s_add_u32 s95, s89, 8192\n\t" \
"buffer_load_dwordx4 %[vg], %[rh], s95 offen offset:0 lds\n\t" \
"v_mfma_f32_16x16x32_bf16 v[216:219], a[76:79], v[104:107], v[216:219]\n\t" \
"v_mfma_f32_16x16x32_bf16 v[220:223], a[76:79], v[108:111], v[220:223]\n\t" \
"ds_read_b128 v[148:151], v248 offset:24576\n\t" \
"v_mfma_f32_16x16x32_bf16 v[224:227], a[132:135], v[104:107], v[224:227]\n\t" \
"v_mfma_f32_16x16x32_bf16 v[228:231], a[132:135], v[108:111], v[228:231]\n\t" \
"ds_read_b128 v[152:155], v248 offset:11264\n\t" \
"v_mfma_f32_16x16x32_bf16 v[208:211], a[24:27], v[112:115], v[208:211]\n\t" \
"v_mfma_f32_16x16x32_bf16 v[212:215], a[24:27], v[116:119], v[212:215]\n\t" \
"ds_read_b128 v[156:159], v248 offset:25600\n\t" \
"v_mfma_f32_16x16x32_bf16 v[216:219], a[80:83], v[112:115], v[216:219]\n\t" \
"v_mfma_f32_16x16x32_bf16 v[220:223], a[80:83], v[116:119], v[220:223]\n\t" \
"ds_read_b128 v[160:163], v248 offset:12288\n\t" \
"buffer_load_dwordx4 %[vg], %[rh], s95 offen offset:1024 lds\n\t" \
"v_mfma_f32_16x16x32_bf16 v[224:227], a[136:139], v[112:115], v[224:227]\n\t" \
"v_mfma_f32_16x16x32_bf16 v[228:231], a[136:139], v[116:119], v[228:231]\n\t" \
"ds_read_b128 v[164:167], v248 offset:26624\n\t" \
"ds_read_b128 v[168:171], v248 offset:13312\n\t" \
"ds_read_b128 v[172:175], v248 offset:27648\n\t" \
"s_add_u32 s89, s89, 57344\n\t" \
"s_min_u32 s89, s89, 1806336\n\t" \
"s_mov_b32 s87, s92\n\t" \
"s_add_u32 s95, s92, 28672\n\t" \
"s_cmp_eq_u32 s95, 114688\n\t" \
"s_cselect_b32 s95, 0, s95\n\t" \
"v_add_u32 v249, s95, %[lr]\n\t" \
"s_waitcnt vmcnt(16) lgkmcnt(0)\n\t" \
"s_barrier\n\t" \
"s_add_u32 s94, s94, 128\n\t" \
"v_mfma_f32_16x16x32_bf16 v[208:211], a[28:31], v[120:123], v[208:211]\n\t" \
"ds_read_b128 v[64:67], v249 offset:0\n\t" \
"v_mfma_f32_16x16x32_bf16 v[212:215], a[28:31], v[124:127], v[212:215]\n\t" \
"v_mfma_f32_16x16x32_bf16 v[216:219], a[84:87], v[120:123], v[216:219]\n\t" \
"ds_read_b128 v[68:71], v249 offset:14336\n\t" \
"v_mfma_f32_16x16x32_bf16 v[220:223], a[84:87], v[124:127], v[220:223]\n\t" \
"v_mfma_f32_16x16x32_bf16 v[224:227], a[140:143], v[120:123], v[224:227]\n\t" \
"ds_read_b128 v[72:75], v249 offset:1024\n\t" \
"v_mfma_f32_16x16x32_bf16 v[228:231], a[140:143], v[124:127], v[228:231]\n\t" \
"v_mfma_f32_16x16x32_bf16 v[208:211], a[32:35], v[128:131], v[208:211]\n\t" \
"ds_read_b128 v[76:79], v249 offset:15360\n\t" \
"v_mfma_f32_16x16x32_bf16 v[212:215], a[32:35], v[132:135], v[212:215]\n\t" \
"s_add_u32 s90, s87, %[lg]\n\t" \
"s_add_u32 s90, s90, 0\n\t" \
"s_mov_b32 m0, s90\n\t" \
"s_add_u32 s95, s89, 0\n\t" \
"buffer_load_dwordx4 %[vg], %[rh], s95 offen offset:0 lds\n\t" \
"v_mfma_f32_16x16x32_bf16 v[216:219], a[88:91], v[128:131], v[216:219]\n\t" \
"ds_read_b128 v[80:83], v249 offset:2048\n\t" \
"v_mfma_f32_16x16x32_bf16 v[220:223], a[88:91], v[132:135], v[220:223]\n\t" \
"v_mfma_f32_16x16x32_bf16 v[224:227], a[144:147], v[128:131], v[224:227]\n\t" \
"ds_read_b128 v[84:87], v249 offset:16384\n\t" \
"v_mfma_f32_16x16x32_bf16 v[228:231], a[144:147], v[132:135], v[228:231]\n\t" \
"v_mfma_f32_16x16x32_bf16 v[208:211], a[36:39], v[136:139], v[208:211]\n\t" \
"ds_read_b128 v[88:91], v249 offset:3072\n\t" \
"v_mfma_f32_16x16x32_bf16 v[212:215], a[36:39], v[140:143], v[212:215]\n\t" \
"v_mfma_f32_16x16x32_bf16 v[216:219], a[92:95], v[136:139], v[216:219]\n\t" \
"ds_read_b128 v[92:95], v249 offset:17408\n\t" \
"v_mfma_f32_16x16x32_bf16 v[220:223], a[92:95], v[140:143], v[220:223]\n\t" \
"buffer_load_dwordx4 %[vg], %[rh], s95 offen offset:1024 lds\n\t" \
"v_mfma_f32_16x16x32_bf16 v[224:227], a[148:151], v[136:139], v[224:227]\n\t" \
"ds_read_b128 v[96:99], v249 offset:4096\n\t" \
"v_mfma_f32_16x16x32_bf16 v[228:231], a[148:151], v[140:143], v[228:231]\n\t" \
"v_mfma_f32_16x16x32_bf16 v[208:211], a[40:43], v[144:147], v[208:211]\n\t" \
"ds_read_b128 v[100:103], v249 offset:18432\n\t" \
"v_mfma_f32_16x16x32_bf16 v[212:215], a[40:43], v[148:151], v[212:215]\n\t" \
"v_mfma_f32_16x16x32_bf16 v[216:219], a[96:99], v[144:147], v[216:219]\n\t" \
"ds_read_b128 v[104:107], v249 offset:5120\n\t" \
"v_mfma_f32_16x16x32_bf16 v[220:223], a[96:99], v[148:151], v[220:223]\n\t" \
"v_mfma_f32_16x16x32_bf16 v[224:227], a[152:155], v[144:147], v[224:227]\n\t" \
"ds_read_b128 v[108:111], v249 offset:19456\n\t" \
"v_mfma_f32_16x16x32_bf16 v[228:231], a[152:155], v[148:151], v[228:231]\n\t" \
"buffer_load_dwordx4 %[vg], %[rh], s95 offen offset:2048 lds\n\t" \
"v_mfma_f32_16x16x32_bf16 v[208:211], a[44:47], v[152:155], v[208:211]\n\t" \
"ds_read_b128 v[112:115], v249 offset:6144\n\t" \
"v_mfma_f32_16x16x32_bf16 v[212:215], a[44:47], v[156:159], v[212:215]\n\t" \
"v_mfma_f32_16x16x32_bf16 v[216:219], a[100:103], v[152:155], v[216:219]\n\t" \
"ds_read_b128 v[116:119], v249 offset:20480\n\t" \
"v_mfma_f32_16x16x32_bf16 v[220:223], a[100:103], v[156:159], v[220:223]\n\t" \
"v_mfma_f32_16x16x32_bf16 v[224:227], a[156:159], v[152:155], v[224:227]\n\t" \
"v_mfma_f32_16x16x32_bf16 v[228:231], a[156:159], v[156:159], v[228:231]\n\t" \
"v_mfma_f32_16x16x32_bf16 v[208:211], a[48:51], v[160:163], v[208:211]\n\t" \
"v_mfma_f32_16x16x32_bf16 v[212:215], a[48:51], v[164:167], v[212:215]\n\t" \
"buffer_load_dwordx4 %[vg], %[rh], s95 offen offset:3072 lds\n\t" \
"v_mfma_f32_16x16x32_bf16 v[216:219], a[104:107], v[160:163], v[216:219]\n\t" \
"v_mfma_f32_16x16x32_bf16 v[220:223], a[104:107], v[164:167], v[220:223]\n\t" \
"v_mfma_f32_16x16x32_bf16 v[224:227], a[160:163], v[160:163], v[224:227]\n\t" \
"v_mfma_f32_16x16x32_bf16 v[228:231], a[160:163], v[164:167], v[228:231]\n\t" \
"v_mfma_f32_16x16x32_bf16 v[208:211], a[52:55], v[168:171], v[208:211]\n\t" \
"v_mfma_f32_16x16x32_bf16 v[212:215], a[52:55], v[172:175], v[212:215]\n\t" \
"v_mfma_f32_16x16x32_bf16 v[216:219], a[108:111], v[168:171], v[216:219]\n\t" \
"v_mfma_f32_16x16x32_bf16 v[220:223], a[108:111], v[172:175], v[220:223]\n\t" \
"s_add_u32 s90, s87, %[lg]\n\t" \
"s_add_u32 s90, s90, 4096\n\t" \
"s_mov_b32 m0, s90\n\t" \
"s_add_u32 s95, s89, 4096\n\t" \
"buffer_load_dwordx4 %[vg], %[rh], s95 offen offset:0 lds\n\t" \
"v_mfma_f32_16x16x32_bf16 v[224:227], a[164:167], v[168:171], v[224:227]\n\t" \
"v_mfma_f32_16x16x32_bf16 v[228:231], a[164:167], v[172:175], v[228:231]\n\t" \
"s_add_u32 s92, s92, 28672\n\t" \
"s_cmp_eq_u32 s92, 114688\n\t" \
"s_cselect_b32 s92, 0, s92\n\t" \
"v_mov_b32 v248, v249\n\t" \
"s_add_u32 s91, s91, 917504\n\t" \
"s_add_u32 s88, s88, 2\n\t" \
"s_cmp_lt_u32 s88, 32\n\t" \
"s_nop 0\n\t" \
"s_cbranch_scc1 Lp3%=\n\t" \
"v_cvt_pk_bf16_f32 v240, v208, v209\n\t" \
"v_cvt_pk_bf16_f32 v241, v210, v211\n\t" \
"buffer_store_dwordx2 v[240:241], %[vs0], %[ro], s91 offen offset:0 sc1\n\t" \
"v_cvt_pk_bf16_f32 v244, v212, v213\n\t" \
"v_cvt_pk_bf16_f32 v245, v214, v215\n\t" \
"buffer_store_dwordx2 v[244:245], %[vs1], %[ro], s91 offen offset:0 sc1\n\t" \
"v_cvt_pk_bf16_f32 v240, v216, v217\n\t" \
"v_cvt_pk_bf16_f32 v241, v218, v219\n\t" \
"buffer_store_dwordx2 v[240:241], %[vs0], %[ro], s91 offen offset:512 sc1\n\t" \
"v_cvt_pk_bf16_f32 v244, v220, v221\n\t" \
"v_cvt_pk_bf16_f32 v245, v222, v223\n\t" \
"buffer_store_dwordx2 v[244:245], %[vs1], %[ro], s91 offen offset:512 sc1\n\t" \
"v_cvt_pk_bf16_f32 v240, v224, v225\n\t" \
"v_cvt_pk_bf16_f32 v241, v226, v227\n\t" \
"buffer_store_dwordx2 v[240:241], %[vs0], %[ro], s91 offen offset:1024 sc1\n\t" \
"v_cvt_pk_bf16_f32 v244, v228, v229\n\t" \
"v_cvt_pk_bf16_f32 v245, v230, v231\n\t" \
"buffer_store_dwordx2 v[244:245], %[vs1], %[ro], s91 offen offset:1024 sc1\n\t" \
"s_waitcnt vmcnt(0) lgkmcnt(0)\n\t" \
"s_barrier\n\t" \
"s_add_u32 s94, s94, 128\n\t" \
"s_add_u32 s94, s94, 128\n\t" \
"s_mov_b32 m0, s93\n\t" \
"s_nop 1\n\t"

#define ASM_P3_0W2_CLOB "v64", "v65", "v66", "v67", "v68", "v69", "v70", "v71", "v72", "v73", "v74", "v75", "v76", "v77", "v78", "v79", "v80", "v81", "v82", "v83", "v84", "v85", "v86", "v87", "v88", "v89", "v90", "v91", "v92", "v93", "v94", "v95", "v96", "v97", "v98", "v99", "v100", "v101", "v102", "v103", "v104", "v105", "v106", "v107", "v108", "v109", "v110", "v111", "v112", "v113", "v114", "v115", "v116", "v117", "v118", "v119", "v120", "v121", "v122", "v123", "v124", "v125", "v126", "v127", "v128", "v129", "v130", "v131", "v132", "v133", "v134", "v135", "v136", "v137", "v138", "v139", "v140", "v141", "v142", "v143", "v144", "v145", "v146", "v147", "v148", "v149", "v150", "v151", "v152", "v153", "v154", "v155", "v156", "v157", "v158", "v159", "v160", "v161", "v162", "v163", "v164", "v165", "v166", "v167", "v168", "v169", "v170", "v171", "v172", "v173", "v174", "v175", "v176", "v177", "v178", "v179", "v180", "v181", "v182", "v183", "v184", "v185", "v186", "v187", "v188", "v189", "v190", "v191", "v192", "v193", "v194", "v195", "v196", "v197", "v198", "v199", "v208", "v209", "v210", "v211", "v212", "v213", "v214", "v215", "v216", "v217", "v218", "v219", "v220", "v221", "v222", "v223", "v224", "v225", "v226", "v227", "v228", "v229", "v230", "v231", "v240", "v241", "v244", "v245", "v248", "v249", "v250", "a0", "a1", "a2", "a3", "a4", "a5", "a6", "a7", "a8", "a9", "a10", "a11", "a12", "a13", "a14", "a15", "a16", "a17", "a18", "a19", "a20", "a21", "a22", "a23", "a24", "a25", "a26", "a27", "a28", "a29", "a30", "a31", "a32", "a33", "a34", "a35", "a36", "a37", "a38", "a39", "a40", "a41", "a42", "a43", "a44", "a45", "a46", "a47", "a48", "a49", "a50", "a51", "a52", "a53", "a54", "a55", "a56", "a57", "a58", "a59", "a60", "a61", "a62", "a63", "a64", "a65", "a66", "a67", "a68", "a69", "a70", "a71", "a72", "a73", "a74", "a75", "a76", "a77", "a78", "a79", "a80", "a81", "a82", "a83", "a84", "a85", "a86", "a87", "a88", "a89", "a90", "a91", "a92", "a93", "a94", "a95", "a96", "a97", "a98", "a99", "a100", "a101", "a102", "a103", "a104", "a105", "a106", "a107", "a108", "a109", "a110", "a111", "a112", "a113", "a114", "a115", "a116", "a117", "a118", "a119", "a120", "a121", "a122", "a123", "a124", "a125", "a126", "a127", "a128", "a129", "a130", "a131", "a132", "a133", "a134", "a135", "a136", "a137", "a138", "a139", "a140", "a141", "a142", "a143", "a144", "a145", "a146", "a147", "a148", "a149", "a150", "a151", "a152", "a153", "a154", "a155", "a156", "a157", "a158", "a159", "a160", "a161", "a162", "a163", "a164", "a165", "a166", "a167", "s86", "s87", "s88", "s89", "s90", "s91", "s92", "s93", "s94", "s95", "vcc", "scc", "memory"

#define ASM_P3_1W0_TEXT \
"s_mov_b32 s93, m0\n\t" \
"s_add_u32 s89, %[src0], 172032\n\t" \
"s_mov_b32 s87, 86016\n\t" \
"s_waitcnt vmcnt(0)\n\t" \
"s_barrier\n\t" \
"v_mov_b32 v250, 1\n\t" \
"v_mov_b32 v248, %[lr]\n\t" \
"ds_read_b128 v[64:67], v248 offset:0\n\t" \
"ds_read_b128 v[68:71], v248 offset:14336\n\t" \
"ds_read_b128 v[72:75], v248 offset:1024\n\t" \
"ds_read_b128 v[76:79], v248 offset:15360\n\t" \
"ds_read_b128 v[80:83], v248 offset:2048\n\t" \
"ds_read_b128 v[84:87], v248 offset:16384\n\t" \
"ds_read_b128 v[88:91], v248 offset:3072\n\t" \
"ds_read_b128 v[92:95], v248 offset:17408\n\t" \
"ds_read_b128 v[96:99], v248 offset:4096\n\t" \
"ds_read_b128 v[100:103], v248 offset:18432\n\t" \
"ds_read_b128 v[104:107], v248 offset:5120\n\t" \
"ds_read_b128 v[108:111], v248 offset:19456\n\t" \
"ds_read_b128 v[112:115], v248 offset:6144\n\t" \
"ds_read_b128 v[116:119], v248 offset:20480\n\t" \
"s_mov_b32 s92, 0\n\t" \
"s_mov_b32 s91, %[so0]\n\t" \
"s_mov_b32 s94, %[ao0]\n\t" \
"s_waitcnt lgkmcnt(0)\n\t" \
"v_mfma_f32_16x16x32_bf16 v[176:179], v[64:67], a[0:3], 0\n\t" \
"v_mfma_f32_16x16x32_bf16 v[180:183], v[68:71], a[0:3], 0\n\t" \
"ds_read_b128 v[120:123], v248 offset:7168\n\t" \
"v_mfma_f32_16x16x32_bf16 v[184:187], v[64:67], a[56:59], 0\n\t" \
"v_mfma_f32_16x16x32_bf16 v[188:191], v[68:71], a[56:59], 0\n\t" \
"ds_read_b128 v[124:127], v248 offset:21504\n\t" \
"v_mfma_f32_16x16x32_bf16 v[192:195], v[64:67], a[112:115], 0\n\t" \
"v_mfma_f32_16x16x32_bf16 v[196:199], v[68:71], a[112:115], 0\n\t" \
"ds_read_b128 v[128:131], v248 offset:8192\n\t" \
"v_mfma_f32_16x16x32_bf16 v[200:203], v[64:67], a[168:171], 0\n\t" \
"v_mfma_f32_16x16x32_bf16 v[204:207], v[68:71], a[168:171], 0\n\t" \
"ds_read_b128 v[132:135], v248 offset:22528\n\t" \
"v_mfma_f32_16x16x32_bf16 v[176:179], v[72:75], a[4:7], v[176:179]\n\t" \
"v_mfma_f32_16x16x32_bf16 v[180:183], v[76:79], a[4:7], v[180:183]\n\t" \
"ds_read_b128 v[136:139], v248 offset:9216\n\t" \
"v_mfma_f32_16x16x32_bf16 v[184:187], v[72:75], a[60:63], v[184:187]\n\t" \
"v_mfma_f32_16x16x32_bf16 v[188:191], v[76:79], a[60:63], v[188:191]\n\t" \
"ds_read_b128 v[140:143], v248 offset:23552\n\t" \
"v_mfma_f32_16x16x32_bf16 v[192:195], v[72:75], a[116:119], v[192:195]\n\t" \
"v_mfma_f32_16x16x32_bf16 v[196:199], v[76:79], a[116:119], v[196:199]\n\t" \
"ds_read_b128 v[144:147], v248 offset:10240\n\t" \
"v_mfma_f32_16x16x32_bf16 v[200:203], v[72:75], a[172:175], v[200:203]\n\t" \
"v_mfma_f32_16x16x32_bf16 v[204:207], v[76:79], a[172:175], v[204:207]\n\t" \
"ds_read_b128 v[148:151], v248 offset:24576\n\t" \
"v_mfma_f32_16x16x32_bf16 v[176:179], v[80:83], a[8:11], v[176:179]\n\t" \
"v_mfma_f32_16x16x32_bf16 v[180:183], v[84:87], a[8:11], v[180:183]\n\t" \
"ds_read_b128 v[152:155], v248 offset:11264\n\t" \
"v_mfma_f32_16x16x32_bf16 v[184:187], v[80:83], a[64:67], v[184:187]\n\t" \
"v_mfma_f32_16x16x32_bf16 v[188:191], v[84:87], a[64:67], v[188:191]\n\t" \
"ds_read_b128 v[156:159], v248 offset:25600\n\t" \
"s_add_u32 s90, s87, %[lg]\n\t" \
"s_add_u32 s90, s90, 0\n\t" \
"s_mov_b32 m0, s90\n\t" \
"s_add_u32 s95, s89, 0\n\t" \
"buffer_load_dwordx4 %[vg], %[rh], s95 offen offset:2048 lds\n\t" \
"v_mfma_f32_16x16x32_bf16 v[192:195], v[80:83], a[120:123], v[192:195]\n\t" \
"v_mfma_f32_16x16x32_bf16 v[196:199], v[84:87], a[120:123], v[196:199]\n\t" \
"ds_read_b128 v[160:163], v248 offset:12288\n\t" \
"v_mfma_f32_16x16x32_bf16 v[200:203], v[80:83], a[176:179], v[200:203]\n\t" \
"v_mfma_f32_16x16x32_bf16 v[204:207], v[84:87], a[176:179], v[204:207]\n\t" \
"ds_read_b128 v[164:167], v248 offset:26624\n\t" \
"v_mfma_f32_16x16x32_bf16 v[176:179], v[88:91], a[12:15], v[176:179]\n\t" \
"v_mfma_f32_16x16x32_bf16 v[180:183], v[92:95], a[12:15], v[180:183]\n\t" \
"ds_read_b128 v[168:171], v248 offset:13312\n\t" \
"v_mfma_f32_16x16x32_bf16 v[184:187], v[88:91], a[68:71], v[184:187]\n\t" \
"v_mfma_f32_16x16x32_bf16 v[188:191], v[92:95], a[68:71], v[188:191]\n\t" \
"ds_read_b128 v[172:175], v248 offset:27648\n\t" \
"v_mfma_f32_16x16x32_bf16 v[192:195], v[88:91], a[124:127], v[192:195]\n\t" \
"v_mfma_f32_16x16x32_bf16 v[196:199], v[92:95], a[124:127], v[196:199]\n\t" \
"v_mfma_f32_16x16x32_bf16 v[200:203], v[88:91], a[180:183], v[200:203]\n\t" \
"v_mfma_f32_16x16x32_bf16 v[204:207], v[92:95], a[180:183], v[204:207]\n\t" \
"v_mfma_f32_16x16x32_bf16 v[176:179], v[96:99], a[16:19], v[176:179]\n\t" \
"v_mfma_f32_16x16x32_bf16 v[180:183], v[100:103], a[16:19], v[180:183]\n\t" \
"v_mfma_f32_16x16x32_bf16 v[184:187], v[96:99], a[72:75], v[184:187]\n\t" \
"v_mfma_f32_16x16x32_bf16 v[188:191], v[100:103], a[72:75], v[188:191]\n\t" \
"v_mfma_f32_16x16x32_bf16 v[192:195], v[96:99], a[128:131], v[192:195]\n\t" \
"v_mfma_f32_16x16x32_bf16 v[196:199], v[100:103], a[128:131], v[196:199]\n\t" \
"v_mfma_f32_16x16x32_bf16 v[200:203], v[96:99], a[184:187], v[200:203]\n\t" \
"v_mfma_f32_16x16x32_bf16 v[204:207], v[100:103], a[184:187], v[204:207]\n\t" \
"buffer_load_dwordx4 %[vg], %[rh], s95 offen offset:3072 lds\n\t" \
"v_mfma_f32_16x16x32_bf16 v[176:179], v[104:107], a[20:23], v[176:179]\n\t" \
"v_mfma_f32_16x16x32_bf16 v[180:183], v[108:111], a[20:23], v[180:183]\n\t" \
"v_mfma_f32_16x16x32_bf16 v[184:187], v[104:107], a[76:79], v[184:187]\n\t" \
"v_mfma_f32_16x16x32_bf16 v[188:191], v[108:111], a[76:79], v[188:191]\n\t" \
"v_mfma_f32_16x16x32_bf16 v[192:195], v[104:107], a[132:135], v[192:195]\n\t" \
"v_mfma_f32_16x16x32_bf16 v[196:199], v[108:111], a[132:135], v[196:199]\n\t" \
"v_mfma_f32_16x16x32_bf16 v[200:203], v[104:107], a[188:191], v[200:203]\n\t" \
"v_mfma_f32_16x16x32_bf16 v[204:207], v[108:111], a[188:191], v[204:207]\n\t" \
"v_mfma_f32_16x16x32_bf16 v[176:179], v[112:115], a[24:27], v[176:179]\n\t" \
"v_mfma_f32_16x16x32_bf16 v[180:183], v[116:119], a[24:27], v[180:183]\n\t" \
"v_mfma_f32_16x16x32_bf16 v[184:187], v[112:115], a[80:83], v[184:187]\n\t" \
"v_mfma_f32_16x16x32_bf16 v[188:191], v[116:119], a[80:83], v[188:191]\n\t" \
"v_mfma_f32_16x16x32_bf16 v[192:195], v[112:115], a[136:139], v[192:195]\n\t" \
"v_mfma_f32_16x16x32_bf16 v[196:199], v[116:119], a[136:139], v[196:199]\n\t" \
"v_mfma_f32_16x16x32_bf16 v[200:203], v[112:115], a[192:195], v[200:203]\n\t" \
"v_mfma_f32_16x16x32_bf16 v[204:207], v[116:119], a[192:195], v[204:207]\n\t" \
"s_add_u32 s89, s89, 57344\n\t" \
"s_min_u32 s89, s89, 1806336\n\t" \
"s_mov_b32 s87, s92\n\t" \
"s_add_u32 s95, s92, 28672\n\t" \
"s_cmp_eq_u32 s95, 114688\n\t" \
"s_cselect_b32 s95, 0, s95\n\t" \
"v_add_u32 v249, s95, %[lr]\n\t" \
"s_waitcnt vmcnt(63) lgkmcnt(0)\n\t" \
"s_barrier\n\t" \
"v_mfma_f32_16x16x32_bf16 v[176:179], v[120:123], a[28:31], v[176:179]\n\t" \
"ds_read_b128 v[64:67], v249 offset:0\n\t" \
"v_mfma_f32_16x16x32_bf16 v[180:183], v[124:127], a[28:31], v[180:183]\n\t" \
"v_mfma_f32_16x16x32_bf16 v[184:187], v[120:123], a[84:87], v[184:187]\n\t" \
"ds_read_b128 v[68:71], v249 offset:14336\n\t" \
"v_mfma_f32_16x16x32_bf16 v[188:191], v[124:127], a[84:87], v[188:191]\n\t" \
"v_mfma_f32_16x16x32_bf16 v[192:195], v[120:123], a[140:143], v[192:195]\n\t" \
"ds_read_b128 v[72:75], v249 offset:1024\n\t" \
"v_mfma_f32_16x16x32_bf16 v[196:199], v[124:127], a[140:143], v[196:199]\n\t" \
"v_mfma_f32_16x16x32_bf16 v[200:203], v[120:123], a[196:199], v[200:203]\n\t" \
"ds_read_b128 v[76:79], v249 offset:15360\n\t" \
"v_mfma_f32_16x16x32_bf16 v[204:207], v[124:127], a[196:199], v[204:207]\n\t" \
"v_mfma_f32_16x16x32_bf16 v[176:179], v[128:131], a[32:35], v[176:179]\n\t" \
"ds_read_b128 v[80:83], v249 offset:2048\n\t" \
"v_mfma_f32_16x16x32_bf16 v[180:183], v[132:135], a[32:35], v[180:183]\n\t" \
"v_mfma_f32_16x16x32_bf16 v[184:187], v[128:131], a[88:91], v[184:187]\n\t" \
"ds_read_b128 v[84:87], v249 offset:16384\n\t" \
"v_mfma_f32_16x16x32_bf16 v[188:191], v[132:135], a[88:91], v[188:191]\n\t" \
"v_mfma_f32_16x16x32_bf16 v[192:195], v[128:131], a[144:147], v[192:195]\n\t" \
"ds_read_b128 v[88:91], v249 offset:3072\n\t" \
"v_mfma_f32_16x16x32_bf16 v[196:199], v[132:135], a[144:147], v[196:199]\n\t" \
"v_mfma_f32_16x16x32_bf16 v[200:203], v[128:131], a[200:203], v[200:203]\n\t" \
"ds_read_b128 v[92:95], v249 offset:17408\n\t" \
"v_mfma_f32_16x16x32_bf16 v[204:207], v[132:135], a[200:203], v[204:207]\n\t" \
"v_mfma_f32_16x16x32_bf16 v[176:179], v[136:139], a[36:39], v[176:179]\n\t" \
"ds_read_b128 v[96:99], v249 offset:4096\n\t" \
"v_mfma_f32_16x16x32_bf16 v[180:183], v[140:143], a[36:39], v[180:183]\n\t" \
"v_mfma_f32_16x16x32_bf16 v[184:187], v[136:139], a[92:95], v[184:187]\n\t" \
"ds_read_b128 v[100:103], v249 offset:18432\n\t" \
"v_mfma_f32_16x16x32_bf16 v[188:191], v[140:143], a[92:95], v[188:191]\n\t" \
"s_add_u32 s90, s87, %[lg]\n\t" \
"s_add_u32 s90, s90, 0\n\t" \
"s_mov_b32 m0, s90\n\t" \
"s_add_u32 s95, s89, 0\n\t" \
"buffer_load_dwordx4 %[vg], %[rh], s95 offen offset:0 lds\n\t" \
"v_mfma_f32_16x16x32_bf16 v[192:195], v[136:139], a[148:151], v[192:195]\n\t" \
"ds_read_b128 v[104:107], v249 offset:5120\n\t" \
"v_mfma_f32_16x16x32_bf16 v[196:199], v[140:143], a[148:151], v[196:199]\n\t" \
"v_mfma_f32_16x16x32_bf16 v[200:203], v[136:139], a[204:207], v[200:203]\n\t" \
"ds_read_b128 v[108:111], v249 offset:19456\n\t" \
"v_mfma_f32_16x16x32_bf16 v[204:207], v[140:143], a[204:207], v[204:207]\n\t" \
"v_mfma_f32_16x16x32_bf16 v[176:179], v[144:147], a[40:43], v[176:179]\n\t" \
"ds_read_b128 v[112:115], v249 offset:6144\n\t" \
"v_mfma_f32_16x16x32_bf16 v[180:183], v[148:151], a[40:43], v[180:183]\n\t" \
"v_mfma_f32_16x16x32_bf16 v[184:187], v[144:147], a[96:99], v[184:187]\n\t" \
"ds_read_b128 v[116:119], v249 offset:20480\n\t" \
"v_mfma_f32_16x16x32_bf16 v[188:191], v[148:151], a[96:99], v[188:191]\n\t" \
"v_mfma_f32_16x16x32_bf16 v[192:195], v[144:147], a[152:155], v[192:195]\n\t" \
"v_mfma_f32_16x16x32_bf16 v[196:199], v[148:151], a[152:155], v[196:199]\n\t" \
"v_mfma_f32_16x16x32_bf16 v[200:203], v[144:147], a[208:211], v[200:203]\n\t" \
"v_mfma_f32_16x16x32_bf16 v[204:207], v[148:151], a[208:211], v[204:207]\n\t" \
"v_mfma_f32_16x16x32_bf16 v[176:179], v[152:155], a[44:47], v[176:179]\n\t" \
"v_mfma_f32_16x16x32_bf16 v[180:183], v[156:159], a[44:47], v[180:183]\n\t" \
"v_mfma_f32_16x16x32_bf16 v[184:187], v[152:155], a[100:103], v[184:187]\n\t" \
"v_mfma_f32_16x16x32_bf16 v[188:191], v[156:159], a[100:103], v[188:191]\n\t" \
"v_mfma_f32_16x16x32_bf16 v[192:195], v[152:155], a[156:159], v[192:195]\n\t" \
"v_mfma_f32_16x16x32_bf16 v[196:199], v[156:159], a[156:159], v[196:199]\n\t" \
"v_mfma_f32_16x16x32_bf16 v[200:203], v[152:155], a[212:215], v[200:203]\n\t" \
"v_mfma_f32_16x16x32_bf16 v[204:207], v[156:159], a[212:215], v[204:207]\n\t" \
"buffer_load_dwordx4 %[vg], %[rh], s95 offen offset:1024 lds\n\t" \
"v_mfma_f32_16x16x32_bf16 v[176:179], v[160:163], a[48:51], v[176:179]\n\t" \
"v_mfma_f32_16x16x32_bf16 v[180:183], v[164:167], a[48:51], v[180:183]\n\t" \
"v_mfma_f32_16x16x32_bf16 v[184:187], v[160:163], a[104:107], v[184:187]\n\t" \
"v_mfma_f32_16x16x32_bf16 v[188:191], v[164:167], a[104:107], v[188:191]\n\t" \
"v_mfma_f32_16x16x32_bf16 v[192:195], v[160:163], a[160:163], v[192:195]\n\t" \
"v_mfma_f32_16x16x32_bf16 v[196:199], v[164:167], a[160:163], v[196:199]\n\t" \
"v_mfma_f32_16x16x32_bf16 v[200:203], v[160:163], a[216:219], v[200:203]\n\t" \
"v_mfma_f32_16x16x32_bf16 v[204:207], v[164:167], a[216:219], v[204:207]\n\t" \
"v_mfma_f32_16x16x32_bf16 v[176:179], v[168:171], a[52:55], v[176:179]\n\t" \
"v_mfma_f32_16x16x32_bf16 v[180:183], v[172:175], a[52:55], v[180:183]\n\t" \
"v_mfma_f32_16x16x32_bf16 v[184:187], v[168:171], a[108:111], v[184:187]\n\t" \
"v_mfma_f32_16x16x32_bf16 v[188:191], v[172:175], a[108:111], v[188:191]\n\t" \
"v_mfma_f32_16x16x32_bf16 v[192:195], v[168:171], a[164:167], v[192:195]\n\t" \
"v_mfma_f32_16x16x32_bf16 v[196:199], v[172:175], a[164:167], v[196:199]\n\t" \
"v_mfma_f32_16x16x32_bf16 v[200:203], v[168:171], a[220:223], v[200:203]\n\t" \
"v_mfma_f32_16x16x32_bf16 v[204:207], v[172:175], a[220:223], v[204:207]\n\t" \
"s_add_u32 s92, s92, 28672\n\t" \
"s_cmp_eq_u32 s92, 114688\n\t" \
"s_cselect_b32 s92, 0, s92\n\t" \
"v_mov_b32 v248, v249\n\t" \
"s_add_u32 s91, s91, 917504\n\t" \
"v_cvt_pk_bf16_f32 v240, v176, v177\n\t" \
"v_cvt_pk_bf16_f32 v241, v178, v179\n\t" \
"v_cvt_pk_bf16_f32 v242, v180, v181\n\t" \
"v_cvt_pk_bf16_f32 v243, v182, v183\n\t" \
"buffer_store_dwordx4 v[240:243], %[vs0], %[ro], s91 offen offset:0 sc1\n\t" \
"v_cvt_pk_bf16_f32 v244, v184, v185\n\t" \
"v_cvt_pk_bf16_f32 v245, v186, v187\n\t" \
"v_cvt_pk_bf16_f32 v246, v188, v189\n\t" \
"v_cvt_pk_bf16_f32 v247, v190, v191\n\t" \
"buffer_store_dwordx4 v[244:247], %[vs0], %[ro], s91 offen offset:1024 sc1\n\t" \
"v_cvt_pk_bf16_f32 v240, v192, v193\n\t" \
"v_cvt_pk_bf16_f32 v241, v194, v195\n\t" \
"v_cvt_pk_bf16_f32 v242, v196, v197\n\t" \
"v_cvt_pk_bf16_f32 v243, v198, v199\n\t" \
"buffer_store_dwordx4 v[240:243], %[vs0], %[ro], s91 offen offset:2048 sc1\n\t" \
"v_cvt_pk_bf16_f32 v244, v200, v201\n\t" \
"v_cvt_pk_bf16_f32 v245, v202, v203\n\t" \
"v_cvt_pk_bf16_f32 v246, v204, v205\n\t" \
"v_cvt_pk_bf16_f32 v247, v206, v207\n\t" \
"buffer_store_dwordx4 v[244:247], %[vs0], %[ro], s91 offen offset:3072 sc1\n\t" \
"s_waitcnt vmcnt(0)\n\t" \
"s_barrier\n\t" \
"s_mov_b64 exec, 1\n\t" \
"buffer_atomic_add v250, off, %[rc], s94\n\t" \
"s_mov_b64 exec, -1\n\t" \
"s_add_u32 s94, s94, 128\n\t" \
"s_waitcnt lgkmcnt(0)\n\t" \
"v_mfma_f32_16x16x32_bf16 v[208:211], v[64:67], a[0:3], 0\n\t" \
"v_mfma_f32_16x16x32_bf16 v[212:215], v[68:71], a[0:3], 0\n\t" \
"ds_read_b128 v[120:123], v248 offset:7168\n\t" \
"v_mfma_f32_16x16x32_bf16 v[216:219], v[64:67], a[56:59], 0\n\t" \
"v_mfma_f32_16x16x32_bf16 v[220:223], v[68:71], a[56:59], 0\n\t" \
"ds_read_b128 v[124:127], v248 offset:21504\n\t" \
"v_mfma_f32_16x16x32_bf16 v[224:227], v[64:67], a[112:115], 0\n\t" \
"v_mfma_f32_16x16x32_bf16 v[228:231], v[68:71], a[112:115], 0\n\t" \
"ds_read_b128 v[128:131], v248 offset:8192\n\t" \
"v_mfma_f32_16x16x32_bf16 v[232:235], v[64:67], a[168:171], 0\n\t" \
"v_mfma_f32_16x16x32_bf16 v[236:239], v[68:71], a[168:171], 0\n\t" \
"ds_read_b128 v[132:135], v248 offset:22528\n\t" \
"v_mfma_f32_16x16x32_bf16 v[208:211], v[72:75], a[4:7], v[208:211]\n\t" \
"v_mfma_f32_16x16x32_bf16 v[212:215], v[76:79], a[4:7], v[212:215]\n\t" \
"ds_read_b128 v[136:139], v248 offset:9216\n\t" \
"v_mfma_f32_16x16x32_bf16 v[216:219], v[72:75], a[60:63], v[216:219]\n\t" \
"v_mfma_f32_16x16x32_bf16 v[220:223], v[76:79], a[60:63], v[220:223]\n\t" \
"ds_read_b128 v[140:143], v248 offset:23552\n\t" \
"v_mfma_f32_16x16x32_bf16 v[224:227], v[72:75], a[116:119], v[224:227]\n\t" \
"v_mfma_f32_16x16x32_bf16 v[228:231], v[76:79], a[116:119], v[228:231]\n\t" \
"ds_read_b128 v[144:147], v248 offset:10240\n\t" \
"v_mfma_f32_16x16x32_bf16 v[232:235], v[72:75], a[172:175], v[232:235]\n\t" \
"v_mfma_f32_16x16x32_bf16 v[236:239], v[76:79], a[172:175], v[236:239]\n\t" \
"ds_read_b128 v[148:151], v248 offset:24576\n\t" \
"v_mfma_f32_16x16x32_bf16 v[208:211], v[80:83], a[8:11], v[208:211]\n\t" \
"v_mfma_f32_16x16x32_bf16 v[212:215], v[84:87], a[8:11], v[212:215]\n\t" \
"ds_read_b128 v[152:155], v248 offset:11264\n\t" \
"v_mfma_f32_16x16x32_bf16 v[216:219], v[80:83], a[64:67], v[216:219]\n\t" \
"v_mfma_f32_16x16x32_bf16 v[220:223], v[84:87], a[64:67], v[220:223]\n\t" \
"ds_read_b128 v[156:159], v248 offset:25600\n\t" \
"s_add_u32 s90, s87, %[lg]\n\t" \
"s_add_u32 s90, s90, 0\n\t" \
"s_mov_b32 m0, s90\n\t" \
"s_add_u32 s95, s89, 0\n\t" \
"buffer_load_dwordx4 %[vg], %[rh], s95 offen offset:2048 lds\n\t" \
"v_mfma_f32_16x16x32_bf16 v[224:227], v[80:83], a[120:123], v[224:227]\n\t" \
"v_mfma_f32_16x16x32_bf16 v[228:231], v[84:87], a[120:123], v[228:231]\n\t" \
"ds_read_b128 v[160:163], v248 offset:12288\n\t" \
"v_mfma_f32_16x16x32_bf16 v[232:235], v[80:83], a[176:179], v[232:235]\n\t" \
"v_mfma_f32_16x16x32_bf16 v[236:239], v[84:87], a[176:179], v[236:239]\n\t" \
"ds_read_b128 v[164:167], v248 offset:26624\n\t" \
"v_mfma_f32_16x16x32_bf16 v[208:211], v[88:91], a[12:15], v[208:211]\n\t" \
"v_mfma_f32_16x16x32_bf16 v[212:215], v[92:95], a[12:15], v[212:215]\n\t" \
"ds_read_b128 v[168:171], v248 offset:13312\n\t" \
"v_mfma_f32_16x16x32_bf16 v[216:219], v[88:91], a[68:71], v[216:219]\n\t" \
"v_mfma_f32_16x16x32_bf16 v[220:223], v[92:95], a[68:71], v[220:223]\n\t" \
"ds_read_b128 v[172:175], v248 offset:27648\n\t" \
"v_mfma_f32_16x16x32_bf16 v[224:227], v[88:91], a[124:127], v[224:227]\n\t" \
"v_mfma_f32_16x16x32_bf16 v[228:231], v[92:95], a[124:127], v[228:231]\n\t" \
"v_mfma_f32_16x16x32_bf16 v[232:235], v[88:91], a[180:183], v[232:235]\n\t" \
"v_mfma_f32_16x16x32_bf16 v[236:239], v[92:95], a[180:183], v[236:239]\n\t" \
"v_mfma_f32_16x16x32_bf16 v[208:211], v[96:99], a[16:19], v[208:211]\n\t" \
"v_mfma_f32_16x16x32_bf16 v[212:215], v[100:103], a[16:19], v[212:215]\n\t" \
"v_mfma_f32_16x16x32_bf16 v[216:219], v[96:99], a[72:75], v[216:219]\n\t" \
"v_mfma_f32_16x16x32_bf16 v[220:223], v[100:103], a[72:75], v[220:223]\n\t" \
"v_mfma_f32_16x16x32_bf16 v[224:227], v[96:99], a[128:131], v[224:227]\n\t" \
"v_mfma_f32_16x16x32_bf16 v[228:231], v[100:103], a[128:131], v[228:231]\n\t" \
"v_mfma_f32_16x16x32_bf16 v[232:235], v[96:99], a[184:187], v[232:235]\n\t" \
"v_mfma_f32_16x16x32_bf16 v[236:239], v[100:103], a[184:187], v[236:239]\n\t" \
"buffer_load_dwordx4 %[vg], %[rh], s95 offen offset:3072 lds\n\t" \
"v_mfma_f32_16x16x32_bf16 v[208:211], v[104:107], a[20:23], v[208:211]\n\t" \
"v_mfma_f32_16x16x32_bf16 v[212:215], v[108:111], a[20:23], v[212:215]\n\t" \
"v_mfma_f32_16x16x32_bf16 v[216:219], v[104:107], a[76:79], v[216:219]\n\t" \
"v_mfma_f32_16x16x32_bf16 v[220:223], v[108:111], a[76:79], v[220:223]\n\t" \
"v_mfma_f32_16x16x32_bf16 v[224:227], v[104:107], a[132:135], v[224:227]\n\t" \
"v_mfma_f32_16x16x32_bf16 v[228:231], v[108:111], a[132:135], v[228:231]\n\t" \
"v_mfma_f32_16x16x32_bf16 v[232:235], v[104:107], a[188:191], v[232:235]\n\t" \
"v_mfma_f32_16x16x32_bf16 v[236:239], v[108:111], a[188:191], v[236:239]\n\t" \
"v_mfma_f32_16x16x32_bf16 v[208:211], v[112:115], a[24:27], v[208:211]\n\t" \
"v_mfma_f32_16x16x32_bf16 v[212:215], v[116:119], a[24:27], v[212:215]\n\t" \
"v_mfma_f32_16x16x32_bf16 v[216:219], v[112:115], a[80:83], v[216:219]\n\t" \
"v_mfma_f32_16x16x32_bf16 v[220:223], v[116:119], a[80:83], v[220:223]\n\t" \
"v_mfma_f32_16x16x32_bf16 v[224:227], v[112:115], a[136:139], v[224:227]\n\t" \
"v_mfma_f32_16x16x32_bf16 v[228:231], v[116:119], a[136:139], v[228:231]\n\t" \
"v_mfma_f32_16x16x32_bf16 v[232:235], v[112:115], a[192:195], v[232:235]\n\t" \
"v_mfma_f32_16x16x32_bf16 v[236:239], v[116:119], a[192:195], v[236:239]\n\t" \
"s_add_u32 s89, s89, 57344\n\t" \
"s_min_u32 s89, s89, 1806336\n\t" \
"s_mov_b32 s87, s92\n\t" \
"s_add_u32 s95, s92, 28672\n\t" \
"s_cmp_eq_u32 s95, 114688\n\t" \
"s_cselect_b32 s95, 0, s95\n\t" \
"v_add_u32 v249, s95, %[lr]\n\t" \
"s_waitcnt vmcnt(63) lgkmcnt(0)\n\t" \
"s_barrier\n\t" \
"v_mfma_f32_16x16x32_bf16 v[208:211], v[120:123], a[28:31], v[208:211]\n\t" \
"ds_read_b128 v[64:67], v249 offset:0\n\t" \
"v_mfma_f32_16x16x32_bf16 v[212:215], v[124:127], a[28:31], v[212:215]\n\t" \
"v_mfma_f32_16x16x32_bf16 v[216:219], v[120:123], a[84:87], v[216:219]\n\t" \
"ds_read_b128 v[68:71], v249 offset:14336\n\t" \
"v_mfma_f32_16x16x32_bf16 v[220:223], v[124:127], a[84:87], v[220:223]\n\t" \
"v_mfma_f32_16x16x32_bf16 v[224:227], v[120:123], a[140:143], v[224:227]\n\t" \
"ds_read_b128 v[72:75], v249 offset:1024\n\t" \
"v_mfma_f32_16x16x32_bf16 v[228:231], v[124:127], a[140:143], v[228:231]\n\t" \
"v_mfma_f32_16x16x32_bf16 v[232:235], v[120:123], a[196:199], v[232:235]\n\t" \
"ds_read_b128 v[76:79], v249 offset:15360\n\t" \
"v_mfma_f32_16x16x32_bf16 v[236:239], v[124:127], a[196:199], v[236:239]\n\t" \
"v_mfma_f32_16x16x32_bf16 v[208:211], v[128:131], a[32:35], v[208:211]\n\t" \
"ds_read_b128 v[80:83], v249 offset:2048\n\t" \
"v_mfma_f32_16x16x32_bf16 v[212:215], v[132:135], a[32:35], v[212:215]\n\t" \
"v_mfma_f32_16x16x32_bf16 v[216:219], v[128:131], a[88:91], v[216:219]\n\t" \
"ds_read_b128 v[84:87], v249 offset:16384\n\t" \
"v_mfma_f32_16x16x32_bf16 v[220:223], v[132:135], a[88:91], v[220:223]\n\t" \
"v_mfma_f32_16x16x32_bf16 v[224:227], v[128:131], a[144:147], v[224:227]\n\t" \
"ds_read_b128 v[88:91], v249 offset:3072\n\t" \
"v_mfma_f32_16x16x32_bf16 v[228:231], v[132:135], a[144:147], v[228:231]\n\t" \
"v_mfma_f32_16x16x32_bf16 v[232:235], v[128:131], a[200:203], v[232:235]\n\t" \
"ds_read_b128 v[92:95], v249 offset:17408\n\t" \
"v_mfma_f32_16x16x32_bf16 v[236:239], v[132:135], a[200:203], v[236:239]\n\t" \
"v_mfma_f32_16x16x32_bf16 v[208:211], v[136:139], a[36:39], v[208:211]\n\t" \
"ds_read_b128 v[96:99], v249 offset:4096\n\t" \
"v_mfma_f32_16x16x32_bf16 v[212:215], v[140:143], a[36:39], v[212:215]\n\t" \
"v_mfma_f32_16x16x32_bf16 v[216:219], v[136:139], a[92:95], v[216:219]\n\t" \
"ds_read_b128 v[100:103], v249 offset:18432\n\t" \
"v_mfma_f32_16x16x32_bf16 v[220:223], v[140:143], a[92:95], v[220:223]\n\t" \
"s_add_u32 s90, s87, %[lg]\n\t" \
"s_add_u32 s90, s90, 0\n\t" \
"s_mov_b32 m0, s90\n\t" \
"s_add_u32 s95, s89, 0\n\t" \
"buffer_load_dwordx4 %[vg], %[rh], s95 offen offset:0 lds\n\t" \
"v_mfma_f32_16x16x32_bf16 v[224:227], v[136:139], a[148:151], v[224:227]\n\t" \
"ds_read_b128 v[104:107], v249 offset:5120\n\t" \
"v_mfma_f32_16x16x32_bf16 v[228:231], v[140:143], a[148:151], v[228:231]\n\t" \
"v_mfma_f32_16x16x32_bf16 v[232:235], v[136:139], a[204:207], v[232:235]\n\t" \
"ds_read_b128 v[108:111], v249 offset:19456\n\t" \
"v_mfma_f32_16x16x32_bf16 v[236:239], v[140:143], a[204:207], v[236:239]\n\t" \
"v_mfma_f32_16x16x32_bf16 v[208:211], v[144:147], a[40:43], v[208:211]\n\t" \
"ds_read_b128 v[112:115], v249 offset:6144\n\t" \
"v_mfma_f32_16x16x32_bf16 v[212:215], v[148:151], a[40:43], v[212:215]\n\t" \
"v_mfma_f32_16x16x32_bf16 v[216:219], v[144:147], a[96:99], v[216:219]\n\t" \
"ds_read_b128 v[116:119], v249 offset:20480\n\t" \
"v_mfma_f32_16x16x32_bf16 v[220:223], v[148:151], a[96:99], v[220:223]\n\t" \
"v_mfma_f32_16x16x32_bf16 v[224:227], v[144:147], a[152:155], v[224:227]\n\t" \
"v_mfma_f32_16x16x32_bf16 v[228:231], v[148:151], a[152:155], v[228:231]\n\t" \
"v_mfma_f32_16x16x32_bf16 v[232:235], v[144:147], a[208:211], v[232:235]\n\t" \
"v_mfma_f32_16x16x32_bf16 v[236:239], v[148:151], a[208:211], v[236:239]\n\t" \
"v_mfma_f32_16x16x32_bf16 v[208:211], v[152:155], a[44:47], v[208:211]\n\t" \
"v_mfma_f32_16x16x32_bf16 v[212:215], v[156:159], a[44:47], v[212:215]\n\t" \
"v_mfma_f32_16x16x32_bf16 v[216:219], v[152:155], a[100:103], v[216:219]\n\t" \
"v_mfma_f32_16x16x32_bf16 v[220:223], v[156:159], a[100:103], v[220:223]\n\t" \
"v_mfma_f32_16x16x32_bf16 v[224:227], v[152:155], a[156:159], v[224:227]\n\t" \
"v_mfma_f32_16x16x32_bf16 v[228:231], v[156:159], a[156:159], v[228:231]\n\t" \
"v_mfma_f32_16x16x32_bf16 v[232:235], v[152:155], a[212:215], v[232:235]\n\t" \
"v_mfma_f32_16x16x32_bf16 v[236:239], v[156:159], a[212:215], v[236:239]\n\t" \
"buffer_load_dwordx4 %[vg], %[rh], s95 offen offset:1024 lds\n\t" \
"v_mfma_f32_16x16x32_bf16 v[208:211], v[160:163], a[48:51], v[208:211]\n\t" \
"v_mfma_f32_16x16x32_bf16 v[212:215], v[164:167], a[48:51], v[212:215]\n\t" \
"v_mfma_f32_16x16x32_bf16 v[216:219], v[160:163], a[104:107], v[216:219]\n\t" \
"v_mfma_f32_16x16x32_bf16 v[220:223], v[164:167], a[104:107], v[220:223]\n\t" \
"v_mfma_f32_16x16x32_bf16 v[224:227], v[160:163], a[160:163], v[224:227]\n\t" \
"v_mfma_f32_16x16x32_bf16 v[228:231], v[164:167], a[160:163], v[228:231]\n\t" \
"v_mfma_f32_16x16x32_bf16 v[232:235], v[160:163], a[216:219], v[232:235]\n\t" \
"v_mfma_f32_16x16x32_bf16 v[236:239], v[164:167], a[216:219], v[236:239]\n\t" \
"v_mfma_f32_16x16x32_bf16 v[208:211], v[168:171], a[52:55], v[208:211]\n\t" \
"v_mfma_f32_16x16x32_bf16 v[212:215], v[172:175], a[52:55], v[212:215]\n\t" \
"v_mfma_f32_16x16x32_bf16 v[216:219], v[168:171], a[108:111], v[216:219]\n\t" \
"v_mfma_f32_16x16x32_bf16 v[220:223], v[172:175], a[108:111], v[220:223]\n\t" \
"v_mfma_f32_16x16x32_bf16 v[224:227], v[168:171], a[164:167], v[224:227]\n\t" \
"v_mfma_f32_16x16x32_bf16 v[228:231], v[172:175], a[164:167], v[228:231]\n\t" \
"v_mfma_f32_16x16x32_bf16 v[232:235], v[168:171], a[220:223], v[232:235]\n\t" \
"v_mfma_f32_16x16x32_bf16 v[236:239], v[172:175], a[220:223], v[236:239]\n\t" \
"s_add_u32 s92, s92, 28672\n\t" \
"s_cmp_eq_u32 s92, 114688\n\t" \
"s_cselect_b32 s92, 0, s92\n\t" \
"v_mov_b32 v248, v249\n\t" \
"s_add_u32 s91, s91, 917504\n\t" \
"s_waitcnt lgkmcnt(0)\n\t" \
"v_mfma_f32_16x16x32_bf16 v[176:179], v[64:67], a[0:3], 0\n\t" \
"v_mfma_f32_16x16x32_bf16 v[180:183], v[68:71], a[0:3], 0\n\t" \
"ds_read_b128 v[120:123], v248 offset:7168\n\t" \
"v_mfma_f32_16x16x32_bf16 v[184:187], v[64:67], a[56:59], 0\n\t" \
"v_cvt_pk_bf16_f32 v240, v208, v209\n\t" \
"v_mfma_f32_16x16x32_bf16 v[188:191], v[68:71], a[56:59], 0\n\t" \
"v_cvt_pk_bf16_f32 v241, v210, v211\n\t" \
"v_mfma_f32_16x16x32_bf16 v[192:195], v[64:67], a[112:115], 0\n\t" \
"v_cvt_pk_bf16_f32 v242, v212, v213\n\t" \
"v_mfma_f32_16x16x32_bf16 v[196:199], v[68:71], a[112:115], 0\n\t" \
"v_cvt_pk_bf16_f32 v243, v214, v215\n\t" \
"v_mfma_f32_16x16x32_bf16 v[200:203], v[64:67], a[168:171], 0\n\t" \
"buffer_store_dwordx4 v[240:243], %[vs0], %[ro], s91 offen offset:0 sc1\n\t" \
"v_mfma_f32_16x16x32_bf16 v[204:207], v[68:71], a[168:171], 0\n\t" \
"v_cvt_pk_bf16_f32 v244, v216, v217\n\t" \
"v_mfma_f32_16x16x32_bf16 v[176:179], v[72:75], a[4:7], v[176:179]\n\t" \
"v_cvt_pk_bf16_f32 v245, v218, v219\n\t" \
"v_mfma_f32_16x16x32_bf16 v[180:183], v[76:79], a[4:7], v[180:183]\n\t" \
"v_cvt_pk_bf16_f32 v246, v220, v221\n\t" \
"v_mfma_f32_16x16x32_bf16 v[184:187], v[72:75], a[60:63], v[184:187]\n\t" \
"v_cvt_pk_bf16_f32 v247, v222, v223\n\t" \
"v_mfma_f32_16x16x32_bf16 v[188:191], v[76:79], a[60:63], v[188:191]\n\t" \
"buffer_store_dwordx4 v[244:247], %[vs0], %[ro], s91 offen offset:1024 sc1\n\t" \
"v_mfma_f32_16x16x32_bf16 v[192:195], v[72:75], a[116:119], v[192:195]\n\t" \
"v_cvt_pk_bf16_f32 v240, v224, v225\n\t" \
"v_mfma_f32_16x16x32_bf16 v[196:199], v[76:79], a[116:119], v[196:199]\n\t" \
"v_cvt_pk_bf16_f32 v241, v226, v227\n\t" \
"v_mfma_f32_16x16x32_bf16 v[200:203], v[72:75], a[172:175], v[200:203]\n\t" \
"v_cvt_pk_bf16_f32 v242, v228, v229\n\t" \
"v_mfma_f32_16x16x32_bf16 v[204:207], v[76:79], a[172:175], v[204:207]\n\t" \
"v_cvt_pk_bf16_f32 v243, v230, v231\n\t" \
"v_mfma_f32_16x16x32_bf16 v[176:179], v[80:83], a[8:11], v[176:179]\n\t" \
"buffer_store_dwordx4 v[240:243], %[vs0], %[ro], s91 offen offset:2048 sc1\n\t" \
"v_mfma_f32_16x16x32_bf16 v[180:183], v[84:87], a[8:11], v[180:183]\n\t" \
"v_cvt_pk_bf16_f32 v244, v232, v233\n\t" \
"v_mfma_f32_16x16x32_bf16 v[184:187], v[80:83], a[64:67], v[184:187]\n\t" \
"v_cvt_pk_bf16_f32 v245, v234, v235\n\t" \
"v_mfma_f32_16x16x32_bf16 v[188:191], v[84:87], a[64:67], v[188:191]\n\t" \
"v_cvt_pk_bf16_f32 v246, v236, v237\n\t" \
"s_add_u32 s90, s87, %[lg]\n\t" \
"s_add_u32 s90, s90, 0\n\t" \
"s_mov_b32 m0, s90\n\t" \
"s_add_u32 s95, s89, 0\n\t" \
"buffer_load_dwordx4 %[vg], %[rh], s95 offen offset:2048 lds\n\t" \
"v_mfma_f32_16x16x32_bf16 v[192:195], v[80:83], a[120:123], v[192:195]\n\t" \
"v_cvt_pk_bf16_f32 v247, v238, v239\n\t" \
"v_mfma_f32_16x16x32_bf16 v[196:199], v[84:87], a[120:123], v[196:199]\n\t" \
"buffer_store_dwordx4 v[244:247], %[vs0], %[ro], s91 offen offset:3072 sc1\n\t" \
"v_mfma_f32_16x16x32_bf16 v[200:203], v[80:83], a[176:179], v[200:203]\n\t" \
"v_mfma_f32_16x16x32_bf16 v[204:207], v[84:87], a[176:179], v[204:207]\n\t" \
"ds_read_b128 v[124:127], v248 offset:21504\n\t" \
"v_mfma_f32_16x16x32_bf16 v[176:179], v[88:91], a[12:15], v[176:179]\n\t" \
"v_mfma_f32_16x16x32_bf16 v[180:183], v[92:95], a[12:15], v[180:183]\n\t" \
"ds_read_b128 v[128:131], v248 offset:8192\n\t" \
"v_mfma_f32_16x16x32_bf16 v[184:187], v[88:91], a[68:71], v[184:187]\n\t" \
"v_mfma_f32_16x16x32_bf16 v[188:191], v[92:95], a[68:71], v[188:191]\n\t" \
"ds_read_b128 v[132:135], v248 offset:22528\n\t" \
"v_mfma_f32_16x16x32_bf16 v[192:195], v[88:91], a[124:127], v[192:195]\n\t" \
"v_mfma_f32_16x16x32_bf16 v[196:199], v[92:95], a[124:127], v[196:199]\n\t" \
"ds_read_b128 v[136:139], v248 offset:9216\n\t" \
"v_mfma_f32_16x16x32_bf16 v[200:203], v[88:91], a[180:183], v[200:203]\n\t" \
"v_mfma_f32_16x16x32_bf16 v[204:207], v[92:95], a[180:183], v[204:207]\n\t" \
"ds_read_b128 v[140:143], v248 offset:23552\n\t" \
"v_mfma_f32_16x16x32_bf16 v[176:179], v[96:99], a[16:19], v[176:179]\n\t" \
"v_mfma_f32_16x16x32_bf16 v[180:183], v[100:103], a[16:19], v[180:183]\n\t" \
"ds_read_b128 v[144:147], v248 offset:10240\n\t" \
"v_mfma_f32_16x16x32_bf16 v[184:187], v[96:99], a[72:75], v[184:187]\n\t" \
"v_mfma_f32_16x16x32_bf16 v[188:191], v[100:103], a[72:75], v[188:191]\n\t" \
"ds_read_b128 v[148:151], v248 offset:24576\n\t" \
"v_mfma_f32_16x16x32_bf16 v[192:195], v[96:99], a[128:131], v[192:195]\n\t" \
"v_mfma_f32_16x16x32_bf16 v[196:199], v[100:103], a[128:131], v[196:199]\n\t" \
"ds_read_b128 v[152:155], v248 offset:11264\n\t" \
"v_mfma_f32_16x16x32_bf16 v[200:203], v[96:99], a[184:187], v[200:203]\n\t" \
"v_mfma_f32_16x16x32_bf16 v[204:207], v[100:103], a[184:187], v[204:207]\n\t" \
"ds_read_b128 v[156:159], v248 offset:25600\n\t" \
"buffer_load_dwordx4 %[vg], %[rh], s95 offen offset:3072 lds\n\t" \
"v_mfma_f32_16x16x32_bf16 v[176:179], v[104:107], a[20:23], v[176:179]\n\t" \
"v_mfma_f32_16x16x32_bf16 v[180:183], v[108:111], a[20:23], v[180:183]\n\t" \
"ds_read_b128 v[160:163], v248 offset:12288\n\t" \
"v_mfma_f32_16x16x32_bf16 v[184:187], v[104:107], a[76:79], v[184:187]\n\t" \
"v_mfma_f32_16x16x32_bf16 v[188:191], v[108:111], a[76:79], v[188:191]\n\t" \
"ds_read_b128 v[164:167], v248 offset:26624\n\t" \
"v_mfma_f32_16x16x32_bf16 v[192:195], v[104:107], a[132:135], v[192:195]\n\t" \
"v_mfma_f32_16x16x32_bf16 v[196:199], v[108:111], a[132:135], v[196:199]\n\t" \
"ds_read_b128 v[168:171], v248 offset:13312\n\t" \
"v_mfma_f32_16x16x32_bf16 v[200:203], v[104:107], a[188:191], v[200:203]\n\t" \
"v_mfma_f32_16x16x32_bf16 v[204:207], v[108:111], a[188:191], v[204:207]\n\t" \
"ds_read_b128 v[172:175], v248 offset:27648\n\t" \
"v_mfma_f32_16x16x32_bf16 v[176:179], v[112:115], a[24:27], v[176:179]\n\t" \
"v_mfma_f32_16x16x32_bf16 v[180:183], v[116:119], a[24:27], v[180:183]\n\t" \
"v_mfma_f32_16x16x32_bf16 v[184:187], v[112:115], a[80:83], v[184:187]\n\t" \
"v_mfma_f32_16x16x32_bf16 v[188:191], v[116:119], a[80:83], v[188:191]\n\t" \
"v_mfma_f32_16x16x32_bf16 v[192:195], v[112:115], a[136:139], v[192:195]\n\t" \
"v_mfma_f32_16x16x32_bf16 v[196:199], v[116:119], a[136:139], v[196:199]\n\t" \
"v_mfma_f32_16x16x32_bf16 v[200:203], v[112:115], a[192:195], v[200:203]\n\t" \
"v_mfma_f32_16x16x32_bf16 v[204:207], v[116:119], a[192:195], v[204:207]\n\t" \
"s_add_u32 s89, s89, 57344\n\t" \
"s_min_u32 s89, s89, 1806336\n\t" \
"s_mov_b32 s87, s92\n\t" \
"s_add_u32 s95, s92, 28672\n\t" \
"s_cmp_eq_u32 s95, 114688\n\t" \
"s_cselect_b32 s95, 0, s95\n\t" \
"v_add_u32 v249, s95, %[lr]\n\t" \
"s_waitcnt vmcnt(8) lgkmcnt(0)\n\t" \
"s_barrier\n\t" \
"v_mfma_f32_16x16x32_bf16 v[176:179], v[120:123], a[28:31], v[176:179]\n\t" \
"ds_read_b128 v[64:67], v249 offset:0\n\t" \
"v_mfma_f32_16x16x32_bf16 v[180:183], v[124:127], a[28:31], v[180:183]\n\t" \
"v_mfma_f32_16x16x32_bf16 v[184:187], v[120:123], a[84:87], v[184:187]\n\t" \
"ds_read_b128 v[68:71], v249 offset:14336\n\t" \
"v_mfma_f32_16x16x32_bf16 v[188:191], v[124:127], a[84:87], v[188:191]\n\t" \
"v_mfma_f32_16x16x32_bf16 v[192:195], v[120:123], a[140:143], v[192:195]\n\t" \
"ds_read_b128 v[72:75], v249 offset:1024\n\t" \
"v_mfma_f32_16x16x32_bf16 v[196:199], v[124:127], a[140:143], v[196:199]\n\t" \
"v_mfma_f32_16x16x32_bf16 v[200:203], v[120:123], a[196:199], v[200:203]\n\t" \
"ds_read_b128 v[76:79], v249 offset:15360\n\t" \
"v_mfma_f32_16x16x32_bf16 v[204:207], v[124:127], a[196:199], v[204:207]\n\t" \
"v_mfma_f32_16x16x32_bf16 v[176:179], v[128:131], a[32:35], v[176:179]\n\t" \
"ds_read_b128 v[80:83], v249 offset:2048\n\t" \
"v_mfma_f32_16x16x32_bf16 v[180:183], v[132:135], a[32:35], v[180:183]\n\t" \
"v_mfma_f32_16x16x32_bf16 v[184:187], v[128:131], a[88:91], v[184:187]\n\t" \
"ds_read_b128 v[84:87], v249 offset:16384\n\t" \
"v_mfma_f32_16x16x32_bf16 v[188:191], v[132:135], a[88:91], v[188:191]\n\t" \
"v_mfma_f32_16x16x32_bf16 v[192:195], v[128:131], a[144:147], v[192:195]\n\t" \
"ds_read_b128 v[88:91], v249 offset:3072\n\t" \
"v_mfma_f32_16x16x32_bf16 v[196:199], v[132:135], a[144:147], v[196:199]\n\t" \
"v_mfma_f32_16x16x32_bf16 v[200:203], v[128:131], a[200:203], v[200:203]\n\t" \
"ds_read_b128 v[92:95], v249 offset:17408\n\t" \
"v_mfma_f32_16x16x32_bf16 v[204:207], v[132:135], a[200:203], v[204:207]\n\t" \
"v_mfma_f32_16x16x32_bf16 v[176:179], v[136:139], a[36:39], v[176:179]\n\t" \
"ds_read_b128 v[96:99], v249 offset:4096\n\t" \
"v_mfma_f32_16x16x32_bf16 v[180:183], v[140:143], a[36:39], v[180:183]\n\t" \
"v_mfma_f32_16x16x32_bf16 v[184:187], v[136:139], a[92:95], v[184:187]\n\t" \
"ds_read_b128 v[100:103], v249 offset:18432\n\t" \
"v_mfma_f32_16x16x32_bf16 v[188:191], v[140:143], a[92:95], v[188:191]\n\t" \
"s_add_u32 s90, s87, %[lg]\n\t" \
"s_add_u32 s90, s90, 0\n\t" \
"s_mov_b32 m0, s90\n\t" \
"s_add_u32 s95, s89, 0\n\t" \
"buffer_load_dwordx4 %[vg], %[rh], s95 offen offset:0 lds\n\t" \
"v_mfma_f32_16x16x32_bf16 v[192:195], v[136:139], a[148:151], v[192:195]\n\t" \
"ds_read_b128 v[104:107], v249 offset:5120\n\t" \
"v_mfma_f32_16x16x32_bf16 v[196:199], v[140:143], a[148:151], v[196:199]\n\t" \
"v_mfma_f32_16x16x32_bf16 v[200:203], v[136:139], a[204:207], v[200:203]\n\t" \
"ds_read_b128 v[108:111], v249 offset:19456\n\t" \
"v_mfma_f32_16x16x32_bf16 v[204:207], v[140:143], a[204:207], v[204:207]\n\t" \
"v_mfma_f32_16x16x32_bf16 v[176:179], v[144:147], a[40:43], v[176:179]\n\t" \
"ds_read_b128 v[112:115], v249 offset:6144\n\t" \
"v_mfma_f32_16x16x32_bf16 v[180:183], v[148:151], a[40:43], v[180:183]\n\t" \
"v_mfma_f32_16x16x32_bf16 v[184:187], v[144:147], a[96:99], v[184:187]\n\t" \
"ds_read_b128 v[116:119], v249 offset:20480\n\t" \
"v_mfma_f32_16x16x32_bf16 v[188:191], v[148:151], a[96:99], v[188:191]\n\t" \
"v_mfma_f32_16x16x32_bf16 v[192:195], v[144:147], a[152:155], v[192:195]\n\t" \
"v_mfma_f32_16x16x32_bf16 v[196:199], v[148:151], a[152:155], v[196:199]\n\t" \
"v_mfma_f32_16x16x32_bf16 v[200:203], v[144:147], a[208:211], v[200:203]\n\t" \
"v_mfma_f32_16x16x32_bf16 v[204:207], v[148:151], a[208:211], v[204:207]\n\t" \
"v_mfma_f32_16x16x32_bf16 v[176:179], v[152:155], a[44:47], v[176:179]\n\t" \
"v_mfma_f32_16x16x32_bf16 v[180:183], v[156:159], a[44:47], v[180:183]\n\t" \
"v_mfma_f32_16x16x32_bf16 v[184:187], v[152:155], a[100:103], v[184:187]\n\t" \
"v_mfma_f32_16x16x32_bf16 v[188:191], v[156:159], a[100:103], v[188:191]\n\t" \
"v_mfma_f32_16x16x32_bf16 v[192:195], v[152:155], a[156:159], v[192:195]\n\t" \
"v_mfma_f32_16x16x32_bf16 v[196:199], v[156:159], a[156:159], v[196:199]\n\t" \
"v_mfma_f32_16x16x32_bf16 v[200:203], v[152:155], a[212:215], v[200:203]\n\t" \
"v_mfma_f32_16x16x32_bf16 v[204:207], v[156:159], a[212:215], v[204:207]\n\t" \
"buffer_load_dwordx4 %[vg], %[rh], s95 offen offset:1024 lds\n\t" \
"v_mfma_f32_16x16x32_bf16 v[176:179], v[160:163], a[48:51], v[176:179]\n\t" \
"v_mfma_f32_16x16x32_bf16 v[180:183], v[164:167], a[48:51], v[180:183]\n\t" \
"v_mfma_f32_16x16x32_bf16 v[184:187], v[160:163], a[104:107], v[184:187]\n\t" \
"v_mfma_f32_16x16x32_bf16 v[188:191], v[164:167], a[104:107], v[188:191]\n\t" \
"v_mfma_f32_16x16x32_bf16 v[192:195], v[160:163], a[160:163], v[192:195]\n\t" \
"v_mfma_f32_16x16x32_bf16 v[196:199], v[164:167], a[160:163], v[196:199]\n\t" \
"v_mfma_f32_16x16x32_bf16 v[200:203], v[160:163], a[216:219], v[200:203]\n\t" \
"v_mfma_f32_16x16x32_bf16 v[204:207], v[164:167], a[216:219], v[204:207]\n\t" \
"v_mfma_f32_16x16x32_bf16 v[176:179], v[168:171], a[52:55], v[176:179]\n\t" \
"v_mfma_f32_16x16x32_bf16 v[180:183], v[172:175], a[52:55], v[180:183]\n\t" \
"v_mfma_f32_16x16x32_bf16 v[184:187], v[168:171], a[108:111], v[184:187]\n\t" \
"v_mfma_f32_16x16x32_bf16 v[188:191], v[172:175], a[108:111], v[188:191]\n\t" \
"v_mfma_f32_16x16x32_bf16 v[192:195], v[168:171], a[164:167], v[192:195]\n\t" \
"v_mfma_f32_16x16x32_bf16 v[196:199], v[172:175], a[164:167], v[196:199]\n\t" \
"v_mfma_f32_16x16x32_bf16 v[200:203], v[168:171], a[220:223], v[200:203]\n\t" \
"v_mfma_f32_16x16x32_bf16 v[204:207], v[172:175], a[220:223], v[204:207]\n\t" \
"s_add_u32 s92, s92, 28672\n\t" \
"s_cmp_eq_u32 s92, 114688\n\t" \
"s_cselect_b32 s92, 0, s92\n\t" \
"v_mov_b32 v248, v249\n\t" \
"s_add_u32 s91, s91, 917504\n\t" \
"s_waitcnt lgkmcnt(0)\n\t" \
"v_mfma_f32_16x16x32_bf16 v[208:211], v[64:67], a[0:3], 0\n\t" \
"v_mfma_f32_16x16x32_bf16 v[212:215], v[68:71], a[0:3], 0\n\t" \
"ds_read_b128 v[120:123], v248 offset:7168\n\t" \
"v_mfma_f32_16x16x32_bf16 v[216:219], v[64:67], a[56:59], 0\n\t" \
"v_cvt_pk_bf16_f32 v240, v176, v177\n\t" \
"v_mfma_f32_16x16x32_bf16 v[220:223], v[68:71], a[56:59], 0\n\t" \
"v_cvt_pk_bf16_f32 v241, v178, v179\n\t" \
"v_mfma_f32_16x16x32_bf16 v[224:227], v[64:67], a[112:115], 0\n\t" \
"v_cvt_pk_bf16_f32 v242, v180, v181\n\t" \
"v_mfma_f32_16x16x32_bf16 v[228:231], v[68:71], a[112:115], 0\n\t" \
"v_cvt_pk_bf16_f32 v243, v182, v183\n\t" \
"v_mfma_f32_16x16x32_bf16 v[232:235], v[64:67], a[168:171], 0\n\t" \
"buffer_store_dwordx4 v[240:243], %[vs0], %[ro], s91 offen offset:0 sc1\n\t" \
"v_mfma_f32_16x16x32_bf16 v[236:239], v[68:71], a[168:171], 0\n\t" \
"v_cvt_pk_bf16_f32 v244, v184, v185\n\t" \
"v_mfma_f32_16x16x32_bf16 v[208:211], v[72:75], a[4:7], v[208:211]\n\t" \
"v_cvt_pk_bf16_f32 v245, v186, v187\n\t" \
"v_mfma_f32_16x16x32_bf16 v[212:215], v[76:79], a[4:7], v[212:215]\n\t" \
"v_cvt_pk_bf16_f32 v246, v188, v189\n\t" \
"v_mfma_f32_16x16x32_bf16 v[216:219], v[72:75], a[60:63], v[216:219]\n\t" \
"v_cvt_pk_bf16_f32 v247, v190, v191\n\t" \
"v_mfma_f32_16x16x32_bf16 v[220:223], v[76:79], a[60:63], v[220:223]\n\t" \
"buffer_store_dwordx4 v[244:247], %[vs0], %[ro], s91 offen offset:1024 sc1\n\t" \
"v_mfma_f32_16x16x32_bf16 v[224:227], v[72:75], a[116:119], v[224:227]\n\t" \
"v_cvt_pk_bf16_f32 v240, v192, v193\n\t" \
"v_mfma_f32_16x16x32_bf16 v[228:231], v[76:79], a[116:119], v[228:231]\n\t" \
"v_cvt_pk_bf16_f32 v241, v194, v195\n\t" \
"v_mfma_f32_16x16x32_bf16 v[232:235], v[72:75], a[172:175], v[232:235]\n\t" \
"v_cvt_pk_bf16_f32 v242, v196, v197\n\t" \
"v_mfma_f32_16x16x32_bf16 v[236:239], v[76:79], a[172:175], v[236:239]\n\t" \
"v_cvt_pk_bf16_f32 v243, v198, v199\n\t" \
"v_mfma_f32_16x16x32_bf16 v[208:211], v[80:83], a[8:11], v[208:211]\n\t" \
"buffer_store_dwordx4 v[240:243], %[vs0], %[ro], s91 offen offset:2048 sc1\n\t" \
"v_mfma_f32_16x16x32_bf16 v[212:215], v[84:87], a[8:11], v[212:215]\n\t" \
"v_cvt_pk_bf16_f32 v244, v200, v201\n\t" \
"v_mfma_f32_16x16x32_bf16 v[216:219], v[80:83], a[64:67], v[216:219]\n\t" \
"v_cvt_pk_bf16_f32 v245, v202, v203\n\t" \
"v_mfma_f32_16x16x32_bf16 v[220:223], v[84:87], a[64:67], v[220:223]\n\t" \
"v_cvt_pk_bf16_f32 v246, v204, v205\n\t" \
"s_add_u32 s90, s87, %[lg]\n\t" \
"s_add_u32 s90, s90, 0\n\t" \
"s_mov_b32 m0, s90\n\t" \
"s_add_u32 s95, s89, 0\n\t" \
"buffer_load_dwordx4 %[vg], %[rh], s95 offen offset:2048 lds\n\t" \
"v_mfma_f32_16x16x32_bf16 v[224:227], v[80:83], a[120:123], v[224:227]\n\t" \
"v_cvt_pk_bf16_f32 v247, v206, v207\n\t" \
"v_mfma_f32_16x16x32_bf16 v[228:231], v[84:87], a[120:123], v[228:231]\n\t" \
"buffer_store_dwordx4 v[244:247], %[vs0], %[ro], s91 offen offset:3072 sc1\n\t" \
"v_mfma_f32_16x16x32_bf16 v[232:235], v[80:83], a[176:179], v[232:235]\n\t" \
"v_mfma_f32_16x16x32_bf16 v[236:239], v[84:87], a[176:179], v[236:239]\n\t" \
"ds_read_b128 v[124:127], v248 offset:21504\n\t" \
"v_mfma_f32_16x16x32_bf16 v[208:211], v[88:91], a[12:15], v[208:211]\n\t" \
"v_mfma_f32_16x16x32_bf16 v[212:215], v[92:95], a[12:15], v[212:215]\n\t" \
"ds_read_b128 v[128:131], v248 offset:8192\n\t" \
"v_mfma_f32_16x16x32_bf16 v[216:219], v[88:91], a[68:71], v[216:219]\n\t" \
"v_mfma_f32_16x16x32_bf16 v[220:223], v[92:95], a[68:71], v[220:223]\n\t" \
"ds_read_b128 v[132:135], v248 offset:22528\n\t" \
"v_mfma_f32_16x16x32_bf16 v[224:227], v[88:91], a[124:127], v[224:227]\n\t" \
"v_mfma_f32_16x16x32_bf16 v[228:231], v[92:95], a[124:127], v[228:231]\n\t" \
"ds_read_b128 v[136:139], v248 offset:9216\n\t" \
"v_mfma_f32_16x16x32_bf16 v[232:235], v[88:91], a[180:183], v[232:235]\n\t" \
"v_mfma_f32_16x16x32_bf16 v[236:239], v[92:95], a[180:183], v[236:239]\n\t" \
"ds_read_b128 v[140:143], v248 offset:23552\n\t" \
"v_mfma_f32_16x16x32_bf16 v[208:211], v[96:99], a[16:19], v[208:211]\n\t" \
"v_mfma_f32_16x16x32_bf16 v[212:215], v[100:103], a[16:19], v[212:215]\n\t" \
"ds_read_b128 v[144:147], v248 offset:10240\n\t" \
"v_mfma_f32_16x16x32_bf16 v[216:219], v[96:99], a[72:75], v[216:219]\n\t" \
"v_mfma_f32_16x16x32_bf16 v[220:223], v[100:103], a[72:75], v[220:223]\n\t" \
"ds_read_b128 v[148:151], v248 offset:24576\n\t" \
"v_mfma_f32_16x16x32_bf16 v[224:227], v[96:99], a[128:131], v[224:227]\n\t" \
"v_mfma_f32_16x16x32_bf16 v[228:231], v[100:103], a[128:131], v[228:231]\n\t" \
"ds_read_b128 v[152:155], v248 offset:11264\n\t" \
"v_mfma_f32_16x16x32_bf16 v[232:235], v[96:99], a[184:187], v[232:235]\n\t" \
"v_mfma_f32_16x16x32_bf16 v[236:239], v[100:103], a[184:187], v[236:239]\n\t" \
"ds_read_b128 v[156:159], v248 offset:25600\n\t" \
"buffer_load_dwordx4 %[vg], %[rh], s95 offen offset:3072 lds\n\t" \
"v_mfma_f32_16x16x32_bf16 v[208:211], v[104:107], a[20:23], v[208:211]\n\t" \
"v_mfma_f32_16x16x32_bf16 v[212:215], v[108:111], a[20:23], v[212:215]\n\t" \
"ds_read_b128 v[160:163], v248 offset:12288\n\t" \
"v_mfma_f32_16x16x32_bf16 v[216:219], v[104:107], a[76:79], v[216:219]\n\t" \
"v_mfma_f32_16x16x32_bf16 v[220:223], v[108:111], a[76:79], v[220:223]\n\t" \
"ds_read_b128 v[164:167], v248 offset:26624\n\t" \
"v_mfma_f32_16x16x32_bf16 v[224:227], v[104:107], a[132:135], v[224:227]\n\t" \
"v_mfma_f32_16x16x32_bf16 v[228:231], v[108:111], a[132:135], v[228:231]\n\t" \
"ds_read_b128 v[168:171], v248 offset:13312\n\t" \
"v_mfma_f32_16x16x32_bf16 v[232:235], v[104:107], a[188:191], v[232:235]\n\t" \
"v_mfma_f32_16x16x32_bf16 v[236:239], v[108:111], a[188:191], v[236:239]\n\t" \
"ds_read_b128 v[172:175], v248 offset:27648\n\t" \
"v_mfma_f32_16x16x32_bf16 v[208:211], v[112:115], a[24:27], v[208:211]\n\t" \
"v_mfma_f32_16x16x32_bf16 v[212:215], v[116:119], a[24:27], v[212:215]\n\t" \
"v_mfma_f32_16x16x32_bf16 v[216:219], v[112:115], a[80:83], v[216:219]\n\t" \
"v_mfma_f32_16x16x32_bf16 v[220:223], v[116:119], a[80:83], v[220:223]\n\t" \
"v_mfma_f32_16x16x32_bf16 v[224:227], v[112:115], a[136:139], v[224:227]\n\t" \
"v_mfma_f32_16x16x32_bf16 v[228:231], v[116:119], a[136:139], v[228:231]\n\t" \
"v_mfma_f32_16x16x32_bf16 v[232:235], v[112:115], a[192:195], v[232:235]\n\t" \
"v_mfma_f32_16x16x32_bf16 v[236:239], v[116:119], a[192:195], v[236:239]\n\t" \
"s_add_u32 s89, s89, 57344\n\t" \
"s_min_u32 s89, s89, 1806336\n\t" \
"s_mov_b32 s87, s92\n\t" \
"s_add_u32 s95, s92, 28672\n\t" \
"s_cmp_eq_u32 s95, 114688\n\t" \
"s_cselect_b32 s95, 0, s95\n\t" \
"v_add_u32 v249, s95, %[lr]\n\t" \
"s_waitcnt vmcnt(8) lgkmcnt(0)\n\t" \
"s_barrier\n\t" \
"s_mov_b64 exec, 1\n\t" \
"buffer_atomic_add v250, off, %[rc], s94\n\t" \
"s_mov_b64 exec, -1\n\t" \
"s_add_u32 s94, s94, 128\n\t" \
"v_mfma_f32_16x16x32_bf16 v[208:211], v[120:123], a[28:31], v[208:211]\n\t" \
"ds_read_b128 v[64:67], v249 offset:0\n\t" \
"v_mfma_f32_16x16x32_bf16 v[212:215], v[124:127], a[28:31], v[212:215]\n\t" \
"v_mfma_f32_16x16x32_bf16 v[216:219], v[120:123], a[84:87], v[216:219]\n\t" \
"ds_read_b128 v[68:71], v249 offset:14336\n\t" \
"v_mfma_f32_16x16x32_bf16 v[220:223], v[124:127], a[84:87], v[220:223]\n\t" \
"v_mfma_f32_16x16x32_bf16 v[224:227], v[120:123], a[140:143], v[224:227]\n\t" \
"ds_read_b128 v[72:75], v249 offset:1024\n\t" \
"v_mfma_f32_16x16x32_bf16 v[228:231], v[124:127], a[140:143], v[228:231]\n\t" \
"v_mfma_f32_16x16x32_bf16 v[232:235], v[120:123], a[196:199], v[232:235]\n\t" \
"ds_read_b128 v[76:79], v249 offset:15360\n\t" \
"v_mfma_f32_16x16x32_bf16 v[236:239], v[124:127], a[196:199], v[236:239]\n\t" \
"v_mfma_f32_16x16x32_bf16 v[208:211], v[128:131], a[32:35], v[208:211]\n\t" \
"ds_read_b128 v[80:83], v249 offset:2048\n\t" \
"v_mfma_f32_16x16x32_bf16 v[212:215], v[132:135], a[32:35], v[212:215]\n\t" \
"v_mfma_f32_16x16x32_bf16 v[216:219], v[128:131], a[88:91], v[216:219]\n\t" \
"ds_read_b128 v[84:87], v249 offset:16384\n\t" \
"v_mfma_f32_16x16x32_bf16 v[220:223], v[132:135], a[88:91], v[220:223]\n\t" \
"v_mfma_f32_16x16x32_bf16 v[224:227], v[128:131], a[144:147], v[224:227]\n\t" \
"ds_read_b128 v[88:91], v249 offset:3072\n\t" \
"v_mfma_f32_16x16x32_bf16 v[228:231], v[132:135], a[144:147], v[228:231]\n\t" \
"v_mfma_f32_16x16x32_bf16 v[232:235], v[128:131], a[200:203], v[232:235]\n\t" \
"ds_read_b128 v[92:95], v249 offset:17408\n\t" \
"v_mfma_f32_16x16x32_bf16 v[236:239], v[132:135], a[200:203], v[236:239]\n\t" \
"v_mfma_f32_16x16x32_bf16 v[208:211], v[136:139], a[36:39], v[208:211]\n\t" \
"ds_read_b128 v[96:99], v249 offset:4096\n\t" \
"v_mfma_f32_16x16x32_bf16 v[212:215], v[140:143], a[36:39], v[212:215]\n\t" \
"v_mfma_f32_16x16x32_bf16 v[216:219], v[136:139], a[92:95], v[216:219]\n\t" \
"ds_read_b128 v[100:103], v249 offset:18432\n\t" \
"v_mfma_f32_16x16x32_bf16 v[220:223], v[140:143], a[92:95], v[220:223]\n\t" \
"s_add_u32 s90, s87, %[lg]\n\t" \
"s_add_u32 s90, s90, 0\n\t" \
"s_mov_b32 m0, s90\n\t" \
"s_add_u32 s95, s89, 0\n\t" \
"buffer_load_dwordx4 %[vg], %[rh], s95 offen offset:0 lds\n\t" \
"v_mfma_f32_16x16x32_bf16 v[224:227], v[136:139], a[148:151], v[224:227]\n\t" \
"ds_read_b128 v[104:107], v249 offset:5120\n\t" \
"v_mfma_f32_16x16x32_bf16 v[228:231], v[140:143], a[148:151], v[228:231]\n\t" \
"v_mfma_f32_16x16x32_bf16 v[232:235], v[136:139], a[204:207], v[232:235]\n\t" \
"ds_read_b128 v[108:111], v249 offset:19456\n\t" \
"v_mfma_f32_16x16x32_bf16 v[236:239], v[140:143], a[204:207], v[236:239]\n\t" \
"v_mfma_f32_16x16x32_bf16 v[208:211], v[144:147], a[40:43], v[208:211]\n\t" \
"ds_read_b128 v[112:115], v249 offset:6144\n\t" \
"v_mfma_f32_16x16x32_bf16 v[212:215], v[148:151], a[40:43], v[212:215]\n\t" \
"v_mfma_f32_16x16x32_bf16 v[216:219], v[144:147], a[96:99], v[216:219]\n\t" \
"ds_read_b128 v[116:119], v249 offset:20480\n\t" \
"v_mfma_f32_16x16x32_bf16 v[220:223], v[148:151], a[96:99], v[220:223]\n\t" \
"v_mfma_f32_16x16x32_bf16 v[224:227], v[144:147], a[152:155], v[224:227]\n\t" \
"v_mfma_f32_16x16x32_bf16 v[228:231], v[148:151], a[152:155], v[228:231]\n\t" \
"v_mfma_f32_16x16x32_bf16 v[232:235], v[144:147], a[208:211], v[232:235]\n\t" \
"v_mfma_f32_16x16x32_bf16 v[236:239], v[148:151], a[208:211], v[236:239]\n\t" \
"v_mfma_f32_16x16x32_bf16 v[208:211], v[152:155], a[44:47], v[208:211]\n\t" \
"v_mfma_f32_16x16x32_bf16 v[212:215], v[156:159], a[44:47], v[212:215]\n\t" \
"v_mfma_f32_16x16x32_bf16 v[216:219], v[152:155], a[100:103], v[216:219]\n\t" \
"v_mfma_f32_16x16x32_bf16 v[220:223], v[156:159], a[100:103], v[220:223]\n\t" \
"v_mfma_f32_16x16x32_bf16 v[224:227], v[152:155], a[156:159], v[224:227]\n\t" \
"v_mfma_f32_16x16x32_bf16 v[228:231], v[156:159], a[156:159], v[228:231]\n\t" \
"v_mfma_f32_16x16x32_bf16 v[232:235], v[152:155], a[212:215], v[232:235]\n\t" \
"v_mfma_f32_16x16x32_bf16 v[236:239], v[156:159], a[212:215], v[236:239]\n\t" \
"buffer_load_dwordx4 %[vg], %[rh], s95 offen offset:1024 lds\n\t" \
"v_mfma_f32_16x16x32_bf16 v[208:211], v[160:163], a[48:51], v[208:211]\n\t" \
"v_mfma_f32_16x16x32_bf16 v[212:215], v[164:167], a[48:51], v[212:215]\n\t" \
"v_mfma_f32_16x16x32_bf16 v[216:219], v[160:163], a[104:107], v[216:219]\n\t" \
"v_mfma_f32_16x16x32_bf16 v[220:223], v[164:167], a[104:107], v[220:223]\n\t" \
"v_mfma_f32_16x16x32_bf16 v[224:227], v[160:163], a[160:163], v[224:227]\n\t" \
"v_mfma_f32_16x16x32_bf16 v[228:231], v[164:167], a[160:163], v[228:231]\n\t" \
"v_mfma_f32_16x16x32_bf16 v[232:235], v[160:163], a[216:219], v[232:235]\n\t" \
"v_mfma_f32_16x16x32_bf16 v[236:239], v[164:167], a[216:219], v[236:239]\n\t" \
"v_mfma_f32_16x16x32_bf16 v[208:211], v[168:171], a[52:55], v[208:211]\n\t" \
"v_mfma_f32_16x16x32_bf16 v[212:215], v[172:175], a[52:55], v[212:215]\n\t" \
"v_mfma_f32_16x16x32_bf16 v[216:219], v[168:171], a[108:111], v[216:219]\n\t" \
"v_mfma_f32_16x16x32_bf16 v[220:223], v[172:175], a[108:111], v[220:223]\n\t" \
"v_mfma_f32_16x16x32_bf16 v[224:227], v[168:171], a[164:167], v[224:227]\n\t" \
"v_mfma_f32_16x16x32_bf16 v[228:231], v[172:175], a[164:167], v[228:231]\n\t" \
"v_mfma_f32_16x16x32_bf16 v[232:235], v[168:171], a[220:223], v[232:235]\n\t" \
"v_mfma_f32_16x16x32_bf16 v[236:239], v[172:175], a[220:223], v[236:239]\n\t" \
"s_add_u32 s92, s92, 28672\n\t" \
"s_cmp_eq_u32 s92, 114688\n\t" \
"s_cselect_b32 s92, 0, s92\n\t" \
"v_mov_b32 v248, v249\n\t" \
"s_add_u32 s91, s91, 917504\n\t" \
"s_mov_b32 s88, 4\n\t" \
"s_nop 1\n\t" \
"Lp3%=:\n\t" \
"s_waitcnt lgkmcnt(0)\n\t" \
"v_mfma_f32_16x16x32_bf16 v[176:179], v[64:67], a[0:3], 0\n\t" \
"v_mfma_f32_16x16x32_bf16 v[180:183], v[68:71], a[0:3], 0\n\t" \
"ds_read_b128 v[120:123], v248 offset:7168\n\t" \
"v_mfma_f32_16x16x32_bf16 v[184:187], v[64:67], a[56:59], 0\n\t" \
"v_cvt_pk_bf16_f32 v240, v208, v209\n\t" \
"v_mfma_f32_16x16x32_bf16 v[188:191], v[68:71], a[56:59], 0\n\t" \
"v_cvt_pk_bf16_f32 v241, v210, v211\n\t" \
"v_mfma_f32_16x16x32_bf16 v[192:195], v[64:67], a[112:115], 0\n\t" \
"v_cvt_pk_bf16_f32 v242, v212, v213\n\t" \
"v_mfma_f32_16x16x32_bf16 v[196:199], v[68:71], a[112:115], 0\n\t" \
"v_cvt_pk_bf16_f32 v243, v214, v215\n\t" \
"v_mfma_f32_16x16x32_bf16 v[200:203], v[64:67], a[168:171], 0\n\t" \
"buffer_store_dwordx4 v[240:243], %[vs0], %[ro], s91 offen offset:0 sc1\n\t" \
"v_mfma_f32_16x16x32_bf16 v[204:207], v[68:71], a[168:171], 0\n\t" \
"v_cvt_pk_bf16_f32 v244, v216, v217\n\t" \
"v_mfma_f32_16x16x32_bf16 v[176:179], v[72:75], a[4:7], v[176:179]\n\t" \
"v_cvt_pk_bf16_f32 v245, v218, v219\n\t" \
"v_mfma_f32_16x16x32_bf16 v[180:183], v[76:79], a[4:7], v[180:183]\n\t" \
"v_cvt_pk_bf16_f32 v246, v220, v221\n\t" \
"v_mfma_f32_16x16x32_bf16 v[184:187], v[72:75], a[60:63], v[184:187]\n\t" \
"v_cvt_pk_bf16_f32 v247, v222, v223\n\t" \
"v_mfma_f32_16x16x32_bf16 v[188:191], v[76:79], a[60:63], v[188:191]\n\t" \
"buffer_store_dwordx4 v[244:247], %[vs0], %[ro], s91 offen offset:1024 sc1\n\t" \
"v_mfma_f32_16x16x32_bf16 v[192:195], v[72:75], a[116:119], v[192:195]\n\t" \
"v_cvt_pk_bf16_f32 v240, v224, v225\n\t" \
"v_mfma_f32_16x16x32_bf16 v[196:199], v[76:79], a[116:119], v[196:199]\n\t" \
"v_cvt_pk_bf16_f32 v241, v226, v227\n\t" \
"v_mfma_f32_16x16x32_bf16 v[200:203], v[72:75], a[172:175], v[200:203]\n\t" \
"v_cvt_pk_bf16_f32 v242, v228, v229\n\t" \
"v_mfma_f32_16x16x32_bf16 v[204:207], v[76:79], a[172:175], v[204:207]\n\t" \
"v_cvt_pk_bf16_f32 v243, v230, v231\n\t" \
"v_mfma_f32_16x16x32_bf16 v[176:179], v[80:83], a[8:11], v[176:179]\n\t" \
"buffer_store_dwordx4 v[240:243], %[vs0], %[ro], s91 offen offset:2048 sc1\n\t" \
"v_mfma_f32_16x16x32_bf16 v[180:183], v[84:87], a[8:11], v[180:183]\n\t" \
"v_cvt_pk_bf16_f32 v244, v232, v233\n\t" \
"v_mfma_f32_16x16x32_bf16 v[184:187], v[80:83], a[64:67], v[184:187]\n\t" \
"v_cvt_pk_bf16_f32 v245, v234, v235\n\t" \
"v_mfma_f32_16x16x32_bf16 v[188:191], v[84:87], a[64:67], v[188:191]\n\t" \
"v_cvt_pk_bf16_f32 v246, v236, v237\n\t" \
"s_add_u32 s90, s87, %[lg]\n\t" \
"s_add_u32 s90, s90, 0\n\t" \
"s_mov_b32 m0, s90\n\t" \
"s_add_u32 s95, s89, 0\n\t" \
"buffer_load_dwordx4 %[vg], %[rh], s95 offen offset:2048 lds\n\t" \
"v_mfma_f32_16x16x32_bf16 v[192:195], v[80:83], a[120:123], v[192:195]\n\t" \
"v_cvt_pk_bf16_f32 v247, v238, v239\n\t" \
"v_mfma_f32_16x16x32_bf16 v[196:199], v[84:87], a[120:123], v[196:199]\n\t" \
"buffer_store_dwordx4 v[244:247], %[vs0], %[ro], s91 offen offset:3072 sc1\n\t" \
"v_mfma_f32_16x16x32_bf16 v[200:203], v[80:83], a[176:179], v[200:203]\n\t" \
"v_mfma_f32_16x16x32_bf16 v[204:207], v[84:87], a[176:179], v[204:207]\n\t" \
"ds_read_b128 v[124:127], v248 offset:21504\n\t" \
"v_mfma_f32_16x16x32_bf16 v[176:179], v[88:91], a[12:15], v[176:179]\n\t" \
"v_mfma_f32_16x16x32_bf16 v[180:183], v[92:95], a[12:15], v[180:183]\n\t" \
"ds_read_b128 v[128:131], v248 offset:8192\n\t" \
"v_mfma_f32_16x16x32_bf16 v[184:187], v[88:91], a[68:71], v[184:187]\n\t" \
"v_mfma_f32_16x16x32_bf16 v[188:191], v[92:95], a[68:71], v[188:191]\n\t" \
"ds_read_b128 v[132:135], v248 offset:22528\n\t" \
"v_mfma_f32_16x16x32_bf16 v[192:195], v[88:91], a[124:127], v[192:195]\n\t" \
"v_mfma_f32_16x16x32_bf16 v[196:199], v[92:95], a[124:127], v[196:199]\n\t" \
"ds_read_b128 v[136:139], v248 offset:9216\n\t" \
"v_mfma_f32_16x16x32_bf16 v[200:203], v[88:91], a[180:183], v[200:203]\n\t" \
"v_mfma_f32_16x16x32_bf16 v[204:207], v[92:95], a[180:183], v[204:207]\n\t" \
"ds_read_b128 v[140:143], v248 offset:23552\n\t" \
"v_mfma_f32_16x16x32_bf16 v[176:179], v[96:99], a[16:19], v[176:179]\n\t" \
"v_mfma_f32_16x16x32_bf16 v[180:183], v[100:103], a[16:19], v[180:183]\n\t" \
"ds_read_b128 v[144:147], v248 offset:10240\n\t" \
"v_mfma_f32_16x16x32_bf16 v[184:187], v[96:99], a[72:75], v[184:187]\n\t" \
"v_mfma_f32_16x16x32_bf16 v[188:191], v[100:103], a[72:75], v[188:191]\n\t" \
"ds_read_b128 v[148:151], v248 offset:24576\n\t" \
"v_mfma_f32_16x16x32_bf16 v[192:195], v[96:99], a[128:131], v[192:195]\n\t" \
"v_mfma_f32_16x16x32_bf16 v[196:199], v[100:103], a[128:131], v[196:199]\n\t" \
"ds_read_b128 v[152:155], v248 offset:11264\n\t" \
"v_mfma_f32_16x16x32_bf16 v[200:203], v[96:99], a[184:187], v[200:203]\n\t" \
"v_mfma_f32_16x16x32_bf16 v[204:207], v[100:103], a[184:187], v[204:207]\n\t" \
"ds_read_b128 v[156:159], v248 offset:25600\n\t" \
"buffer_load_dwordx4 %[vg], %[rh], s95 offen offset:3072 lds\n\t" \
"v_mfma_f32_16x16x32_bf16 v[176:179], v[104:107], a[20:23], v[176:179]\n\t" \
"v_mfma_f32_16x16x32_bf16 v[180:183], v[108:111], a[20:23], v[180:183]\n\t" \
"ds_read_b128 v[160:163], v248 offset:12288\n\t" \
"v_mfma_f32_16x16x32_bf16 v[184:187], v[104:107], a[76:79], v[184:187]\n\t" \
"v_mfma_f32_16x16x32_bf16 v[188:191], v[108:111], a[76:79], v[188:191]\n\t" \
"ds_read_b128 v[164:167], v248 offset:26624\n\t" \
"v_mfma_f32_16x16x32_bf16 v[192:195], v[104:107], a[132:135], v[192:195]\n\t" \
"v_mfma_f32_16x16x32_bf16 v[196:199], v[108:111], a[132:135], v[196:199]\n\t" \
"ds_read_b128 v[168:171], v248 offset:13312\n\t" \
"v_mfma_f32_16x16x32_bf16 v[200:203], v[104:107], a[188:191], v[200:203]\n\t" \
"v_mfma_f32_16x16x32_bf16 v[204:207], v[108:111], a[188:191], v[204:207]\n\t" \
"ds_read_b128 v[172:175], v248 offset:27648\n\t" \
"v_mfma_f32_16x16x32_bf16 v[176:179], v[112:115], a[24:27], v[176:179]\n\t" \
"v_mfma_f32_16x16x32_bf16 v[180:183], v[116:119], a[24:27], v[180:183]\n\t" \
"v_mfma_f32_16x16x32_bf16 v[184:187], v[112:115], a[80:83], v[184:187]\n\t" \
"v_mfma_f32_16x16x32_bf16 v[188:191], v[116:119], a[80:83], v[188:191]\n\t" \
"v_mfma_f32_16x16x32_bf16 v[192:195], v[112:115], a[136:139], v[192:195]\n\t" \
"v_mfma_f32_16x16x32_bf16 v[196:199], v[116:119], a[136:139], v[196:199]\n\t" \
"v_mfma_f32_16x16x32_bf16 v[200:203], v[112:115], a[192:195], v[200:203]\n\t" \
"v_mfma_f32_16x16x32_bf16 v[204:207], v[116:119], a[192:195], v[204:207]\n\t" \
"s_add_u32 s89, s89, 57344\n\t" \
"s_min_u32 s89, s89, 1806336\n\t" \
"s_mov_b32 s87, s92\n\t" \
"s_add_u32 s95, s92, 28672\n\t" \
"s_cmp_eq_u32 s95, 114688\n\t" \
"s_cselect_b32 s95, 0, s95\n\t" \
"v_add_u32 v249, s95, %[lr]\n\t" \
"s_waitcnt vmcnt(8) lgkmcnt(0)\n\t" \
"s_barrier\n\t" \
"s_mov_b64 exec, 1\n\t" \
"buffer_atomic_add v250, off, %[rc], s94\n\t" \
"s_mov_b64 exec, -1\n\t" \
"s_add_u32 s94, s94, 128\n\t" \
"v_mfma_f32_16x16x32_bf16 v[176:179], v[120:123], a[28:31], v[176:179]\n\t" \
"ds_read_b128 v[64:67], v249 offset:0\n\t" \
"v_mfma_f32_16x16x32_bf16 v[180:183], v[124:127], a[28:31], v[180:183]\n\t" \
"v_mfma_f32_16x16x32_bf16 v[184:187], v[120:123], a[84:87], v[184:187]\n\t" \
"ds_read_b128 v[68:71], v249 offset:14336\n\t" \
"v_mfma_f32_16x16x32_bf16 v[188:191], v[124:127], a[84:87], v[188:191]\n\t" \
"v_mfma_f32_16x16x32_bf16 v[192:195], v[120:123], a[140:143], v[192:195]\n\t" \
"ds_read_b128 v[72:75], v249 offset:1024\n\t" \
"v_mfma_f32_16x16x32_bf16 v[196:199], v[124:127], a[140:143], v[196:199]\n\t" \
"v_mfma_f32_16x16x32_bf16 v[200:203], v[120:123], a[196:199], v[200:203]\n\t" \
"ds_read_b128 v[76:79], v249 offset:15360\n\t" \
"v_mfma_f32_16x16x32_bf16 v[204:207], v[124:127], a[196:199], v[204:207]\n\t" \
"v_mfma_f32_16x16x32_bf16 v[176:179], v[128:131], a[32:35], v[176:179]\n\t" \
"ds_read_b128 v[80:83], v249 offset:2048\n\t" \
"v_mfma_f32_16x16x32_bf16 v[180:183], v[132:135], a[32:35], v[180:183]\n\t" \
"v_mfma_f32_16x16x32_bf16 v[184:187], v[128:131], a[88:91], v[184:187]\n\t" \
"ds_read_b128 v[84:87], v249 offset:16384\n\t" \
"v_mfma_f32_16x16x32_bf16 v[188:191], v[132:135], a[88:91], v[188:191]\n\t" \
"v_mfma_f32_16x16x32_bf16 v[192:195], v[128:131], a[144:147], v[192:195]\n\t" \
"ds_read_b128 v[88:91], v249 offset:3072\n\t" \
"v_mfma_f32_16x16x32_bf16 v[196:199], v[132:135], a[144:147], v[196:199]\n\t" \
"v_mfma_f32_16x16x32_bf16 v[200:203], v[128:131], a[200:203], v[200:203]\n\t" \
"ds_read_b128 v[92:95], v249 offset:17408\n\t" \
"v_mfma_f32_16x16x32_bf16 v[204:207], v[132:135], a[200:203], v[204:207]\n\t" \
"v_mfma_f32_16x16x32_bf16 v[176:179], v[136:139], a[36:39], v[176:179]\n\t" \
"ds_read_b128 v[96:99], v249 offset:4096\n\t" \
"v_mfma_f32_16x16x32_bf16 v[180:183], v[140:143], a[36:39], v[180:183]\n\t" \
"v_mfma_f32_16x16x32_bf16 v[184:187], v[136:139], a[92:95], v[184:187]\n\t" \
"ds_read_b128 v[100:103], v249 offset:18432\n\t" \
"v_mfma_f32_16x16x32_bf16 v[188:191], v[140:143], a[92:95], v[188:191]\n\t" \
"s_add_u32 s90, s87, %[lg]\n\t" \
"s_add_u32 s90, s90, 0\n\t" \
"s_mov_b32 m0, s90\n\t" \
"s_add_u32 s95, s89, 0\n\t" \
"buffer_load_dwordx4 %[vg], %[rh], s95 offen offset:0 lds\n\t" \
"v_mfma_f32_16x16x32_bf16 v[192:195], v[136:139], a[148:151], v[192:195]\n\t" \
"ds_read_b128 v[104:107], v249 offset:5120\n\t" \
"v_mfma_f32_16x16x32_bf16 v[196:199], v[140:143], a[148:151], v[196:199]\n\t" \
"v_mfma_f32_16x16x32_bf16 v[200:203], v[136:139], a[204:207], v[200:203]\n\t" \
"ds_read_b128 v[108:111], v249 offset:19456\n\t" \
"v_mfma_f32_16x16x32_bf16 v[204:207], v[140:143], a[204:207], v[204:207]\n\t" \
"v_mfma_f32_16x16x32_bf16 v[176:179], v[144:147], a[40:43], v[176:179]\n\t" \
"ds_read_b128 v[112:115], v249 offset:6144\n\t" \
"v_mfma_f32_16x16x32_bf16 v[180:183], v[148:151], a[40:43], v[180:183]\n\t" \
"v_mfma_f32_16x16x32_bf16 v[184:187], v[144:147], a[96:99], v[184:187]\n\t" \
"ds_read_b128 v[116:119], v249 offset:20480\n\t" \
"v_mfma_f32_16x16x32_bf16 v[188:191], v[148:151], a[96:99], v[188:191]\n\t" \
"v_mfma_f32_16x16x32_bf16 v[192:195], v[144:147], a[152:155], v[192:195]\n\t" \
"v_mfma_f32_16x16x32_bf16 v[196:199], v[148:151], a[152:155], v[196:199]\n\t" \
"v_mfma_f32_16x16x32_bf16 v[200:203], v[144:147], a[208:211], v[200:203]\n\t" \
"v_mfma_f32_16x16x32_bf16 v[204:207], v[148:151], a[208:211], v[204:207]\n\t" \
"v_mfma_f32_16x16x32_bf16 v[176:179], v[152:155], a[44:47], v[176:179]\n\t" \
"v_mfma_f32_16x16x32_bf16 v[180:183], v[156:159], a[44:47], v[180:183]\n\t" \
"v_mfma_f32_16x16x32_bf16 v[184:187], v[152:155], a[100:103], v[184:187]\n\t" \
"v_mfma_f32_16x16x32_bf16 v[188:191], v[156:159], a[100:103], v[188:191]\n\t" \
"v_mfma_f32_16x16x32_bf16 v[192:195], v[152:155], a[156:159], v[192:195]\n\t" \
"v_mfma_f32_16x16x32_bf16 v[196:199], v[156:159], a[156:159], v[196:199]\n\t" \
"v_mfma_f32_16x16x32_bf16 v[200:203], v[152:155], a[212:215], v[200:203]\n\t" \
"v_mfma_f32_16x16x32_bf16 v[204:207], v[156:159], a[212:215], v[204:207]\n\t" \
"buffer_load_dwordx4 %[vg], %[rh], s95 offen offset:1024 lds\n\t" \
"v_mfma_f32_16x16x32_bf16 v[176:179], v[160:163], a[48:51], v[176:179]\n\t" \
"v_mfma_f32_16x16x32_bf16 v[180:183], v[164:167], a[48:51], v[180:183]\n\t" \
"v_mfma_f32_16x16x32_bf16 v[184:187], v[160:163], a[104:107], v[184:187]\n\t" \
"v_mfma_f32_16x16x32_bf16 v[188:191], v[164:167], a[104:107], v[188:191]\n\t" \
"v_mfma_f32_16x16x32_bf16 v[192:195], v[160:163], a[160:163], v[192:195]\n\t" \
"v_mfma_f32_16x16x32_bf16 v[196:199], v[164:167], a[160:163], v[196:199]\n\t" \
"v_mfma_f32_16x16x32_bf16 v[200:203], v[160:163], a[216:219], v[200:203]\n\t" \
"v_mfma_f32_16x16x32_bf16 v[204:207], v[164:167], a[216:219], v[204:207]\n\t" \
"v_mfma_f32_16x16x32_bf16 v[176:179], v[168:171], a[52:55], v[176:179]\n\t" \
"v_mfma_f32_16x16x32_bf16 v[180:183], v[172:175], a[52:55], v[180:183]\n\t" \
"v_mfma_f32_16x16x32_bf16 v[184:187], v[168:171], a[108:111], v[184:187]\n\t" \
"v_mfma_f32_16x16x32_bf16 v[188:191], v[172:175], a[108:111], v[188:191]\n\t" \
"v_mfma_f32_16x16x32_bf16 v[192:195], v[168:171], a[164:167], v[192:195]\n\t" \
"v_mfma_f32_16x16x32_bf16 v[196:199], v[172:175], a[164:167], v[196:199]\n\t" \
"v_mfma_f32_16x16x32_bf16 v[200:203], v[168:171], a[220:223], v[200:203]\n\t" \
"v_mfma_f32_16x16x32_bf16 v[204:207], v[172:175], a[220:223], v[204:207]\n\t" \
"s_add_u32 s92, s92, 28672\n\t" \
"s_cmp_eq_u32 s92, 114688\n\t" \
"s_cselect_b32 s92, 0, s92\n\t" \
"v_mov_b32 v248, v249\n\t" \
"s_add_u32 s91, s91, 917504\n\t" \
"s_waitcnt lgkmcnt(0)\n\t" \
"v_mfma_f32_16x16x32_bf16 v[208:211], v[64:67], a[0:3], 0\n\t" \
"v_mfma_f32_16x16x32_bf16 v[212:215], v[68:71], a[0:3], 0\n\t" \
"ds_read_b128 v[120:123], v248 offset:7168\n\t" \
"v_mfma_f32_16x16x32_bf16 v[216:219], v[64:67], a[56:59], 0\n\t" \
"v_cvt_pk_bf16_f32 v240, v176, v177\n\t" \
"v_mfma_f32_16x16x32_bf16 v[220:223], v[68:71], a[56:59], 0\n\t" \
"v_cvt_pk_bf16_f32 v241, v178, v179\n\t" \
"v_mfma_f32_16x16x32_bf16 v[224:227], v[64:67], a[112:115], 0\n\t" \
"v_cvt_pk_bf16_f32 v242, v180, v181\n\t" \
"v_mfma_f32_16x16x32_bf16 v[228:231], v[68:71], a[112:115], 0\n\t" \
"v_cvt_pk_bf16_f32 v243, v182, v183\n\t" \
"v_mfma_f32_16x16x32_bf16 v[232:235], v[64:67], a[168:171], 0\n\t" \
"buffer_store_dwordx4 v[240:243], %[vs0], %[ro], s91 offen offset:0 sc1\n\t" \
"v_mfma_f32_16x16x32_bf16 v[236:239], v[68:71], a[168:171], 0\n\t" \
"v_cvt_pk_bf16_f32 v244, v184, v185\n\t" \
"v_mfma_f32_16x16x32_bf16 v[208:211], v[72:75], a[4:7], v[208:211]\n\t" \
"v_cvt_pk_bf16_f32 v245, v186, v187\n\t" \
"v_mfma_f32_16x16x32_bf16 v[212:215], v[76:79], a[4:7], v[212:215]\n\t" \
"v_cvt_pk_bf16_f32 v246, v188, v189\n\t" \
"v_mfma_f32_16x16x32_bf16 v[216:219], v[72:75], a[60:63], v[216:219]\n\t" \
"v_cvt_pk_bf16_f32 v247, v190, v191\n\t" \
"v_mfma_f32_16x16x32_bf16 v[220:223], v[76:79], a[60:63], v[220:223]\n\t" \
"buffer_store_dwordx4 v[244:247], %[vs0], %[ro], s91 offen offset:1024 sc1\n\t" \
"v_mfma_f32_16x16x32_bf16 v[224:227], v[72:75], a[116:119], v[224:227]\n\t" \
"v_cvt_pk_bf16_f32 v240, v192, v193\n\t" \
"v_mfma_f32_16x16x32_bf16 v[228:231], v[76:79], a[116:119], v[228:231]\n\t" \
"v_cvt_pk_bf16_f32 v241, v194, v195\n\t" \
"v_mfma_f32_16x16x32_bf16 v[232:235], v[72:75], a[172:175], v[232:235]\n\t" \
"v_cvt_pk_bf16_f32 v242, v196, v197\n\t" \
"v_mfma_f32_16x16x32_bf16 v[236:239], v[76:79], a[172:175], v[236:239]\n\t" \
"v_cvt_pk_bf16_f32 v243, v198, v199\n\t" \
"v_mfma_f32_16x16x32_bf16 v[208:211], v[80:83], a[8:11], v[208:211]\n\t" \
"buffer_store_dwordx4 v[240:243], %[vs0], %[ro], s91 offen offset:2048 sc1\n\t" \
"v_mfma_f32_16x16x32_bf16 v[212:215], v[84:87], a[8:11], v[212:215]\n\t" \
"v_cvt_pk_bf16_f32 v244, v200, v201\n\t" \
"v_mfma_f32_16x16x32_bf16 v[216:219], v[80:83], a[64:67], v[216:219]\n\t" \
"v_cvt_pk_bf16_f32 v245, v202, v203\n\t" \
"v_mfma_f32_16x16x32_bf16 v[220:223], v[84:87], a[64:67], v[220:223]\n\t" \
"v_cvt_pk_bf16_f32 v246, v204, v205\n\t" \
"s_add_u32 s90, s87, %[lg]\n\t" \
"s_add_u32 s90, s90, 0\n\t" \
"s_mov_b32 m0, s90\n\t" \
"s_add_u32 s95, s89, 0\n\t" \
"buffer_load_dwordx4 %[vg], %[rh], s95 offen offset:2048 lds\n\t" \
"v_mfma_f32_16x16x32_bf16 v[224:227], v[80:83], a[120:123], v[224:227]\n\t" \
"v_cvt_pk_bf16_f32 v247, v206, v207\n\t" \
"v_mfma_f32_16x16x32_bf16 v[228:231], v[84:87], a[120:123], v[228:231]\n\t" \
"buffer_store_dwordx4 v[244:247], %[vs0], %[ro], s91 offen offset:3072 sc1\n\t" \
"v_mfma_f32_16x16x32_bf16 v[232:235], v[80:83], a[176:179], v[232:235]\n\t" \
"v_mfma_f32_16x16x32_bf16 v[236:239], v[84:87], a[176:179], v[236:239]\n\t" \
"ds_read_b128 v[124:127], v248 offset:21504\n\t" \
"v_mfma_f32_16x16x32_bf16 v[208:211], v[88:91], a[12:15], v[208:211]\n\t" \
"v_mfma_f32_16x16x32_bf16 v[212:215], v[92:95], a[12:15], v[212:215]\n\t" \
"ds_read_b128 v[128:131], v248 offset:8192\n\t" \
"v_mfma_f32_16x16x32_bf16 v[216:219], v[88:91], a[68:71], v[216:219]\n\t" \
"v_mfma_f32_16x16x32_bf16 v[220:223], v[92:95], a[68:71], v[220:223]\n\t" \
"ds_read_b128 v[132:135], v248 offset:22528\n\t" \
"v_mfma_f32_16x16x32_bf16 v[224:227], v[88:91], a[124:127], v[224:227]\n\t" \
"v_mfma_f32_16x16x32_bf16 v[228:231], v[92:95], a[124:127], v[228:231]\n\t" \
"ds_read_b128 v[136:139], v248 offset:9216\n\t" \
"v_mfma_f32_16x16x32_bf16 v[232:235], v[88:91], a[180:183], v[232:235]\n\t" \
"v_mfma_f32_16x16x32_bf16 v[236:239], v[92:95], a[180:183], v[236:239]\n\t" \
"ds_read_b128 v[140:143], v248 offset:23552\n\t" \
"v_mfma_f32_16x16x32_bf16 v[208:211], v[96:99], a[16:19], v[208:211]\n\t" \
"v_mfma_f32_16x16x32_bf16 v[212:215], v[100:103], a[16:19], v[212:215]\n\t" \
"ds_read_b128 v[144:147], v248 offset:10240\n\t" \
"v_mfma_f32_16x16x32_bf16 v[216:219], v[96:99], a[72:75], v[216:219]\n\t" \
"v_mfma_f32_16x16x32_bf16 v[220:223], v[100:103], a[72:75], v[220:223]\n\t" \
"ds_read_b128 v[148:151], v248 offset:24576\n\t" \
"v_mfma_f32_16x16x32_bf16 v[224:227], v[96:99], a[128:131], v[224:227]\n\t" \
"v_mfma_f32_16x16x32_bf16 v[228:231], v[100:103], a[128:131], v[228:231]\n\t" \
"ds_read_b128 v[152:155], v248 offset:11264\n\t" \
"v_mfma_f32_16x16x32_bf16 v[232:235], v[96:99], a[184:187], v[232:235]\n\t" \
"v_mfma_f32_16x16x32_bf16 v[236:239], v[100:103], a[184:187], v[236:239]\n\t" \
"ds_read_b128 v[156:159], v248 offset:25600\n\t" \
"buffer_load_dwordx4 %[vg], %[rh], s95 offen offset:3072 lds\n\t" \
"v_mfma_f32_16x16x32_bf16 v[208:211], v[104:107], a[20:23], v[208:211]\n\t" \
"v_mfma_f32_16x16x32_bf16 v[212:215], v[108:111], a[20:23], v[212:215]\n\t" \
"ds_read_b128 v[160:163], v248 offset:12288\n\t" \
"v_mfma_f32_16x16x32_bf16 v[216:219], v[104:107], a[76:79], v[216:219]\n\t" \
"v_mfma_f32_16x16x32_bf16 v[220:223], v[108:111], a[76:79], v[220:223]\n\t" \
"ds_read_b128 v[164:167], v248 offset:26624\n\t" \
"v_mfma_f32_16x16x32_bf16 v[224:227], v[104:107], a[132:135], v[224:227]\n\t" \
"v_mfma_f32_16x16x32_bf16 v[228:231], v[108:111], a[132:135], v[228:231]\n\t" \
"ds_read_b128 v[168:171], v248 offset:13312\n\t" \
"v_mfma_f32_16x16x32_bf16 v[232:235], v[104:107], a[188:191], v[232:235]\n\t" \
"v_mfma_f32_16x16x32_bf16 v[236:239], v[108:111], a[188:191], v[236:239]\n\t" \
"ds_read_b128 v[172:175], v248 offset:27648\n\t" \
"v_mfma_f32_16x16x32_bf16 v[208:211], v[112:115], a[24:27], v[208:211]\n\t" \
"v_mfma_f32_16x16x32_bf16 v[212:215], v[116:119], a[24:27], v[212:215]\n\t" \
"v_mfma_f32_16x16x32_bf16 v[216:219], v[112:115], a[80:83], v[216:219]\n\t" \
"v_mfma_f32_16x16x32_bf16 v[220:223], v[116:119], a[80:83], v[220:223]\n\t" \
"v_mfma_f32_16x16x32_bf16 v[224:227], v[112:115], a[136:139], v[224:227]\n\t" \
"v_mfma_f32_16x16x32_bf16 v[228:231], v[116:119], a[136:139], v[228:231]\n\t" \
"v_mfma_f32_16x16x32_bf16 v[232:235], v[112:115], a[192:195], v[232:235]\n\t" \
"v_mfma_f32_16x16x32_bf16 v[236:239], v[116:119], a[192:195], v[236:239]\n\t" \
"s_add_u32 s89, s89, 57344\n\t" \
"s_min_u32 s89, s89, 1806336\n\t" \
"s_mov_b32 s87, s92\n\t" \
"s_add_u32 s95, s92, 28672\n\t" \
"s_cmp_eq_u32 s95, 114688\n\t" \
"s_cselect_b32 s95, 0, s95\n\t" \
"v_add_u32 v249, s95, %[lr]\n\t" \
"s_waitcnt vmcnt(8) lgkmcnt(0)\n\t" \
"s_barrier\n\t" \
"s_mov_b64 exec, 1\n\t" \
"buffer_atomic_add v250, off, %[rc], s94\n\t" \
"s_mov_b64 exec, -1\n\t" \
"s_add_u32 s94, s94, 128\n\t" \
"v_mfma_f32_16x16x32_bf16 v[208:211], v[120:123], a[28:31], v[208:211]\n\t" \
"ds_read_b128 v[64:67], v249 offset:0\n\t" \
"v_mfma_f32_16x16x32_bf16 v[212:215], v[124:127], a[28:31], v[212:215]\n\t" \
"v_mfma_f32_16x16x32_bf16 v[216:219], v[120:123], a[84:87], v[216:219]\n\t" \
"ds_read_b128 v[68:71], v249 offset:14336\n\t" \
"v_mfma_f32_16x16x32_bf16 v[220:223], v[124:127], a[84:87], v[220:223]\n\t" \
"v_mfma_f32_16x16x32_bf16 v[224:227], v[120:123], a[140:143], v[224:227]\n\t" \
"ds_read_b128 v[72:75], v249 offset:1024\n\t" \
"v_mfma_f32_16x16x32_bf16 v[228:231], v[124:127], a[140:143], v[228:231]\n\t" \
"v_mfma_f32_16x16x32_bf16 v[232:235], v[120:123], a[196:199], v[232:235]\n\t" \
"ds_read_b128 v[76:79], v249 offset:15360\n\t" \
"v_mfma_f32_16x16x32_bf16 v[236:239], v[124:127], a[196:199], v[236:239]\n\t" \
"v_mfma_f32_16x16x32_bf16 v[208:211], v[128:131], a[32:35], v[208:211]\n\t" \
"ds_read_b128 v[80:83], v249 offset:2048\n\t" \
"v_mfma_f32_16x16x32_bf16 v[212:215], v[132:135], a[32:35], v[212:215]\n\t" \
"v_mfma_f32_16x16x32_bf16 v[216:219], v[128:131], a[88:91], v[216:219]\n\t" \
"ds_read_b128 v[84:87], v249 offset:16384\n\t" \
"v_mfma_f32_16x16x32_bf16 v[220:223], v[132:135], a[88:91], v[220:223]\n\t" \
"v_mfma_f32_16x16x32_bf16 v[224:227], v[128:131], a[144:147], v[224:227]\n\t" \
"ds_read_b128 v[88:91], v249 offset:3072\n\t" \
"v_mfma_f32_16x16x32_bf16 v[228:231], v[132:135], a[144:147], v[228:231]\n\t" \
"v_mfma_f32_16x16x32_bf16 v[232:235], v[128:131], a[200:203], v[232:235]\n\t" \
"ds_read_b128 v[92:95], v249 offset:17408\n\t" \
"v_mfma_f32_16x16x32_bf16 v[236:239], v[132:135], a[200:203], v[236:239]\n\t" \
"v_mfma_f32_16x16x32_bf16 v[208:211], v[136:139], a[36:39], v[208:211]\n\t" \
"ds_read_b128 v[96:99], v249 offset:4096\n\t" \
"v_mfma_f32_16x16x32_bf16 v[212:215], v[140:143], a[36:39], v[212:215]\n\t" \
"v_mfma_f32_16x16x32_bf16 v[216:219], v[136:139], a[92:95], v[216:219]\n\t" \
"ds_read_b128 v[100:103], v249 offset:18432\n\t" \
"v_mfma_f32_16x16x32_bf16 v[220:223], v[140:143], a[92:95], v[220:223]\n\t" \
"s_add_u32 s90, s87, %[lg]\n\t" \
"s_add_u32 s90, s90, 0\n\t" \
"s_mov_b32 m0, s90\n\t" \
"s_add_u32 s95, s89, 0\n\t" \
"buffer_load_dwordx4 %[vg], %[rh], s95 offen offset:0 lds\n\t" \
"v_mfma_f32_16x16x32_bf16 v[224:227], v[136:139], a[148:151], v[224:227]\n\t" \
"ds_read_b128 v[104:107], v249 offset:5120\n\t" \
"v_mfma_f32_16x16x32_bf16 v[228:231], v[140:143], a[148:151], v[228:231]\n\t" \
"v_mfma_f32_16x16x32_bf16 v[232:235], v[136:139], a[204:207], v[232:235]\n\t" \
"ds_read_b128 v[108:111], v249 offset:19456\n\t" \
"v_mfma_f32_16x16x32_bf16 v[236:239], v[140:143], a[204:207], v[236:239]\n\t" \
"v_mfma_f32_16x16x32_bf16 v[208:211], v[144:147], a[40:43], v[208:211]\n\t" \
"ds_read_b128 v[112:115], v249 offset:6144\n\t" \
"v_mfma_f32_16x16x32_bf16 v[212:215], v[148:151], a[40:43], v[212:215]\n\t" \
"v_mfma_f32_16x16x32_bf16 v[216:219], v[144:147], a[96:99], v[216:219]\n\t" \
"ds_read_b128 v[116:119], v249 offset:20480\n\t" \
"v_mfma_f32_16x16x32_bf16 v[220:223], v[148:151], a[96:99], v[220:223]\n\t" \
"v_mfma_f32_16x16x32_bf16 v[224:227], v[144:147], a[152:155], v[224:227]\n\t" \
"v_mfma_f32_16x16x32_bf16 v[228:231], v[148:151], a[152:155], v[228:231]\n\t" \
"v_mfma_f32_16x16x32_bf16 v[232:235], v[144:147], a[208:211], v[232:235]\n\t" \
"v_mfma_f32_16x16x32_bf16 v[236:239], v[148:151], a[208:211], v[236:239]\n\t" \
"v_mfma_f32_16x16x32_bf16 v[208:211], v[152:155], a[44:47], v[208:211]\n\t" \
"v_mfma_f32_16x16x32_bf16 v[212:215], v[156:159], a[44:47], v[212:215]\n\t" \
"v_mfma_f32_16x16x32_bf16 v[216:219], v[152:155], a[100:103], v[216:219]\n\t" \
"v_mfma_f32_16x16x32_bf16 v[220:223], v[156:159], a[100:103], v[220:223]\n\t" \
"v_mfma_f32_16x16x32_bf16 v[224:227], v[152:155], a[156:159], v[224:227]\n\t" \
"v_mfma_f32_16x16x32_bf16 v[228:231], v[156:159], a[156:159], v[228:231]\n\t" \
"v_mfma_f32_16x16x32_bf16 v[232:235], v[152:155], a[212:215], v[232:235]\n\t" \
"v_mfma_f32_16x16x32_bf16 v[236:239], v[156:159], a[212:215], v[236:239]\n\t" \
"buffer_load_dwordx4 %[vg], %[rh], s95 offen offset:1024 lds\n\t" \
"v_mfma_f32_16x16x32_bf16 v[208:211], v[160:163], a[48:51], v[208:211]\n\t" \
"v_mfma_f32_16x16x32_bf16 v[212:215], v[164:167], a[48:51], v[212:215]\n\t" \
"v_mfma_f32_16x16x32_bf16 v[216:219], v[160:163], a[104:107], v[216:219]\n\t" \
"v_mfma_f32_16x16x32_bf16 v[220:223], v[164:167], a[104:107], v[220:223]\n\t" \
"v_mfma_f32_16x16x32_bf16 v[224:227], v[160:163], a[160:163], v[224:227]\n\t" \
"v_mfma_f32_16x16x32_bf16 v[228:231], v[164:167], a[160:163], v[228:231]\n\t" \
"v_mfma_f32_16x16x32_bf16 v[232:235], v[160:163], a[216:219], v[232:235]\n\t" \
"v_mfma_f32_16x16x32_bf16 v[236:239], v[164:167], a[216:219], v[236:239]\n\t" \
"v_mfma_f32_16x16x32_bf16 v[208:211], v[168:171], a[52:55], v[208:211]\n\t" \
"v_mfma_f32_16x16x32_bf16 v[212:215], v[172:175], a[52:55], v[212:215]\n\t" \
"v_mfma_f32_16x16x32_bf16 v[216:219], v[168:171], a[108:111], v[216:219]\n\t" \
"v_mfma_f32_16x16x32_bf16 v[220:223], v[172:175], a[108:111], v[220:223]\n\t" \
"v_mfma_f32_16x16x32_bf16 v[224:227], v[168:171], a[164:167], v[224:227]\n\t" \
"v_mfma_f32_16x16x32_bf16 v[228:231], v[172:175], a[164:167], v[228:231]\n\t" \
"v_mfma_f32_16x16x32_bf16 v[232:235], v[168:171], a[220:223], v[232:235]\n\t" \
"v_mfma_f32_16x16x32_bf16 v[236:239], v[172:175], a[220:223], v[236:239]\n\t" \
"s_add_u32 s92, s92, 28672\n\t" \
"s_cmp_eq_u32 s92, 114688\n\t" \
"s_cselect_b32 s92, 0, s92\n\t" \
"v_mov_b32 v248, v249\n\t" \
"s_add_u32 s91, s91, 917504\n\t" \
"s_add_u32 s88, s88, 2\n\t" \
"s_cmp_lt_u32 s88, 32\n\t" \
"s_nop 0\n\t" \
"s_cbranch_scc1 Lp3%=\n\t" \
"v_cvt_pk_bf16_f32 v240, v208, v209\n\t" \
"v_cvt_pk_bf16_f32 v241, v210, v211\n\t" \
"v_cvt_pk_bf16_f32 v242, v212, v213\n\t" \
"v_cvt_pk_bf16_f32 v243, v214, v215\n\t" \
"buffer_store_dwordx4 v[240:243], %[vs0], %[ro], s91 offen offset:0 sc1\n\t" \
"v_cvt_pk_bf16_f32 v244, v216, v217\n\t" \
"v_cvt_pk_bf16_f32 v245, v218, v219\n\t" \
"v_cvt_pk_bf16_f32 v246, v220, v221\n\t" \
"v_cvt_pk_bf16_f32 v247, v222, v223\n\t" \
"buffer_store_dwordx4 v[244:247], %[vs0], %[ro], s91 offen offset:1024 sc1\n\t" \
"v_cvt_pk_bf16_f32 v240, v224, v225\n\t" \
"v_cvt_pk_bf16_f32 v241, v226, v227\n\t" \
"v_cvt_pk_bf16_f32 v242, v228, v229\n\t" \
"v_cvt_pk_bf16_f32 v243, v230, v231\n\t" \
"buffer_store_dwordx4 v[240:243], %[vs0], %[ro], s91 offen offset:2048 sc1\n\t" \
"v_cvt_pk_bf16_f32 v244, v232, v233\n\t" \
"v_cvt_pk_bf16_f32 v245, v234, v235\n\t" \
"v_cvt_pk_bf16_f32 v246, v236, v237\n\t" \
"v_cvt_pk_bf16_f32 v247, v238, v239\n\t" \
"buffer_store_dwordx4 v[244:247], %[vs0], %[ro], s91 offen offset:3072 sc1\n\t" \
"s_waitcnt vmcnt(0) lgkmcnt(0)\n\t" \
"s_barrier\n\t" \
"s_mov_b64 exec, 1\n\t" \
"buffer_atomic_add v250, off, %[rc], s94\n\t" \
"s_mov_b64 exec, -1\n\t" \
"s_add_u32 s94, s94, 128\n\t" \
"s_mov_b64 exec, 1\n\t" \
"buffer_atomic_add v250, off, %[rc], s94\n\t" \
"s_mov_b64 exec, -1\n\t" \
"s_add_u32 s94, s94, 128\n\t" \
"s_mov_b32 m0, s93\n\t" \
"s_nop 1\n\t"

#define ASM_P3_1W0_CLOB "v64", "v65", "v66", "v67", "v68", "v69", "v70", "v71", "v72", "v73", "v74", "v75", "v76", "v77", "v78", "v79", "v80", "v81", "v82", "v83", "v84", "v85", "v86", "v87", "v88", "v89", "v90", "v91", "v92", "v93", "v94", "v95", "v96", "v97", "v98", "v99", "v100", "v101", "v102", "v103", "v104", "v105", "v106", "v107", "v108", "v109", "v110", "v111", "v112", "v113", "v114", "v115", "v116", "v117", "v118", "v119", "v120", "v121", "v122", "v123", "v124", "v125", "v126", "v127", "v128", "v129", "v130", "v131", "v132", "v133", "v134", "v135", "v136", "v137", "v138", "v139", "v140", "v141", "v142", "v143", "v144", "v145", "v146", "v147", "v148", "v149", "v150", "v151", "v152", "v153", "v154", "v155", "v156", "v157", "v158", "v159", "v160", "v161", "v162", "v163", "v164", "v165", "v166", "v167", "v168", "v169", "v170", "v171", "v172", "v173", "v174", "v175", "v176", "v177", "v178", "v179", "v180", "v181", "v182", "v183", "v184", "v185", "v186", "v187", "v188", "v189", "v190", "v191", "v192", "v193", "v194", "v195", "v196", "v197", "v198", "v199", "v200", "v201", "v202", "v203", "v204", "v205", "v206", "v207", "v208", "v209", "v210", "v211", "v212", "v213", "v214", "v215", "v216", "v217", "v218", "v219", "v220", "v221", "v222", "v223", "v224", "v225", "v226", "v227", "v228", "v229", "v230", "v231", "v232", "v233", "v234", "v235", "v236", "v237", "v238", "v239", "v240", "v241", "v242", "v243", "v244", "v245", "v246", "v247", "v248", "v249", "v250", "a0", "a1", "a2", "a3", "a4", "a5", "a6", "a7", "a8", "a9", "a10", "a11", "a12", "a13", "a14", "a15", "a16", "a17", "a18", "a19", "a20", "a21", "a22", "a23", "a24", "a25", "a26", "a27", "a28", "a29", "a30", "a31", "a32", "a33", "a34", "a35", "a36", "a37", "a38", "a39", "a40", "a41", "a42", "a43", "a44", "a45", "a46", "a47", "a48", "a49", "a50", "a51", "a52", "a53", "a54", "a55", "a56", "a57", "a58", "a59", "a60", "a61", "a62", "a63", "a64", "a65", "a66", "a67", "a68", "a69", "a70", "a71", "a72", "a73", "a74", "a75", "a76", "a77", "a78", "a79", "a80", "a81", "a82", "a83", "a84", "a85", "a86", "a87", "a88", "a89", "a90", "a91", "a92", "a93", "a94", "a95", "a96", "a97", "a98", "a99", "a100", "a101", "a102", "a103", "a104", "a105", "a106", "a107", "a108", "a109", "a110", "a111", "a112", "a113", "a114", "a115", "a116", "a117", "a118", "a119", "a120", "a121", "a122", "a123", "a124", "a125", "a126", "a127", "a128", "a129", "a130", "a131", "a132", "a133", "a134", "a135", "a136", "a137", "a138", "a139", "a140", "a141", "a142", "a143", "a144", "a145", "a146", "a147", "a148", "a149", "a150", "a151", "a152", "a153", "a154", "a155", "a156", "a157", "a158", "a159", "a160", "a161", "a162", "a163", "a164", "a165", "a166", "a167", "a168", "a169", "a170", "a171", "a172", "a173", "a174", "a175", "a176", "a177", "a178", "a179", "a180", "a181", "a182", "a183", "a184", "a185", "a186", "a187", "a188", "a189", "a190", "a191", "a192", "a193", "a194", "a195", "a196", "a197", "a198", "a199", "a200", "a201", "a202", "a203", "a204", "a205", "a206", "a207", "a208", "a209", "a210", "a211", "a212", "a213", "a214", "a215", "a216", "a217", "a218", "a219", "a220", "a221", "a222", "a223", "s86", "s87", "s88", "s89", "s90", "s91", "s92", "s93", "s94", "s95", "vcc", "scc", "memory"

#define ASM_P3_1W1_TEXT \
"s_mov_b32 s93, m0\n\t" \
"s_add_u32 s89, %[src0], 172032\n\t" \
"s_mov_b32 s87, 86016\n\t" \
"s_waitcnt vmcnt(0)\n\t" \
"s_barrier\n\t" \
"v_mov_b32 v250, 1\n\t" \
"v_mov_b32 v248, %[lr]\n\t" \
"ds_read_b128 v[64:67], v248 offset:0\n\t" \
"ds_read_b128 v[68:71], v248 offset:14336\n\t" \
"ds_read_b128 v[72:75], v248 offset:1024\n\t" \
"ds_read_b128 v[76:79], v248 offset:15360\n\t" \
"ds_read_b128 v[80:83], v248 offset:2048\n\t" \
"ds_read_b128 v[84:87], v248 offset:16384\n\t" \
"ds_read_b128 v[88:91], v248 offset:3072\n\t" \
"ds_read_b128 v[92:95], v248 offset:17408\n\t" \
"ds_read_b128 v[96:99], v248 offset:4096\n\t" \
"ds_read_b128 v[100:103], v248 offset:18432\n\t" \
"ds_read_b128 v[104:107], v248 offset:5120\n\t" \
"ds_read_b128 v[108:111], v248 offset:19456\n\t" \
"ds_read_b128 v[112:115], v248 offset:6144\n\t" \
"ds_read_b128 v[116:119], v248 offset:20480\n\t" \
"s_mov_b32 s92, 0\n\t" \
"s_mov_b32 s91, %[so0]\n\t" \
"s_mov_b32 s94, %[ao0]\n\t" \
"s_waitcnt lgkmcnt(0)\n\t" \
"v_mfma_f32_16x16x32_bf16 v[176:179], v[64:67], a[0:3], 0\n\t" \
"v_mfma_f32_16x16x32_bf16 v[180:183], v[68:71], a[0:3], 0\n\t" \
"ds_read_b128 v[120:123], v248 offset:7168\n\t" \
"v_mfma_f32_16x16x32_bf16 v[184:187], v[64:67], a[56:59], 0\n\t" \
"v_mfma_f32_16x16x32_bf16 v[188:191], v[68:71], a[56:59], 0\n\t" \
"ds_read_b128 v[124:127], v248 offset:21504\n\t" \
"v_mfma_f32_16x16x32_bf16 v[192:195], v[64:67], a[112:115], 0\n\t" \
"v_mfma_f32_16x16x32_bf16 v[196:199], v[68:71], a[112:115], 0\n\t" \
"ds_read_b128 v[128:131], v248 offset:8192\n\t" \
"v_mfma_f32_16x16x32_bf16 v[200:203], v[64:67], a[168:171], 0\n\t" \
"v_mfma_f32_16x16x32_bf16 v[204:207], v[68:71], a[168:171], 0\n\t" \
"ds_read_b128 v[132:135], v248 offset:22528\n\t" \
"v_mfma_f32_16x16x32_bf16 v[176:179], v[72:75], a[4:7], v[176:179]\n\t" \
"v_mfma_f32_16x16x32_bf16 v[180:183], v[76:79], a[4:7], v[180:183]\n\t" \
"ds_read_b128 v[136:139], v248 offset:9216\n\t" \
"v_mfma_f32_16x16x32_bf16 v[184:187], v[72:75], a[60:63], v[184:187]\n\t" \
"v_mfma_f32_16x16x32_bf16 v[188:191], v[76:79], a[60:63], v[188:191]\n\t" \
"ds_read_b128 v[140:143], v248 offset:23552\n\t" \
"v_mfma_f32_16x16x32_bf16 v[192:195], v[72:75], a[116:119], v[192:195]\n\t" \
"v_mfma_f32_16x16x32_bf16 v[196:199], v[76:79], a[116:119], v[196:199]\n\t" \
"ds_read_b128 v[144:147], v248 offset:10240\n\t" \
"v_mfma_f32_16x16x32_bf16 v[200:203], v[72:75], a[172:175], v[200:203]\n\t" \
"v_mfma_f32_16x16x32_bf16 v[204:207], v[76:79], a[172:175], v[204:207]\n\t" \
"ds_read_b128 v[148:151], v248 offset:24576\n\t" \
"v_mfma_f32_16x16x32_bf16 v[176:179], v[80:83], a[8:11], v[176:179]\n\t" \
"v_mfma_f32_16x16x32_bf16 v[180:183], v[84:87], a[8:11], v[180:183]\n\t" \
"ds_read_b128 v[152:155], v248 offset:11264\n\t" \
"v_mfma_f32_16x16x32_bf16 v[184:187], v[80:83], a[64:67], v[184:187]\n\t" \
"v_mfma_f32_16x16x32_bf16 v[188:191], v[84:87], a[64:67], v[188:191]\n\t" \
"ds_read_b128 v[156:159], v248 offset:25600\n\t" \
"s_add_u32 s90, s87, %[lg]\n\t" \
"s_add_u32 s90, s90, 0\n\t" \
"s_mov_b32 m0, s90\n\t" \
"s_add_u32 s95, s89, 0\n\t" \
"buffer_load_dwordx4 %[vg], %[rh], s95 offen offset:2048 lds\n\t" \
"v_mfma_f32_16x16x32_bf16 v[192:195], v[80:83], a[120:123], v[192:195]\n\t" \
"v_mfma_f32_16x16x32_bf16 v[196:199], v[84:87], a[120:123], v[196:199]\n\t" \
"ds_read_b128 v[160:163], v248 offset:12288\n\t" \
"v_mfma_f32_16x16x32_bf16 v[200:203], v[80:83], a[176:179], v[200:203]\n\t" \
"v_mfma_f32_16x16x32_bf16 v[204:207], v[84:87], a[176:179], v[204:207]\n\t" \
"ds_read_b128 v[164:167], v248 offset:26624\n\t" \
"v_mfma_f32_16x16x32_bf16 v[176:179], v[88:91], a[12:15], v[176:179]\n\t" \
"v_mfma_f32_16x16x32_bf16 v[180:183], v[92:95], a[12:15], v[180:183]\n\t" \
"ds_read_b128 v[168:171], v248 offset:13312\n\t" \
"v_mfma_f32_16x16x32_bf16 v[184:187], v[88:91], a[68:71], v[184:187]\n\t" \
"v_mfma_f32_16x16x32_bf16 v[188:191], v[92:95], a[68:71], v[188:191]\n\t" \
"ds_read_b128 v[172:175], v248 offset:27648\n\t" \
"v_mfma_f32_16x16x32_bf16 v[192:195], v[88:91], a[124:127], v[192:195]\n\t" \
"v_mfma_f32_16x16x32_bf16 v[196:199], v[92:95], a[124:127], v[196:199]\n\t" \
"v_mfma_f32_16x16x32_bf16 v[200:203], v[88:91], a[180:183], v[200:203]\n\t" \
"v_mfma_f32_16x16x32_bf16 v[204:207], v[92:95], a[180:183], v[204:207]\n\t" \
"v_mfma_f32_16x16x32_bf16 v[176:179], v[96:99], a[16:19], v[176:179]\n\t" \
"v_mfma_f32_16x16x32_bf16 v[180:183], v[100:103], a[16:19], v[180:183]\n\t" \
"v_mfma_f32_16x16x32_bf16 v[184:187], v[96:99], a[72:75], v[184:187]\n\t" \
"v_mfma_f32_16x16x32_bf16 v[188:191], v[100:103], a[72:75], v[188:191]\n\t" \
"v_mfma_f32_16x16x32_bf16 v[192:195], v[96:99], a[128:131], v[192:195]\n\t" \
"v_mfma_f32_16x16x32_bf16 v[196:199], v[100:103], a[128:131], v[196:199]\n\t" \
"v_mfma_f32_16x16x32_bf16 v[200:203], v[96:99], a[184:187], v[200:203]\n\t" \
"v_mfma_f32_16x16x32_bf16 v[204:207], v[100:103], a[184:187], v[204:207]\n\t" \
"buffer_load_dwordx4 %[vg], %[rh], s95 offen offset:3072 lds\n\t" \
"v_mfma_f32_16x16x32_bf16 v[176:179], v[104:107], a[20:23], v[176:179]\n\t" \
"v_mfma_f32_16x16x32_bf16 v[180:183], v[108:111], a[20:23], v[180:183]\n\t" \
"v_mfma_f32_16x16x32_bf16 v[184:187], v[104:107], a[76:79], v[184:187]\n\t" \
"v_mfma_f32_16x16x32_bf16 v[188:191], v[108:111], a[76:79], v[188:191]\n\t" \
"v_mfma_f32_16x16x32_bf16 v[192:195], v[104:107], a[132:135], v[192:195]\n\t" \
"v_mfma_f32_16x16x32_bf16 v[196:199], v[108:111], a[132:135], v[196:199]\n\t" \
"v_mfma_f32_16x16x32_bf16 v[200:203], v[104:107], a[188:191], v[200:203]\n\t" \
"v_mfma_f32_16x16x32_bf16 v[204:207], v[108:111], a[188:191], v[204:207]\n\t" \
"v_mfma_f32_16x16x32_bf16 v[176:179], v[112:115], a[24:27], v[176:179]\n\t" \
"v_mfma_f32_16x16x32_bf16 v[180:183], v[116:119], a[24:27], v[180:183]\n\t" \
"v_mfma_f32_16x16x32_bf16 v[184:187], v[112:115], a[80:83], v[184:187]\n\t" \
"v_mfma_f32_16x16x32_bf16 v[188:191], v[116:119], a[80:83], v[188:191]\n\t" \
"v_mfma_f32_16x16x32_bf16 v[192:195], v[112:115], a[136:139], v[192:195]\n\t" \
"v_mfma_f32_16x16x32_bf16 v[196:199], v[116:119], a[136:139], v[196:199]\n\t" \
"v_mfma_f32_16x16x32_bf16 v[200:203], v[112:115], a[192:195], v[200:203]\n\t" \
"v_mfma_f32_16x16x32_bf16 v[204:207], v[116:119], a[192:195], v[204:207]\n\t" \
"s_add_u32 s89, s89, 57344\n\t" \
"s_min_u32 s89, s89, 1806336\n\t" \
"s_mov_b32 s87, s92\n\t" \
"s_add_u32 s95, s92, 28672\n\t" \
"s_cmp_eq_u32 s95, 114688\n\t" \
"s_cselect_b32 s95, 0, s95\n\t" \
"v_add_u32 v249, s95, %[lr]\n\t" \
"s_waitcnt vmcnt(63) lgkmcnt(0)\n\t" \
"s_barrier\n\t" \
"v_mfma_f32_16x16x32_bf16 v[176:179], v[120:123], a[28:31], v[176:179]\n\t" \
"ds_read_b128 v[64:67], v249 offset:0\n\t" \
"v_mfma_f32_16x16x32_bf16 v[180:183], v[124:127], a[28:31], v[180:183]\n\t" \
"v_mfma_f32_16x16x32_bf16 v[184:187], v[120:123], a[84:87], v[184:187]\n\t" \
"ds_read_b128 v[68:71], v249 offset:14336\n\t" \
"v_mfma_f32_16x16x32_bf16 v[188:191], v[124:127], a[84:87], v[188:191]\n\t" \
"v_mfma_f32_16x16x32_bf16 v[192:195], v[120:123], a[140:143], v[192:195]\n\t" \
"ds_read_b128 v[72:75], v249 offset:1024\n\t" \
"v_mfma_f32_16x16x32_bf16 v[196:199], v[124:127], a[140:143], v[196:199]\n\t" \
"v_mfma_f32_16x16x32_bf16 v[200:203], v[120:123], a[196:199], v[200:203]\n\t" \
"ds_read_b128 v[76:79], v249 offset:15360\n\t" \
"v_mfma_f32_16x16x32_bf16 v[204:207], v[124:127], a[196:199], v[204:207]\n\t" \
"v_mfma_f32_16x16x32_bf16 v[176:179], v[128:131], a[32:35], v[176:179]\n\t" \
"ds_read_b128 v[80:83], v249 offset:2048\n\t" \
"v_mfma_f32_16x16x32_bf16 v[180:183], v[132:135], a[32:35], v[180:183]\n\t" \
"v_mfma_f32_16x16x32_bf16 v[184:187], v[128:131], a[88:91], v[184:187]\n\t" \
"ds_read_b128 v[84:87], v249 offset:16384\n\t" \
"v_mfma_f32_16x16x32_bf16 v[188:191], v[132:135], a[88:91], v[188:191]\n\t" \
"v_mfma_f32_16x16x32_bf16 v[192:195], v[128:131], a[144:147], v[192:195]\n\t" \
"ds_read_b128 v[88:91], v249 offset:3072\n\t" \
"v_mfma_f32_16x16x32_bf16 v[196:199], v[132:135], a[144:147], v[196:199]\n\t" \
"v_mfma_f32_16x16x32_bf16 v[200:203], v[128:131], a[200:203], v[200:203]\n\t" \
"ds_read_b128 v[92:95], v249 offset:17408\n\t" \
"v_mfma_f32_16x16x32_bf16 v[204:207], v[132:135], a[200:203], v[204:207]\n\t" \
"v_mfma_f32_16x16x32_bf16 v[176:179], v[136:139], a[36:39], v[176:179]\n\t" \
"ds_read_b128 v[96:99], v249 offset:4096\n\t" \
"v_mfma_f32_16x16x32_bf16 v[180:183], v[140:143], a[36:39], v[180:183]\n\t" \
"v_mfma_f32_16x16x32_bf16 v[184:187], v[136:139], a[92:95], v[184:187]\n\t" \
"ds_read_b128 v[100:103], v249 offset:18432\n\t" \
"v_mfma_f32_16x16x32_bf16 v[188:191], v[140:143], a[92:95], v[188:191]\n\t" \
"s_add_u32 s90, s87, %[lg]\n\t" \
"s_add_u32 s90, s90, 0\n\t" \
"s_mov_b32 m0, s90\n\t" \
"s_add_u32 s95, s89, 0\n\t" \
"buffer_load_dwordx4 %[vg], %[rh], s95 offen offset:0 lds\n\t" \
"v_mfma_f32_16x16x32_bf16 v[192:195], v[136:139], a[148:151], v[192:195]\n\t" \
"ds_read_b128 v[104:107], v249 offset:5120\n\t" \
"v_mfma_f32_16x16x32_bf16 v[196:199], v[140:143], a[148:151], v[196:199]\n\t" \
"v_mfma_f32_16x16x32_bf16 v[200:203], v[136:139], a[204:207], v[200:203]\n\t" \
"ds_read_b128 v[108:111], v249 offset:19456\n\t" \
"v_mfma_f32_16x16x32_bf16 v[204:207], v[140:143], a[204:207], v[204:207]\n\t" \
"v_mfma_f32_16x16x32_bf16 v[176:179], v[144:147], a[40:43], v[176:179]\n\t" \
"ds_read_b128 v[112:115], v249 offset:6144\n\t" \
"v_mfma_f32_16x16x32_bf16 v[180:183], v[148:151], a[40:43], v[180:183]\n\t" \
"v_mfma_f32_16x16x32_bf16 v[184:187], v[144:147], a[96:99], v[184:187]\n\t" \
"ds_read_b128 v[116:119], v249 offset:20480\n\t" \
"v_mfma_f32_16x16x32_bf16 v[188:191], v[148:151], a[96:99], v[188:191]\n\t" \
"v_mfma_f32_16x16x32_bf16 v[192:195], v[144:147], a[152:155], v[192:195]\n\t" \
"v_mfma_f32_16x16x32_bf16 v[196:199], v[148:151], a[152:155], v[196:199]\n\t" \
"v_mfma_f32_16x16x32_bf16 v[200:203], v[144:147], a[208:211], v[200:203]\n\t" \
"v_mfma_f32_16x16x32_bf16 v[204:207], v[148:151], a[208:211], v[204:207]\n\t" \
"v_mfma_f32_16x16x32_bf16 v[176:179], v[152:155], a[44:47], v[176:179]\n\t" \
"v_mfma_f32_16x16x32_bf16 v[180:183], v[156:159], a[44:47], v[180:183]\n\t" \
"v_mfma_f32_16x16x32_bf16 v[184:187], v[152:155], a[100:103], v[184:187]\n\t" \
"v_mfma_f32_16x16x32_bf16 v[188:191], v[156:159], a[100:103], v[188:191]\n\t" \
"v_mfma_f32_16x16x32_bf16 v[192:195], v[152:155], a[156:159], v[192:195]\n\t" \
"v_mfma_f32_16x16x32_bf16 v[196:199], v[156:159], a[156:159], v[196:199]\n\t" \
"v_mfma_f32_16x16x32_bf16 v[200:203], v[152:155], a[212:215], v[200:203]\n\t" \
"v_mfma_f32_16x16x32_bf16 v[204:207], v[156:159], a[212:215], v[204:207]\n\t" \
"buffer_load_dwordx4 %[vg], %[rh], s95 offen offset:1024 lds\n\t" \
"v_mfma_f32_16x16x32_bf16 v[176:179], v[160:163], a[48:51], v[176:179]\n\t" \
"v_mfma_f32_16x16x32_bf16 v[180:183], v[164:167], a[48:51], v[180:183]\n\t" \
"v_mfma_f32_16x16x32_bf16 v[184:187], v[160:163], a[104:107], v[184:187]\n\t" \
"v_mfma_f32_16x16x32_bf16 v[188:191], v[164:167], a[104:107], v[188:191]\n\t" \
"v_mfma_f32_16x16x32_bf16 v[192:195], v[160:163], a[160:163], v[192:195]\n\t" \
"v_mfma_f32_16x16x32_bf16 v[196:199], v[164:167], a[160:163], v[196:199]\n\t" \
"v_mfma_f32_16x16x32_bf16 v[200:203], v[160:163], a[216:219], v[200:203]\n\t" \
"v_mfma_f32_16x16x32_bf16 v[204:207], v[164:167], a[216:219], v[204:207]\n\t" \
"v_mfma_f32_16x16x32_bf16 v[176:179], v[168:171], a[52:55], v[176:179]\n\t" \
"v_mfma_f32_16x16x32_bf16 v[180:183], v[172:175], a[52:55], v[180:183]\n\t" \
"v_mfma_f32_16x16x32_bf16 v[184:187], v[168:171], a[108:111], v[184:187]\n\t" \
"v_mfma_f32_16x16x32_bf16 v[188:191], v[172:175], a[108:111], v[188:191]\n\t" \
"v_mfma_f32_16x16x32_bf16 v[192:195], v[168:171], a[164:167], v[192:195]\n\t" \
"v_mfma_f32_16x16x32_bf16 v[196:199], v[172:175], a[164:167], v[196:199]\n\t" \
"v_mfma_f32_16x16x32_bf16 v[200:203], v[168:171], a[220:223], v[200:203]\n\t" \
"v_mfma_f32_16x16x32_bf16 v[204:207], v[172:175], a[220:223], v[204:207]\n\t" \
"s_add_u32 s92, s92, 28672\n\t" \
"s_cmp_eq_u32 s92, 114688\n\t" \
"s_cselect_b32 s92, 0, s92\n\t" \
"v_mov_b32 v248, v249\n\t" \
"s_add_u32 s91, s91, 917504\n\t" \
"v_cvt_pk_bf16_f32 v240, v176, v177\n\t" \
"v_cvt_pk_bf16_f32 v241, v178, v179\n\t" \
"v_cvt_pk_bf16_f32 v242, v180, v181\n\t" \
"v_cvt_pk_bf16_f32 v243, v182, v183\n\t" \
"buffer_store_dwordx4 v[240:243], %[vs0], %[ro], s91 offen offset:0 sc1\n\t" \
"v_cvt_pk_bf16_f32 v244, v184, v185\n\t" \
"v_cvt_pk_bf16_f32 v245, v186, v187\n\t" \
"v_cvt_pk_bf16_f32 v246, v188, v189\n\t" \
"v_cvt_pk_bf16_f32 v247, v190, v191\n\t" \
"buffer_store_dwordx4 v[244:247], %[vs0], %[ro], s91 offen offset:1024 sc1\n\t" \
"v_cvt_pk_bf16_f32 v240, v192, v193\n\t" \
"v_cvt_pk_bf16_f32 v241, v194, v195\n\t" \
"v_cvt_pk_bf16_f32 v242, v196, v197\n\t" \
"v_cvt_pk_bf16_f32 v243, v198, v199\n\t" \
"buffer_store_dwordx4 v[240:243], %[vs0], %[ro], s91 offen offset:2048 sc1\n\t" \
"v_cvt_pk_bf16_f32 v244, v200, v201\n\t" \
"v_cvt_pk_bf16_f32 v245, v202, v203\n\t" \
"v_cvt_pk_bf16_f32 v246, v204, v205\n\t" \
"v_cvt_pk_bf16_f32 v247, v206, v207\n\t" \
"buffer_store_dwordx4 v[244:247], %[vs0], %[ro], s91 offen offset:3072 sc1\n\t" \
"s_waitcnt vmcnt(0)\n\t" \
"s_barrier\n\t" \
"s_add_u32 s94, s94, 128\n\t" \
"s_waitcnt lgkmcnt(0)\n\t" \
"v_mfma_f32_16x16x32_bf16 v[208:211], v[64:67], a[0:3], 0\n\t" \
"v_mfma_f32_16x16x32_bf16 v[212:215], v[68:71], a[0:3], 0\n\t" \
"ds_read_b128 v[120:123], v248 offset:7168\n\t" \
"v_mfma_f32_16x16x32_bf16 v[216:219], v[64:67], a[56:59], 0\n\t" \
"v_mfma_f32_16x16x32_bf16 v[220:223], v[68:71], a[56:59], 0\n\t" \
"ds_read_b128 v[124:127], v248 offset:21504\n\t" \
"v_mfma_f32_16x16x32_bf16 v[224:227], v[64:67], a[112:115], 0\n\t" \
"v_mfma_f32_16x16x32_bf16 v[228:231], v[68:71], a[112:115], 0\n\t" \
"ds_read_b128 v[128:131], v248 offset:8192\n\t" \
"v_mfma_f32_16x16x32_bf16 v[232:235], v[64:67], a[168:171], 0\n\t" \
"v_mfma_f32_16x16x32_bf16 v[236:239], v[68:71], a[168:171], 0\n\t" \
"ds_read_b128 v[132:135], v248 offset:22528\n\t" \
"v_mfma_f32_16x16x32_bf16 v[208:211], v[72:75], a[4:7], v[208:211]\n\t" \
"v_mfma_f32_16x16x32_bf16 v[212:215], v[76:79], a[4:7], v[212:215]\n\t" \
"ds_read_b128 v[136:139], v248 offset:9216\n\t" \
"v_mfma_f32_16x16x32_bf16 v[216:219], v[72:75], a[60:63], v[216:219]\n\t" \
"v_mfma_f32_16x16x32_bf16 v[220:223], v[76:79], a[60:63], v[220:223]\n\t" \
"ds_read_b128 v[140:143], v248 offset:23552\n\t" \
"v_mfma_f32_16x16x32_bf16 v[224:227], v[72:75], a[116:119], v[224:227]\n\t" \
"v_mfma_f32_16x16x32_bf16 v[228:231], v[76:79], a[116:119], v[228:231]\n\t" \
"ds_read_b128 v[144:147], v248 offset:10240\n\t" \
"v_mfma_f32_16x16x32_bf16 v[232:235], v[72:75], a[172:175], v[232:235]\n\t" \
"v_mfma_f32_16x16x32_bf16 v[236:239], v[76:79], a[172:175], v[236:239]\n\t" \
"ds_read_b128 v[148:151], v248 offset:24576\n\t" \
"v_mfma_f32_16x16x32_bf16 v[208:211], v[80:83], a[8:11], v[208:211]\n\t" \
"v_mfma_f32_16x16x32_bf16 v[212:215], v[84:87], a[8:11], v[212:215]\n\t" \
"ds_read_b128 v[152:155], v248 offset:11264\n\t" \
"v_mfma_f32_16x16x32_bf16 v[216:219], v[80:83], a[64:67], v[216:219]\n\t" \
"v_mfma_f32_16x16x32_bf16 v[220:223], v[84:87], a[64:67], v[220:223]\n\t" \
"ds_read_b128 v[156:159], v248 offset:25600\n\t" \
"s_add_u32 s90, s87, %[lg]\n\t" \
"s_add_u32 s90, s90, 0\n\t" \
"s_mov_b32 m0, s90\n\t" \
"s_add_u32 s95, s89, 0\n\t" \
"buffer_load_dwordx4 %[vg], %[rh], s95 offen offset:2048 lds\n\t" \
"v_mfma_f32_16x16x32_bf16 v[224:227], v[80:83], a[120:123], v[224:227]\n\t" \
"v_mfma_f32_16x16x32_bf16 v[228:231], v[84:87], a[120:123], v[228:231]\n\t" \
"ds_read_b128 v[160:163], v248 offset:12288\n\t" \
"v_mfma_f32_16x16x32_bf16 v[232:235], v[80:83], a[176:179], v[232:235]\n\t" \
"v_mfma_f32_16x16x32_bf16 v[236:239], v[84:87], a[176:179], v[236:239]\n\t" \
"ds_read_b128 v[164:167], v248 offset:26624\n\t" \
"v_mfma_f32_16x16x32_bf16 v[208:211], v[88:91], a[12:15], v[208:211]\n\t" \
"v_mfma_f32_16x16x32_bf16 v[212:215], v[92:95], a[12:15], v[212:215]\n\t" \
"ds_read_b128 v[168:171], v248 offset:13312\n\t" \
"v_mfma_f32_16x16x32_bf16 v[216:219], v[88:91], a[68:71], v[216:219]\n\t" \
"v_mfma_f32_16x16x32_bf16 v[220:223], v[92:95], a[68:71], v[220:223]\n\t" \
"ds_read_b128 v[172:175], v248 offset:27648\n\t" \
"v_mfma_f32_16x16x32_bf16 v[224:227], v[88:91], a[124:127], v[224:227]\n\t" \
"v_mfma_f32_16x16x32_bf16 v[228:231], v[92:95], a[124:127], v[228:231]\n\t" \
"v_mfma_f32_16x16x32_bf16 v[232:235], v[88:91], a[180:183], v[232:235]\n\t" \
"v_mfma_f32_16x16x32_bf16 v[236:239], v[92:95], a[180:183], v[236:239]\n\t" \
"v_mfma_f32_16x16x32_bf16 v[208:211], v[96:99], a[16:19], v[208:211]\n\t" \
"v_mfma_f32_16x16x32_bf16 v[212:215], v[100:103], a[16:19], v[212:215]\n\t" \
"v_mfma_f32_16x16x32_bf16 v[216:219], v[96:99], a[72:75], v[216:219]\n\t" \
"v_mfma_f32_16x16x32_bf16 v[220:223], v[100:103], a[72:75], v[220:223]\n\t" \
"v_mfma_f32_16x16x32_bf16 v[224:227], v[96:99], a[128:131], v[224:227]\n\t" \
"v_mfma_f32_16x16x32_bf16 v[228:231], v[100:103], a[128:131], v[228:231]\n\t" \
"v_mfma_f32_16x16x32_bf16 v[232:235], v[96:99], a[184:187], v[232:235]\n\t" \
"v_mfma_f32_16x16x32_bf16 v[236:239], v[100:103], a[184:187], v[236:239]\n\t" \
"buffer_load_dwordx4 %[vg], %[rh], s95 offen offset:3072 lds\n\t" \
"v_mfma_f32_16x16x32_bf16 v[208:211], v[104:107], a[20:23], v[208:211]\n\t" \
"v_mfma_f32_16x16x32_bf16 v[212:215], v[108:111], a[20:23], v[212:215]\n\t" \
"v_mfma_f32_16x16x32_bf16 v[216:219], v[104:107], a[76:79], v[216:219]\n\t" \
"v_mfma_f32_16x16x32_bf16 v[220:223], v[108:111], a[76:79], v[220:223]\n\t" \
"v_mfma_f32_16x16x32_bf16 v[224:227], v[104:107], a[132:135], v[224:227]\n\t" \
"v_mfma_f32_16x16x32_bf16 v[228:231], v[108:111], a[132:135], v[228:231]\n\t" \
"v_mfma_f32_16x16x32_bf16 v[232:235], v[104:107], a[188:191], v[232:235]\n\t" \
"v_mfma_f32_16x16x32_bf16 v[236:239], v[108:111], a[188:191], v[236:239]\n\t" \
"v_mfma_f32_16x16x32_bf16 v[208:211], v[112:115], a[24:27], v[208:211]\n\t" \
"v_mfma_f32_16x16x32_bf16 v[212:215], v[116:119], a[24:27], v[212:215]\n\t" \
"v_mfma_f32_16x16x32_bf16 v[216:219], v[112:115], a[80:83], v[216:219]\n\t" \
"v_mfma_f32_16x16x32_bf16 v[220:223], v[116:119], a[80:83], v[220:223]\n\t" \
"v_mfma_f32_16x16x32_bf16 v[224:227], v[112:115], a[136:139], v[224:227]\n\t" \
"v_mfma_f32_16x16x32_bf16 v[228:231], v[116:119], a[136:139], v[228:231]\n\t" \
"v_mfma_f32_16x16x32_bf16 v[232:235], v[112:115], a[192:195], v[232:235]\n\t" \
"v_mfma_f32_16x16x32_bf16 v[236:239], v[116:119], a[192:195], v[236:239]\n\t" \
"s_add_u32 s89, s89, 57344\n\t" \
"s_min_u32 s89, s89, 1806336\n\t" \
"s_mov_b32 s87, s92\n\t" \
"s_add_u32 s95, s92, 28672\n\t" \
"s_cmp_eq_u32 s95, 114688\n\t" \
"s_cselect_b32 s95, 0, s95\n\t" \
"v_add_u32 v249, s95, %[lr]\n\t" \
"s_waitcnt vmcnt(63) lgkmcnt(0)\n\t" \
"s_barrier\n\t" \
"v_mfma_f32_16x16x32_bf16 v[208:211], v[120:123], a[28:31], v[208:211]\n\t" \
"ds_read_b128 v[64:67], v249 offset:0\n\t" \
"v_mfma_f32_16x16x32_bf16 v[212:215], v[124:127], a[28:31], v[212:215]\n\t" \
"v_mfma_f32_16x16x32_bf16 v[216:219], v[120:123], a[84:87], v[216:219]\n\t" \
"ds_read_b128 v[68:71], v249 offset:14336\n\t" \
"v_mfma_f32_16x16x32_bf16 v[220:223], v[124:127], a[84:87], v[220:223]\n\t" \
"v_mfma_f32_16x16x32_bf16 v[224:227], v[120:123], a[140:143], v[224:227]\n\t" \
"ds_read_b128 v[72:75], v249 offset:1024\n\t" \
"v_mfma_f32_16x16x32_bf16 v[228:231], v[124:127], a[140:143], v[228:231]\n\t" \
"v_mfma_f32_16x16x32_bf16 v[232:235], v[120:123], a[196:199], v[232:235]\n\t" \
"ds_read_b128 v[76:79], v249 offset:15360\n\t" \
"v_mfma_f32_16x16x32_bf16 v[236:239], v[124:127], a[196:199], v[236:239]\n\t" \
"v_mfma_f32_16x16x32_bf16 v[208:211], v[128:131], a[32:35], v[208:211]\n\t" \
"ds_read_b128 v[80:83], v249 offset:2048\n\t" \
"v_mfma_f32_16x16x32_bf16 v[212:215], v[132:135], a[32:35], v[212:215]\n\t" \
"v_mfma_f32_16x16x32_bf16 v[216:219], v[128:131], a[88:91], v[216:219]\n\t" \
"ds_read_b128 v[84:87], v249 offset:16384\n\t" \
"v_mfma_f32_16x16x32_bf16 v[220:223], v[132:135], a[88:91], v[220:223]\n\t" \
"v_mfma_f32_16x16x32_bf16 v[224:227], v[128:131], a[144:147], v[224:227]\n\t" \
"ds_read_b128 v[88:91], v249 offset:3072\n\t" \
"v_mfma_f32_16x16x32_bf16 v[228:231], v[132:135], a[144:147], v[228:231]\n\t" \
"v_mfma_f32_16x16x32_bf16 v[232:235], v[128:131], a[200:203], v[232:235]\n\t" \
"ds_read_b128 v[92:95], v249 offset:17408\n\t" \
"v_mfma_f32_16x16x32_bf16 v[236:239], v[132:135], a[200:203], v[236:239]\n\t" \
"v_mfma_f32_16x16x32_bf16 v[208:211], v[136:139], a[36:39], v[208:211]\n\t" \
"ds_read_b128 v[96:99], v249 offset:4096\n\t" \
"v_mfma_f32_16x16x32_bf16 v[212:215], v[140:143], a[36:39], v[212:215]\n\t" \
"v_mfma_f32_16x16x32_bf16 v[216:219], v[136:139], a[92:95], v[216:219]\n\t" \
"ds_read_b128 v[100:103], v249 offset:18432\n\t" \
"v_mfma_f32_16x16x32_bf16 v[220:223], v[140:143], a[92:95], v[220:223]\n\t" \
"s_add_u32 s90, s87, %[lg]\n\t" \
"s_add_u32 s90, s90, 0\n\t" \
"s_mov_b32 m0, s90\n\t" \
"s_add_u32 s95, s89, 0\n\t" \
"buffer_load_dwordx4 %[vg], %[rh], s95 offen offset:0 lds\n\t" \
"v_mfma_f32_16x16x32_bf16 v[224:227], v[136:139], a[148:151], v[224:227]\n\t" \
"ds_read_b128 v[104:107], v249 offset:5120\n\t" \
"v_mfma_f32_16x16x32_bf16 v[228:231], v[140:143], a[148:151], v[228:231]\n\t" \
"v_mfma_f32_16x16x32_bf16 v[232:235], v[136:139], a[204:207], v[232:235]\n\t" \
"ds_read_b128 v[108:111], v249 offset:19456\n\t" \
"v_mfma_f32_16x16x32_bf16 v[236:239], v[140:143], a[204:207], v[236:239]\n\t" \
"v_mfma_f32_16x16x32_bf16 v[208:211], v[144:147], a[40:43], v[208:211]\n\t" \
"ds_read_b128 v[112:115], v249 offset:6144\n\t" \
"v_mfma_f32_16x16x32_bf16 v[212:215], v[148:151], a[40:43], v[212:215]\n\t" \
"v_mfma_f32_16x16x32_bf16 v[216:219], v[144:147], a[96:99], v[216:219]\n\t" \
"ds_read_b128 v[116:119], v249 offset:20480\n\t" \
"v_mfma_f32_16x16x32_bf16 v[220:223], v[148:151], a[96:99], v[220:223]\n\t" \
"v_mfma_f32_16x16x32_bf16 v[224:227], v[144:147], a[152:155], v[224:227]\n\t" \
"v_mfma_f32_16x16x32_bf16 v[228:231], v[148:151], a[152:155], v[228:231]\n\t" \
"v_mfma_f32_16x16x32_bf16 v[232:235], v[144:147], a[208:211], v[232:235]\n\t" \
"v_mfma_f32_16x16x32_bf16 v[236:239], v[148:151], a[208:211], v[236:239]\n\t" \
"v_mfma_f32_16x16x32_bf16 v[208:211], v[152:155], a[44:47], v[208:211]\n\t" \
"v_mfma_f32_16x16x32_bf16 v[212:215], v[156:159], a[44:47], v[212:215]\n\t" \
"v_mfma_f32_16x16x32_bf16 v[216:219], v[152:155], a[100:103], v[216:219]\n\t" \
"v_mfma_f32_16x16x32_bf16 v[220:223], v[156:159], a[100:103], v[220:223]\n\t" \
"v_mfma_f32_16x16x32_bf16 v[224:227], v[152:155], a[156:159], v[224:227]\n\t" \
"v_mfma_f32_16x16x32_bf16 v[228:231], v[156:159], a[156:159], v[228:231]\n\t" \
"v_mfma_f32_16x16x32_bf16 v[232:235], v[152:155], a[212:215], v[232:235]\n\t" \
"v_mfma_f32_16x16x32_bf16 v[236:239], v[156:159], a[212:215], v[236:239]\n\t" \
"buffer_load_dwordx4 %[vg], %[rh], s95 offen offset:1024 lds\n\t" \
"v_mfma_f32_16x16x32_bf16 v[208:211], v[160:163], a[48:51], v[208:211]\n\t" \
"v_mfma_f32_16x16x32_bf16 v[212:215], v[164:167], a[48:51], v[212:215]\n\t" \
"v_mfma_f32_16x16x32_bf16 v[216:219], v[160:163], a[104:107], v[216:219]\n\t" \
"v_mfma_f32_16x16x32_bf16 v[220:223], v[164:167], a[104:107], v[220:223]\n\t" \
"v_mfma_f32_16x16x32_bf16 v[224:227], v[160:163], a[160:163], v[224:227]\n\t" \
"v_mfma_f32_16x16x32_bf16 v[228:231], v[164:167], a[160:163], v[228:231]\n\t" \
"v_mfma_f32_16x16x32_bf16 v[232:235], v[160:163], a[216:219], v[232:235]\n\t" \
"v_mfma_f32_16x16x32_bf16 v[236:239], v[164:167], a[216:219], v[236:239]\n\t" \
"v_mfma_f32_16x16x32_bf16 v[208:211], v[168:171], a[52:55], v[208:211]\n\t" \
"v_mfma_f32_16x16x32_bf16 v[212:215], v[172:175], a[52:55], v[212:215]\n\t" \
"v_mfma_f32_16x16x32_bf16 v[216:219], v[168:171], a[108:111], v[216:219]\n\t" \
"v_mfma_f32_16x16x32_bf16 v[220:223], v[172:175], a[108:111], v[220:223]\n\t" \
"v_mfma_f32_16x16x32_bf16 v[224:227], v[168:171], a[164:167], v[224:227]\n\t" \
"v_mfma_f32_16x16x32_bf16 v[228:231], v[172:175], a[164:167], v[228:231]\n\t" \
"v_mfma_f32_16x16x32_bf16 v[232:235], v[168:171], a[220:223], v[232:235]\n\t" \
"v_mfma_f32_16x16x32_bf16 v[236:239], v[172:175], a[220:223], v[236:239]\n\t" \
"s_add_u32 s92, s92, 28672\n\t" \
"s_cmp_eq_u32 s92, 114688\n\t" \
"s_cselect_b32 s92, 0, s92\n\t" \
"v_mov_b32 v248, v249\n\t" \
"s_add_u32 s91, s91, 917504\n\t" \
"s_waitcnt lgkmcnt(0)\n\t" \
"v_mfma_f32_16x16x32_bf16 v[176:179], v[64:67], a[0:3], 0\n\t" \
"v_mfma_f32_16x16x32_bf16 v[180:183], v[68:71], a[0:3], 0\n\t" \
"ds_read_b128 v[120:123], v248 offset:7168\n\t" \
"v_mfma_f32_16x16x32_bf16 v[184:187], v[64:67], a[56:59], 0\n\t" \
"v_cvt_pk_bf16_f32 v240, v208, v209\n\t" \
"v_mfma_f32_16x16x32_bf16 v[188:191], v[68:71], a[56:59], 0\n\t" \
"v_cvt_pk_bf16_f32 v241, v210, v211\n\t" \
"v_mfma_f32_16x16x32_bf16 v[192:195], v[64:67], a[112:115], 0\n\t" \
"v_cvt_pk_bf16_f32 v242, v212, v213\n\t" \
"v_mfma_f32_16x16x32_bf16 v[196:199], v[68:71], a[112:115], 0\n\t" \
"v_cvt_pk_bf16_f32 v243, v214, v215\n\t" \
"v_mfma_f32_16x16x32_bf16 v[200:203], v[64:67], a[168:171], 0\n\t" \
"buffer_store_dwordx4 v[240:243], %[vs0], %[ro], s91 offen offset:0 sc1\n\t" \
"v_mfma_f32_16x16x32_bf16 v[204:207], v[68:71], a[168:171], 0\n\t" \
"v_cvt_pk_bf16_f32 v244, v216, v217\n\t" \
"v_mfma_f32_16x16x32_bf16 v[176:179], v[72:75], a[4:7], v[176:179]\n\t" \
"v_cvt_pk_bf16_f32 v245, v218, v219\n\t" \
"v_mfma_f32_16x16x32_bf16 v[180:183], v[76:79], a[4:7], v[180:183]\n\t" \
"v_cvt_pk_bf16_f32 v246, v220, v221\n\t" \
"v_mfma_f32_16x16x32_bf16 v[184:187], v[72:75], a[60:63], v[184:187]\n\t" \
"v_cvt_pk_bf16_f32 v247, v222, v223\n\t" \
"v_mfma_f32_16x16x32_bf16 v[188:191], v[76:79], a[60:63], v[188:191]\n\t" \
"buffer_store_dwordx4 v[244:247], %[vs0], %[ro], s91 offen offset:1024 sc1\n\t" \
"v_mfma_f32_16x16x32_bf16 v[192:195], v[72:75], a[116:119], v[192:195]\n\t" \
"v_cvt_pk_bf16_f32 v240, v224, v225\n\t" \
"v_mfma_f32_16x16x32_bf16 v[196:199], v[76:79], a[116:119], v[196:199]\n\t" \
"v_cvt_pk_bf16_f32 v241, v226, v227\n\t" \
"v_mfma_f32_16x16x32_bf16 v[200:203], v[72:75], a[172:175], v[200:203]\n\t" \
"v_cvt_pk_bf16_f32 v242, v228, v229\n\t" \
"v_mfma_f32_16x16x32_bf16 v[204:207], v[76:79], a[172:175], v[204:207]\n\t" \
"v_cvt_pk_bf16_f32 v243, v230, v231\n\t" \
"v_mfma_f32_16x16x32_bf16 v[176:179], v[80:83], a[8:11], v[176:179]\n\t" \
"buffer_store_dwordx4 v[240:243], %[vs0], %[ro], s91 offen offset:2048 sc1\n\t" \
"v_mfma_f32_16x16x32_bf16 v[180:183], v[84:87], a[8:11], v[180:183]\n\t" \
"v_cvt_pk_bf16_f32 v244, v232, v233\n\t" \
"v_mfma_f32_16x16x32_bf16 v[184:187], v[80:83], a[64:67], v[184:187]\n\t" \
"v_cvt_pk_bf16_f32 v245, v234, v235\n\t" \
"v_mfma_f32_16x16x32_bf16 v[188:191], v[84:87], a[64:67], v[188:191]\n\t" \
"v_cvt_pk_bf16_f32 v246, v236, v237\n\t" \
"s_add_u32 s90, s87, %[lg]\n\t" \
"s_add_u32 s90, s90, 0\n\t" \
"s_mov_b32 m0, s90\n\t" \
"s_add_u32 s95, s89, 0\n\t" \
"buffer_load_dwordx4 %[vg], %[rh], s95 offen offset:2048 lds\n\t" \
"v_mfma_f32_16x16x32_bf16 v[192:195], v[80:83], a[120:123], v[192:195]\n\t" \
"v_cvt_pk_bf16_f32 v247, v238, v239\n\t" \
"v_mfma_f32_16x16x32_bf16 v[196:199], v[84:87], a[120:123], v[196:199]\n\t" \
"buffer_store_dwordx4 v[244:247], %[vs0], %[ro], s91 offen offset:3072 sc1\n\t" \
"v_mfma_f32_16x16x32_bf16 v[200:203], v[80:83], a[176:179], v[200:203]\n\t" \
"v_mfma_f32_16x16x32_bf16 v[204:207], v[84:87], a[176:179], v[204:207]\n\t" \
"ds_read_b128 v[124:127], v248 offset:21504\n\t" \
"v_mfma_f32_16x16x32_bf16 v[176:179], v[88:91], a[12:15], v[176:179]\n\t" \
"v_mfma_f32_16x16x32_bf16 v[180:183], v[92:95], a[12:15], v[180:183]\n\t" \
"ds_read_b128 v[128:131], v248 offset:8192\n\t" \
"v_mfma_f32_16x16x32_bf16 v[184:187], v[88:91], a[68:71], v[184:187]\n\t" \
"v_mfma_f32_16x16x32_bf16 v[188:191], v[92:95], a[68:71], v[188:191]\n\t" \
"ds_read_b128 v[132:135], v248 offset:22528\n\t" \
"v_mfma_f32_16x16x32_bf16 v[192:195], v[88:91], a[124:127], v[192:195]\n\t" \
"v_mfma_f32_16x16x32_bf16 v[196:199], v[92:95], a[124:127], v[196:199]\n\t" \
"ds_read_b128 v[136:139], v248 offset:9216\n\t" \
"v_mfma_f32_16x16x32_bf16 v[200:203], v[88:91], a[180:183], v[200:203]\n\t" \
"v_mfma_f32_16x16x32_bf16 v[204:207], v[92:95], a[180:183], v[204:207]\n\t" \
"ds_read_b128 v[140:143], v248 offset:23552\n\t" \
"v_mfma_f32_16x16x32_bf16 v[176:179], v[96:99], a[16:19], v[176:179]\n\t" \
"v_mfma_f32_16x16x32_bf16 v[180:183], v[100:103], a[16:19], v[180:183]\n\t" \
"ds_read_b128 v[144:147], v248 offset:10240\n\t" \
"v_mfma_f32_16x16x32_bf16 v[184:187], v[96:99], a[72:75], v[184:187]\n\t" \
"v_mfma_f32_16x16x32_bf16 v[188:191], v[100:103], a[72:75], v[188:191]\n\t" \
"ds_read_b128 v[148:151], v248 offset:24576\n\t" \
"v_mfma_f32_16x16x32_bf16 v[192:195], v[96:99], a[128:131], v[192:195]\n\t" \
"v_mfma_f32_16x16x32_bf16 v[196:199], v[100:103], a[128:131], v[196:199]\n\t" \
"ds_read_b128 v[152:155], v248 offset:11264\n\t" \
"v_mfma_f32_16x16x32_bf16 v[200:203], v[96:99], a[184:187], v[200:203]\n\t" \
"v_mfma_f32_16x16x32_bf16 v[204:207], v[100:103], a[184:187], v[204:207]\n\t" \
"ds_read_b128 v[156:159], v248 offset:25600\n\t" \
"buffer_load_dwordx4 %[vg], %[rh], s95 offen offset:3072 lds\n\t" \
"v_mfma_f32_16x16x32_bf16 v[176:179], v[104:107], a[20:23], v[176:179]\n\t" \
"v_mfma_f32_16x16x32_bf16 v[180:183], v[108:111], a[20:23], v[180:183]\n\t" \
"ds_read_b128 v[160:163], v248 offset:12288\n\t" \
"v_mfma_f32_16x16x32_bf16 v[184:187], v[104:107], a[76:79], v[184:187]\n\t" \
"v_mfma_f32_16x16x32_bf16 v[188:191], v[108:111], a[76:79], v[188:191]\n\t" \
"ds_read_b128 v[164:167], v248 offset:26624\n\t" \
"v_mfma_f32_16x16x32_bf16 v[192:195], v[104:107], a[132:135], v[192:195]\n\t" \
"v_mfma_f32_16x16x32_bf16 v[196:199], v[108:111], a[132:135], v[196:199]\n\t" \
"ds_read_b128 v[168:171], v248 offset:13312\n\t" \
"v_mfma_f32_16x16x32_bf16 v[200:203], v[104:107], a[188:191], v[200:203]\n\t" \
"v_mfma_f32_16x16x32_bf16 v[204:207], v[108:111], a[188:191], v[204:207]\n\t" \
"ds_read_b128 v[172:175], v248 offset:27648\n\t" \
"v_mfma_f32_16x16x32_bf16 v[176:179], v[112:115], a[24:27], v[176:179]\n\t" \
"v_mfma_f32_16x16x32_bf16 v[180:183], v[116:119], a[24:27], v[180:183]\n\t" \
"v_mfma_f32_16x16x32_bf16 v[184:187], v[112:115], a[80:83], v[184:187]\n\t" \
"v_mfma_f32_16x16x32_bf16 v[188:191], v[116:119], a[80:83], v[188:191]\n\t" \
"v_mfma_f32_16x16x32_bf16 v[192:195], v[112:115], a[136:139], v[192:195]\n\t" \
"v_mfma_f32_16x16x32_bf16 v[196:199], v[116:119], a[136:139], v[196:199]\n\t" \
"v_mfma_f32_16x16x32_bf16 v[200:203], v[112:115], a[192:195], v[200:203]\n\t" \
"v_mfma_f32_16x16x32_bf16 v[204:207], v[116:119], a[192:195], v[204:207]\n\t" \
"s_add_u32 s89, s89, 57344\n\t" \
"s_min_u32 s89, s89, 1806336\n\t" \
"s_mov_b32 s87, s92\n\t" \
"s_add_u32 s95, s92, 28672\n\t" \
"s_cmp_eq_u32 s95, 114688\n\t" \
"s_cselect_b32 s95, 0, s95\n\t" \
"v_add_u32 v249, s95, %[lr]\n\t" \
"s_waitcnt vmcnt(8) lgkmcnt(0)\n\t" \
"s_barrier\n\t" \
"v_mfma_f32_16x16x32_bf16 v[176:179], v[120:123], a[28:31], v[176:179]\n\t" \
"ds_read_b128 v[64:67], v249 offset:0\n\t" \
"v_mfma_f32_16x16x32_bf16 v[180:183], v[124:127], a[28:31], v[180:183]\n\t" \
"v_mfma_f32_16x16x32_bf16 v[184:187], v[120:123], a[84:87], v[184:187]\n\t" \
"ds_read_b128 v[68:71], v249 offset:14336\n\t" \
"v_mfma_f32_16x16x32_bf16 v[188:191], v[124:127], a[84:87], v[188:191]\n\t" \
"v_mfma_f32_16x16x32_bf16 v[192:195], v[120:123], a[140:143], v[192:195]\n\t" \
"ds_read_b128 v[72:75], v249 offset:1024\n\t" \
"v_mfma_f32_16x16x32_bf16 v[196:199], v[124:127], a[140:143], v[196:199]\n\t" \
"v_mfma_f32_16x16x32_bf16 v[200:203], v[120:123], a[196:199], v[200:203]\n\t" \
"ds_read_b128 v[76:79], v249 offset:15360\n\t" \
"v_mfma_f32_16x16x32_bf16 v[204:207], v[124:127], a[196:199], v[204:207]\n\t" \
"v_mfma_f32_16x16x32_bf16 v[176:179], v[128:131], a[32:35], v[176:179]\n\t" \
"ds_read_b128 v[80:83], v249 offset:2048\n\t" \
"v_mfma_f32_16x16x32_bf16 v[180:183], v[132:135], a[32:35], v[180:183]\n\t" \
"v_mfma_f32_16x16x32_bf16 v[184:187], v[128:131], a[88:91], v[184:187]\n\t" \
"ds_read_b128 v[84:87], v249 offset:16384\n\t" \
"v_mfma_f32_16x16x32_bf16 v[188:191], v[132:135], a[88:91], v[188:191]\n\t" \
"v_mfma_f32_16x16x32_bf16 v[192:195], v[128:131], a[144:147], v[192:195]\n\t" \
"ds_read_b128 v[88:91], v249 offset:3072\n\t" \
"v_mfma_f32_16x16x32_bf16 v[196:199], v[132:135], a[144:147], v[196:199]\n\t" \
"v_mfma_f32_16x16x32_bf16 v[200:203], v[128:131], a[200:203], v[200:203]\n\t" \
"ds_read_b128 v[92:95], v249 offset:17408\n\t" \
"v_mfma_f32_16x16x32_bf16 v[204:207], v[132:135], a[200:203], v[204:207]\n\t" \
"v_mfma_f32_16x16x32_bf16 v[176:179], v[136:139], a[36:39], v[176:179]\n\t" \
"ds_read_b128 v[96:99], v249 offset:4096\n\t" \
"v_mfma_f32_16x16x32_bf16 v[180:183], v[140:143], a[36:39], v[180:183]\n\t" \
"v_mfma_f32_16x16x32_bf16 v[184:187], v[136:139], a[92:95], v[184:187]\n\t" \
"ds_read_b128 v[100:103], v249 offset:18432\n\t" \
"v_mfma_f32_16x16x32_bf16 v[188:191], v[140:143], a[92:95], v[188:191]\n\t" \
"s_add_u32 s90, s87, %[lg]\n\t" \
"s_add_u32 s90, s90, 0\n\t" \
"s_mov_b32 m0, s90\n\t" \
"s_add_u32 s95, s89, 0\n\t" \
"buffer_load_dwordx4 %[vg], %[rh], s95 offen offset:0 lds\n\t" \
"v_mfma_f32_16x16x32_bf16 v[192:195], v[136:139], a[148:151], v[192:195]\n\t" \
"ds_read_b128 v[104:107], v249 offset:5120\n\t" \
"v_mfma_f32_16x16x32_bf16 v[196:199], v[140:143], a[148:151], v[196:199]\n\t" \
"v_mfma_f32_16x16x32_bf16 v[200:203], v[136:139], a[204:207], v[200:203]\n\t" \
"ds_read_b128 v[108:111], v249 offset:19456\n\t" \
"v_mfma_f32_16x16x32_bf16 v[204:207], v[140:143], a[204:207], v[204:207]\n\t" \
"v_mfma_f32_16x16x32_bf16 v[176:179], v[144:147], a[40:43], v[176:179]\n\t" \
"ds_read_b128 v[112:115], v249 offset:6144\n\t" \
"v_mfma_f32_16x16x32_bf16 v[180:183], v[148:151], a[40:43], v[180:183]\n\t" \
"v_mfma_f32_16x16x32_bf16 v[184:187], v[144:147], a[96:99], v[184:187]\n\t" \
"ds_read_b128 v[116:119], v249 offset:20480\n\t" \
"v_mfma_f32_16x16x32_bf16 v[188:191], v[148:151], a[96:99], v[188:191]\n\t" \
"v_mfma_f32_16x16x32_bf16 v[192:195], v[144:147], a[152:155], v[192:195]\n\t" \
"v_mfma_f32_16x16x32_bf16 v[196:199], v[148:151], a[152:155], v[196:199]\n\t" \
"v_mfma_f32_16x16x32_bf16 v[200:203], v[144:147], a[208:211], v[200:203]\n\t" \
"v_mfma_f32_16x16x32_bf16 v[204:207], v[148:151], a[208:211], v[204:207]\n\t" \
"v_mfma_f32_16x16x32_bf16 v[176:179], v[152:155], a[44:47], v[176:179]\n\t" \
"v_mfma_f32_16x16x32_bf16 v[180:183], v[156:159], a[44:47], v[180:183]\n\t" \
"v_mfma_f32_16x16x32_bf16 v[184:187], v[152:155], a[100:103], v[184:187]\n\t" \
"v_mfma_f32_16x16x32_bf16 v[188:191], v[156:159], a[100:103], v[188:191]\n\t" \
"v_mfma_f32_16x16x32_bf16 v[192:195], v[152:155], a[156:159], v[192:195]\n\t" \
"v_mfma_f32_16x16x32_bf16 v[196:199], v[156:159], a[156:159], v[196:199]\n\t" \
"v_mfma_f32_16x16x32_bf16 v[200:203], v[152:155], a[212:215], v[200:203]\n\t" \
"v_mfma_f32_16x16x32_bf16 v[204:207], v[156:159], a[212:215], v[204:207]\n\t" \
"buffer_load_dwordx4 %[vg], %[rh], s95 offen offset:1024 lds\n\t" \
"v_mfma_f32_16x16x32_bf16 v[176:179], v[160:163], a[48:51], v[176:179]\n\t" \
"v_mfma_f32_16x16x32_bf16 v[180:183], v[164:167], a[48:51], v[180:183]\n\t" \
"v_mfma_f32_16x16x32_bf16 v[184:187], v[160:163], a[104:107], v[184:187]\n\t" \
"v_mfma_f32_16x16x32_bf16 v[188:191], v[164:167], a[104:107], v[188:191]\n\t" \
"v_mfma_f32_16x16x32_bf16 v[192:195], v[160:163], a[160:163], v[192:195]\n\t" \
"v_mfma_f32_16x16x32_bf16 v[196:199], v[164:167], a[160:163], v[196:199]\n\t" \
"v_mfma_f32_16x16x32_bf16 v[200:203], v[160:163], a[216:219], v[200:203]\n\t" \
"v_mfma_f32_16x16x32_bf16 v[204:207], v[164:167], a[216:219], v[204:207]\n\t" \
"v_mfma_f32_16x16x32_bf16 v[176:179], v[168:171], a[52:55], v[176:179]\n\t" \
"v_mfma_f32_16x16x32_bf16 v[180:183], v[172:175], a[52:55], v[180:183]\n\t" \
"v_mfma_f32_16x16x32_bf16 v[184:187], v[168:171], a[108:111], v[184:187]\n\t" \
"v_mfma_f32_16x16x32_bf16 v[188:191], v[172:175], a[108:111], v[188:191]\n\t" \
"v_mfma_f32_16x16x32_bf16 v[192:195], v[168:171], a[164:167], v[192:195]\n\t" \
"v_mfma_f32_16x16x32_bf16 v[196:199], v[172:175], a[164:167], v[196:199]\n\t" \
"v_mfma_f32_16x16x32_bf16 v[200:203], v[168:171], a[220:223], v[200:203]\n\t" \
"v_mfma_f32_16x16x32_bf16 v[204:207], v[172:175], a[220:223], v[204:207]\n\t" \
"s_add_u32 s92, s92, 28672\n\t" \
"s_cmp_eq_u32 s92, 114688\n\t" \
"s_cselect_b32 s92, 0, s92\n\t" \
"v_mov_b32 v248, v249\n\t" \
"s_add_u32 s91, s91, 917504\n\t" \
"s_waitcnt lgkmcnt(0)\n\t" \
"v_mfma_f32_16x16x32_bf16 v[208:211], v[64:67], a[0:3], 0\n\t" \
"v_mfma_f32_16x16x32_bf16 v[212:215], v[68:71], a[0:3], 0\n\t" \
"ds_read_b128 v[120:123], v248 offset:7168\n\t" \
"v_mfma_f32_16x16x32_bf16 v[216:219], v[64:67], a[56:59], 0\n\t" \
"v_cvt_pk_bf16_f32 v240, v176, v177\n\t" \
"v_mfma_f32_16x16x32_bf16 v[220:223], v[68:71], a[56:59], 0\n\t" \
"v_cvt_pk_bf16_f32 v241, v178, v179\n\t" \
"v_mfma_f32_16x16x32_bf16 v[224:227], v[64:67], a[112:115], 0\n\t" \
"v_cvt_pk_bf16_f32 v242, v180, v181\n\t" \
"v_mfma_f32_16x16x32_bf16 v[228:231], v[68:71], a[112:115], 0\n\t" \
"v_cvt_pk_bf16_f32 v243, v182, v183\n\t" \
"v_mfma_f32_16x16x32_bf16 v[232:235], v[64:67], a[168:171], 0\n\t" \
"buffer_store_dwordx4 v[240:243], %[vs0], %[ro], s91 offen offset:0 sc1\n\t" \
"v_mfma_f32_16x16x32_bf16 v[236:239], v[68:71], a[168:171], 0\n\t" \
"v_cvt_pk_bf16_f32 v244, v184, v185\n\t" \
"v_mfma_f32_16x16x32_bf16 v[208:211], v[72:75], a[4:7], v[208:211]\n\t" \
"v_cvt_pk_bf16_f32 v245, v186, v187\n\t" \
"v_mfma_f32_16x16x32_bf16 v[212:215], v[76:79], a[4:7], v[212:215]\n\t" \
"v_cvt_pk_bf16_f32 v246, v188, v189\n\t" \
"v_mfma_f32_16x16x32_bf16 v[216:219], v[72:75], a[60:63], v[216:219]\n\t" \
"v_cvt_pk_bf16_f32 v247, v190, v191\n\t" \
"v_mfma_f32_16x16x32_bf16 v[220:223], v[76:79], a[60:63], v[220:223]\n\t" \
"buffer_store_dwordx4 v[244:247], %[vs0], %[ro], s91 offen offset:1024 sc1\n\t" \
"v_mfma_f32_16x16x32_bf16 v[224:227], v[72:75], a[116:119], v[224:227]\n\t" \
"v_cvt_pk_bf16_f32 v240, v192, v193\n\t" \
"v_mfma_f32_16x16x32_bf16 v[228:231], v[76:79], a[116:119], v[228:231]\n\t" \
"v_cvt_pk_bf16_f32 v241, v194, v195\n\t" \
"v_mfma_f32_16x16x32_bf16 v[232:235], v[72:75], a[172:175], v[232:235]\n\t" \
"v_cvt_pk_bf16_f32 v242, v196, v197\n\t" \
"v_mfma_f32_16x16x32_bf16 v[236:239], v[76:79], a[172:175], v[236:239]\n\t" \
"v_cvt_pk_bf16_f32 v243, v198, v199\n\t" \
"v_mfma_f32_16x16x32_bf16 v[208:211], v[80:83], a[8:11], v[208:211]\n\t" \
"buffer_store_dwordx4 v[240:243], %[vs0], %[ro], s91 offen offset:2048 sc1\n\t" \
"v_mfma_f32_16x16x32_bf16 v[212:215], v[84:87], a[8:11], v[212:215]\n\t" \
"v_cvt_pk_bf16_f32 v244, v200, v201\n\t" \
"v_mfma_f32_16x16x32_bf16 v[216:219], v[80:83], a[64:67], v[216:219]\n\t" \
"v_cvt_pk_bf16_f32 v245, v202, v203\n\t" \
"v_mfma_f32_16x16x32_bf16 v[220:223], v[84:87], a[64:67], v[220:223]\n\t" \
"v_cvt_pk_bf16_f32 v246, v204, v205\n\t" \
"s_add_u32 s90, s87, %[lg]\n\t" \
"s_add_u32 s90, s90, 0\n\t" \
"s_mov_b32 m0, s90\n\t" \
"s_add_u32 s95, s89, 0\n\t" \
"buffer_load_dwordx4 %[vg], %[rh], s95 offen offset:2048 lds\n\t" \
"v_mfma_f32_16x16x32_bf16 v[224:227], v[80:83], a[120:123], v[224:227]\n\t" \
"v_cvt_pk_bf16_f32 v247, v206, v207\n\t" \
"v_mfma_f32_16x16x32_bf16 v[228:231], v[84:87], a[120:123], v[228:231]\n\t" \
"buffer_store_dwordx4 v[244:247], %[vs0], %[ro], s91 offen offset:3072 sc1\n\t" \
"v_mfma_f32_16x16x32_bf16 v[232:235], v[80:83], a[176:179], v[232:235]\n\t" \
"v_mfma_f32_16x16x32_bf16 v[236:239], v[84:87], a[176:179], v[236:239]\n\t" \
"ds_read_b128 v[124:127], v248 offset:21504\n\t" \
"v_mfma_f32_16x16x32_bf16 v[208:211], v[88:91], a[12:15], v[208:211]\n\t" \
"v_mfma_f32_16x16x32_bf16 v[212:215], v[92:95], a[12:15], v[212:215]\n\t" \
"ds_read_b128 v[128:131], v248 offset:8192\n\t" \
"v_mfma_f32_16x16x32_bf16 v[216:219], v[88:91], a[68:71], v[216:219]\n\t" \
"v_mfma_f32_16x16x32_bf16 v[220:223], v[92:95], a[68:71], v[220:223]\n\t" \
"ds_read_b128 v[132:135], v248 offset:22528\n\t" \
"v_mfma_f32_16x16x32_bf16 v[224:227], v[88:91], a[124:127], v[224:227]\n\t" \
"v_mfma_f32_16x16x32_bf16 v[228:231], v[92:95], a[124:127], v[228:231]\n\t" \
"ds_read_b128 v[136:139], v248 offset:9216\n\t" \
"v_mfma_f32_16x16x32_bf16 v[232:235], v[88:91], a[180:183], v[232:235]\n\t" \
"v_mfma_f32_16x16x32_bf16 v[236:239], v[92:95], a[180:183], v[236:239]\n\t" \
"ds_read_b128 v[140:143], v248 offset:23552\n\t" \
"v_mfma_f32_16x16x32_bf16 v[208:211], v[96:99], a[16:19], v[208:211]\n\t" \
"v_mfma_f32_16x16x32_bf16 v[212:215], v[100:103], a[16:19], v[212:215]\n\t" \
"ds_read_b128 v[144:147], v248 offset:10240\n\t" \
"v_mfma_f32_16x16x32_bf16 v[216:219], v[96:99], a[72:75], v[216:219]\n\t" \
"v_mfma_f32_16x16x32_bf16 v[220:223], v[100:103], a[72:75], v[220:223]\n\t" \
"ds_read_b128 v[148:151], v248 offset:24576\n\t" \
"v_mfma_f32_16x16x32_bf16 v[224:227], v[96:99], a[128:131], v[224:227]\n\t" \
"v_mfma_f32_16x16x32_bf16 v[228:231], v[100:103], a[128:131], v[228:231]\n\t" \
"ds_read_b128 v[152:155], v248 offset:11264\n\t" \
"v_mfma_f32_16x16x32_bf16 v[232:235], v[96:99], a[184:187], v[232:235]\n\t" \
"v_mfma_f32_16x16x32_bf16 v[236:239], v[100:103], a[184:187], v[236:239]\n\t" \
"ds_read_b128 v[156:159], v248 offset:25600\n\t" \
"buffer_load_dwordx4 %[vg], %[rh], s95 offen offset:3072 lds\n\t" \
"v_mfma_f32_16x16x32_bf16 v[208:211], v[104:107], a[20:23], v[208:211]\n\t" \
"v_mfma_f32_16x16x32_bf16 v[212:215], v[108:111], a[20:23], v[212:215]\n\t" \
"ds_read_b128 v[160:163], v248 offset:12288\n\t" \
"v_mfma_f32_16x16x32_bf16 v[216:219], v[104:107], a[76:79], v[216:219]\n\t" \
"v_mfma_f32_16x16x32_bf16 v[220:223], v[108:111], a[76:79], v[220:223]\n\t" \
"ds_read_b128 v[164:167], v248 offset:26624\n\t" \
"v_mfma_f32_16x16x32_bf16 v[224:227], v[104:107], a[132:135], v[224:227]\n\t" \
"v_mfma_f32_16x16x32_bf16 v[228:231], v[108:111], a[132:135], v[228:231]\n\t" \
"ds_read_b128 v[168:171], v248 offset:13312\n\t" \
"v_mfma_f32_16x16x32_bf16 v[232:235], v[104:107], a[188:191], v[232:235]\n\t" \
"v_mfma_f32_16x16x32_bf16 v[236:239], v[108:111], a[188:191], v[236:239]\n\t" \
"ds_read_b128 v[172:175], v248 offset:27648\n\t" \
"v_mfma_f32_16x16x32_bf16 v[208:211], v[112:115], a[24:27], v[208:211]\n\t" \
"v_mfma_f32_16x16x32_bf16 v[212:215], v[116:119], a[24:27], v[212:215]\n\t" \
"v_mfma_f32_16x16x32_bf16 v[216:219], v[112:115], a[80:83], v[216:219]\n\t" \
"v_mfma_f32_16x16x32_bf16 v[220:223], v[116:119], a[80:83], v[220:223]\n\t" \
"v_mfma_f32_16x16x32_bf16 v[224:227], v[112:115], a[136:139], v[224:227]\n\t" \
"v_mfma_f32_16x16x32_bf16 v[228:231], v[116:119], a[136:139], v[228:231]\n\t" \
"v_mfma_f32_16x16x32_bf16 v[232:235], v[112:115], a[192:195], v[232:235]\n\t" \
"v_mfma_f32_16x16x32_bf16 v[236:239], v[116:119], a[192:195], v[236:239]\n\t" \
"s_add_u32 s89, s89, 57344\n\t" \
"s_min_u32 s89, s89, 1806336\n\t" \
"s_mov_b32 s87, s92\n\t" \
"s_add_u32 s95, s92, 28672\n\t" \
"s_cmp_eq_u32 s95, 114688\n\t" \
"s_cselect_b32 s95, 0, s95\n\t" \
"v_add_u32 v249, s95, %[lr]\n\t" \
"s_waitcnt vmcnt(8) lgkmcnt(0)\n\t" \
"s_barrier\n\t" \
"s_add_u32 s94, s94, 128\n\t" \
"v_mfma_f32_16x16x32_bf16 v[208:211], v[120:123], a[28:31], v[208:211]\n\t" \
"ds_read_b128 v[64:67], v249 offset:0\n\t" \
"v_mfma_f32_16x16x32_bf16 v[212:215], v[124:127], a[28:31], v[212:215]\n\t" \
"v_mfma_f32_16x16x32_bf16 v[216:219], v[120:123], a[84:87], v[216:219]\n\t" \
"ds_read_b128 v[68:71], v249 offset:14336\n\t" \
"v_mfma_f32_16x16x32_bf16 v[220:223], v[124:127], a[84:87], v[220:223]\n\t" \
"v_mfma_f32_16x16x32_bf16 v[224:227], v[120:123], a[140:143], v[224:227]\n\t" \
"ds_read_b128 v[72:75], v249 offset:1024\n\t" \
"v_mfma_f32_16x16x32_bf16 v[228:231], v[124:127], a[140:143], v[228:231]\n\t" \
"v_mfma_f32_16x16x32_bf16 v[232:235], v[120:123], a[196:199], v[232:235]\n\t" \
"ds_read_b128 v[76:79], v249 offset:15360\n\t" \
"v_mfma_f32_16x16x32_bf16 v[236:239], v[124:127], a[196:199], v[236:239]\n\t" \
"v_mfma_f32_16x16x32_bf16 v[208:211], v[128:131], a[32:35], v[208:211]\n\t" \
"ds_read_b128 v[80:83], v249 offset:2048\n\t" \
"v_mfma_f32_16x16x32_bf16 v[212:215], v[132:135], a[32:35], v[212:215]\n\t" \
"v_mfma_f32_16x16x32_bf16 v[216:219], v[128:131], a[88:91], v[216:219]\n\t" \
"ds_read_b128 v[84:87], v249 offset:16384\n\t" \
"v_mfma_f32_16x16x32_bf16 v[220:223], v[132:135], a[88:91], v[220:223]\n\t" \
"v_mfma_f32_16x16x32_bf16 v[224:227], v[128:131], a[144:147], v[224:227]\n\t" \
"ds_read_b128 v[88:91], v249 offset:3072\n\t" \
"v_mfma_f32_16x16x32_bf16 v[228:231], v[132:135], a[144:147], v[228:231]\n\t" \
"v_mfma_f32_16x16x32_bf16 v[232:235], v[128:131], a[200:203], v[232:235]\n\t" \
"ds_read_b128 v[92:95], v249 offset:17408\n\t" \
"v_mfma_f32_16x16x32_bf16 v[236:239], v[132:135], a[200:203], v[236:239]\n\t" \
"v_mfma_f32_16x16x32_bf16 v[208:211], v[136:139], a[36:39], v[208:211]\n\t" \
"ds_read_b128 v[96:99], v249 offset:4096\n\t" \
"v_mfma_f32_16x16x32_bf16 v[212:215], v[140:143], a[36:39], v[212:215]\n\t" \
"v_mfma_f32_16x16x32_bf16 v[216:219], v[136:139], a[92:95], v[216:219]\n\t" \
"ds_read_b128 v[100:103], v249 offset:18432\n\t" \
"v_mfma_f32_16x16x32_bf16 v[220:223], v[140:143], a[92:95], v[220:223]\n\t" \
"s_add_u32 s90, s87, %[lg]\n\t" \
"s_add_u32 s90, s90, 0\n\t" \
"s_mov_b32 m0, s90\n\t" \
"s_add_u32 s95, s89, 0\n\t" \
"buffer_load_dwordx4 %[vg], %[rh], s95 offen offset:0 lds\n\t" \
"v_mfma_f32_16x16x32_bf16 v[224:227], v[136:139], a[148:151], v[224:227]\n\t" \
"ds_read_b128 v[104:107], v249 offset:5120\n\t" \
"v_mfma_f32_16x16x32_bf16 v[228:231], v[140:143], a[148:151], v[228:231]\n\t" \
"v_mfma_f32_16x16x32_bf16 v[232:235], v[136:139], a[204:207], v[232:235]\n\t" \
"ds_read_b128 v[108:111], v249 offset:19456\n\t" \
"v_mfma_f32_16x16x32_bf16 v[236:239], v[140:143], a[204:207], v[236:239]\n\t" \
"v_mfma_f32_16x16x32_bf16 v[208:211], v[144:147], a[40:43], v[208:211]\n\t" \
"ds_read_b128 v[112:115], v249 offset:6144\n\t" \
"v_mfma_f32_16x16x32_bf16 v[212:215], v[148:151], a[40:43], v[212:215]\n\t" \
"v_mfma_f32_16x16x32_bf16 v[216:219], v[144:147], a[96:99], v[216:219]\n\t" \
"ds_read_b128 v[116:119], v249 offset:20480\n\t" \
"v_mfma_f32_16x16x32_bf16 v[220:223], v[148:151], a[96:99], v[220:223]\n\t" \
"v_mfma_f32_16x16x32_bf16 v[224:227], v[144:147], a[152:155], v[224:227]\n\t" \
"v_mfma_f32_16x16x32_bf16 v[228:231], v[148:151], a[152:155], v[228:231]\n\t" \
"v_mfma_f32_16x16x32_bf16 v[232:235], v[144:147], a[208:211], v[232:235]\n\t" \
"v_mfma_f32_16x16x32_bf16 v[236:239], v[148:151], a[208:211], v[236:239]\n\t" \
"v_mfma_f32_16x16x32_bf16 v[208:211], v[152:155], a[44:47], v[208:211]\n\t" \
"v_mfma_f32_16x16x32_bf16 v[212:215], v[156:159], a[44:47], v[212:215]\n\t" \
"v_mfma_f32_16x16x32_bf16 v[216:219], v[152:155], a[100:103], v[216:219]\n\t" \
"v_mfma_f32_16x16x32_bf16 v[220:223], v[156:159], a[100:103], v[220:223]\n\t" \
"v_mfma_f32_16x16x32_bf16 v[224:227], v[152:155], a[156:159], v[224:227]\n\t" \
"v_mfma_f32_16x16x32_bf16 v[228:231], v[156:159], a[156:159], v[228:231]\n\t" \
"v_mfma_f32_16x16x32_bf16 v[232:235], v[152:155], a[212:215], v[232:235]\n\t" \
"v_mfma_f32_16x16x32_bf16 v[236:239], v[156:159], a[212:215], v[236:239]\n\t" \
"buffer_load_dwordx4 %[vg], %[rh], s95 offen offset:1024 lds\n\t" \
"v_mfma_f32_16x16x32_bf16 v[208:211], v[160:163], a[48:51], v[208:211]\n\t" \
"v_mfma_f32_16x16x32_bf16 v[212:215], v[164:167], a[48:51], v[212:215]\n\t" \
"v_mfma_f32_16x16x32_bf16 v[216:219], v[160:163], a[104:107], v[216:219]\n\t" \
"v_mfma_f32_16x16x32_bf16 v[220:223], v[164:167], a[104:107], v[220:223]\n\t" \
"v_mfma_f32_16x16x32_bf16 v[224:227], v[160:163], a[160:163], v[224:227]\n\t" \
"v_mfma_f32_16x16x32_bf16 v[228:231], v[164:167], a[160:163], v[228:231]\n\t" \
"v_mfma_f32_16x16x32_bf16 v[232:235], v[160:163], a[216:219], v[232:235]\n\t" \
"v_mfma_f32_16x16x32_bf16 v[236:239], v[164:167], a[216:219], v[236:239]\n\t" \
"v_mfma_f32_16x16x32_bf16 v[208:211], v[168:171], a[52:55], v[208:211]\n\t" \
"v_mfma_f32_16x16x32_bf16 v[212:215], v[172:175], a[52:55], v[212:215]\n\t" \
"v_mfma_f32_16x16x32_bf16 v[216:219], v[168:171], a[108:111], v[216:219]\n\t" \
"v_mfma_f32_16x16x32_bf16 v[220:223], v[172:175], a[108:111], v[220:223]\n\t" \
"v_mfma_f32_16x16x32_bf16 v[224:227], v[168:171], a[164:167], v[224:227]\n\t" \
"v_mfma_f32_16x16x32_bf16 v[228:231], v[172:175], a[164:167], v[228:231]\n\t" \
"v_mfma_f32_16x16x32_bf16 v[232:235], v[168:171], a[220:223], v[232:235]\n\t" \
"v_mfma_f32_16x16x32_bf16 v[236:239], v[172:175], a[220:223], v[236:239]\n\t" \
"s_add_u32 s92, s92, 28672\n\t" \
"s_cmp_eq_u32 s92, 114688\n\t" \
"s_cselect_b32 s92, 0, s92\n\t" \
"v_mov_b32 v248, v249\n\t" \
"s_add_u32 s91, s91, 917504\n\t" \
"s_mov_b32 s88, 4\n\t" \
"s_nop 1\n\t" \
"Lp3%=:\n\t" \
"s_waitcnt lgkmcnt(0)\n\t" \
"v_mfma_f32_16x16x32_bf16 v[176:179], v[64:67], a[0:3], 0\n\t" \
"v_mfma_f32_16x16x32_bf16 v[180:183], v[68:71], a[0:3], 0\n\t" \
"ds_read_b128 v[120:123], v248 offset:7168\n\t" \
"v_mfma_f32_16x16x32_bf16 v[184:187], v[64:67], a[56:59], 0\n\t" \
"v_cvt_pk_bf16_f32 v240, v208, v209\n\t" \
"v_mfma_f32_16x16x32_bf16 v[188:191], v[68:71], a[56:59], 0\n\t" \
"v_cvt_pk_bf16_f32 v241, v210, v211\n\t" \
"v_mfma_f32_16x16x32_bf16 v[192:195], v[64:67], a[112:115], 0\n\t" \
"v_cvt_pk_bf16_f32 v242, v212, v213\n\t" \
"v_mfma_f32_16x16x32_bf16 v[196:199], v[68:71], a[112:115], 0\n\t" \
"v_cvt_pk_bf16_f32 v243, v214, v215\n\t" \
"v_mfma_f32_16x16x32_bf16 v[200:203], v[64:67], a[168:171], 0\n\t" \
"buffer_store_dwordx4 v[240:243], %[vs0], %[ro], s91 offen offset:0 sc1\n\t" \
"v_mfma_f32_16x16x32_bf16 v[204:207], v[68:71], a[168:171], 0\n\t" \
"v_cvt_pk_bf16_f32 v244, v216, v217\n\t" \
"v_mfma_f32_16x16x32_bf16 v[176:179], v[72:75], a[4:7], v[176:179]\n\t" \
"v_cvt_pk_bf16_f32 v245, v218, v219\n\t" \
"v_mfma_f32_16x16x32_bf16 v[180:183], v[76:79], a[4:7], v[180:183]\n\t" \
"v_cvt_pk_bf16_f32 v246, v220, v221\n\t" \
"v_mfma_f32_16x16x32_bf16 v[184:187], v[72:75], a[60:63], v[184:187]\n\t" \
"v_cvt_pk_bf16_f32 v247, v222, v223\n\t" \
"v_mfma_f32_16x16x32_bf16 v[188:191], v[76:79], a[60:63], v[188:191]\n\t" \
"buffer_store_dwordx4 v[244:247], %[vs0], %[ro], s91 offen offset:1024 sc1\n\t" \
"v_mfma_f32_16x16x32_bf16 v[192:195], v[72:75], a[116:119], v[192:195]\n\t" \
"v_cvt_pk_bf16_f32 v240, v224, v225\n\t" \
"v_mfma_f32_16x16x32_bf16 v[196:199], v[76:79], a[116:119], v[196:199]\n\t" \
"v_cvt_pk_bf16_f32 v241, v226, v227\n\t" \
"v_mfma_f32_16x16x32_bf16 v[200:203], v[72:75], a[172:175], v[200:203]\n\t" \
"v_cvt_pk_bf16_f32 v242, v228, v229\n\t" \
"v_mfma_f32_16x16x32_bf16 v[204:207], v[76:79], a[172:175], v[204:207]\n\t" \
"v_cvt_pk_bf16_f32 v243, v230, v231\n\t" \
"v_mfma_f32_16x16x32_bf16 v[176:179], v[80:83], a[8:11], v[176:179]\n\t" \
"buffer_store_dwordx4 v[240:243], %[vs0], %[ro], s91 offen offset:2048 sc1\n\t" \
"v_mfma_f32_16x16x32_bf16 v[180:183], v[84:87], a[8:11], v[180:183]\n\t" \
"v_cvt_pk_bf16_f32 v244, v232, v233\n\t" \
"v_mfma_f32_16x16x32_bf16 v[184:187], v[80:83], a[64:67], v[184:187]\n\t" \
"v_cvt_pk_bf16_f32 v245, v234, v235\n\t" \
"v_mfma_f32_16x16x32_bf16 v[188:191], v[84:87], a[64:67], v[188:191]\n\t" \
"v_cvt_pk_bf16_f32 v246, v236, v237\n\t" \
"s_add_u32 s90, s87, %[lg]\n\t" \
"s_add_u32 s90, s90, 0\n\t" \
"s_mov_b32 m0, s90\n\t" \
"s_add_u32 s95, s89, 0\n\t" \
"buffer_load_dwordx4 %[vg], %[rh], s95 offen offset:2048 lds\n\t" \
"v_mfma_f32_16x16x32_bf16 v[192:195], v[80:83], a[120:123], v[192:195]\n\t" \
"v_cvt_pk_bf16_f32 v247, v238, v239\n\t" \
"v_mfma_f32_16x16x32_bf16 v[196:199], v[84:87], a[120:123], v[196:199]\n\t" \
"buffer_store_dwordx4 v[244:247], %[vs0], %[ro], s91 offen offset:3072 sc1\n\t" \
"v_mfma_f32_16x16x32_bf16 v[200:203], v[80:83], a[176:179], v[200:203]\n\t" \
"v_mfma_f32_16x16x32_bf16 v[204:207], v[84:87], a[176:179], v[204:207]\n\t" \
"ds_read_b128 v[124:127], v248 offset:21504\n\t" \
"v_mfma_f32_16x16x32_bf16 v[176:179], v[88:91], a[12:15], v[176:179]\n\t" \
"v_mfma_f32_16x16x32_bf16 v[180:183], v[92:95], a[12:15], v[180:183]\n\t" \
"ds_read_b128 v[128:131], v248 offset:8192\n\t" \
"v_mfma_f32_16x16x32_bf16 v[184:187], v[88:91], a[68:71], v[184:187]\n\t" \
"v_mfma_f32_16x16x32_bf16 v[188:191], v[92:95], a[68:71], v[188:191]\n\t" \
"ds_read_b128 v[132:135], v248 offset:22528\n\t" \
"v_mfma_f32_16x16x32_bf16 v[192:195], v[88:91], a[124:127], v[192:195]\n\t" \
"v_mfma_f32_16x16x32_bf16 v[196:199], v[92:95], a[124:127], v[196:199]\n\t" \
"ds_read_b128 v[136:139], v248 offset:9216\n\t" \
"v_mfma_f32_16x16x32_bf16 v[200:203], v[88:91], a[180:183], v[200:203]\n\t" \
"v_mfma_f32_16x16x32_bf16 v[204:207], v[92:95], a[180:183], v[204:207]\n\t" \
"ds_read_b128 v[140:143], v248 offset:23552\n\t" \
"v_mfma_f32_16x16x32_bf16 v[176:179], v[96:99], a[16:19], v[176:179]\n\t" \
"v_mfma_f32_16x16x32_bf16 v[180:183], v[100:103], a[16:19], v[180:183]\n\t" \
"ds_read_b128 v[144:147], v248 offset:10240\n\t" \
"v_mfma_f32_16x16x32_bf16 v[184:187], v[96:99], a[72:75], v[184:187]\n\t" \
"v_mfma_f32_16x16x32_bf16 v[188:191], v[100:103], a[72:75], v[188:191]\n\t" \
"ds_read_b128 v[148:151], v248 offset:24576\n\t" \
"v_mfma_f32_16x16x32_bf16 v[192:195], v[96:99], a[128:131], v[192:195]\n\t" \
"v_mfma_f32_16x16x32_bf16 v[196:199], v[100:103], a[128:131], v[196:199]\n\t" \
"ds_read_b128 v[152:155], v248 offset:11264\n\t" \
"v_mfma_f32_16x16x32_bf16 v[200:203], v[96:99], a[184:187], v[200:203]\n\t" \
"v_mfma_f32_16x16x32_bf16 v[204:207], v[100:103], a[184:187], v[204:207]\n\t" \
"ds_read_b128 v[156:159], v248 offset:25600\n\t" \
"buffer_load_dwordx4 %[vg], %[rh], s95 offen offset:3072 lds\n\t" \
"v_mfma_f32_16x16x32_bf16 v[176:179], v[104:107], a[20:23], v[176:179]\n\t" \
"v_mfma_f32_16x16x32_bf16 v[180:183], v[108:111], a[20:23], v[180:183]\n\t" \
"ds_read_b128 v[160:163], v248 offset:12288\n\t" \
"v_mfma_f32_16x16x32_bf16 v[184:187], v[104:107], a[76:79], v[184:187]\n\t" \
"v_mfma_f32_16x16x32_bf16 v[188:191], v[108:111], a[76:79], v[188:191]\n\t" \
"ds_read_b128 v[164:167], v248 offset:26624\n\t" \
"v_mfma_f32_16x16x32_bf16 v[192:195], v[104:107], a[132:135], v[192:195]\n\t" \
"v_mfma_f32_16x16x32_bf16 v[196:199], v[108:111], a[132:135], v[196:199]\n\t" \
"ds_read_b128 v[168:171], v248 offset:13312\n\t" \
"v_mfma_f32_16x16x32_bf16 v[200:203], v[104:107], a[188:191], v[200:203]\n\t" \
"v_mfma_f32_16x16x32_bf16 v[204:207], v[108:111], a[188:191], v[204:207]\n\t" \
"ds_read_b128 v[172:175], v248 offset:27648\n\t" \
"v_mfma_f32_16x16x32_bf16 v[176:179], v[112:115], a[24:27], v[176:179]\n\t" \
"v_mfma_f32_16x16x32_bf16 v[180:183], v[116:119], a[24:27], v[180:183]\n\t" \
"v_mfma_f32_16x16x32_bf16 v[184:187], v[112:115], a[80:83], v[184:187]\n\t" \
"v_mfma_f32_16x16x32_bf16 v[188:191], v[116:119], a[80:83], v[188:191]\n\t" \
"v_mfma_f32_16x16x32_bf16 v[192:195], v[112:115], a[136:139], v[192:195]\n\t" \
"v_mfma_f32_16x16x32_bf16 v[196:199], v[116:119], a[136:139], v[196:199]\n\t" \
"v_mfma_f32_16x16x32_bf16 v[200:203], v[112:115], a[192:195], v[200:203]\n\t" \
"v_mfma_f32_16x16x32_bf16 v[204:207], v[116:119], a[192:195], v[204:207]\n\t" \
"s_add_u32 s89, s89, 57344\n\t" \
"s_min_u32 s89, s89, 1806336\n\t" \
"s_mov_b32 s87, s92\n\t" \
"s_add_u32 s95, s92, 28672\n\t" \
"s_cmp_eq_u32 s95, 114688\n\t" \
"s_cselect_b32 s95, 0, s95\n\t" \
"v_add_u32 v249, s95, %[lr]\n\t" \
"s_waitcnt vmcnt(8) lgkmcnt(0)\n\t" \
"s_barrier\n\t" \
"s_add_u32 s94, s94, 128\n\t" \
"v_mfma_f32_16x16x32_bf16 v[176:179], v[120:123], a[28:31], v[176:179]\n\t" \
"ds_read_b128 v[64:67], v249 offset:0\n\t" \
"v_mfma_f32_16x16x32_bf16 v[180:183], v[124:127], a[28:31], v[180:183]\n\t" \
"v_mfma_f32_16x16x32_bf16 v[184:187], v[120:123], a[84:87], v[184:187]\n\t" \
"ds_read_b128 v[68:71], v249 offset:14336\n\t" \
"v_mfma_f32_16x16x32_bf16 v[188:191], v[124:127], a[84:87], v[188:191]\n\t" \
"v_mfma_f32_16x16x32_bf16 v[192:195], v[120:123], a[140:143], v[192:195]\n\t" \
"ds_read_b128 v[72:75], v249 offset:1024\n\t" \
"v_mfma_f32_16x16x32_bf16 v[196:199], v[124:127], a[140:143], v[196:199]\n\t" \
"v_mfma_f32_16x16x32_bf16 v[200:203], v[120:123], a[196:199], v[200:203]\n\t" \
"ds_read_b128 v[76:79], v249 offset:15360\n\t" \
"v_mfma_f32_16x16x32_bf16 v[204:207], v[124:127], a[196:199], v[204:207]\n\t" \
"v_mfma_f32_16x16x32_bf16 v[176:179], v[128:131], a[32:35], v[176:179]\n\t" \
"ds_read_b128 v[80:83], v249 offset:2048\n\t" \
"v_mfma_f32_16x16x32_bf16 v[180:183], v[132:135], a[32:35], v[180:183]\n\t" \
"v_mfma_f32_16x16x32_bf16 v[184:187], v[128:131], a[88:91], v[184:187]\n\t" \
"ds_read_b128 v[84:87], v249 offset:16384\n\t" \
"v_mfma_f32_16x16x32_bf16 v[188:191], v[132:135], a[88:91], v[188:191]\n\t" \
"v_mfma_f32_16x16x32_bf16 v[192:195], v[128:131], a[144:147], v[192:195]\n\t" \
"ds_read_b128 v[88:91], v249 offset:3072\n\t" \
"v_mfma_f32_16x16x32_bf16 v[196:199], v[132:135], a[144:147], v[196:199]\n\t" \
"v_mfma_f32_16x16x32_bf16 v[200:203], v[128:131], a[200:203], v[200:203]\n\t" \
"ds_read_b128 v[92:95], v249 offset:17408\n\t" \
"v_mfma_f32_16x16x32_bf16 v[204:207], v[132:135], a[200:203], v[204:207]\n\t" \
"v_mfma_f32_16x16x32_bf16 v[176:179], v[136:139], a[36:39], v[176:179]\n\t" \
"ds_read_b128 v[96:99], v249 offset:4096\n\t" \
"v_mfma_f32_16x16x32_bf16 v[180:183], v[140:143], a[36:39], v[180:183]\n\t" \
"v_mfma_f32_16x16x32_bf16 v[184:187], v[136:139], a[92:95], v[184:187]\n\t" \
"ds_read_b128 v[100:103], v249 offset:18432\n\t" \
"v_mfma_f32_16x16x32_bf16 v[188:191], v[140:143], a[92:95], v[188:191]\n\t" \
"s_add_u32 s90, s87, %[lg]\n\t" \
"s_add_u32 s90, s90, 0\n\t" \
"s_mov_b32 m0, s90\n\t" \
"s_add_u32 s95, s89, 0\n\t" \
"buffer_load_dwordx4 %[vg], %[rh], s95 offen offset:0 lds\n\t" \
"v_mfma_f32_16x16x32_bf16 v[192:195], v[136:139], a[148:151], v[192:195]\n\t" \
"ds_read_b128 v[104:107], v249 offset:5120\n\t" \
"v_mfma_f32_16x16x32_bf16 v[196:199], v[140:143], a[148:151], v[196:199]\n\t" \
"v_mfma_f32_16x16x32_bf16 v[200:203], v[136:139], a[204:207], v[200:203]\n\t" \
"ds_read_b128 v[108:111], v249 offset:19456\n\t" \
"v_mfma_f32_16x16x32_bf16 v[204:207], v[140:143], a[204:207], v[204:207]\n\t" \
"v_mfma_f32_16x16x32_bf16 v[176:179], v[144:147], a[40:43], v[176:179]\n\t" \
"ds_read_b128 v[112:115], v249 offset:6144\n\t" \
"v_mfma_f32_16x16x32_bf16 v[180:183], v[148:151], a[40:43], v[180:183]\n\t" \
"v_mfma_f32_16x16x32_bf16 v[184:187], v[144:147], a[96:99], v[184:187]\n\t" \
"ds_read_b128 v[116:119], v249 offset:20480\n\t" \
"v_mfma_f32_16x16x32_bf16 v[188:191], v[148:151], a[96:99], v[188:191]\n\t" \
"v_mfma_f32_16x16x32_bf16 v[192:195], v[144:147], a[152:155], v[192:195]\n\t" \
"v_mfma_f32_16x16x32_bf16 v[196:199], v[148:151], a[152:155], v[196:199]\n\t" \
"v_mfma_f32_16x16x32_bf16 v[200:203], v[144:147], a[208:211], v[200:203]\n\t" \
"v_mfma_f32_16x16x32_bf16 v[204:207], v[148:151], a[208:211], v[204:207]\n\t" \
"v_mfma_f32_16x16x32_bf16 v[176:179], v[152:155], a[44:47], v[176:179]\n\t" \
"v_mfma_f32_16x16x32_bf16 v[180:183], v[156:159], a[44:47], v[180:183]\n\t" \
"v_mfma_f32_16x16x32_bf16 v[184:187], v[152:155], a[100:103], v[184:187]\n\t" \
"v_mfma_f32_16x16x32_bf16 v[188:191], v[156:159], a[100:103], v[188:191]\n\t" \
"v_mfma_f32_16x16x32_bf16 v[192:195], v[152:155], a[156:159], v[192:195]\n\t" \
"v_mfma_f32_16x16x32_bf16 v[196:199], v[156:159], a[156:159], v[196:199]\n\t" \
"v_mfma_f32_16x16x32_bf16 v[200:203], v[152:155], a[212:215], v[200:203]\n\t" \
"v_mfma_f32_16x16x32_bf16 v[204:207], v[156:159], a[212:215], v[204:207]\n\t" \
"buffer_load_dwordx4 %[vg], %[rh], s95 offen offset:1024 lds\n\t" \
"v_mfma_f32_16x16x32_bf16 v[176:179], v[160:163], a[48:51], v[176:179]\n\t" \
"v_mfma_f32_16x16x32_bf16 v[180:183], v[164:167], a[48:51], v[180:183]\n\t" \
"v_mfma_f32_16x16x32_bf16 v[184:187], v[160:163], a[104:107], v[184:187]\n\t" \
"v_mfma_f32_16x16x32_bf16 v[188:191], v[164:167], a[104:107], v[188:191]\n\t" \
"v_mfma_f32_16x16x32_bf16 v[192:195], v[160:163], a[160:163], v[192:195]\n\t" \
"v_mfma_f32_16x16x32_bf16 v[196:199], v[164:167], a[160:163], v[196:199]\n\t" \
"v_mfma_f32_16x16x32_bf16 v[200:203], v[160:163], a[216:219], v[200:203]\n\t" \
"v_mfma_f32_16x16x32_bf16 v[204:207], v[164:167], a[216:219], v[204:207]\n\t" \
"v_mfma_f32_16x16x32_bf16 v[176:179], v[168:171], a[52:55], v[176:179]\n\t" \
"v_mfma_f32_16x16x32_bf16 v[180:183], v[172:175], a[52:55], v[180:183]\n\t" \
"v_mfma_f32_16x16x32_bf16 v[184:187], v[168:171], a[108:111], v[184:187]\n\t" \
"v_mfma_f32_16x16x32_bf16 v[188:191], v[172:175], a[108:111], v[188:191]\n\t" \
"v_mfma_f32_16x16x32_bf16 v[192:195], v[168:171], a[164:167], v[192:195]\n\t" \
"v_mfma_f32_16x16x32_bf16 v[196:199], v[172:175], a[164:167], v[196:199]\n\t" \
"v_mfma_f32_16x16x32_bf16 v[200:203], v[168:171], a[220:223], v[200:203]\n\t" \
"v_mfma_f32_16x16x32_bf16 v[204:207], v[172:175], a[220:223], v[204:207]\n\t" \
"s_add_u32 s92, s92, 28672\n\t" \
"s_cmp_eq_u32 s92, 114688\n\t" \
"s_cselect_b32 s92, 0, s92\n\t" \
"v_mov_b32 v248, v249\n\t" \
"s_add_u32 s91, s91, 917504\n\t" \
"s_waitcnt lgkmcnt(0)\n\t" \
"v_mfma_f32_16x16x32_bf16 v[208:211], v[64:67], a[0:3], 0\n\t" \
"v_mfma_f32_16x16x32_bf16 v[212:215], v[68:71], a[0:3], 0\n\t" \
"ds_read_b128 v[120:123], v248 offset:7168\n\t" \
"v_mfma_f32_16x16x32_bf16 v[216:219], v[64:67], a[56:59], 0\n\t" \
"v_cvt_pk_bf16_f32 v240, v176, v177\n\t" \
"v_mfma_f32_16x16x32_bf16 v[220:223], v[68:71], a[56:59], 0\n\t" \
"v_cvt_pk_bf16_f32 v241, v178, v179\n\t" \
"v_mfma_f32_16x16x32_bf16 v[224:227], v[64:67], a[112:115], 0\n\t" \
"v_cvt_pk_bf16_f32 v242, v180, v181\n\t" \
"v_mfma_f32_16x16x32_bf16 v[228:231], v[68:71], a[112:115], 0\n\t" \
"v_cvt_pk_bf16_f32 v243, v182, v183\n\t" \
"v_mfma_f32_16x16x32_bf16 v[232:235], v[64:67], a[168:171], 0\n\t" \
"buffer_store_dwordx4 v[240:243], %[vs0], %[ro], s91 offen offset:0 sc1\n\t" \
"v_mfma_f32_16x16x32_bf16 v[236:239], v[68:71], a[168:171], 0\n\t" \
"v_cvt_pk_bf16_f32 v244, v184, v185\n\t" \
"v_mfma_f32_16x16x32_bf16 v[208:211], v[72:75], a[4:7], v[208:211]\n\t" \
"v_cvt_pk_bf16_f32 v245, v186, v187\n\t" \
"v_mfma_f32_16x16x32_bf16 v[212:215], v[76:79], a[4:7], v[212:215]\n\t" \
"v_cvt_pk_bf16_f32 v246, v188, v189\n\t" \
"v_mfma_f32_16x16x32_bf16 v[216:219], v[72:75], a[60:63], v[216:219]\n\t" \
"v_cvt_pk_bf16_f32 v247, v190, v191\n\t" \
"v_mfma_f32_16x16x32_bf16 v[220:223], v[76:79], a[60:63], v[220:223]\n\t" \
"buffer_store_dwordx4 v[244:247], %[vs0], %[ro], s91 offen offset:1024 sc1\n\t" \
"v_mfma_f32_16x16x32_bf16 v[224:227], v[72:75], a[116:119], v[224:227]\n\t" \
"v_cvt_pk_bf16_f32 v240, v192, v193\n\t" \
"v_mfma_f32_16x16x32_bf16 v[228:231], v[76:79], a[116:119], v[228:231]\n\t" \
"v_cvt_pk_bf16_f32 v241, v194, v195\n\t" \
"v_mfma_f32_16x16x32_bf16 v[232:235], v[72:75], a[172:175], v[232:235]\n\t" \
"v_cvt_pk_bf16_f32 v242, v196, v197\n\t" \
"v_mfma_f32_16x16x32_bf16 v[236:239], v[76:79], a[172:175], v[236:239]\n\t" \
"v_cvt_pk_bf16_f32 v243, v198, v199\n\t" \
"v_mfma_f32_16x16x32_bf16 v[208:211], v[80:83], a[8:11], v[208:211]\n\t" \
"buffer_store_dwordx4 v[240:243], %[vs0], %[ro], s91 offen offset:2048 sc1\n\t" \
"v_mfma_f32_16x16x32_bf16 v[212:215], v[84:87], a[8:11], v[212:215]\n\t" \
"v_cvt_pk_bf16_f32 v244, v200, v201\n\t" \
"v_mfma_f32_16x16x32_bf16 v[216:219], v[80:83], a[64:67], v[216:219]\n\t" \
"v_cvt_pk_bf16_f32 v245, v202, v203\n\t" \
"v_mfma_f32_16x16x32_bf16 v[220:223], v[84:87], a[64:67], v[220:223]\n\t" \
"v_cvt_pk_bf16_f32 v246, v204, v205\n\t" \
"s_add_u32 s90, s87, %[lg]\n\t" \
"s_add_u32 s90, s90, 0\n\t" \
"s_mov_b32 m0, s90\n\t" \
"s_add_u32 s95, s89, 0\n\t" \
"buffer_load_dwordx4 %[vg], %[rh], s95 offen offset:2048 lds\n\t" \
"v_mfma_f32_16x16x32_bf16 v[224:227], v[80:83], a[120:123], v[224:227]\n\t" \
"v_cvt_pk_bf16_f32 v247, v206, v207\n\t" \
"v_mfma_f32_16x16x32_bf16 v[228:231], v[84:87], a[120:123], v[228:231]\n\t" \
"buffer_store_dwordx4 v[244:247], %[vs0], %[ro], s91 offen offset:3072 sc1\n\t" \
"v_mfma_f32_16x16x32_bf16 v[232:235], v[80:83], a[176:179], v[232:235]\n\t" \
"v_mfma_f32_16x16x32_bf16 v[236:239], v[84:87], a[176:179], v[236:239]\n\t" \
"ds_read_b128 v[124:127], v248 offset:21504\n\t" \
"v_mfma_f32_16x16x32_bf16 v[208:211], v[88:91], a[12:15], v[208:211]\n\t" \
"v_mfma_f32_16x16x32_bf16 v[212:215], v[92:95], a[12:15], v[212:215]\n\t" \
"ds_read_b128 v[128:131], v248 offset:8192\n\t" \
"v_mfma_f32_16x16x32_bf16 v[216:219], v[88:91], a[68:71], v[216:219]\n\t" \
"v_mfma_f32_16x16x32_bf16 v[220:223], v[92:95], a[68:71], v[220:223]\n\t" \
"ds_read_b128 v[132:135], v248 offset:22528\n\t" \
"v_mfma_f32_16x16x32_bf16 v[224:227], v[88:91], a[124:127], v[224:227]\n\t" \
"v_mfma_f32_16x16x32_bf16 v[228:231], v[92:95], a[124:127], v[228:231]\n\t" \
"ds_read_b128 v[136:139], v248 offset:9216\n\t" \
"v_mfma_f32_16x16x32_bf16 v[232:235], v[88:91], a[180:183], v[232:235]\n\t" \
"v_mfma_f32_16x16x32_bf16 v[236:239], v[92:95], a[180:183], v[236:239]\n\t" \
"ds_read_b128 v[140:143], v248 offset:23552\n\t" \
"v_mfma_f32_16x16x32_bf16 v[208:211], v[96:99], a[16:19], v[208:211]\n\t" \
"v_mfma_f32_16x16x32_bf16 v[212:215], v[100:103], a[16:19], v[212:215]\n\t" \
"ds_read_b128 v[144:147], v248 offset:10240\n\t" \
"v_mfma_f32_16x16x32_bf16 v[216:219], v[96:99], a[72:75], v[216:219]\n\t" \
"v_mfma_f32_16x16x32_bf16 v[220:223], v[100:103], a[72:75], v[220:223]\n\t" \
"ds_read_b128 v[148:151], v248 offset:24576\n\t" \
"v_mfma_f32_16x16x32_bf16 v[224:227], v[96:99], a[128:131], v[224:227]\n\t" \
"v_mfma_f32_16x16x32_bf16 v[228:231], v[100:103], a[128:131], v[228:231]\n\t" \
"ds_read_b128 v[152:155], v248 offset:11264\n\t" \
"v_mfma_f32_16x16x32_bf16 v[232:235], v[96:99], a[184:187], v[232:235]\n\t" \
"v_mfma_f32_16x16x32_bf16 v[236:239], v[100:103], a[184:187], v[236:239]\n\t" \
"ds_read_b128 v[156:159], v248 offset:25600\n\t" \
"buffer_load_dwordx4 %[vg], %[rh], s95 offen offset:3072 lds\n\t" \
"v_mfma_f32_16x16x32_bf16 v[208:211], v[104:107], a[20:23], v[208:211]\n\t" \
"v_mfma_f32_16x16x32_bf16 v[212:215], v[108:111], a[20:23], v[212:215]\n\t" \
"ds_read_b128 v[160:163], v248 offset:12288\n\t" \
"v_mfma_f32_16x16x32_bf16 v[216:219], v[104:107], a[76:79], v[216:219]\n\t" \
"v_mfma_f32_16x16x32_bf16 v[220:223], v[108:111], a[76:79], v[220:223]\n\t" \
"ds_read_b128 v[164:167], v248 offset:26624\n\t" \
"v_mfma_f32_16x16x32_bf16 v[224:227], v[104:107], a[132:135], v[224:227]\n\t" \
"v_mfma_f32_16x16x32_bf16 v[228:231], v[108:111], a[132:135], v[228:231]\n\t" \
"ds_read_b128 v[168:171], v248 offset:13312\n\t" \
"v_mfma_f32_16x16x32_bf16 v[232:235], v[104:107], a[188:191], v[232:235]\n\t" \
"v_mfma_f32_16x16x32_bf16 v[236:239], v[108:111], a[188:191], v[236:239]\n\t" \
"ds_read_b128 v[172:175], v248 offset:27648\n\t" \
"v_mfma_f32_16x16x32_bf16 v[208:211], v[112:115], a[24:27], v[208:211]\n\t" \
"v_mfma_f32_16x16x32_bf16 v[212:215], v[116:119], a[24:27], v[212:215]\n\t" \
"v_mfma_f32_16x16x32_bf16 v[216:219], v[112:115], a[80:83], v[216:219]\n\t" \
"v_mfma_f32_16x16x32_bf16 v[220:223], v[116:119], a[80:83], v[220:223]\n\t" \
"v_mfma_f32_16x16x32_bf16 v[224:227], v[112:115], a[136:139], v[224:227]\n\t" \
"v_mfma_f32_16x16x32_bf16 v[228:231], v[116:119], a[136:139], v[228:231]\n\t" \
"v_mfma_f32_16x16x32_bf16 v[232:235], v[112:115], a[192:195], v[232:235]\n\t" \
"v_mfma_f32_16x16x32_bf16 v[236:239], v[116:119], a[192:195], v[236:239]\n\t" \
"s_add_u32 s89, s89, 57344\n\t" \
"s_min_u32 s89, s89, 1806336\n\t" \
"s_mov_b32 s87, s92\n\t" \
"s_add_u32 s95, s92, 28672\n\t" \
"s_cmp_eq_u32 s95, 114688\n\t" \
"s_cselect_b32 s95, 0, s95\n\t" \
"v_add_u32 v249, s95, %[lr]\n\t" \
"s_waitcnt vmcnt(8) lgkmcnt(0)\n\t" \
"s_barrier\n\t" \
"s_add_u32 s94, s94, 128\n\t" \
"v_mfma_f32_16x16x32_bf16 v[208:211], v[120:123], a[28:31], v[208:211]\n\t" \
"ds_read_b128 v[64:67], v249 offset:0\n\t" \
"v_mfma_f32_16x16x32_bf16 v[212:215], v[124:127], a[28:31], v[212:215]\n\t" \
"v_mfma_f32_16x16x32_bf16 v[216:219], v[120:123], a[84:87], v[216:219]\n\t" \
"ds_read_b128 v[68:71], v249 offset:14336\n\t" \
"v_mfma_f32_16x16x32_bf16 v[220:223], v[124:127], a[84:87], v[220:223]\n\t" \
"v_mfma_f32_16x16x32_bf16 v[224:227], v[120:123], a[140:143], v[224:227]\n\t" \
"ds_read_b128 v[72:75], v249 offset:1024\n\t" \
"v_mfma_f32_16x16x32_bf16 v[228:231], v[124:127], a[140:143], v[228:231]\n\t" \
"v_mfma_f32_16x16x32_bf16 v[232:235], v[120:123], a[196:199], v[232:235]\n\t" \
"ds_read_b128 v[76:79], v249 offset:15360\n\t" \
"v_mfma_f32_16x16x32_bf16 v[236:239], v[124:127], a[196:199], v[236:239]\n\t" \
"v_mfma_f32_16x16x32_bf16 v[208:211], v[128:131], a[32:35], v[208:211]\n\t" \
"ds_read_b128 v[80:83], v249 offset:2048\n\t" \
"v_mfma_f32_16x16x32_bf16 v[212:215], v[132:135], a[32:35], v[212:215]\n\t" \
"v_mfma_f32_16x16x32_bf16 v[216:219], v[128:131], a[88:91], v[216:219]\n\t" \
"ds_read_b128 v[84:87], v249 offset:16384\n\t" \
"v_mfma_f32_16x16x32_bf16 v[220:223], v[132:135], a[88:91], v[220:223]\n\t" \
"v_mfma_f32_16x16x32_bf16 v[224:227], v[128:131], a[144:147], v[224:227]\n\t" \
"ds_read_b128 v[88:91], v249 offset:3072\n\t" \
"v_mfma_f32_16x16x32_bf16 v[228:231], v[132:135], a[144:147], v[228:231]\n\t" \
"v_mfma_f32_16x16x32_bf16 v[232:235], v[128:131], a[200:203], v[232:235]\n\t" \
"ds_read_b128 v[92:95], v249 offset:17408\n\t" \
"v_mfma_f32_16x16x32_bf16 v[236:239], v[132:135], a[200:203], v[236:239]\n\t" \
"v_mfma_f32_16x16x32_bf16 v[208:211], v[136:139], a[36:39], v[208:211]\n\t" \
"ds_read_b128 v[96:99], v249 offset:4096\n\t" \
"v_mfma_f32_16x16x32_bf16 v[212:215], v[140:143], a[36:39], v[212:215]\n\t" \
"v_mfma_f32_16x16x32_bf16 v[216:219], v[136:139], a[92:95], v[216:219]\n\t" \
"ds_read_b128 v[100:103], v249 offset:18432\n\t" \
"v_mfma_f32_16x16x32_bf16 v[220:223], v[140:143], a[92:95], v[220:223]\n\t" \
"s_add_u32 s90, s87, %[lg]\n\t" \
"s_add_u32 s90, s90, 0\n\t" \
"s_mov_b32 m0, s90\n\t" \
"s_add_u32 s95, s89, 0\n\t" \
"buffer_load_dwordx4 %[vg], %[rh], s95 offen offset:0 lds\n\t" \
"v_mfma_f32_16x16x32_bf16 v[224:227], v[136:139], a[148:151], v[224:227]\n\t" \
"ds_read_b128 v[104:107], v249 offset:5120\n\t" \
"v_mfma_f32_16x16x32_bf16 v[228:231], v[140:143], a[148:151], v[228:231]\n\t" \
"v_mfma_f32_16x16x32_bf16 v[232:235], v[136:139], a[204:207], v[232:235]\n\t" \
"ds_read_b128 v[108:111], v249 offset:19456\n\t" \
"v_mfma_f32_16x16x32_bf16 v[236:239], v[140:143], a[204:207], v[236:239]\n\t" \
"v_mfma_f32_16x16x32_bf16 v[208:211], v[144:147], a[40:43], v[208:211]\n\t" \
"ds_read_b128 v[112:115], v249 offset:6144\n\t" \
"v_mfma_f32_16x16x32_bf16 v[212:215], v[148:151], a[40:43], v[212:215]\n\t" \
"v_mfma_f32_16x16x32_bf16 v[216:219], v[144:147], a[96:99], v[216:219]\n\t" \
"ds_read_b128 v[116:119], v249 offset:20480\n\t" \
"v_mfma_f32_16x16x32_bf16 v[220:223], v[148:151], a[96:99], v[220:223]\n\t" \
"v_mfma_f32_16x16x32_bf16 v[224:227], v[144:147], a[152:155], v[224:227]\n\t" \
"v_mfma_f32_16x16x32_bf16 v[228:231], v[148:151], a[152:155], v[228:231]\n\t" \
"v_mfma_f32_16x16x32_bf16 v[232:235], v[144:147], a[208:211], v[232:235]\n\t" \
"v_mfma_f32_16x16x32_bf16 v[236:239], v[148:151], a[208:211], v[236:239]\n\t" \
"v_mfma_f32_16x16x32_bf16 v[208:211], v[152:155], a[44:47], v[208:211]\n\t" \
"v_mfma_f32_16x16x32_bf16 v[212:215], v[156:159], a[44:47], v[212:215]\n\t" \
"v_mfma_f32_16x16x32_bf16 v[216:219], v[152:155], a[100:103], v[216:219]\n\t" \
"v_mfma_f32_16x16x32_bf16 v[220:223], v[156:159], a[100:103], v[220:223]\n\t" \
"v_mfma_f32_16x16x32_bf16 v[224:227], v[152:155], a[156:159], v[224:227]\n\t" \
"v_mfma_f32_16x16x32_bf16 v[228:231], v[156:159], a[156:159], v[228:231]\n\t" \
"v_mfma_f32_16x16x32_bf16 v[232:235], v[152:155], a[212:215], v[232:235]\n\t" \
"v_mfma_f32_16x16x32_bf16 v[236:239], v[156:159], a[212:215], v[236:239]\n\t" \
"buffer_load_dwordx4 %[vg], %[rh], s95 offen offset:1024 lds\n\t" \
"v_mfma_f32_16x16x32_bf16 v[208:211], v[160:163], a[48:51], v[208:211]\n\t" \
"v_mfma_f32_16x16x32_bf16 v[212:215], v[164:167], a[48:51], v[212:215]\n\t" \
"v_mfma_f32_16x16x32_bf16 v[216:219], v[160:163], a[104:107], v[216:219]\n\t" \
"v_mfma_f32_16x16x32_bf16 v[220:223], v[164:167], a[104:107], v[220:223]\n\t" \
"v_mfma_f32_16x16x32_bf16 v[224:227], v[160:163], a[160:163], v[224:227]\n\t" \
"v_mfma_f32_16x16x32_bf16 v[228:231], v[164:167], a[160:163], v[228:231]\n\t" \
"v_mfma_f32_16x16x32_bf16 v[232:235], v[160:163], a[216:219], v[232:235]\n\t" \
"v_mfma_f32_16x16x32_bf16 v[236:239], v[164:167], a[216:219], v[236:239]\n\t" \
"v_mfma_f32_16x16x32_bf16 v[208:211], v[168:171], a[52:55], v[208:211]\n\t" \
"v_mfma_f32_16x16x32_bf16 v[212:215], v[172:175], a[52:55], v[212:215]\n\t" \
"v_mfma_f32_16x16x32_bf16 v[216:219], v[168:171], a[108:111], v[216:219]\n\t" \
"v_mfma_f32_16x16x32_bf16 v[220:223], v[172:175], a[108:111], v[220:223]\n\t" \
"v_mfma_f32_16x16x32_bf16 v[224:227], v[168:171], a[164:167], v[224:227]\n\t" \
"v_mfma_f32_16x16x32_bf16 v[228:231], v[172:175], a[164:167], v[228:231]\n\t" \
"v_mfma_f32_16x16x32_bf16 v[232:235], v[168:171], a[220:223], v[232:235]\n\t" \
"v_mfma_f32_16x16x32_bf16 v[236:239], v[172:175], a[220:223], v[236:239]\n\t" \
"s_add_u32 s92, s92, 28672\n\t" \
"s_cmp_eq_u32 s92, 114688\n\t" \
"s_cselect_b32 s92, 0, s92\n\t" \
"v_mov_b32 v248, v249\n\t" \
"s_add_u32 s91, s91, 917504\n\t" \
"s_add_u32 s88, s88, 2\n\t" \
"s_cmp_lt_u32 s88, 32\n\t" \
"s_nop 0\n\t" \
"s_cbranch_scc1 Lp3%=\n\t" \
"v_cvt_pk_bf16_f32 v240, v208, v209\n\t" \
"v_cvt_pk_bf16_f32 v241, v210, v211\n\t" \
"v_cvt_pk_bf16_f32 v242, v212, v213\n\t" \
"v_cvt_pk_bf16_f32 v243, v214, v215\n\t" \
"buffer_store_dwordx4 v[240:243], %[vs0], %[ro], s91 offen offset:0 sc1\n\t" \
"v_cvt_pk_bf16_f32 v244, v216, v217\n\t" \
"v_cvt_pk_bf16_f32 v245, v218, v219\n\t" \
"v_cvt_pk_bf16_f32 v246, v220, v221\n\t" \
"v_cvt_pk_bf16_f32 v247, v222, v223\n\t" \
"buffer_store_dwordx4 v[244:247], %[vs0], %[ro], s91 offen offset:1024 sc1\n\t" \
"v_cvt_pk_bf16_f32 v240, v224, v225\n\t" \
"v_cvt_pk_bf16_f32 v241, v226, v227\n\t" \
"v_cvt_pk_bf16_f32 v242, v228, v229\n\t" \
"v_cvt_pk_bf16_f32 v243, v230, v231\n\t" \
"buffer_store_dwordx4 v[240:243], %[vs0], %[ro], s91 offen offset:2048 sc1\n\t" \
"v_cvt_pk_bf16_f32 v244, v232, v233\n\t" \
"v_cvt_pk_bf16_f32 v245, v234, v235\n\t" \
"v_cvt_pk_bf16_f32 v246, v236, v237\n\t" \
"v_cvt_pk_bf16_f32 v247, v238, v239\n\t" \
"buffer_store_dwordx4 v[244:247], %[vs0], %[ro], s91 offen offset:3072 sc1\n\t" \
"s_waitcnt vmcnt(0) lgkmcnt(0)\n\t" \
"s_barrier\n\t" \
"s_add_u32 s94, s94, 128\n\t" \
"s_add_u32 s94, s94, 128\n\t" \
"s_mov_b32 m0, s93\n\t" \
"s_nop 1\n\t"

#define ASM_P3_1W1_CLOB "v64", "v65", "v66", "v67", "v68", "v69", "v70", "v71", "v72", "v73", "v74", "v75", "v76", "v77", "v78", "v79", "v80", "v81", "v82", "v83", "v84", "v85", "v86", "v87", "v88", "v89", "v90", "v91", "v92", "v93", "v94", "v95", "v96", "v97", "v98", "v99", "v100", "v101", "v102", "v103", "v104", "v105", "v106", "v107", "v108", "v109", "v110", "v111", "v112", "v113", "v114", "v115", "v116", "v117", "v118", "v119", "v120", "v121", "v122", "v123", "v124", "v125", "v126", "v127", "v128", "v129", "v130", "v131", "v132", "v133", "v134", "v135", "v136", "v137", "v138", "v139", "v140", "v141", "v142", "v143", "v144", "v145", "v146", "v147", "v148", "v149", "v150", "v151", "v152", "v153", "v154", "v155", "v156", "v157", "v158", "v159", "v160", "v161", "v162", "v163", "v164", "v165", "v166", "v167", "v168", "v169", "v170", "v171", "v172", "v173", "v174", "v175", "v176", "v177", "v178", "v179", "v180", "v181", "v182", "v183", "v184", "v185", "v186", "v187", "v188", "v189", "v190", "v191", "v192", "v193", "v194", "v195", "v196", "v197", "v198", "v199", "v200", "v201", "v202", "v203", "v204", "v205", "v206", "v207", "v208", "v209", "v210", "v211", "v212", "v213", "v214", "v215", "v216", "v217", "v218", "v219", "v220", "v221", "v222", "v223", "v224", "v225", "v226", "v227", "v228", "v229", "v230", "v231", "v232", "v233", "v234", "v235", "v236", "v237", "v238", "v239", "v240", "v241", "v242", "v243", "v244", "v245", "v246", "v247", "v248", "v249", "v250", "a0", "a1", "a2", "a3", "a4", "a5", "a6", "a7", "a8", "a9", "a10", "a11", "a12", "a13", "a14", "a15", "a16", "a17", "a18", "a19", "a20", "a21", "a22", "a23", "a24", "a25", "a26", "a27", "a28", "a29", "a30", "a31", "a32", "a33", "a34", "a35", "a36", "a37", "a38", "a39", "a40", "a41", "a42", "a43", "a44", "a45", "a46", "a47", "a48", "a49", "a50", "a51", "a52", "a53", "a54", "a55", "a56", "a57", "a58", "a59", "a60", "a61", "a62", "a63", "a64", "a65", "a66", "a67", "a68", "a69", "a70", "a71", "a72", "a73", "a74", "a75", "a76", "a77", "a78", "a79", "a80", "a81", "a82", "a83", "a84", "a85", "a86", "a87", "a88", "a89", "a90", "a91", "a92", "a93", "a94", "a95", "a96", "a97", "a98", "a99", "a100", "a101", "a102", "a103", "a104", "a105", "a106", "a107", "a108", "a109", "a110", "a111", "a112", "a113", "a114", "a115", "a116", "a117", "a118", "a119", "a120", "a121", "a122", "a123", "a124", "a125", "a126", "a127", "a128", "a129", "a130", "a131", "a132", "a133", "a134", "a135", "a136", "a137", "a138", "a139", "a140", "a141", "a142", "a143", "a144", "a145", "a146", "a147", "a148", "a149", "a150", "a151", "a152", "a153", "a154", "a155", "a156", "a157", "a158", "a159", "a160", "a161", "a162", "a163", "a164", "a165", "a166", "a167", "a168", "a169", "a170", "a171", "a172", "a173", "a174", "a175", "a176", "a177", "a178", "a179", "a180", "a181", "a182", "a183", "a184", "a185", "a186", "a187", "a188", "a189", "a190", "a191", "a192", "a193", "a194", "a195", "a196", "a197", "a198", "a199", "a200", "a201", "a202", "a203", "a204", "a205", "a206", "a207", "a208", "a209", "a210", "a211", "a212", "a213", "a214", "a215", "a216", "a217", "a218", "a219", "a220", "a221", "a222", "a223", "s86", "s87", "s88", "s89", "s90", "s91", "s92", "s93", "s94", "s95", "vcc", "scc", "memory"

#define ASM_P3_1W2_TEXT \
"s_mov_b32 s93, m0\n\t" \
"s_add_u32 s89, %[src0], 172032\n\t" \
"s_mov_b32 s87, 86016\n\t" \
"s_waitcnt vmcnt(0)\n\t" \
"s_barrier\n\t" \
"v_mov_b32 v250, 1\n\t" \
"v_mov_b32 v248, %[lr]\n\t" \
"ds_read_b128 v[64:67], v248 offset:0\n\t" \
"ds_read_b128 v[68:71], v248 offset:14336\n\t" \
"ds_read_b128 v[72:75], v248 offset:1024\n\t" \
"ds_read_b128 v[76:79], v248 offset:15360\n\t" \
"ds_read_b128 v[80:83], v248 offset:2048\n\t" \
"ds_read_b128 v[84:87], v248 offset:16384\n\t" \
"ds_read_b128 v[88:91], v248 offset:3072\n\t" \
"ds_read_b128 v[92:95], v248 offset:17408\n\t" \
"ds_read_b128 v[96:99], v248 offset:4096\n\t" \
"ds_read_b128 v[100:103], v248 offset:18432\n\t" \
"ds_read_b128 v[104:107], v248 offset:5120\n\t" \
"ds_read_b128 v[108:111], v248 offset:19456\n\t" \
"ds_read_b128 v[112:115], v248 offset:6144\n\t" \
"ds_read_b128 v[116:119], v248 offset:20480\n\t" \
"s_mov_b32 s92, 0\n\t" \
"s_mov_b32 s91, %[so0]\n\t" \
"s_mov_b32 s94, %[ao0]\n\t" \
"s_waitcnt lgkmcnt(0)\n\t" \
"v_mfma_f32_16x16x32_bf16 v[176:179], v[64:67], a[0:3], 0\n\t" \
"v_mfma_f32_16x16x32_bf16 v[180:183], v[68:71], a[0:3], 0\n\t" \
"ds_read_b128 v[120:123], v248 offset:7168\n\t" \
"v_mfma_f32_16x16x32_bf16 v[184:187], v[64:67], a[56:59], 0\n\t" \
"v_mfma_f32_16x16x32_bf16 v[188:191], v[68:71], a[56:59], 0\n\t" \
"ds_read_b128 v[124:127], v248 offset:21504\n\t" \
"v_mfma_f32_16x16x32_bf16 v[192:195], v[64:67], a[112:115], 0\n\t" \
"v_mfma_f32_16x16x32_bf16 v[196:199], v[68:71], a[112:115], 0\n\t" \
"ds_read_b128 v[128:131], v248 offset:8192\n\t" \
"v_mfma_f32_16x16x32_bf16 v[176:179], v[72:75], a[4:7], v[176:179]\n\t" \
"v_mfma_f32_16x16x32_bf16 v[180:183], v[76:79], a[4:7], v[180:183]\n\t" \
"ds_read_b128 v[132:135], v248 offset:22528\n\t" \
"s_add_u32 s90, s87, %[lg]\n\t" \
"s_add_u32 s90, s90, 4096\n\t" \
"s_mov_b32 m0, s90\n\t" \
"s_add_u32 s95, s89, 4096\n\t" \
"buffer_load_dwordx4 %[vg], %[rh], s95 offen offset:1024 lds\n\t" \
"v_mfma_f32_16x16x32_bf16 v[184:187], v[72:75], a[60:63], v[184:187]\n\t" \
"v_mfma_f32_16x16x32_bf16 v[188:191], v[76:79], a[60:63], v[188:191]\n\t" \
"ds_read_b128 v[136:139], v248 offset:9216\n\t" \
"v_mfma_f32_16x16x32_bf16 v[192:195], v[72:75], a[116:119], v[192:195]\n\t" \
"v_mfma_f32_16x16x32_bf16 v[196:199], v[76:79], a[116:119], v[196:199]\n\t" \
"ds_read_b128 v[140:143], v248 offset:23552\n\t" \
"v_mfma_f32_16x16x32_bf16 v[176:179], v[80:83], a[8:11], v[176:179]\n\t" \
"v_mfma_f32_16x16x32_bf16 v[180:183], v[84:87], a[8:11], v[180:183]\n\t" \
"ds_read_b128 v[144:147], v248 offset:10240\n\t" \
"v_mfma_f32_16x16x32_bf16 v[184:187], v[80:83], a[64:67], v[184:187]\n\t" \
"v_mfma_f32_16x16x32_bf16 v[188:191], v[84:87], a[64:67], v[188:191]\n\t" \
"ds_read_b128 v[148:151], v248 offset:24576\n\t" \
"buffer_load_dwordx4 %[vg], %[rh], s95 offen offset:2048 lds\n\t" \
"v_mfma_f32_16x16x32_bf16 v[192:195], v[80:83], a[120:123], v[192:195]\n\t" \
"v_mfma_f32_16x16x32_bf16 v[196:199], v[84:87], a[120:123], v[196:199]\n\t" \
"ds_read_b128 v[152:155], v248 offset:11264\n\t" \
"v_mfma_f32_16x16x32_bf16 v[176:179], v[88:91], a[12:15], v[176:179]\n\t" \
"v_mfma_f32_16x16x32_bf16 v[180:183], v[92:95], a[12:15], v[180:183]\n\t" \
"ds_read_b128 v[156:159], v248 offset:25600\n\t" \
"v_mfma_f32_16x16x32_bf16 v[184:187], v[88:91], a[68:71], v[184:187]\n\t" \
"v_mfma_f32_16x16x32_bf16 v[188:191], v[92:95], a[68:71], v[188:191]\n\t" \
"ds_read_b128 v[160:163], v248 offset:12288\n\t" \
"v_mfma_f32_16x16x32_bf16 v[192:195], v[88:91], a[124:127], v[192:195]\n\t" \
"v_mfma_f32_16x16x32_bf16 v[196:199], v[92:95], a[124:127], v[196:199]\n\t" \
"ds_read_b128 v[164:167], v248 offset:26624\n\t" \
"buffer_load_dwordx4 %[vg], %[rh], s95 offen offset:3072 lds\n\t" \
"v_mfma_f32_16x16x32_bf16 v[176:179], v[96:99], a[16:19], v[176:179]\n\t" \
"v_mfma_f32_16x16x32_bf16 v[180:183], v[100:103], a[16:19], v[180:183]\n\t" \
"ds_read_b128 v[168:171], v248 offset:13312\n\t" \
"v_mfma_f32_16x16x32_bf16 v[184:187], v[96:99], a[72:75], v[184:187]\n\t" \
"v_mfma_f32_16x16x32_bf16 v[188:191], v[100:103], a[72:75], v[188:191]\n\t" \
"ds_read_b128 v[172:175], v248 offset:27648\n\t" \
"v_mfma_f32_16x16x32_bf16 v[192:195], v[96:99], a[128:131], v[192:195]\n\t" \
"v_mfma_f32_16x16x32_bf16 v[196:199], v[100:103], a[128:131], v[196:199]\n\t" \
"v_mfma_f32_16x16x32_bf16 v[176:179], v[104:107], a[20:23], v[176:179]\n\t" \
"v_mfma_f32_16x16x32_bf16 v[180:183], v[108:111], a[20:23], v[180:183]\n\t" \
"s_add_u32 s90, s87, %[lg]\n\t" \
"s_add_u32 s90, s90, 8192\n\t" \
"s_mov_b32 m0, s90\n\t" \
"s_add_u32 s95, s89, 8192\n\t" \
"buffer_load_dwordx4 %[vg], %[rh], s95 offen offset:0 lds\n\t" \
"v_mfma_f32_16x16x32_bf16 v[184:187], v[104:107], a[76:79], v[184:187]\n\t" \
"v_mfma_f32_16x16x32_bf16 v[188:191], v[108:111], a[76:79], v[188:191]\n\t" \
"v_mfma_f32_16x16x32_bf16 v[192:195], v[104:107], a[132:135], v[192:195]\n\t" \
"v_mfma_f32_16x16x32_bf16 v[196:199], v[108:111], a[132:135], v[196:199]\n\t" \
"v_mfma_f32_16x16x32_bf16 v[176:179], v[112:115], a[24:27], v[176:179]\n\t" \
"v_mfma_f32_16x16x32_bf16 v[180:183], v[116:119], a[24:27], v[180:183]\n\t" \
"v_mfma_f32_16x16x32_bf16 v[184:187], v[112:115], a[80:83], v[184:187]\n\t" \
"v_mfma_f32_16x16x32_bf16 v[188:191], v[116:119], a[80:83], v[188:191]\n\t" \
"buffer_load_dwordx4 %[vg], %[rh], s95 offen offset:1024 lds\n\t" \
"v_mfma_f32_16x16x32_bf16 v[192:195], v[112:115], a[136:139], v[192:195]\n\t" \
"v_mfma_f32_16x16x32_bf16 v[196:199], v[116:119], a[136:139], v[196:199]\n\t" \
"s_add_u32 s89, s89, 57344\n\t" \
"s_min_u32 s89, s89, 1806336\n\t" \
"s_mov_b32 s87, s92\n\t" \
"s_add_u32 s95, s92, 28672\n\t" \
"s_cmp_eq_u32 s95, 114688\n\t" \
"s_cselect_b32 s95, 0, s95\n\t" \
"v_add_u32 v249, s95, %[lr]\n\t" \
"s_waitcnt vmcnt(63) lgkmcnt(0)\n\t" \
"s_barrier\n\t" \
"v_mfma_f32_16x16x32_bf16 v[176:179], v[120:123], a[28:31], v[176:179]\n\t" \
"ds_read_b128 v[64:67], v249 offset:0\n\t" \
"v_mfma_f32_16x16x32_bf16 v[180:183], v[124:127], a[28:31], v[180:183]\n\t" \
"v_mfma_f32_16x16x32_bf16 v[184:187], v[120:123], a[84:87], v[184:187]\n\t" \
"ds_read_b128 v[68:71], v249 offset:14336\n\t" \
"v_mfma_f32_16x16x32_bf16 v[188:191], v[124:127], a[84:87], v[188:191]\n\t" \
"v_mfma_f32_16x16x32_bf16 v[192:195], v[120:123], a[140:143], v[192:195]\n\t" \
"ds_read_b128 v[72:75], v249 offset:1024\n\t" \
"v_mfma_f32_16x16x32_bf16 v[196:199], v[124:127], a[140:143], v[196:199]\n\t" \
"v_mfma_f32_16x16x32_bf16 v[176:179], v[128:131], a[32:35], v[176:179]\n\t" \
"ds_read_b128 v[76:79], v249 offset:15360\n\t" \
"v_mfma_f32_16x16x32_bf16 v[180:183], v[132:135], a[32:35], v[180:183]\n\t" \
"s_add_u32 s90, s87, %[lg]\n\t" \
"s_add_u32 s90, s90, 0\n\t" \
"s_mov_b32 m0, s90\n\t" \
"s_add_u32 s95, s89, 0\n\t" \
"buffer_load_dwordx4 %[vg], %[rh], s95 offen offset:0 lds\n\t" \
"v_mfma_f32_16x16x32_bf16 v[184:187], v[128:131], a[88:91], v[184:187]\n\t" \
"ds_read_b128 v[80:83], v249 offset:2048\n\t" \
"v_mfma_f32_16x16x32_bf16 v[188:191], v[132:135], a[88:91], v[188:191]\n\t" \
"v_mfma_f32_16x16x32_bf16 v[192:195], v[128:131], a[144:147], v[192:195]\n\t" \
"ds_read_b128 v[84:87], v249 offset:16384\n\t" \
"v_mfma_f32_16x16x32_bf16 v[196:199], v[132:135], a[144:147], v[196:199]\n\t" \
"v_mfma_f32_16x16x32_bf16 v[176:179], v[136:139], a[36:39], v[176:179]\n\t" \
"ds_read_b128 v[88:91], v249 offset:3072\n\t" \
"v_mfma_f32_16x16x32_bf16 v[180:183], v[140:143], a[36:39], v[180:183]\n\t" \
"v_mfma_f32_16x16x32_bf16 v[184:187], v[136:139], a[92:95], v[184:187]\n\t" \
"ds_read_b128 v[92:95], v249 offset:17408\n\t" \
"v_mfma_f32_16x16x32_bf16 v[188:191], v[140:143], a[92:95], v[188:191]\n\t" \
"buffer_load_dwordx4 %[vg], %[rh], s95 offen offset:1024 lds\n\t" \
"v_mfma_f32_16x16x32_bf16 v[192:195], v[136:139], a[148:151], v[192:195]\n\t" \
"ds_read_b128 v[96:99], v249 offset:4096\n\t" \
"v_mfma_f32_16x16x32_bf16 v[196:199], v[140:143], a[148:151], v[196:199]\n\t" \
"v_mfma_f32_16x16x32_bf16 v[176:179], v[144:147], a[40:43], v[176:179]\n\t" \
"ds_read_b128 v[100:103], v249 offset:18432\n\t" \
"v_mfma_f32_16x16x32_bf16 v[180:183], v[148:151], a[40:43], v[180:183]\n\t" \
"v_mfma_f32_16x16x32_bf16 v[184:187], v[144:147], a[96:99], v[184:187]\n\t" \
"ds_read_b128 v[104:107], v249 offset:5120\n\t" \
"v_mfma_f32_16x16x32_bf16 v[188:191], v[148:151], a[96:99], v[188:191]\n\t" \
"v_mfma_f32_16x16x32_bf16 v[192:195], v[144:147], a[152:155], v[192:195]\n\t" \
"ds_read_b128 v[108:111], v249 offset:19456\n\t" \
"v_mfma_f32_16x16x32_bf16 v[196:199], v[148:151], a[152:155], v[196:199]\n\t" \
"buffer_load_dwordx4 %[vg], %[rh], s95 offen offset:2048 lds\n\t" \
"v_mfma_f32_16x16x32_bf16 v[176:179], v[152:155], a[44:47], v[176:179]\n\t" \
"ds_read_b128 v[112:115], v249 offset:6144\n\t" \
"v_mfma_f32_16x16x32_bf16 v[180:183], v[156:159], a[44:47], v[180:183]\n\t" \
"v_mfma_f32_16x16x32_bf16 v[184:187], v[152:155], a[100:103], v[184:187]\n\t" \
"ds_read_b128 v[116:119], v249 offset:20480\n\t" \
"v_mfma_f32_16x16x32_bf16 v[188:191], v[156:159], a[100:103], v[188:191]\n\t" \
"v_mfma_f32_16x16x32_bf16 v[192:195], v[152:155], a[156:159], v[192:195]\n\t" \
"v_mfma_f32_16x16x32_bf16 v[196:199], v[156:159], a[156:159], v[196:199]\n\t" \
"v_mfma_f32_16x16x32_bf16 v[176:179], v[160:163], a[48:51], v[176:179]\n\t" \
"v_mfma_f32_16x16x32_bf16 v[180:183], v[164:167], a[48:51], v[180:183]\n\t" \
"buffer_load_dwordx4 %[vg], %[rh], s95 offen offset:3072 lds\n\t" \
"v_mfma_f32_16x16x32_bf16 v[184:187], v[160:163], a[104:107], v[184:187]\n\t" \
"v_mfma_f32_16x16x32_bf16 v[188:191], v[164:167], a[104:107], v[188:191]\n\t" \
"v_mfma_f32_16x16x32_bf16 v[192:195], v[160:163], a[160:163], v[192:195]\n\t" \
"v_mfma_f32_16x16x32_bf16 v[196:199], v[164:167], a[160:163], v[196:199]\n\t" \
"v_mfma_f32_16x16x32_bf16 v[176:179], v[168:171], a[52:55], v[176:179]\n\t" \
"v_mfma_f32_16x16x32_bf16 v[180:183], v[172:175], a[52:55], v[180:183]\n\t" \
"v_mfma_f32_16x16x32_bf16 v[184:187], v[168:171], a[108:111], v[184:187]\n\t" \
"v_mfma_f32_16x16x32_bf16 v[188:191], v[172:175], a[108:111], v[188:191]\n\t" \
"s_add_u32 s90, s87, %[lg]\n\t" \
"s_add_u32 s90, s90, 4096\n\t" \
"s_mov_b32 m0, s90\n\t" \
"s_add_u32 s95, s89, 4096\n\t" \
"buffer_load_dwordx4 %[vg], %[rh], s95 offen offset:0 lds\n\t" \
"v_mfma_f32_16x16x32_bf16 v[192:195], v[168:171], a[164:167], v[192:195]\n\t" \
"v_mfma_f32_16x16x32_bf16 v[196:199], v[172:175], a[164:167], v[196:199]\n\t" \
"s_add_u32 s92, s92, 28672\n\t" \
"s_cmp_eq_u32 s92, 114688\n\t" \
"s_cselect_b32 s92, 0, s92\n\t" \
"v_mov_b32 v248, v249\n\t" \
"s_add_u32 s91, s91, 917504\n\t" \
"v_cvt_pk_bf16_f32 v240, v176, v177\n\t" \
"v_cvt_pk_bf16_f32 v241, v178, v179\n\t" \
"v_cvt_pk_bf16_f32 v242, v180, v181\n\t" \
"v_cvt_pk_bf16_f32 v243, v182, v183\n\t" \
"buffer_store_dwordx4 v[240:243], %[vs0], %[ro], s91 offen offset:0 sc1\n\t" \
"v_cvt_pk_bf16_f32 v244, v184, v185\n\t" \
"v_cvt_pk_bf16_f32 v245, v186, v187\n\t" \
"v_cvt_pk_bf16_f32 v246, v188, v189\n\t" \
"v_cvt_pk_bf16_f32 v247, v190, v191\n\t" \
"buffer_store_dwordx4 v[244:247], %[vs0], %[ro], s91 offen offset:1024 sc1\n\t" \
"v_cvt_pk_bf16_f32 v240, v192, v193\n\t" \
"v_cvt_pk_bf16_f32 v241, v194, v195\n\t" \
"v_cvt_pk_bf16_f32 v242, v196, v197\n\t" \
"v_cvt_pk_bf16_f32 v243, v198, v199\n\t" \
"buffer_store_dwordx4 v[240:243], %[vs0], %[ro], s91 offen offset:2048 sc1\n\t" \
"s_waitcnt vmcnt(0)\n\t" \
"s_barrier\n\t" \
"s_add_u32 s94, s94, 128\n\t" \
"s_waitcnt lgkmcnt(0)\n\t" \
"v_mfma_f32_16x16x32_bf16 v[208:211], v[64:67], a[0:3], 0\n\t" \
"v_mfma_f32_16x16x32_bf16 v[212:215], v[68:71], a[0:3], 0\n\t" \
"ds_read_b128 v[120:123], v248 offset:7168\n\t" \
"v_mfma_f32_16x16x32_bf16 v[216:219], v[64:67], a[56:59], 0\n\t" \
"v_mfma_f32_16x16x32_bf16 v[220:223], v[68:71], a[56:59], 0\n\t" \
"ds_read_b128 v[124:127], v248 offset:21504\n\t" \
"v_mfma_f32_16x16x32_bf16 v[224:227], v[64:67], a[112:115], 0\n\t" \
"v_mfma_f32_16x16x32_bf16 v[228:231], v[68:71], a[112:115], 0\n\t" \
"ds_read_b128 v[128:131], v248 offset:8192\n\t" \
"v_mfma_f32_16x16x32_bf16 v[208:211], v[72:75], a[4:7], v[208:211]\n\t" \
"v_mfma_f32_16x16x32_bf16 v[212:215], v[76:79], a[4:7], v[212:215]\n\t" \
"ds_read_b128 v[132:135], v248 offset:22528\n\t" \
"s_add_u32 s90, s87, %[lg]\n\t" \
"s_add_u32 s90, s90, 4096\n\t" \
"s_mov_b32 m0, s90\n\t" \
"s_add_u32 s95, s89, 4096\n\t" \
"buffer_load_dwordx4 %[vg], %[rh], s95 offen offset:1024 lds\n\t" \
"v_mfma_f32_16x16x32_bf16 v[216:219], v[72:75], a[60:63], v[216:219]\n\t" \
"v_mfma_f32_16x16x32_bf16 v[220:223], v[76:79], a[60:63], v[220:223]\n\t" \
"ds_read_b128 v[136:139], v248 offset:9216\n\t" \
"v_mfma_f32_16x16x32_bf16 v[224:227], v[72:75], a[116:119], v[224:227]\n\t" \
"v_mfma_f32_16x16x32_bf16 v[228:231], v[76:79], a[116:119], v[228:231]\n\t" \
"ds_read_b128 v[140:143], v248 offset:23552\n\t" \
"v_mfma_f32_16x16x32_bf16 v[208:211], v[80:83], a[8:11], v[208:211]\n\t" \
"v_mfma_f32_16x16x32_bf16 v[212:215], v[84:87], a[8:11], v[212:215]\n\t" \
"ds_read_b128 v[144:147], v248 offset:10240\n\t" \
"v_mfma_f32_16x16x32_bf16 v[216:219], v[80:83], a[64:67], v[216:219]\n\t" \
"v_mfma_f32_16x16x32_bf16 v[220:223], v[84:87], a[64:67], v[220:223]\n\t" \
"ds_read_b128 v[148:151], v248 offset:24576\n\t" \
"buffer_load_dwordx4 %[vg], %[rh], s95 offen offset:2048 lds\n\t" \
"v_mfma_f32_16x16x32_bf16 v[224:227], v[80:83], a[120:123], v[224:227]\n\t" \
"v_mfma_f32_16x16x32_bf16 v[228:231], v[84:87], a[120:123], v[228:231]\n\t" \
"ds_read_b128 v[152:155], v248 offset:11264\n\t" \
"v_mfma_f32_16x16x32_bf16 v[208:211], v[88:91], a[12:15], v[208:211]\n\t" \
"v_mfma_f32_16x16x32_bf16 v[212:215], v[92:95], a[12:15], v[212:215]\n\t" \
"ds_read_b128 v[156:159], v248 offset:25600\n\t" \
"v_mfma_f32_16x16x32_bf16 v[216:219], v[88:91], a[68:71], v[216:219]\n\t" \
"v_mfma_f32_16x16x32_bf16 v[220:223], v[92:95], a[68:71], v[220:223]\n\t" \
"ds_read_b128 v[160:163], v248 offset:12288\n\t" \
"v_mfma_f32_16x16x32_bf16 v[224:227], v[88:91], a[124:127], v[224:227]\n\t" \
"v_mfma_f32_16x16x32_bf16 v[228:231], v[92:95], a[124:127], v[228:231]\n\t" \
"ds_read_b128 v[164:167], v248 offset:26624\n\t" \
"buffer_load_dwordx4 %[vg], %[rh], s95 offen offset:3072 lds\n\t" \
"v_mfma_f32_16x16x32_bf16 v[208:211], v[96:99], a[16:19], v[208:211]\n\t" \
"v_mfma_f32_16x16x32_bf16 v[212:215], v[100:103], a[16:19], v[212:215]\n\t" \
"ds_read_b128 v[168:171], v248 offset:13312\n\t" \
"v_mfma_f32_16x16x32_bf16 v[216:219], v[96:99], a[72:75], v[216:219]\n\t" \
"v_mfma_f32_16x16x32_bf16 v[220:223], v[100:103], a[72:75], v[220:223]\n\t" \
"ds_read_b128 v[172:175], v248 offset:27648\n\t" \
"v_mfma_f32_16x16x32_bf16 v[224:227], v[96:99], a[128:131], v[224:227]\n\t" \
"v_mfma_f32_16x16x32_bf16 v[228:231], v[100:103], a[128:131], v[228:231]\n\t" \
"v_mfma_f32_16x16x32_bf16 v[208:211], v[104:107], a[20:23], v[208:211]\n\t" \
"v_mfma_f32_16x16x32_bf16 v[212:215], v[108:111], a[20:23], v[212:215]\n\t" \
"s_add_u32 s90, s87, %[lg]\n\t" \
"s_add_u32 s90, s90, 8192\n\t" \
"s_mov_b32 m0, s90\n\t" \
"s_add_u32 s95, s89, 8192\n\t" \
"buffer_load_dwordx4 %[vg], %[rh], s95 offen offset:0 lds\n\t" \
"v_mfma_f32_16x16x32_bf16 v[216:219], v[104:107], a[76:79], v[216:219]\n\t" \
"v_mfma_f32_16x16x32_bf16 v[220:223], v[108:111], a[76:79], v[220:223]\n\t" \
"v_mfma_f32_16x16x32_bf16 v[224:227], v[104:107], a[132:135], v[224:227]\n\t" \
"v_mfma_f32_16x16x32_bf16 v[228:231], v[108:111], a[132:135], v[228:231]\n\t" \
"v_mfma_f32_16x16x32_bf16 v[208:211], v[112:115], a[24:27], v[208:211]\n\t" \
"v_mfma_f32_16x16x32_bf16 v[212:215], v[116:119], a[24:27], v[212:215]\n\t" \
"v_mfma_f32_16x16x32_bf16 v[216:219], v[112:115], a[80:83], v[216:219]\n\t" \
"v_mfma_f32_16x16x32_bf16 v[220:223], v[116:119], a[80:83], v[220:223]\n\t" \
"buffer_load_dwordx4 %[vg], %[rh], s95 offen offset:1024 lds\n\t" \
"v_mfma_f32_16x16x32_bf16 v[224:227], v[112:115], a[136:139], v[224:227]\n\t" \
"v_mfma_f32_16x16x32_bf16 v[228:231], v[116:119], a[136:139], v[228:231]\n\t" \
"s_add_u32 s89, s89, 57344\n\t" \
"s_min_u32 s89, s89, 1806336\n\t" \
"s_mov_b32 s87, s92\n\t" \
"s_add_u32 s95, s92, 28672\n\t" \
"s_cmp_eq_u32 s95, 114688\n\t" \
"s_cselect_b32 s95, 0, s95\n\t" \
"v_add_u32 v249, s95, %[lr]\n\t" \
"s_waitcnt vmcnt(63) lgkmcnt(0)\n\t" \
"s_barrier\n\t" \
"v_mfma_f32_16x16x32_bf16 v[208:211], v[120:123], a[28:31], v[208:211]\n\t" \
"ds_read_b128 v[64:67], v249 offset:0\n\t" \
"v_mfma_f32_16x16x32_bf16 v[212:215], v[124:127], a[28:31], v[212:215]\n\t" \
"v_mfma_f32_16x16x32_bf16 v[216:219], v[120:123], a[84:87], v[216:219]\n\t" \
"ds_read_b128 v[68:71], v249 offset:14336\n\t" \
"v_mfma_f32_16x16x32_bf16 v[220:223], v[124:127], a[84:87], v[220:223]\n\t" \
"v_mfma_f32_16x16x32_bf16 v[224:227], v[120:123], a[140:143], v[224:227]\n\t" \
"ds_read_b128 v[72:75], v249 offset:1024\n\t" \
"v_mfma_f32_16x16x32_bf16 v[228:231], v[124:127], a[140:143], v[228:231]\n\t" \
"v_mfma_f32_16x16x32_bf16 v[208:211], v[128:131], a[32:35], v[208:211]\n\t" \
"ds_read_b128 v[76:79], v249 offset:15360\n\t" \
"v_mfma_f32_16x16x32_bf16 v[212:215], v[132:135], a[32:35], v[212:215]\n\t" \
"s_add_u32 s90, s87, %[lg]\n\t" \
"s_add_u32 s90, s90, 0\n\t" \
"s_mov_b32 m0, s90\n\t" \
"s_add_u32 s95, s89, 0\n\t" \
"buffer_load_dwordx4 %[vg], %[rh], s95 offen offset:0 lds\n\t" \
"v_mfma_f32_16x16x32_bf16 v[216:219], v[128:131], a[88:91], v[216:219]\n\t" \
"ds_read_b128 v[80:83], v249 offset:2048\n\t" \
"v_mfma_f32_16x16x32_bf16 v[220:223], v[132:135], a[88:91], v[220:223]\n\t" \
"v_mfma_f32_16x16x32_bf16 v[224:227], v[128:131], a[144:147], v[224:227]\n\t" \
"ds_read_b128 v[84:87], v249 offset:16384\n\t" \
"v_mfma_f32_16x16x32_bf16 v[228:231], v[132:135], a[144:147], v[228:231]\n\t" \
"v_mfma_f32_16x16x32_bf16 v[208:211], v[136:139], a[36:39], v[208:211]\n\t" \
"ds_read_b128 v[88:91], v249 offset:3072\n\t" \
"v_mfma_f32_16x16x32_bf16 v[212:215], v[140:143], a[36:39], v[212:215]\n\t" \
"v_mfma_f32_16x16x32_bf16 v[216:219], v[136:139], a[92:95], v[216:219]\n\t" \
"ds_read_b128 v[92:95], v249 offset:17408\n\t" \
"v_mfma_f32_16x16x32_bf16 v[220:223], v[140:143], a[92:95], v[220:223]\n\t" \
"buffer_load_dwordx4 %[vg], %[rh], s95 offen offset:1024 lds\n\t" \
"v_mfma_f32_16x16x32_bf16 v[224:227], v[136:139], a[148:151], v[224:227]\n\t" \
"ds_read_b128 v[96:99], v249 offset:4096\n\t" \
"v_mfma_f32_16x16x32_bf16 v[228:231], v[140:143], a[148:151], v[228:231]\n\t" \
"v_mfma_f32_16x16x32_bf16 v[208:211], v[144:147], a[40:43], v[208:211]\n\t" \
"ds_read_b128 v[100:103], v249 offset:18432\n\t" \
"v_mfma_f32_16x16x32_bf16 v[212:215], v[148:151], a[40:43], v[212:215]\n\t" \
"v_mfma_f32_16x16x32_bf16 v[216:219], v[144:147], a[96:99], v[216:219]\n\t" \
"ds_read_b128 v[104:107], v249 offset:5120\n\t" \
"v_mfma_f32_16x16x32_bf16 v[220:223], v[148:151], a[96:99], v[220:223]\n\t" \
"v_mfma_f32_16x16x32_bf16 v[224:227], v[144:147], a[152:155], v[224:227]\n\t" \
"ds_read_b128 v[108:111], v249 offset:19456\n\t" \
"v_mfma_f32_16x16x32_bf16 v[228:231], v[148:151], a[152:155], v[228:231]\n\t" \
"buffer_load_dwordx4 %[vg], %[rh], s95 offen offset:2048 lds\n\t" \
"v_mfma_f32_16x16x32_bf16 v[208:211], v[152:155], a[44:47], v[208:211]\n\t" \
"ds_read_b128 v[112:115], v249 offset:6144\n\t" \
"v_mfma_f32_16x16x32_bf16 v[212:215], v[156:159], a[44:47], v[212:215]\n\t" \
"v_mfma_f32_16x16x32_bf16 v[216:219], v[152:155], a[100:103], v[216:219]\n\t" \
"ds_read_b128 v[116:119], v249 offset:20480\n\t" \
"v_mfma_f32_16x16x32_bf16 v[220:223], v[156:159], a[100:103], v[220:223]\n\t" \
"v_mfma_f32_16x16x32_bf16 v[224:227], v[152:155], a[156:159], v[224:227]\n\t" \
"v_mfma_f32_16x16x32_bf16 v[228:231], v[156:159], a[156:159], v[228:231]\n\t" \
"v_mfma_f32_16x16x32_bf16 v[208:211], v[160:163], a[48:51], v[208:211]\n\t" \
"v_mfma_f32_16x16x32_bf16 v[212:215], v[164:167], a[48:51], v[212:215]\n\t" \
"buffer_load_dwordx4 %[vg], %[rh], s95 offen offset:3072 lds\n\t" \
"v_mfma_f32_16x16x32_bf16 v[216:219], v[160:163], a[104:107], v[216:219]\n\t" \
"v_mfma_f32_16x16x32_bf16 v[220:223], v[164:167], a[104:107], v[220:223]\n\t" \
"v_mfma_f32_16x16x32_bf16 v[224:227], v[160:163], a[160:163], v[224:227]\n\t" \
"v_mfma_f32_16x16x32_bf16 v[228:231], v[164:167], a[160:163], v[228:231]\n\t" \
"v_mfma_f32_16x16x32_bf16 v[208:211], v[168:171], a[52:55], v[208:211]\n\t" \
"v_mfma_f32_16x16x32_bf16 v[212:215], v[172:175], a[52:55], v[212:215]\n\t" \
"v_mfma_f32_16x16x32_bf16 v[216:219], v[168:171], a[108:111], v[216:219]\n\t" \
"v_mfma_f32_16x16x32_bf16 v[220:223], v[172:175], a[108:111], v[220:223]\n\t" \
"s_add_u32 s90, s87, %[lg]\n\t" \
"s_add_u32 s90, s90, 4096\n\t" \
"s_mov_b32 m0, s90\n\t" \
"s_add_u32 s95, s89, 4096\n\t" \
"buffer_load_dwordx4 %[vg], %[rh], s95 offen offset:0 lds\n\t" \
"v_mfma_f32_16x16x32_bf16 v[224:227], v[168:171], a[164:167], v[224:227]\n\t" \
"v_mfma_f32_16x16x32_bf16 v[228:231], v[172:175], a[164:167], v[228:231]\n\t" \
"s_add_u32 s92, s92, 28672\n\t" \
"s_cmp_eq_u32 s92, 114688\n\t" \
"s_cselect_b32 s92, 0, s92\n\t" \
"v_mov_b32 v248, v249\n\t" \
"s_add_u32 s91, s91, 917504\n\t" \
"s_waitcnt lgkmcnt(0)\n\t" \
"v_mfma_f32_16x16x32_bf16 v[176:179], v[64:67], a[0:3], 0\n\t" \
"v_mfma_f32_16x16x32_bf16 v[180:183], v[68:71], a[0:3], 0\n\t" \
"ds_read_b128 v[120:123], v248 offset:7168\n\t" \
"v_mfma_f32_16x16x32_bf16 v[184:187], v[64:67], a[56:59], 0\n\t" \
"v_cvt_pk_bf16_f32 v240, v208, v209\n\t" \
"v_mfma_f32_16x16x32_bf16 v[188:191], v[68:71], a[56:59], 0\n\t" \
"v_cvt_pk_bf16_f32 v241, v210, v211\n\t" \
"v_mfma_f32_16x16x32_bf16 v[192:195], v[64:67], a[112:115], 0\n\t" \
"v_cvt_pk_bf16_f32 v242, v212, v213\n\t" \
"v_mfma_f32_16x16x32_bf16 v[196:199], v[68:71], a[112:115], 0\n\t" \
"v_cvt_pk_bf16_f32 v243, v214, v215\n\t" \
"v_mfma_f32_16x16x32_bf16 v[176:179], v[72:75], a[4:7], v[176:179]\n\t" \
"buffer_store_dwordx4 v[240:243], %[vs0], %[ro], s91 offen offset:0 sc1\n\t" \
"v_mfma_f32_16x16x32_bf16 v[180:183], v[76:79], a[4:7], v[180:183]\n\t" \
"v_cvt_pk_bf16_f32 v244, v216, v217\n\t" \
"s_add_u32 s90, s87, %[lg]\n\t" \
"s_add_u32 s90, s90, 4096\n\t" \
"s_mov_b32 m0, s90\n\t" \
"s_add_u32 s95, s89, 4096\n\t" \
"buffer_load_dwordx4 %[vg], %[rh], s95 offen offset:1024 lds\n\t" \
"v_mfma_f32_16x16x32_bf16 v[184:187], v[72:75], a[60:63], v[184:187]\n\t" \
"v_cvt_pk_bf16_f32 v245, v218, v219\n\t" \
"v_mfma_f32_16x16x32_bf16 v[188:191], v[76:79], a[60:63], v[188:191]\n\t" \
"v_cvt_pk_bf16_f32 v246, v220, v221\n\t" \
"v_mfma_f32_16x16x32_bf16 v[192:195], v[72:75], a[116:119], v[192:195]\n\t" \
"v_cvt_pk_bf16_f32 v247, v222, v223\n\t" \
"v_mfma_f32_16x16x32_bf16 v[196:199], v[76:79], a[116:119], v[196:199]\n\t" \
"buffer_store_dwordx4 v[244:247], %[vs0], %[ro], s91 offen offset:1024 sc1\n\t" \
"v_mfma_f32_16x16x32_bf16 v[176:179], v[80:83], a[8:11], v[176:179]\n\t" \
"v_cvt_pk_bf16_f32 v240, v224, v225\n\t" \
"v_mfma_f32_16x16x32_bf16 v[180:183], v[84:87], a[8:11], v[180:183]\n\t" \
"v_cvt_pk_bf16_f32 v241, v226, v227\n\t" \
"v_mfma_f32_16x16x32_bf16 v[184:187], v[80:83], a[64:67], v[184:187]\n\t" \
"v_cvt_pk_bf16_f32 v242, v228, v229\n\t" \
"v_mfma_f32_16x16x32_bf16 v[188:191], v[84:87], a[64:67], v[188:191]\n\t" \
"v_cvt_pk_bf16_f32 v243, v230, v231\n\t" \
"buffer_load_dwordx4 %[vg], %[rh], s95 offen offset:2048 lds\n\t" \
"v_mfma_f32_16x16x32_bf16 v[192:195], v[80:83], a[120:123], v[192:195]\n\t" \
"buffer_store_dwordx4 v[240:243], %[vs0], %[ro], s91 offen offset:2048 sc1\n\t" \
"v_mfma_f32_16x16x32_bf16 v[196:199], v[84:87], a[120:123], v[196:199]\n\t" \
"ds_read_b128 v[124:127], v248 offset:21504\n\t" \
"v_mfma_f32_16x16x32_bf16 v[176:179], v[88:91], a[12:15], v[176:179]\n\t" \
"v_mfma_f32_16x16x32_bf16 v[180:183], v[92:95], a[12:15], v[180:183]\n\t" \
"ds_read_b128 v[128:131], v248 offset:8192\n\t" \
"v_mfma_f32_16x16x32_bf16 v[184:187], v[88:91], a[68:71], v[184:187]\n\t" \
"v_mfma_f32_16x16x32_bf16 v[188:191], v[92:95], a[68:71], v[188:191]\n\t" \
"ds_read_b128 v[132:135], v248 offset:22528\n\t" \
"v_mfma_f32_16x16x32_bf16 v[192:195], v[88:91], a[124:127], v[192:195]\n\t" \
"v_mfma_f32_16x16x32_bf16 v[196:199], v[92:95], a[124:127], v[196:199]\n\t" \
"ds_read_b128 v[136:139], v248 offset:9216\n\t" \
"buffer_load_dwordx4 %[vg], %[rh], s95 offen offset:3072 lds\n\t" \
"v_mfma_f32_16x16x32_bf16 v[176:179], v[96:99], a[16:19], v[176:179]\n\t" \
"v_mfma_f32_16x16x32_bf16 v[180:183], v[100:103], a[16:19], v[180:183]\n\t" \
"ds_read_b128 v[140:143], v248 offset:23552\n\t" \
"v_mfma_f32_16x16x32_bf16 v[184:187], v[96:99], a[72:75], v[184:187]\n\t" \
"v_mfma_f32_16x16x32_bf16 v[188:191], v[100:103], a[72:75], v[188:191]\n\t" \
"ds_read_b128 v[144:147], v248 offset:10240\n\t" \
"v_mfma_f32_16x16x32_bf16 v[192:195], v[96:99], a[128:131], v[192:195]\n\t" \
"v_mfma_f32_16x16x32_bf16 v[196:199], v[100:103], a[128:131], v[196:199]\n\t" \
"ds_read_b128 v[148:151], v248 offset:24576\n\t" \
"v_mfma_f32_16x16x32_bf16 v[176:179], v[104:107], a[20:23], v[176:179]\n\t" \
"v_mfma_f32_16x16x32_bf16 v[180:183], v[108:111], a[20:23], v[180:183]\n\t" \
"ds_read_b128 v[152:155], v248 offset:11264\n\t" \
"s_add_u32 s90, s87, %[lg]\n\t" \
"s_add_u32 s90, s90, 8192\n\t" \
"s_mov_b32 m0, s90\n\t" \
"s_add_u32 s95, s89, 8192\n\t" \
"buffer_load_dwordx4 %[vg], %[rh], s95 offen offset:0 lds\n\t" \
"v_mfma_f32_16x16x32_bf16 v[184:187], v[104:107], a[76:79], v[184:187]\n\t" \
"v_mfma_f32_16x16x32_bf16 v[188:191], v[108:111], a[76:79], v[188:191]\n\t" \
"ds_read_b128 v[156:159], v248 offset:25600\n\t" \
"v_mfma_f32_16x16x32_bf16 v[192:195], v[104:107], a[132:135], v[192:195]\n\t" \
"v_mfma_f32_16x16x32_bf16 v[196:199], v[108:111], a[132:135], v[196:199]\n\t" \
"ds_read_b128 v[160:163], v248 offset:12288\n\t" \
"v_mfma_f32_16x16x32_bf16 v[176:179], v[112:115], a[24:27], v[176:179]\n\t" \
"v_mfma_f32_16x16x32_bf16 v[180:183], v[116:119], a[24:27], v[180:183]\n\t" \
"ds_read_b128 v[164:167], v248 offset:26624\n\t" \
"v_mfma_f32_16x16x32_bf16 v[184:187], v[112:115], a[80:83], v[184:187]\n\t" \
"v_mfma_f32_16x16x32_bf16 v[188:191], v[116:119], a[80:83], v[188:191]\n\t" \
"ds_read_b128 v[168:171], v248 offset:13312\n\t" \
"buffer_load_dwordx4 %[vg], %[rh], s95 offen offset:1024 lds\n\t" \
"v_mfma_f32_16x16x32_bf16 v[192:195], v[112:115], a[136:139], v[192:195]\n\t" \
"v_mfma_f32_16x16x32_bf16 v[196:199], v[116:119], a[136:139], v[196:199]\n\t" \
"ds_read_b128 v[172:175], v248 offset:27648\n\t" \
"s_add_u32 s89, s89, 57344\n\t" \
"s_min_u32 s89, s89, 1806336\n\t" \
"s_mov_b32 s87, s92\n\t" \
"s_add_u32 s95, s92, 28672\n\t" \
"s_cmp_eq_u32 s95, 114688\n\t" \
"s_cselect_b32 s95, 0, s95\n\t" \
"v_add_u32 v249, s95, %[lr]\n\t" \
"s_waitcnt vmcnt(13) lgkmcnt(0)\n\t" \
"s_barrier\n\t" \
"v_mfma_f32_16x16x32_bf16 v[176:179], v[120:123], a[28:31], v[176:179]\n\t" \
"ds_read_b128 v[64:67], v249 offset:0\n\t" \
"v_mfma_f32_16x16x32_bf16 v[180:183], v[124:127], a[28:31], v[180:183]\n\t" \
"v_mfma_f32_16x16x32_bf16 v[184:187], v[120:123], a[84:87], v[184:187]\n\t" \
"ds_read_b128 v[68:71], v249 offset:14336\n\t" \
"v_mfma_f32_16x16x32_bf16 v[188:191], v[124:127], a[84:87], v[188:191]\n\t" \
"v_mfma_f32_16x16x32_bf16 v[192:195], v[120:123], a[140:143], v[192:195]\n\t" \
"ds_read_b128 v[72:75], v249 offset:1024\n\t" \
"v_mfma_f32_16x16x32_bf16 v[196:199], v[124:127], a[140:143], v[196:199]\n\t" \
"v_mfma_f32_16x16x32_bf16 v[176:179], v[128:131], a[32:35], v[176:179]\n\t" \
"ds_read_b128 v[76:79], v249 offset:15360\n\t" \
"v_mfma_f32_16x16x32_bf16 v[180:183], v[132:135], a[32:35], v[180:183]\n\t" \
"s_add_u32 s90, s87, %[lg]\n\t" \
"s_add_u32 s90, s90, 0\n\t" \
"s_mov_b32 m0, s90\n\t" \
"s_add_u32 s95, s89, 0\n\t" \
"buffer_load_dwordx4 %[vg], %[rh], s95 offen offset:0 lds\n\t" \
"v_mfma_f32_16x16x32_bf16 v[184:187], v[128:131], a[88:91], v[184:187]\n\t" \
"ds_read_b128 v[80:83], v249 offset:2048\n\t" \
"v_mfma_f32_16x16x32_bf16 v[188:191], v[132:135], a[88:91], v[188:191]\n\t" \
"v_mfma_f32_16x16x32_bf16 v[192:195], v[128:131], a[144:147], v[192:195]\n\t" \
"ds_read_b128 v[84:87], v249 offset:16384\n\t" \
"v_mfma_f32_16x16x32_bf16 v[196:199], v[132:135], a[144:147], v[196:199]\n\t" \
"v_mfma_f32_16x16x32_bf16 v[176:179], v[136:139], a[36:39], v[176:179]\n\t" \
"ds_read_b128 v[88:91], v249 offset:3072\n\t" \
"v_mfma_f32_16x16x32_bf16 v[180:183], v[140:143], a[36:39], v[180:183]\n\t" \
"v_mfma_f32_16x16x32_bf16 v[184:187], v[136:139], a[92:95], v[184:187]\n\t" \
"ds_read_b128 v[92:95], v249 offset:17408\n\t" \
"v_mfma_f32_16x16x32_bf16 v[188:191], v[140:143], a[92:95], v[188:191]\n\t" \
"buffer_load_dwordx4 %[vg], %[rh], s95 offen offset:1024 lds\n\t" \
"v_mfma_f32_16x16x32_bf16 v[192:195], v[136:139], a[148:151], v[192:195]\n\t" \
"ds_read_b128 v[96:99], v249 offset:4096\n\t" \
"v_mfma_f32_16x16x32_bf16 v[196:199], v[140:143], a[148:151], v[196:199]\n\t" \
"v_mfma_f32_16x16x32_bf16 v[176:179], v[144:147], a[40:43], v[176:179]\n\t" \
"ds_read_b128 v[100:103], v249 offset:18432\n\t" \
"v_mfma_f32_16x16x32_bf16 v[180:183], v[148:151], a[40:43], v[180:183]\n\t" \
"v_mfma_f32_16x16x32_bf16 v[184:187], v[144:147], a[96:99], v[184:187]\n\t" \
"ds_read_b128 v[104:107], v249 offset:5120\n\t" \
"v_mfma_f32_16x16x32_bf16 v[188:191], v[148:151], a[96:99], v[188:191]\n\t" \
"v_mfma_f32_16x16x32_bf16 v[192:195], v[144:147], a[152:155], v[192:195]\n\t" \
"ds_read_b128 v[108:111], v249 offset:19456\n\t" \
"v_mfma_f32_16x16x32_bf16 v[196:199], v[148:151], a[152:155], v[196:199]\n\t" \
"buffer_load_dwordx4 %[vg], %[rh], s95 offen offset:2048 lds\n\t" \
"v_mfma_f32_16x16x32_bf16 v[176:179], v[152:155], a[44:47], v[176:179]\n\t" \
"ds_read_b128 v[112:115], v249 offset:6144\n\t" \
"v_mfma_f32_16x16x32_bf16 v[180:183], v[156:159], a[44:47], v[180:183]\n\t" \
"v_mfma_f32_16x16x32_bf16 v[184:187], v[152:155], a[100:103], v[184:187]\n\t" \
"ds_read_b128 v[116:119], v249 offset:20480\n\t" \
"v_mfma_f32_16x16x32_bf16 v[188:191], v[156:159], a[100:103], v[188:191]\n\t" \
"v_mfma_f32_16x16x32_bf16 v[192:195], v[152:155], a[156:159], v[192:195]\n\t" \
"v_mfma_f32_16x16x32_bf16 v[196:199], v[156:159], a[156:159], v[196:199]\n\t" \
"v_mfma_f32_16x16x32_bf16 v[176:179], v[160:163], a[48:51], v[176:179]\n\t" \
"v_mfma_f32_16x16x32_bf16 v[180:183], v[164:167], a[48:51], v[180:183]\n\t" \
"buffer_load_dwordx4 %[vg], %[rh], s95 offen offset:3072 lds\n\t" \
"v_mfma_f32_16x16x32_bf16 v[184:187], v[160:163], a[104:107], v[184:187]\n\t" \
"v_mfma_f32_16x16x32_bf16 v[188:191], v[164:167], a[104:107], v[188:191]\n\t" \
"v_mfma_f32_16x16x32_bf16 v[192:195], v[160:163], a[160:163], v[192:195]\n\t" \
"v_mfma_f32_16x16x32_bf16 v[196:199], v[164:167], a[160:163], v[196:199]\n\t" \
"v_mfma_f32_16x16x32_bf16 v[176:179], v[168:171], a[52:55], v[176:179]\n\t" \
"v_mfma_f32_16x16x32_bf16 v[180:183], v[172:175], a[52:55], v[180:183]\n\t" \
"v_mfma_f32_16x16x32_bf16 v[184:187], v[168:171], a[108:111], v[184:187]\n\t" \
"v_mfma_f32_16x16x32_bf16 v[188:191], v[172:175], a[108:111], v[188:191]\n\t" \
"s_add_u32 s90, s87, %[lg]\n\t" \
"s_add_u32 s90, s90, 4096\n\t" \
"s_mov_b32 m0, s90\n\t" \
"s_add_u32 s95, s89, 4096\n\t" \
"buffer_load_dwordx4 %[vg], %[rh], s95 offen offset:0 lds\n\t" \
"v_mfma_f32_16x16x32_bf16 v[192:195], v[168:171], a[164:167], v[192:195]\n\t" \
"v_mfma_f32_16x16x32_bf16 v[196:199], v[172:175], a[164:167], v[196:199]\n\t" \
"s_add_u32 s92, s92, 28672\n\t" \
"s_cmp_eq_u32 s92, 114688\n\t" \
"s_cselect_b32 s92, 0, s92\n\t" \
"v_mov_b32 v248, v249\n\t" \
"s_add_u32 s91, s91, 917504\n\t" \
"s_waitcnt lgkmcnt(0)\n\t" \
"v_mfma_f32_16x16x32_bf16 v[208:211], v[64:67], a[0:3], 0\n\t" \
"v_mfma_f32_16x16x32_bf16 v[212:215], v[68:71], a[0:3], 0\n\t" \
"ds_read_b128 v[120:123], v248 offset:7168\n\t" \
"v_mfma_f32_16x16x32_bf16 v[216:219], v[64:67], a[56:59], 0\n\t" \
"v_cvt_pk_bf16_f32 v240, v176, v177\n\t" \
"v_mfma_f32_16x16x32_bf16 v[220:223], v[68:71], a[56:59], 0\n\t" \
"v_cvt_pk_bf16_f32 v241, v178, v179\n\t" \
"v_mfma_f32_16x16x32_bf16 v[224:227], v[64:67], a[112:115], 0\n\t" \
"v_cvt_pk_bf16_f32 v242, v180, v181\n\t" \
"v_mfma_f32_16x16x32_bf16 v[228:231], v[68:71], a[112:115], 0\n\t" \
"v_cvt_pk_bf16_f32 v243, v182, v183\n\t" \
"v_mfma_f32_16x16x32_bf16 v[208:211], v[72:75], a[4:7], v[208:211]\n\t" \
"buffer_store_dwordx4 v[240:243], %[vs0], %[ro], s91 offen offset:0 sc1\n\t" \
"v_mfma_f32_16x16x32_bf16 v[212:215], v[76:79], a[4:7], v[212:215]\n\t" \
"v_cvt_pk_bf16_f32 v244, v184, v185\n\t" \
"s_add_u32 s90, s87, %[lg]\n\t" \
"s_add_u32 s90, s90, 4096\n\t" \
"s_mov_b32 m0, s90\n\t" \
"s_add_u32 s95, s89, 4096\n\t" \
"buffer_load_dwordx4 %[vg], %[rh], s95 offen offset:1024 lds\n\t" \
"v_mfma_f32_16x16x32_bf16 v[216:219], v[72:75], a[60:63], v[216:219]\n\t" \
"v_cvt_pk_bf16_f32 v245, v186, v187\n\t" \
"v_mfma_f32_16x16x32_bf16 v[220:223], v[76:79], a[60:63], v[220:223]\n\t" \
"v_cvt_pk_bf16_f32 v246, v188, v189\n\t" \
"v_mfma_f32_16x16x32_bf16 v[224:227], v[72:75], a[116:119], v[224:227]\n\t" \
"v_cvt_pk_bf16_f32 v247, v190, v191\n\t" \
"v_mfma_f32_16x16x32_bf16 v[228:231], v[76:79], a[116:119], v[228:231]\n\t" \
"buffer_store_dwordx4 v[244:247], %[vs0], %[ro], s91 offen offset:1024 sc1\n\t" \
"v_mfma_f32_16x16x32_bf16 v[208:211], v[80:83], a[8:11], v[208:211]\n\t" \
"v_cvt_pk_bf16_f32 v240, v192, v193\n\t" \
"v_mfma_f32_16x16x32_bf16 v[212:215], v[84:87], a[8:11], v[212:215]\n\t" \
"v_cvt_pk_bf16_f32 v241, v194, v195\n\t" \
"v_mfma_f32_16x16x32_bf16 v[216:219], v[80:83], a[64:67], v[216:219]\n\t" \
"v_cvt_pk_bf16_f32 v242, v196, v197\n\t" \
"v_mfma_f32_16x16x32_bf16 v[220:223], v[84:87], a[64:67], v[220:223]\n\t" \
"v_cvt_pk_bf16_f32 v243, v198, v199\n\t" \
"buffer_load_dwordx4 %[vg], %[rh], s95 offen offset:2048 lds\n\t" \
"v_mfma_f32_16x16x32_bf16 v[224:227], v[80:83], a[120:123], v[224:227]\n\t" \
"buffer_store_dwordx4 v[240:243], %[vs0], %[ro], s91 offen offset:2048 sc1\n\t" \
"v_mfma_f32_16x16x32_bf16 v[228:231], v[84:87], a[120:123], v[228:231]\n\t" \
"ds_read_b128 v[124:127], v248 offset:21504\n\t" \
"v_mfma_f32_16x16x32_bf16 v[208:211], v[88:91], a[12:15], v[208:211]\n\t" \
"v_mfma_f32_16x16x32_bf16 v[212:215], v[92:95], a[12:15], v[212:215]\n\t" \
"ds_read_b128 v[128:131], v248 offset:8192\n\t" \
"v_mfma_f32_16x16x32_bf16 v[216:219], v[88:91], a[68:71], v[216:219]\n\t" \
"v_mfma_f32_16x16x32_bf16 v[220:223], v[92:95], a[68:71], v[220:223]\n\t" \
"ds_read_b128 v[132:135], v248 offset:22528\n\t" \
"v_mfma_f32_16x16x32_bf16 v[224:227], v[88:91], a[124:127], v[224:227]\n\t" \
"v_mfma_f32_16x16x32_bf16 v[228:231], v[92:95], a[124:127], v[228:231]\n\t" \
"ds_read_b128 v[136:139], v248 offset:9216\n\t" \
"buffer_load_dwordx4 %[vg], %[rh], s95 offen offset:3072 lds\n\t" \
"v_mfma_f32_16x16x32_bf16 v[208:211], v[96:99], a[16:19], v[208:211]\n\t" \
"v_mfma_f32_16x16x32_bf16 v[212:215], v[100:103], a[16:19], v[212:215]\n\t" \
"ds_read_b128 v[140:143], v248 offset:23552\n\t" \
"v_mfma_f32_16x16x32_bf16 v[216:219], v[96:99], a[72:75], v[216:219]\n\t" \
"v_mfma_f32_16x16x32_bf16 v[220:223], v[100:103], a[72:75], v[220:223]\n\t" \
"ds_read_b128 v[144:147], v248 offset:10240\n\t" \
"v_mfma_f32_16x16x32_bf16 v[224:227], v[96:99], a[128:131], v[224:227]\n\t" \
"v_mfma_f32_16x16x32_bf16 v[228:231], v[100:103], a[128:131], v[228:231]\n\t" \
"ds_read_b128 v[148:151], v248 offset:24576\n\t" \
"v_mfma_f32_16x16x32_bf16 v[208:211], v[104:107], a[20:23], v[208:211]\n\t" \
"v_mfma_f32_16x16x32_bf16 v[212:215], v[108:111], a[20:23], v[212:215]\n\t" \
"ds_read_b128 v[152:155], v248 offset:11264\n\t" \
"s_add_u32 s90, s87, %[lg]\n\t" \
"s_add_u32 s90, s90, 8192\n\t" \
"s_mov_b32 m0, s90\n\t" \
"s_add_u32 s95, s89, 8192\n\t" \
"buffer_load_dwordx4 %[vg], %[rh], s95 offen offset:0 lds\n\t" \
"v_mfma_f32_16x16x32_bf16 v[216:219], v[104:107], a[76:79], v[216:219]\n\t" \
"v_mfma_f32_16x16x32_bf16 v[220:223], v[108:111], a[76:79], v[220:223]\n\t" \
"ds_read_b128 v[156:159], v248 offset:25600\n\t" \
"v_mfma_f32_16x16x32_bf16 v[224:227], v[104:107], a[132:135], v[224:227]\n\t" \
"v_mfma_f32_16x16x32_bf16 v[228:231], v[108:111], a[132:135], v[228:231]\n\t" \
"ds_read_b128 v[160:163], v248 offset:12288\n\t" \
"v_mfma_f32_16x16x32_bf16 v[208:211], v[112:115], a[24:27], v[208:211]\n\t" \
"v_mfma_f32_16x16x32_bf16 v[212:215], v[116:119], a[24:27], v[212:215]\n\t" \
"ds_read_b128 v[164:167], v248 offset:26624\n\t" \
"v_mfma_f32_16x16x32_bf16 v[216:219], v[112:115], a[80:83], v[216:219]\n\t" \
"v_mfma_f32_16x16x32_bf16 v[220:223], v[116:119], a[80:83], v[220:223]\n\t" \
"ds_read_b128 v[168:171], v248 offset:13312\n\t" \
"buffer_load_dwordx4 %[vg], %[rh], s95 offen offset:1024 lds\n\t" \
"v_mfma_f32_16x16x32_bf16 v[224:227], v[112:115], a[136:139], v[224:227]\n\t" \
"v_mfma_f32_16x16x32_bf16 v[228:231], v[116:119], a[136:139], v[228:231]\n\t" \
"ds_read_b128 v[172:175], v248 offset:27648\n\t" \
"s_add_u32 s89, s89, 57344\n\t" \
"s_min_u32 s89, s89, 1806336\n\t" \
"s_mov_b32 s87, s92\n\t" \
"s_add_u32 s95, s92, 28672\n\t" \
"s_cmp_eq_u32 s95, 114688\n\t" \
"s_cselect_b32 s95, 0, s95\n\t" \
"v_add_u32 v249, s95, %[lr]\n\t" \
"s_waitcnt vmcnt(13) lgkmcnt(0)\n\t" \
"s_barrier\n\t" \
"s_add_u32 s94, s94, 128\n\t" \
"v_mfma_f32_16x16x32_bf16 v[208:211], v[120:123], a[28:31], v[208:211]\n\t" \
"ds_read_b128 v[64:67], v249 offset:0\n\t" \
"v_mfma_f32_16x16x32_bf16 v[212:215], v[124:127], a[28:31], v[212:215]\n\t" \
"v_mfma_f32_16x16x32_bf16 v[216:219], v[120:123], a[84:87], v[216:219]\n\t" \
"ds_read_b128 v[68:71], v249 offset:14336\n\t" \
"v_mfma_f32_16x16x32_bf16 v[220:223], v[124:127], a[84:87], v[220:223]\n\t" \
"v_mfma_f32_16x16x32_bf16 v[224:227], v[120:123], a[140:143], v[224:227]\n\t" \
"ds_read_b128 v[72:75], v249 offset:1024\n\t" \
"v_mfma_f32_16x16x32_bf16 v[228:231], v[124:127], a[140:143], v[228:231]\n\t" \
"v_mfma_f32_16x16x32_bf16 v[208:211], v[128:131], a[32:35], v[208:211]\n\t" \
"ds_read_b128 v[76:79], v249 offset:15360\n\t" \
"v_mfma_f32_16x16x32_bf16 v[212:215], v[132:135], a[32:35], v[212:215]\n\t" \
"s_add_u32 s90, s87, %[lg]\n\t" \
"s_add_u32 s90, s90, 0\n\t" \
"s_mov_b32 m0, s90\n\t" \
"s_add_u32 s95, s89, 0\n\t" \
"buffer_load_dwordx4 %[vg], %[rh], s95 offen offset:0 lds\n\t" \
"v_mfma_f32_16x16x32_bf16 v[216:219], v[128:131], a[88:91], v[216:219]\n\t" \
"ds_read_b128 v[80:83], v249 offset:2048\n\t" \
"v_mfma_f32_16x16x32_bf16 v[220:223], v[132:135], a[88:91], v[220:223]\n\t" \
"v_mfma_f32_16x16x32_bf16 v[224:227], v[128:131], a[144:147], v[224:227]\n\t" \
"ds_read_b128 v[84:87], v249 offset:16384\n\t" \
"v_mfma_f32_16x16x32_bf16 v[228:231], v[132:135], a[144:147], v[228:231]\n\t" \
"v_mfma_f32_16x16x32_bf16 v[208:211], v[136:139], a[36:39], v[208:211]\n\t" \
"ds_read_b128 v[88:91], v249 offset:3072\n\t" \
"v_mfma_f32_16x16x32_bf16 v[212:215], v[140:143], a[36:39], v[212:215]\n\t" \
"v_mfma_f32_16x16x32_bf16 v[216:219], v[136:139], a[92:95], v[216:219]\n\t" \
"ds_read_b128 v[92:95], v249 offset:17408\n\t" \
"v_mfma_f32_16x16x32_bf16 v[220:223], v[140:143], a[92:95], v[220:223]\n\t" \
"buffer_load_dwordx4 %[vg], %[rh], s95 offen offset:1024 lds\n\t" \
"v_mfma_f32_16x16x32_bf16 v[224:227], v[136:139], a[148:151], v[224:227]\n\t" \
"ds_read_b128 v[96:99], v249 offset:4096\n\t" \
"v_mfma_f32_16x16x32_bf16 v[228:231], v[140:143], a[148:151], v[228:231]\n\t" \
"v_mfma_f32_16x16x32_bf16 v[208:211], v[144:147], a[40:43], v[208:211]\n\t" \
"ds_read_b128 v[100:103], v249 offset:18432\n\t" \
"v_mfma_f32_16x16x32_bf16 v[212:215], v[148:151], a[40:43], v[212:215]\n\t" \
"v_mfma_f32_16x16x32_bf16 v[216:219], v[144:147], a[96:99], v[216:219]\n\t" \
"ds_read_b128 v[104:107], v249 offset:5120\n\t" \
"v_mfma_f32_16x16x32_bf16 v[220:223], v[148:151], a[96:99], v[220:223]\n\t" \
"v_mfma_f32_16x16x32_bf16 v[224:227], v[144:147], a[152:155], v[224:227]\n\t" \
"ds_read_b128 v[108:111], v249 offset:19456\n\t" \
"v_mfma_f32_16x16x32_bf16 v[228:231], v[148:151], a[152:155], v[228:231]\n\t" \
"buffer_load_dwordx4 %[vg], %[rh], s95 offen offset:2048 lds\n\t" \
"v_mfma_f32_16x16x32_bf16 v[208:211], v[152:155], a[44:47], v[208:211]\n\t" \
"ds_read_b128 v[112:115], v249 offset:6144\n\t" \
"v_mfma_f32_16x16x32_bf16 v[212:215], v[156:159], a[44:47], v[212:215]\n\t" \
"v_mfma_f32_16x16x32_bf16 v[216:219], v[152:155], a[100:103], v[216:219]\n\t" \
"ds_read_b128 v[116:119], v249 offset:20480\n\t" \
"v_mfma_f32_16x16x32_bf16 v[220:223], v[156:159], a[100:103], v[220:223]\n\t" \
"v_mfma_f32_16x16x32_bf16 v[224:227], v[152:155], a[156:159], v[224:227]\n\t" \
"v_mfma_f32_16x16x32_bf16 v[228:231], v[156:159], a[156:159], v[228:231]\n\t" \
"v_mfma_f32_16x16x32_bf16 v[208:211], v[160:163], a[48:51], v[208:211]\n\t" \
"v_mfma_f32_16x16x32_bf16 v[212:215], v[164:167], a[48:51], v[212:215]\n\t" \
"buffer_load_dwordx4 %[vg], %[rh], s95 offen offset:3072 lds\n\t" \
"v_mfma_f32_16x16x32_bf16 v[216:219], v[160:163], a[104:107], v[216:219]\n\t" \
"v_mfma_f32_16x16x32_bf16 v[220:223], v[164:167], a[104:107], v[220:223]\n\t" \
"v_mfma_f32_16x16x32_bf16 v[224:227], v[160:163], a[160:163], v[224:227]\n\t" \
"v_mfma_f32_16x16x32_bf16 v[228:231], v[164:167], a[160:163], v[228:231]\n\t" \
"v_mfma_f32_16x16x32_bf16 v[208:211], v[168:171], a[52:55], v[208:211]\n\t" \
"v_mfma_f32_16x16x32_bf16 v[212:215], v[172:175], a[52:55], v[212:215]\n\t" \
"v_mfma_f32_16x16x32_bf16 v[216:219], v[168:171], a[108:111], v[216:219]\n\t" \
"v_mfma_f32_16x16x32_bf16 v[220:223], v[172:175], a[108:111], v[220:223]\n\t" \
"s_add_u32 s90, s87, %[lg]\n\t" \
"s_add_u32 s90, s90, 4096\n\t" \
"s_mov_b32 m0, s90\n\t" \
"s_add_u32 s95, s89, 4096\n\t" \
"buffer_load_dwordx4 %[vg], %[rh], s95 offen offset:0 lds\n\t" \
"v_mfma_f32_16x16x32_bf16 v[224:227], v[168:171], a[164:167], v[224:227]\n\t" \
"v_mfma_f32_16x16x32_bf16 v[228:231], v[172:175], a[164:167], v[228:231]\n\t" \
"s_add_u32 s92, s92, 28672\n\t" \
"s_cmp_eq_u32 s92, 114688\n\t" \
"s_cselect_b32 s92, 0, s92\n\t" \
"v_mov_b32 v248, v249\n\t" \
"s_add_u32 s91, s91, 917504\n\t" \
"s_mov_b32 s88, 4\n\t" \
"s_nop 1\n\t" \
"Lp3%=:\n\t" \
"s_waitcnt lgkmcnt(0)\n\t" \
"v_mfma_f32_16x16x32_bf16 v[176:179], v[64:67], a[0:3], 0\n\t" \
"v_mfma_f32_16x16x32_bf16 v[180:183], v[68:71], a[0:3], 0\n\t" \
"ds_read_b128 v[120:123], v248 offset:7168\n\t" \
"v_mfma_f32_16x16x32_bf16 v[184:187], v[64:67], a[56:59], 0\n\t" \
"v_cvt_pk_bf16_f32 v240, v208, v209\n\t" \
"v_mfma_f32_16x16x32_bf16 v[188:191], v[68:71], a[56:59], 0\n\t" \
"v_cvt_pk_bf16_f32 v241, v210, v211\n\t" \
"v_mfma_f32_16x16x32_bf16 v[192:195], v[64:67], a[112:115], 0\n\t" \
"v_cvt_pk_bf16_f32 v242, v212, v213\n\t" \
"v_mfma_f32_16x16x32_bf16 v[196:199], v[68:71], a[112:115], 0\n\t" \
"v_cvt_pk_bf16_f32 v243, v214, v215\n\t" \
"v_mfma_f32_16x16x32_bf16 v[176:179], v[72:75], a[4:7], v[176:179]\n\t" \
"buffer_store_dwordx4 v[240:243], %[vs0], %[ro], s91 offen offset:0 sc1\n\t" \
"v_mfma_f32_16x16x32_bf16 v[180:183], v[76:79], a[4:7], v[180:183]\n\t" \
"v_cvt_pk_bf16_f32 v244, v216, v217\n\t" \
"s_add_u32 s90, s87, %[lg]\n\t" \
"s_add_u32 s90, s90, 4096\n\t" \
"s_mov_b32 m0, s90\n\t" \
"s_add_u32 s95, s89, 4096\n\t" \
"buffer_load_dwordx4 %[vg], %[rh], s95 offen offset:1024 lds\n\t" \
"v_mfma_f32_16x16x32_bf16 v[184:187], v[72:75], a[60:63], v[184:187]\n\t" \
"v_cvt_pk_bf16_f32 v245, v218, v219\n\t" \
"v_mfma_f32_16x16x32_bf16 v[188:191], v[76:79], a[60:63], v[188:191]\n\t" \
"v_cvt_pk_bf16_f32 v246, v220, v221\n\t" \
"v_mfma_f32_16x16x32_bf16 v[192:195], v[72:75], a[116:119], v[192:195]\n\t" \
"v_cvt_pk_bf16_f32 v247, v222, v223\n\t" \
"v_mfma_f32_16x16x32_bf16 v[196:199], v[76:79], a[116:119], v[196:199]\n\t" \
"buffer_store_dwordx4 v[244:247], %[vs0], %[ro], s91 offen offset:1024 sc1\n\t" \
"v_mfma_f32_16x16x32_bf16 v[176:179], v[80:83], a[8:11], v[176:179]\n\t" \
"v_cvt_pk_bf16_f32 v240, v224, v225\n\t" \
"v_mfma_f32_16x16x32_bf16 v[180:183], v[84:87], a[8:11], v[180:183]\n\t" \
"v_cvt_pk_bf16_f32 v241, v226, v227\n\t" \
"v_mfma_f32_16x16x32_bf16 v[184:187], v[80:83], a[64:67], v[184:187]\n\t" \
"v_cvt_pk_bf16_f32 v242, v228, v229\n\t" \
"v_mfma_f32_16x16x32_bf16 v[188:191], v[84:87], a[64:67], v[188:191]\n\t" \
"v_cvt_pk_bf16_f32 v243, v230, v231\n\t" \
"buffer_load_dwordx4 %[vg], %[rh], s95 offen offset:2048 lds\n\t" \
"v_mfma_f32_16x16x32_bf16 v[192:195], v[80:83], a[120:123], v[192:195]\n\t" \
"buffer_store_dwordx4 v[240:243], %[vs0], %[ro], s91 offen offset:2048 sc1\n\t" \
"v_mfma_f32_16x16x32_bf16 v[196:199], v[84:87], a[120:123], v[196:199]\n\t" \
"ds_read_b128 v[124:127], v248 offset:21504\n\t" \
"v_mfma_f32_16x16x32_bf16 v[176:179], v[88:91], a[12:15], v[176:179]\n\t" \
"v_mfma_f32_16x16x32_bf16 v[180:183], v[92:95], a[12:15], v[180:183]\n\t" \
"ds_read_b128 v[128:131], v248 offset:8192\n\t" \
"v_mfma_f32_16x16x32_bf16 v[184:187], v[88:91], a[68:71], v[184:187]\n\t" \
"v_mfma_f32_16x16x32_bf16 v[188:191], v[92:95], a[68:71], v[188:191]\n\t" \
"ds_read_b128 v[132:135], v248 offset:22528\n\t" \
"v_mfma_f32_16x16x32_bf16 v[192:195], v[88:91], a[124:127], v[192:195]\n\t" \
"v_mfma_f32_16x16x32_bf16 v[196:199], v[92:95], a[124:127], v[196:199]\n\t" \
"ds_read_b128 v[136:139], v248 offset:9216\n\t" \
"buffer_load_dwordx4 %[vg], %[rh], s95 offen offset:3072 lds\n\t" \
"v_mfma_f32_16x16x32_bf16 v[176:179], v[96:99], a[16:19], v[176:179]\n\t" \
"v_mfma_f32_16x16x32_bf16 v[180:183], v[100:103], a[16:19], v[180:183]\n\t" \
"ds_read_b128 v[140:143], v248 offset:23552\n\t" \
"v_mfma_f32_16x16x32_bf16 v[184:187], v[96:99], a[72:75], v[184:187]\n\t" \
"v_mfma_f32_16x16x32_bf16 v[188:191], v[100:103], a[72:75], v[188:191]\n\t" \
"ds_read_b128 v[144:147], v248 offset:10240\n\t" \
"v_mfma_f32_16x16x32_bf16 v[192:195], v[96:99], a[128:131], v[192:195]\n\t" \
"v_mfma_f32_16x16x32_bf16 v[196:199], v[100:103], a[128:131], v[196:199]\n\t" \
"ds_read_b128 v[148:151], v248 offset:24576\n\t" \
"v_mfma_f32_16x16x32_bf16 v[176:179], v[104:107], a[20:23], v[176:179]\n\t" \
"v_mfma_f32_16x16x32_bf16 v[180:183], v[108:111], a[20:23], v[180:183]\n\t" \
"ds_read_b128 v[152:155], v248 offset:11264\n\t" \
"s_add_u32 s90, s87, %[lg]\n\t" \
"s_add_u32 s90, s90, 8192\n\t" \
"s_mov_b32 m0, s90\n\t" \
"s_add_u32 s95, s89, 8192\n\t" \
"buffer_load_dwordx4 %[vg], %[rh], s95 offen offset:0 lds\n\t" \
"v_mfma_f32_16x16x32_bf16 v[184:187], v[104:107], a[76:79], v[184:187]\n\t" \
"v_mfma_f32_16x16x32_bf16 v[188:191], v[108:111], a[76:79], v[188:191]\n\t" \
"ds_read_b128 v[156:159], v248 offset:25600\n\t" \
"v_mfma_f32_16x16x32_bf16 v[192:195], v[104:107], a[132:135], v[192:195]\n\t" \
"v_mfma_f32_16x16x32_bf16 v[196:199], v[108:111], a[132:135], v[196:199]\n\t" \
"ds_read_b128 v[160:163], v248 offset:12288\n\t" \
"v_mfma_f32_16x16x32_bf16 v[176:179], v[112:115], a[24:27], v[176:179]\n\t" \
"v_mfma_f32_16x16x32_bf16 v[180:183], v[116:119], a[24:27], v[180:183]\n\t" \
"ds_read_b128 v[164:167], v248 offset:26624\n\t" \
"v_mfma_f32_16x16x32_bf16 v[184:187], v[112:115], a[80:83], v[184:187]\n\t" \
"v_mfma_f32_16x16x32_bf16 v[188:191], v[116:119], a[80:83], v[188:191]\n\t" \
"ds_read_b128 v[168:171], v248 offset:13312\n\t" \
"buffer_load_dwordx4 %[vg], %[rh], s95 offen offset:1024 lds\n\t" \
"v_mfma_f32_16x16x32_bf16 v[192:195], v[112:115], a[136:139], v[192:195]\n\t" \
"v_mfma_f32_16x16x32_bf16 v[196:199], v[116:119], a[136:139], v[196:199]\n\t" \
"ds_read_b128 v[172:175], v248 offset:27648\n\t" \
"s_add_u32 s89, s89, 57344\n\t" \
"s_min_u32 s89, s89, 1806336\n\t" \
"s_mov_b32 s87, s92\n\t" \
"s_add_u32 s95, s92, 28672\n\t" \
"s_cmp_eq_u32 s95, 114688\n\t" \
"s_cselect_b32 s95, 0, s95\n\t" \
"v_add_u32 v249, s95, %[lr]\n\t" \
"s_waitcnt vmcnt(13) lgkmcnt(0)\n\t" \
"s_barrier\n\t" \
"s_add_u32 s94, s94, 128\n\t" \
"v_mfma_f32_16x16x32_bf16 v[176:179], v[120:123], a[28:31], v[176:179]\n\t" \
"ds_read_b128 v[64:67], v249 offset:0\n\t" \
"v_mfma_f32_16x16x32_bf16 v[180:183], v[124:127], a[28:31], v[180:183]\n\t" \
"v_mfma_f32_16x16x32_bf16 v[184:187], v[120:123], a[84:87], v[184:187]\n\t" \
"ds_read_b128 v[68:71], v249 offset:14336\n\t" \
"v_mfma_f32_16x16x32_bf16 v[188:191], v[124:127], a[84:87], v[188:191]\n\t" \
"v_mfma_f32_16x16x32_bf16 v[192:195], v[120:123], a[140:143], v[192:195]\n\t" \
"ds_read_b128 v[72:75], v249 offset:1024\n\t" \
"v_mfma_f32_16x16x32_bf16 v[196:199], v[124:127], a[140:143], v[196:199]\n\t" \
"v_mfma_f32_16x16x32_bf16 v[176:179], v[128:131], a[32:35], v[176:179]\n\t" \
"ds_read_b128 v[76:79], v249 offset:15360\n\t" \
"v_mfma_f32_16x16x32_bf16 v[180:183], v[132:135], a[32:35], v[180:183]\n\t" \
"s_add_u32 s90, s87, %[lg]\n\t" \
"s_add_u32 s90, s90, 0\n\t" \
"s_mov_b32 m0, s90\n\t" \
"s_add_u32 s95, s89, 0\n\t" \
"buffer_load_dwordx4 %[vg], %[rh], s95 offen offset:0 lds\n\t" \
"v_mfma_f32_16x16x32_bf16 v[184:187], v[128:131], a[88:91], v[184:187]\n\t" \
"ds_read_b128 v[80:83], v249 offset:2048\n\t" \
"v_mfma_f32_16x16x32_bf16 v[188:191], v[132:135], a[88:91], v[188:191]\n\t" \
"v_mfma_f32_16x16x32_bf16 v[192:195], v[128:131], a[144:147], v[192:195]\n\t" \
"ds_read_b128 v[84:87], v249 offset:16384\n\t" \
"v_mfma_f32_16x16x32_bf16 v[196:199], v[132:135], a[144:147], v[196:199]\n\t" \
"v_mfma_f32_16x16x32_bf16 v[176:179], v[136:139], a[36:39], v[176:179]\n\t" \
"ds_read_b128 v[88:91], v249 offset:3072\n\t" \
"v_mfma_f32_16x16x32_bf16 v[180:183], v[140:143], a[36:39], v[180:183]\n\t" \
"v_mfma_f32_16x16x32_bf16 v[184:187], v[136:139], a[92:95], v[184:187]\n\t" \
"ds_read_b128 v[92:95], v249 offset:17408\n\t" \
"v_mfma_f32_16x16x32_bf16 v[188:191], v[140:143], a[92:95], v[188:191]\n\t" \
"buffer_load_dwordx4 %[vg], %[rh], s95 offen offset:1024 lds\n\t" \
"v_mfma_f32_16x16x32_bf16 v[192:195], v[136:139], a[148:151], v[192:195]\n\t" \
"ds_read_b128 v[96:99], v249 offset:4096\n\t" \
"v_mfma_f32_16x16x32_bf16 v[196:199], v[140:143], a[148:151], v[196:199]\n\t" \
"v_mfma_f32_16x16x32_bf16 v[176:179], v[144:147], a[40:43], v[176:179]\n\t" \
"ds_read_b128 v[100:103], v249 offset:18432\n\t" \
"v_mfma_f32_16x16x32_bf16 v[180:183], v[148:151], a[40:43], v[180:183]\n\t" \
"v_mfma_f32_16x16x32_bf16 v[184:187], v[144:147], a[96:99], v[184:187]\n\t" \
"ds_read_b128 v[104:107], v249 offset:5120\n\t" \
"v_mfma_f32_16x16x32_bf16 v[188:191], v[148:151], a[96:99], v[188:191]\n\t" \
"v_mfma_f32_16x16x32_bf16 v[192:195], v[144:147], a[152:155], v[192:195]\n\t" \
"ds_read_b128 v[108:111], v249 offset:19456\n\t" \
"v_mfma_f32_16x16x32_bf16 v[196:199], v[148:151], a[152:155], v[196:199]\n\t" \
"buffer_load_dwordx4 %[vg], %[rh], s95 offen offset:2048 lds\n\t" \
"v_mfma_f32_16x16x32_bf16 v[176:179], v[152:155], a[44:47], v[176:179]\n\t" \
"ds_read_b128 v[112:115], v249 offset:6144\n\t" \
"v_mfma_f32_16x16x32_bf16 v[180:183], v[156:159], a[44:47], v[180:183]\n\t" \
"v_mfma_f32_16x16x32_bf16 v[184:187], v[152:155], a[100:103], v[184:187]\n\t" \
"ds_read_b128 v[116:119], v249 offset:20480\n\t" \
"v_mfma_f32_16x16x32_bf16 v[188:191], v[156:159], a[100:103], v[188:191]\n\t" \
"v_mfma_f32_16x16x32_bf16 v[192:195], v[152:155], a[156:159], v[192:195]\n\t" \
"v_mfma_f32_16x16x32_bf16 v[196:199], v[156:159], a[156:159], v[196:199]\n\t" \
"v_mfma_f32_16x16x32_bf16 v[176:179], v[160:163], a[48:51], v[176:179]\n\t" \
"v_mfma_f32_16x16x32_bf16 v[180:183], v[164:167], a[48:51], v[180:183]\n\t" \
"buffer_load_dwordx4 %[vg], %[rh], s95 offen offset:3072 lds\n\t" \
"v_mfma_f32_16x16x32_bf16 v[184:187], v[160:163], a[104:107], v[184:187]\n\t" \
"v_mfma_f32_16x16x32_bf16 v[188:191], v[164:167], a[104:107], v[188:191]\n\t" \
"v_mfma_f32_16x16x32_bf16 v[192:195], v[160:163], a[160:163], v[192:195]\n\t" \
"v_mfma_f32_16x16x32_bf16 v[196:199], v[164:167], a[160:163], v[196:199]\n\t" \
"v_mfma_f32_16x16x32_bf16 v[176:179], v[168:171], a[52:55], v[176:179]\n\t" \
"v_mfma_f32_16x16x32_bf16 v[180:183], v[172:175], a[52:55], v[180:183]\n\t" \
"v_mfma_f32_16x16x32_bf16 v[184:187], v[168:171], a[108:111], v[184:187]\n\t" \
"v_mfma_f32_16x16x32_bf16 v[188:191], v[172:175], a[108:111], v[188:191]\n\t" \
"s_add_u32 s90, s87, %[lg]\n\t" \
"s_add_u32 s90, s90, 4096\n\t" \
"s_mov_b32 m0, s90\n\t" \
"s_add_u32 s95, s89, 4096\n\t" \
"buffer_load_dwordx4 %[vg], %[rh], s95 offen offset:0 lds\n\t" \
"v_mfma_f32_16x16x32_bf16 v[192:195], v[168:171], a[164:167], v[192:195]\n\t" \
"v_mfma_f32_16x16x32_bf16 v[196:199], v[172:175], a[164:167], v[196:199]\n\t" \
"s_add_u32 s92, s92, 28672\n\t" \
"s_cmp_eq_u32 s92, 114688\n\t" \
"s_cselect_b32 s92, 0, s92\n\t" \
"v_mov_b32 v248, v249\n\t" \
"s_add_u32 s91, s91, 917504\n\t" \
"s_waitcnt lgkmcnt(0)\n\t" \
"v_mfma_f32_16x16x32_bf16 v[208:211], v[64:67], a[0:3], 0\n\t" \
"v_mfma_f32_16x16x32_bf16 v[212:215], v[68:71], a[0:3], 0\n\t" \
"ds_read_b128 v[120:123], v248 offset:7168\n\t" \
"v_mfma_f32_16x16x32_bf16 v[216:219], v[64:67], a[56:59], 0\n\t" \
"v_cvt_pk_bf16_f32 v240, v176, v177\n\t" \
"v_mfma_f32_16x16x32_bf16 v[220:223], v[68:71], a[56:59], 0\n\t" \
"v_cvt_pk_bf16_f32 v241, v178, v179\n\t" \
"v_mfma_f32_16x16x32_bf16 v[224:227], v[64:67], a[112:115], 0\n\t" \
"v_cvt_pk_bf16_f32 v242, v180, v181\n\t" \
"v_mfma_f32_16x16x32_bf16 v[228:231], v[68:71], a[112:115], 0\n\t" \
"v_cvt_pk_bf16_f32 v243, v182, v183\n\t" \
"v_mfma_f32_16x16x32_bf16 v[208:211], v[72:75], a[4:7], v[208:211]\n\t" \
"buffer_store_dwordx4 v[240:243], %[vs0], %[ro], s91 offen offset:0 sc1\n\t" \
"v_mfma_f32_16x16x32_bf16 v[212:215], v[76:79], a[4:7], v[212:215]\n\t" \
"v_cvt_pk_bf16_f32 v244, v184, v185\n\t" \
"s_add_u32 s90, s87, %[lg]\n\t" \
"s_add_u32 s90, s90, 4096\n\t" \
"s_mov_b32 m0, s90\n\t" \
"s_add_u32 s95, s89, 4096\n\t" \
"buffer_load_dwordx4 %[vg], %[rh], s95 offen offset:1024 lds\n\t" \
"v_mfma_f32_16x16x32_bf16 v[216:219], v[72:75], a[60:63], v[216:219]\n\t" \
"v_cvt_pk_bf16_f32 v245, v186, v187\n\t" \
"v_mfma_f32_16x16x32_bf16 v[220:223], v[76:79], a[60:63], v[220:223]\n\t" \
"v_cvt_pk_bf16_f32 v246, v188, v189\n\t" \
"v_mfma_f32_16x16x32_bf16 v[224:227], v[72:75], a[116:119], v[224:227]\n\t" \
"v_cvt_pk_bf16_f32 v247, v190, v191\n\t" \
"v_mfma_f32_16x16x32_bf16 v[228:231], v[76:79], a[116:119], v[228:231]\n\t" \
"buffer_store_dwordx4 v[244:247], %[vs0], %[ro], s91 offen offset:1024 sc1\n\t" \
"v_mfma_f32_16x16x32_bf16 v[208:211], v[80:83], a[8:11], v[208:211]\n\t" \
"v_cvt_pk_bf16_f32 v240, v192, v193\n\t" \
"v_mfma_f32_16x16x32_bf16 v[212:215], v[84:87], a[8:11], v[212:215]\n\t" \
"v_cvt_pk_bf16_f32 v241, v194, v195\n\t" \
"v_mfma_f32_16x16x32_bf16 v[216:219], v[80:83], a[64:67], v[216:219]\n\t" \
"v_cvt_pk_bf16_f32 v242, v196, v197\n\t" \
"v_mfma_f32_16x16x32_bf16 v[220:223], v[84:87], a[64:67], v[220:223]\n\t" \
"v_cvt_pk_bf16_f32 v243, v198, v199\n\t" \
"buffer_load_dwordx4 %[vg], %[rh], s95 offen offset:2048 lds\n\t" \
"v_mfma_f32_16x16x32_bf16 v[224:227], v[80:83], a[120:123], v[224:227]\n\t" \
"buffer_store_dwordx4 v[240:243], %[vs0], %[ro], s91 offen offset:2048 sc1\n\t" \
"v_mfma_f32_16x16x32_bf16 v[228:231], v[84:87], a[120:123], v[228:231]\n\t" \
"ds_read_b128 v[124:127], v248 offset:21504\n\t" \
"v_mfma_f32_16x16x32_bf16 v[208:211], v[88:91], a[12:15], v[208:211]\n\t" \
"v_mfma_f32_16x16x32_bf16 v[212:215], v[92:95], a[12:15], v[212:215]\n\t" \
"ds_read_b128 v[128:131], v248 offset:8192\n\t" \
"v_mfma_f32_16x16x32_bf16 v[216:219], v[88:91], a[68:71], v[216:219]\n\t" \
"v_mfma_f32_16x16x32_bf16 v[220:223], v[92:95], a[68:71], v[220:223]\n\t" \
"ds_read_b128 v[132:135], v248 offset:22528\n\t" \
"v_mfma_f32_16x16x32_bf16 v[224:227], v[88:91], a[124:127], v[224:227]\n\t" \
"v_mfma_f32_16x16x32_bf16 v[228:231], v[92:95], a[124:127], v[228:231]\n\t" \
"ds_read_b128 v[136:139], v248 offset:9216\n\t" \
"buffer_load_dwordx4 %[vg], %[rh], s95 offen offset:3072 lds\n\t" \
"v_mfma_f32_16x16x32_bf16 v[208:211], v[96:99], a[16:19], v[208:211]\n\t" \
"v_mfma_f32_16x16x32_bf16 v[212:215], v[100:103], a[16:19], v[212:215]\n\t" \
"ds_read_b128 v[140:143], v248 offset:23552\n\t" \
"v_mfma_f32_16x16x32_bf16 v[216:219], v[96:99], a[72:75], v[216:219]\n\t" \
"v_mfma_f32_16x16x32_bf16 v[220:223], v[100:103], a[72:75], v[220:223]\n\t" \
"ds_read_b128 v[144:147], v248 offset:10240\n\t" \
"v_mfma_f32_16x16x32_bf16 v[224:227], v[96:99], a[128:131], v[224:227]\n\t" \
"v_mfma_f32_16x16x32_bf16 v[228:231], v[100:103], a[128:131], v[228:231]\n\t" \
"ds_read_b128 v[148:151], v248 offset:24576\n\t" \
"v_mfma_f32_16x16x32_bf16 v[208:211], v[104:107], a[20:23], v[208:211]\n\t" \
"v_mfma_f32_16x16x32_bf16 v[212:215], v[108:111], a[20:23], v[212:215]\n\t" \
"ds_read_b128 v[152:155], v248 offset:11264\n\t" \
"s_add_u32 s90, s87, %[lg]\n\t" \
"s_add_u32 s90, s90, 8192\n\t" \
"s_mov_b32 m0, s90\n\t" \
"s_add_u32 s95, s89, 8192\n\t" \
"buffer_load_dwordx4 %[vg], %[rh], s95 offen offset:0 lds\n\t" \
"v_mfma_f32_16x16x32_bf16 v[216:219], v[104:107], a[76:79], v[216:219]\n\t" \
"v_mfma_f32_16x16x32_bf16 v[220:223], v[108:111], a[76:79], v[220:223]\n\t" \
"ds_read_b128 v[156:159], v248 offset:25600\n\t" \
"v_mfma_f32_16x16x32_bf16 v[224:227], v[104:107], a[132:135], v[224:227]\n\t" \
"v_mfma_f32_16x16x32_bf16 v[228:231], v[108:111], a[132:135], v[228:231]\n\t" \
"ds_read_b128 v[160:163], v248 offset:12288\n\t" \
"v_mfma_f32_16x16x32_bf16 v[208:211], v[112:115], a[24:27], v[208:211]\n\t" \
"v_mfma_f32_16x16x32_bf16 v[212:215], v[116:119], a[24:27], v[212:215]\n\t" \
"ds_read_b128 v[164:167], v248 offset:26624\n\t" \
"v_mfma_f32_16x16x32_bf16 v[216:219], v[112:115], a[80:83], v[216:219]\n\t" \
"v_mfma_f32_16x16x32_bf16 v[220:223], v[116:119], a[80:83], v[220:223]\n\t" \
"ds_read_b128 v[168:171], v248 offset:13312\n\t" \
"buffer_load_dwordx4 %[vg], %[rh], s95 offen offset:1024 lds\n\t" \
"v_mfma_f32_16x16x32_bf16 v[224:227], v[112:115], a[136:139], v[224:227]\n\t" \
"v_mfma_f32_16x16x32_bf16 v[228:231], v[116:119], a[136:139], v[228:231]\n\t" \
"ds_read_b128 v[172:175], v248 offset:27648\n\t" \
"s_add_u32 s89, s89, 57344\n\t" \
"s_min_u32 s89, s89, 1806336\n\t" \
"s_mov_b32 s87, s92\n\t" \
"s_add_u32 s95, s92, 28672\n\t" \
"s_cmp_eq_u32 s95, 114688\n\t" \
"s_cselect_b32 s95, 0, s95\n\t" \
"v_add_u32 v249, s95, %[lr]\n\t" \
"s_waitcnt vmcnt(13) lgkmcnt(0)\n\t" \
"s_barrier\n\t" \
"s_add_u32 s94, s94, 128\n\t" \
"v_mfma_f32_16x16x32_bf16 v[208:211], v[120:123], a[28:31], v[208:211]\n\t" \
"ds_read_b128 v[64:67], v249 offset:0\n\t" \
"v_mfma_f32_16x16x32_bf16 v[212:215], v[124:127], a[28:31], v[212:215]\n\t" \
"v_mfma_f32_16x16x32_bf16 v[216:219], v[120:123], a[84:87], v[216:219]\n\t" \
"ds_read_b128 v[68:71], v249 offset:14336\n\t" \
"v_mfma_f32_16x16x32_bf16 v[220:223], v[124:127], a[84:87], v[220:223]\n\t" \
"v_mfma_f32_16x16x32_bf16 v[224:227], v[120:123], a[140:143], v[224:227]\n\t" \
"ds_read_b128 v[72:75], v249 offset:1024\n\t" \
"v_mfma_f32_16x16x32_bf16 v[228:231], v[124:127], a[140:143], v[228:231]\n\t" \
"v_mfma_f32_16x16x32_bf16 v[208:211], v[128:131], a[32:35], v[208:211]\n\t" \
"ds_read_b128 v[76:79], v249 offset:15360\n\t" \
"v_mfma_f32_16x16x32_bf16 v[212:215], v[132:135], a[32:35], v[212:215]\n\t" \
"s_add_u32 s90, s87, %[lg]\n\t" \
"s_add_u32 s90, s90, 0\n\t" \
"s_mov_b32 m0, s90\n\t" \
"s_add_u32 s95, s89, 0\n\t" \
"buffer_load_dwordx4 %[vg], %[rh], s95 offen offset:0 lds\n\t" \
"v_mfma_f32_16x16x32_bf16 v[216:219], v[128:131], a[88:91], v[216:219]\n\t" \
"ds_read_b128 v[80:83], v249 offset:2048\n\t" \
"v_mfma_f32_16x16x32_bf16 v[220:223], v[132:135], a[88:91], v[220:223]\n\t" \
"v_mfma_f32_16x16x32_bf16 v[224:227], v[128:131], a[144:147], v[224:227]\n\t" \
"ds_read_b128 v[84:87], v249 offset:16384\n\t" \
"v_mfma_f32_16x16x32_bf16 v[228:231], v[132:135], a[144:147], v[228:231]\n\t" \
"v_mfma_f32_16x16x32_bf16 v[208:211], v[136:139], a[36:39], v[208:211]\n\t" \
"ds_read_b128 v[88:91], v249 offset:3072\n\t" \
"v_mfma_f32_16x16x32_bf16 v[212:215], v[140:143], a[36:39], v[212:215]\n\t" \
"v_mfma_f32_16x16x32_bf16 v[216:219], v[136:139], a[92:95], v[216:219]\n\t" \
"ds_read_b128 v[92:95], v249 offset:17408\n\t" \
"v_mfma_f32_16x16x32_bf16 v[220:223], v[140:143], a[92:95], v[220:223]\n\t" \
"buffer_load_dwordx4 %[vg], %[rh], s95 offen offset:1024 lds\n\t" \
"v_mfma_f32_16x16x32_bf16 v[224:227], v[136:139], a[148:151], v[224:227]\n\t" \
"ds_read_b128 v[96:99], v249 offset:4096\n\t" \
"v_mfma_f32_16x16x32_bf16 v[228:231], v[140:143], a[148:151], v[228:231]\n\t" \
"v_mfma_f32_16x16x32_bf16 v[208:211], v[144:147], a[40:43], v[208:211]\n\t" \
"ds_read_b128 v[100:103], v249 offset:18432\n\t" \
"v_mfma_f32_16x16x32_bf16 v[212:215], v[148:151], a[40:43], v[212:215]\n\t" \
"v_mfma_f32_16x16x32_bf16 v[216:219], v[144:147], a[96:99], v[216:219]\n\t" \
"ds_read_b128 v[104:107], v249 offset:5120\n\t" \
"v_mfma_f32_16x16x32_bf16 v[220:223], v[148:151], a[96:99], v[220:223]\n\t" \
"v_mfma_f32_16x16x32_bf16 v[224:227], v[144:147], a[152:155], v[224:227]\n\t" \
"ds_read_b128 v[108:111], v249 offset:19456\n\t" \
"v_mfma_f32_16x16x32_bf16 v[228:231], v[148:151], a[152:155], v[228:231]\n\t" \
"buffer_load_dwordx4 %[vg], %[rh], s95 offen offset:2048 lds\n\t" \
"v_mfma_f32_16x16x32_bf16 v[208:211], v[152:155], a[44:47], v[208:211]\n\t" \
"ds_read_b128 v[112:115], v249 offset:6144\n\t" \
"v_mfma_f32_16x16x32_bf16 v[212:215], v[156:159], a[44:47], v[212:215]\n\t" \
"v_mfma_f32_16x16x32_bf16 v[216:219], v[152:155], a[100:103], v[216:219]\n\t" \
"ds_read_b128 v[116:119], v249 offset:20480\n\t" \
"v_mfma_f32_16x16x32_bf16 v[220:223], v[156:159], a[100:103], v[220:223]\n\t" \
"v_mfma_f32_16x16x32_bf16 v[224:227], v[152:155], a[156:159], v[224:227]\n\t" \
"v_mfma_f32_16x16x32_bf16 v[228:231], v[156:159], a[156:159], v[228:231]\n\t" \
"v_mfma_f32_16x16x32_bf16 v[208:211], v[160:163], a[48:51], v[208:211]\n\t" \
"v_mfma_f32_16x16x32_bf16 v[212:215], v[164:167], a[48:51], v[212:215]\n\t" \
"buffer_load_dwordx4 %[vg], %[rh], s95 offen offset:3072 lds\n\t" \
"v_mfma_f32_16x16x32_bf16 v[216:219], v[160:163], a[104:107], v[216:219]\n\t" \
"v_mfma_f32_16x16x32_bf16 v[220:223], v[164:167], a[104:107], v[220:223]\n\t" \
"v_mfma_f32_16x16x32_bf16 v[224:227], v[160:163], a[160:163], v[224:227]\n\t" \
"v_mfma_f32_16x16x32_bf16 v[228:231], v[164:167], a[160:163], v[228:231]\n\t" \
"v_mfma_f32_16x16x32_bf16 v[208:211], v[168:171], a[52:55], v[208:211]\n\t" \
"v_mfma_f32_16x16x32_bf16 v[212:215], v[172:175], a[52:55], v[212:215]\n\t" \
"v_mfma_f32_16x16x32_bf16 v[216:219], v[168:171], a[108:111], v[216:219]\n\t" \
"v_mfma_f32_16x16x32_bf16 v[220:223], v[172:175], a[108:111], v[220:223]\n\t" \
"s_add_u32 s90, s87, %[lg]\n\t" \
"s_add_u32 s90, s90, 4096\n\t" \
"s_mov_b32 m0, s90\n\t" \
"s_add_u32 s95, s89, 4096\n\t" \
"buffer_load_dwordx4 %[vg], %[rh], s95 offen offset:0 lds\n\t" \
"v_mfma_f32_16x16x32_bf16 v[224:227], v[168:171], a[164:167], v[224:227]\n\t" \
"v_mfma_f32_16x16x32_bf16 v[228:231], v[172:175], a[164:167], v[228:231]\n\t" \
"s_add_u32 s92, s92, 28672\n\t" \
"s_cmp_eq_u32 s92, 114688\n\t" \
"s_cselect_b32 s92, 0, s92\n\t" \
"v_mov_b32 v248, v249\n\t" \
"s_add_u32 s91, s91, 917504\n\t" \
"s_add_u32 s88, s88, 2\n\t" \
"s_cmp_lt_u32 s88, 32\n\t" \
"s_nop 0\n\t" \
"s_cbranch_scc1 Lp3%=\n\t" \
"v_cvt_pk_bf16_f32 v240, v208, v209\n\t" \
"v_cvt_pk_bf16_f32 v241, v210, v211\n\t" \
"v_cvt_pk_bf16_f32 v242, v212, v213\n\t" \
"v_cvt_pk_bf16_f32 v243, v214, v215\n\t" \
"buffer_store_dwordx4 v[240:243], %[vs0], %[ro], s91 offen offset:0 sc1\n\t" \
"v_cvt_pk_bf16_f32 v244, v216, v217\n\t" \
"v_cvt_pk_bf16_f32 v245, v218, v219\n\t" \
"v_cvt_pk_bf16_f32 v246, v220, v221\n\t" \
"v_cvt_pk_bf16_f32 v247, v222, v223\n\t" \
"buffer_store_dwordx4 v[244:247], %[vs0], %[ro], s91 offen offset:1024 sc1\n\t" \
"v_cvt_pk_bf16_f32 v240, v224, v225\n\t" \
"v_cvt_pk_bf16_f32 v241, v226, v227\n\t" \
"v_cvt_pk_bf16_f32 v242, v228, v229\n\t" \
"v_cvt_pk_bf16_f32 v243, v230, v231\n\t" \
"buffer_store_dwordx4 v[240:243], %[vs0], %[ro], s91 offen offset:2048 sc1\n\t" \
"s_waitcnt vmcnt(0) lgkmcnt(0)\n\t" \
"s_barrier\n\t" \
"s_add_u32 s94, s94, 128\n\t" \
"s_add_u32 s94, s94, 128\n\t" \
"s_mov_b32 m0, s93\n\t" \
"s_nop 1\n\t"

#define ASM_P3_1W2_CLOB "v64", "v65", "v66", "v67", "v68", "v69", "v70", "v71", "v72", "v73", "v74", "v75", "v76", "v77", "v78", "v79", "v80", "v81", "v82", "v83", "v84", "v85", "v86", "v87", "v88", "v89", "v90", "v91", "v92", "v93", "v94", "v95", "v96", "v97", "v98", "v99", "v100", "v101", "v102", "v103", "v104", "v105", "v106", "v107", "v108", "v109", "v110", "v111", "v112", "v113", "v114", "v115", "v116", "v117", "v118", "v119", "v120", "v121", "v122", "v123", "v124", "v125", "v126", "v127", "v128", "v129", "v130", "v131", "v132", "v133", "v134", "v135", "v136", "v137", "v138", "v139", "v140", "v141", "v142", "v143", "v144", "v145", "v146", "v147", "v148", "v149", "v150", "v151", "v152", "v153", "v154", "v155", "v156", "v157", "v158", "v159", "v160", "v161", "v162", "v163", "v164", "v165", "v166", "v167", "v168", "v169", "v170", "v171", "v172", "v173", "v174", "v175", "v176", "v177", "v178", "v179", "v180", "v181", "v182", "v183", "v184", "v185", "v186", "v187", "v188", "v189", "v190", "v191", "v192", "v193", "v194", "v195", "v196", "v197", "v198", "v199", "v208", "v209", "v210", "v211", "v212", "v213", "v214", "v215", "v216", "v217", "v218", "v219", "v220", "v221", "v222", "v223", "v224", "v225", "v226", "v227", "v228", "v229", "v230", "v231", "v240", "v241", "v242", "v243", "v244", "v245", "v246", "v247", "v248", "v249", "v250", "a0", "a1", "a2", "a3", "a4", "a5", "a6", "a7", "a8", "a9", "a10", "a11", "a12", "a13", "a14", "a15", "a16", "a17", "a18", "a19", "a20", "a21", "a22", "a23", "a24", "a25", "a26", "a27", "a28", "a29", "a30", "a31", "a32", "a33", "a34", "a35", "a36", "a37", "a38", "a39", "a40", "a41", "a42", "a43", "a44", "a45", "a46", "a47", "a48", "a49", "a50", "a51", "a52", "a53", "a54", "a55", "a56", "a57", "a58", "a59", "a60", "a61", "a62", "a63", "a64", "a65", "a66", "a67", "a68", "a69", "a70", "a71", "a72", "a73", "a74", "a75", "a76", "a77", "a78", "a79", "a80", "a81", "a82", "a83", "a84", "a85", "a86", "a87", "a88", "a89", "a90", "a91", "a92", "a93", "a94", "a95", "a96", "a97", "a98", "a99", "a100", "a101", "a102", "a103", "a104", "a105", "a106", "a107", "a108", "a109", "a110", "a111", "a112", "a113", "a114", "a115", "a116", "a117", "a118", "a119", "a120", "a121", "a122", "a123", "a124", "a125", "a126", "a127", "a128", "a129", "a130", "a131", "a132", "a133", "a134", "a135", "a136", "a137", "a138", "a139", "a140", "a141", "a142", "a143", "a144", "a145", "a146", "a147", "a148", "a149", "a150", "a151", "a152", "a153", "a154", "a155", "a156", "a157", "a158", "a159", "a160", "a161", "a162", "a163", "a164", "a165", "a166", "a167", "s86", "s87", "s88", "s89", "s90", "s91", "s92", "s93", "s94", "s95", "vcc", "scc", "memory"

#define ASM_P3D4_TEXT \
"s_mov_b32 s93, m0\n\t" \
"s_mov_b32 s89, %[src0]\n\t" \
"s_mov_b32 s92, 0\n\t" \
"s_add_u32 s90, s92, %[lg]\n\t" \
"s_add_u32 s90, s90, 0\n\t" \
"s_mov_b32 m0, s90\n\t" \
"s_add_u32 s95, s89, 0\n\t" \
"buffer_load_dwordx4 %[vg], %[rh], s95 offen offset:0 lds\n\t" \
"buffer_load_dwordx4 %[vg], %[rh], s95 offen offset:1024 lds\n\t" \
"buffer_load_dwordx4 %[vg], %[rh], s95 offen offset:2048 lds\n\t" \
"buffer_load_dwordx4 %[vg], %[rh], s95 offen offset:3072 lds\n\t" \
"s_add_u32 s89, s89, 57344\n\t" \
"s_mov_b32 s92, 28672\n\t" \
"s_add_u32 s90, s92, %[lg]\n\t" \
"s_add_u32 s90, s90, 0\n\t" \
"s_mov_b32 m0, s90\n\t" \
"s_add_u32 s95, s89, 0\n\t" \
"buffer_load_dwordx4 %[vg], %[rh], s95 offen offset:0 lds\n\t" \
"buffer_load_dwordx4 %[vg], %[rh], s95 offen offset:1024 lds\n\t" \
"buffer_load_dwordx4 %[vg], %[rh], s95 offen offset:2048 lds\n\t" \
"buffer_load_dwordx4 %[vg], %[rh], s95 offen offset:3072 lds\n\t" \
"s_add_u32 s89, s89, 57344\n\t" \
"s_mov_b32 s92, 57344\n\t" \
"s_add_u32 s90, s92, %[lg]\n\t" \
"s_add_u32 s90, s90, 0\n\t" \
"s_mov_b32 m0, s90\n\t" \
"s_add_u32 s95, s89, 0\n\t" \
"buffer_load_dwordx4 %[vg], %[rh], s95 offen offset:0 lds\n\t" \
"buffer_load_dwordx4 %[vg], %[rh], s95 offen offset:1024 lds\n\t" \
"buffer_load_dwordx4 %[vg], %[rh], s95 offen offset:2048 lds\n\t" \
"buffer_load_dwordx4 %[vg], %[rh], s95 offen offset:3072 lds\n\t" \
"s_add_u32 s89, s89, 57344\n\t" \
"s_mov_b32 s92, 86016\n\t" \
"s_add_u32 s90, s92, %[lg]\n\t" \
"s_add_u32 s90, s90, 0\n\t" \
"s_mov_b32 m0, s90\n\t" \
"s_add_u32 s95, s89, 0\n\t" \
"buffer_load_dwordx4 %[vg], %[rh], s95 offen offset:0 lds\n\t" \
"buffer_load_dwordx4 %[vg], %[rh], s95 offen offset:1024 lds\n\t" \
"s_mov_b32 m0, s93\n\t"

#define ASM_P3D_TEXT \
"s_mov_b32 s93, m0\n\t" \
"s_mov_b32 s89, %[src0]\n\t" \
"s_mov_b32 s92, 0\n\t" \
"s_add_u32 s90, s92, %[lg]\n\t" \
"s_add_u32 s90, s90, 0\n\t" \
"s_mov_b32 m0, s90\n\t" \
"s_add_u32 s95, s89, 0\n\t" \
"buffer_load_dwordx4 %[vg], %[rh], s95 offen offset:0 lds\n\t" \
"buffer_load_dwordx4 %[vg], %[rh], s95 offen offset:1024 lds\n\t" \
"buffer_load_dwordx4 %[vg], %[rh], s95 offen offset:2048 lds\n\t" \
"buffer_load_dwordx4 %[vg], %[rh], s95 offen offset:3072 lds\n\t" \
"s_add_u32 s90, s92, %[lg]\n\t" \
"s_add_u32 s90, s90, 4096\n\t" \
"s_mov_b32 m0, s90\n\t" \
"s_add_u32 s95, s89, 4096\n\t" \
"buffer_load_dwordx4 %[vg], %[rh], s95 offen offset:0 lds\n\t" \
"buffer_load_dwordx4 %[vg], %[rh], s95 offen offset:1024 lds\n\t" \
"buffer_load_dwordx4 %[vg], %[rh], s95 offen offset:2048 lds\n\t" \
"buffer_load_dwordx4 %[vg], %[rh], s95 offen offset:3072 lds\n\t" \
"s_add_u32 s90, s92, %[lg]\n\t" \
"s_add_u32 s90, s90, 8192\n\t" \
"s_mov_b32 m0, s90\n\t" \
"s_add_u32 s95, s89, 8192\n\t" \
"buffer_load_dwordx4 %[vg], %[rh], s95 offen offset:0 lds\n\t" \
"buffer_load_dwordx4 %[vg], %[rh], s95 offen offset:1024 lds\n\t" \
"s_add_u32 s89, s89, 57344\n\t" \
"s_mov_b32 s92, 28672\n\t" \
"s_add_u32 s90, s92, %[lg]\n\t" \
"s_add_u32 s90, s90, 0\n\t" \
"s_mov_b32 m0, s90\n\t" \
"s_add_u32 s95, s89, 0\n\t" \
"buffer_load_dwordx4 %[vg], %[rh], s95 offen offset:0 lds\n\t" \
"buffer_load_dwordx4 %[vg], %[rh], s95 offen offset:1024 lds\n\t" \
"buffer_load_dwordx4 %[vg], %[rh], s95 offen offset:2048 lds\n\t" \
"buffer_load_dwordx4 %[vg], %[rh], s95 offen offset:3072 lds\n\t" \
"s_add_u32 s90, s92, %[lg]\n\t" \
"s_add_u32 s90, s90, 4096\n\t" \
"s_mov_b32 m0, s90\n\t" \
"s_add_u32 s95, s89, 4096\n\t" \
"buffer_load_dwordx4 %[vg], %[rh], s95 offen offset:0 lds\n\t" \
"buffer_load_dwordx4 %[vg], %[rh], s95 offen offset:1024 lds\n\t" \
"buffer_load_dwordx4 %[vg], %[rh], s95 offen offset:2048 lds\n\t" \
"buffer_load_dwordx4 %[vg], %[rh], s95 offen offset:3072 lds\n\t" \
"s_add_u32 s90, s92, %[lg]\n\t" \
"s_add_u32 s90, s90, 8192\n\t" \
"s_mov_b32 m0, s90\n\t" \
"s_add_u32 s95, s89, 8192\n\t" \
"buffer_load_dwordx4 %[vg], %[rh], s95 offen offset:0 lds\n\t" \
"buffer_load_dwordx4 %[vg], %[rh], s95 offen offset:1024 lds\n\t" \
"s_add_u32 s89, s89, 57344\n\t" \
"s_mov_b32 s92, 57344\n\t" \
"s_add_u32 s90, s92, %[lg]\n\t" \
"s_add_u32 s90, s90, 0\n\t" \
"s_mov_b32 m0, s90\n\t" \
"s_add_u32 s95, s89, 0\n\t" \
"buffer_load_dwordx4 %[vg], %[rh], s95 offen offset:0 lds\n\t" \
"buffer_load_dwordx4 %[vg], %[rh], s95 offen offset:1024 lds\n\t" \
"buffer_load_dwordx4 %[vg], %[rh], s95 offen offset:2048 lds\n\t" \
"buffer_load_dwordx4 %[vg], %[rh], s95 offen offset:3072 lds\n\t" \
"s_add_u32 s90, s92, %[lg]\n\t" \
"s_add_u32 s90, s90, 4096\n\t" \
"s_mov_b32 m0, s90\n\t" \
"s_add_u32 s95, s89, 4096\n\t" \
"buffer_load_dwordx4 %[vg], %[rh], s95 offen offset:0 lds\n\t" \
"buffer_load_dwordx4 %[vg], %[rh], s95 offen offset:1024 lds\n\t" \
"buffer_load_dwordx4 %[vg], %[rh], s95 offen offset:2048 lds\n\t" \
"buffer_load_dwordx4 %[vg], %[rh], s95 offen offset:3072 lds\n\t" \
"s_add_u32 s90, s92, %[lg]\n\t" \
"s_add_u32 s90, s90, 8192\n\t" \
"s_mov_b32 m0, s90\n\t" \
"s_add_u32 s95, s89, 8192\n\t" \
"buffer_load_dwordx4 %[vg], %[rh], s95 offen offset:0 lds\n\t" \
"buffer_load_dwordx4 %[vg], %[rh], s95 offen offset:1024 lds\n\t" \
"s_add_u32 s89, s89, 57344\n\t" \
"s_mov_b32 s92, 86016\n\t" \
"s_add_u32 s90, s92, %[lg]\n\t" \
"s_add_u32 s90, s90, 0\n\t" \
"s_mov_b32 m0, s90\n\t" \
"s_add_u32 s95, s89, 0\n\t" \
"buffer_load_dwordx4 %[vg], %[rh], s95 offen offset:0 lds\n\t" \
"buffer_load_dwordx4 %[vg], %[rh], s95 offen offset:1024 lds\n\t" \
"buffer_load_dwordx4 %[vg], %[rh], s95 offen offset:2048 lds\n\t" \
"buffer_load_dwordx4 %[vg], %[rh], s95 offen offset:3072 lds\n\t" \
"s_add_u32 s90, s92, %[lg]\n\t" \
"s_add_u32 s90, s90, 4096\n\t" \
"s_mov_b32 m0, s90\n\t" \
"s_add_u32 s95, s89, 4096\n\t" \
"buffer_load_dwordx4 %[vg], %[rh], s95 offen offset:0 lds\n\t" \
"s_mov_b32 m0, s93\n\t"

#define ASM_P3D_CLOB "s86", "s87", "s88", "s89", "s90", "s91", "s92", "s93", "s94", "s95", "vcc", "scc", "memory"

#define ASM_P3W_04_TEXT \
"s_add_u32 s86, %[wa], 0\n\t" \
"buffer_load_dwordx4 a[0:3], %[vw], %[rw], s86 offen offset:0 sc1\n\t" \
"buffer_load_dwordx4 a[4:7], %[vw], %[rw], s86 offen offset:1024 sc1\n\t" \
"s_add_u32 s86, %[wa], 2048\n\t" \
"buffer_load_dwordx4 a[8:11], %[vw], %[rw], s86 offen offset:0 sc1\n\t" \
"buffer_load_dwordx4 a[12:15], %[vw], %[rw], s86 offen offset:1024 sc1\n\t" \
"s_add_u32 s86, %[wa], 57344\n\t" \
"buffer_load_dwordx4 a[16:19], %[vw], %[rw], s86 offen offset:0 sc1\n\t" \
"buffer_load_dwordx4 a[20:23], %[vw], %[rw], s86 offen offset:1024 sc1\n\t" \
"s_add_u32 s86, %[wa], 59392\n\t" \
"buffer_load_dwordx4 a[24:27], %[vw], %[rw], s86 offen offset:0 sc1\n\t" \
"buffer_load_dwordx4 a[28:31], %[vw], %[rw], s86 offen offset:1024 sc1\n\t" \
"s_add_u32 s86, %[wa], 114688\n\t" \
"buffer_load_dwordx4 a[32:35], %[vw], %[rw], s86 offen offset:0 sc1\n\t" \
"buffer_load_dwordx4 a[36:39], %[vw], %[rw], s86 offen offset:1024 sc1\n\t" \
"s_add_u32 s86, %[wa], 116736\n\t" \
"buffer_load_dwordx4 a[40:43], %[vw], %[rw], s86 offen offset:0 sc1\n\t" \
"buffer_load_dwordx4 a[44:47], %[vw], %[rw], s86 offen offset:1024 sc1\n\t" \
"s_add_u32 s86, %[wb], 0\n\t" \
"buffer_load_dwordx4 a[48:51], %[vw], %[rw], s86 offen offset:0 sc1\n\t" \
"buffer_load_dwordx4 a[52:55], %[vw], %[rw], s86 offen offset:1024 sc1\n\t" \
"s_add_u32 s86, %[wa], 4096\n\t" \
"buffer_load_dwordx4 a[56:59], %[vw], %[rw], s86 offen offset:0 sc1\n\t" \
"buffer_load_dwordx4 a[60:63], %[vw], %[rw], s86 offen offset:1024 sc1\n\t" \
"s_add_u32 s86, %[wa], 6144\n\t" \
"buffer_load_dwordx4 a[64:67], %[vw], %[rw], s86 offen offset:0 sc1\n\t" \
"buffer_load_dwordx4 a[68:71], %[vw], %[rw], s86 offen offset:1024 sc1\n\t" \
"s_add_u32 s86, %[wa], 61440\n\t" \
"buffer_load_dwordx4 a[72:75], %[vw], %[rw], s86 offen offset:0 sc1\n\t" \
"buffer_load_dwordx4 a[76:79], %[vw], %[rw], s86 offen offset:1024 sc1\n\t" \
"s_add_u32 s86, %[wa], 63488\n\t" \
"buffer_load_dwordx4 a[80:83], %[vw], %[rw], s86 offen offset:0 sc1\n\t" \
"buffer_load_dwordx4 a[84:87], %[vw], %[rw], s86 offen offset:1024 sc1\n\t" \
"s_add_u32 s86, %[wa], 118784\n\t" \
"buffer_load_dwordx4 a[88:91], %[vw], %[rw], s86 offen offset:0 sc1\n\t" \
"buffer_load_dwordx4 a[92:95], %[vw], %[rw], s86 offen offset:1024 sc1\n\t" \
"s_add_u32 s86, %[wa], 120832\n\t" \
"buffer_load_dwordx4 a[96:99], %[vw], %[rw], s86 offen offset:0 sc1\n\t" \
"buffer_load_dwordx4 a[100:103], %[vw], %[rw], s86 offen offset:1024 sc1\n\t" \
"s_add_u32 s86, %[wb], 2048\n\t" \
"buffer_load_dwordx4 a[104:107], %[vw], %[rw], s86 offen offset:0 sc1\n\t" \
"buffer_load_dwordx4 a[108:111], %[vw], %[rw], s86 offen offset:1024 sc1\n\t" \
"s_add_u32 s86, %[wa], 8192\n\t" \
"buffer_load_dwordx4 a[112:115], %[vw], %[rw], s86 offen offset:0 sc1\n\t" \
"buffer_load_dwordx4 a[116:119], %[vw], %[rw], s86 offen offset:1024 sc1\n\t" \
"s_add_u32 s86, %[wa], 10240\n\t" \
"buffer_load_dwordx4 a[120:123], %[vw], %[rw], s86 offen offset:0 sc1\n\t" \
"buffer_load_dwordx4 a[124:127], %[vw], %[rw], s86 offen offset:1024 sc1\n\t" \
"s_add_u32 s86, %[wa], 65536\n\t" \
"buffer_load_dwordx4 a[128:131], %[vw], %[rw], s86 offen offset:0 sc1\n\t" \
"buffer_load_dwordx4 a[132:135], %[vw], %[rw], s86 offen offset:1024 sc1\n\t" \
"s_add_u32 s86, %[wa], 67584\n\t" \
"buffer_load_dwordx4 a[136:139], %[vw], %[rw], s86 offen offset:0 sc1\n\t" \
"buffer_load_dwordx4 a[140:143], %[vw], %[rw], s86 offen offset:1024 sc1\n\t" \
"s_add_u32 s86, %[wa], 122880\n\t" \
"buffer_load_dwordx4 a[144:147], %[vw], %[rw], s86 offen offset:0 sc1\n\t" \
"buffer_load_dwordx4 a[148:151], %[vw], %[rw], s86 offen offset:1024 sc1\n\t" \
"s_add_u32 s86, %[wa], 124928\n\t" \
"buffer_load_dwordx4 a[152:155], %[vw], %[rw], s86 offen offset:0 sc1\n\t" \
"buffer_load_dwordx4 a[156:159], %[vw], %[rw], s86 offen offset:1024 sc1\n\t" \
"s_add_u32 s86, %[wb], 4096\n\t" \
"buffer_load_dwordx4 a[160:163], %[vw], %[rw], s86 offen offset:0 sc1\n\t" \
"buffer_load_dwordx4 a[164:167], %[vw], %[rw], s86 offen offset:1024 sc1\n\t" \
"s_add_u32 s86, %[wa], 12288\n\t" \
"buffer_load_dwordx4 a[168:171], %[vw], %[rw], s86 offen offset:0 sc1\n\t" \
"buffer_load_dwordx4 a[172:175], %[vw], %[rw], s86 offen offset:1024 sc1\n\t" \
"s_add_u32 s86, %[wa], 14336\n\t" \
"buffer_load_dwordx4 a[176:179], %[vw], %[rw], s86 offen offset:0 sc1\n\t" \
"buffer_load_dwordx4 a[180:183], %[vw], %[rw], s86 offen offset:1024 sc1\n\t" \
"s_add_u32 s86, %[wa], 69632\n\t" \
"buffer_load_dwordx4 a[184:187], %[vw], %[rw], s86 offen offset:0 sc1\n\t" \
"buffer_load_dwordx4 a[188:191], %[vw], %[rw], s86 offen offset:1024 sc1\n\t" \
"s_add_u32 s86, %[wa], 71680\n\t" \
"buffer_load_dwordx4 a[192:195], %[vw], %[rw], s86 offen offset:0 sc1\n\t" \
"buffer_load_dwordx4 a[196:199], %[vw], %[rw], s86 offen offset:1024 sc1\n\t" \
"s_add_u32 s86, %[wa], 126976\n\t" \
"buffer_load_dwordx4 a[200:203], %[vw], %[rw], s86 offen offset:0 sc1\n\t" \
"buffer_load_dwordx4 a[204:207], %[vw], %[rw], s86 offen offset:1024 sc1\n\t" \
"s_add_u32 s86, %[wa], 129024\n\t" \
"buffer_load_dwordx4 a[208:211], %[vw], %[rw], s86 offen offset:0 sc1\n\t" \
"buffer_load_dwordx4 a[212:215], %[vw], %[rw], s86 offen offset:1024 sc1\n\t" \
"s_add_u32 s86, %[wb], 6144\n\t" \
"buffer_load_dwordx4 a[216:219], %[vw], %[rw], s86 offen offset:0 sc1\n\t" \
"buffer_load_dwordx4 a[220:223], %[vw], %[rw], s86 offen offset:1024 sc1\n\t"

#define ASM_P3W_04_CLOB "a0", "a1", "a2", "a3", "a4", "a5", "a6", "a7", "a8", "a9", "a10", "a11", "a12", "a13", "a14", "a15", "a16", "a17", "a18", "a19", "a20", "a21", "a22", "a23", "a24", "a25", "a26", "a27", "a28", "a29", "a30", "a31", "a32", "a33", "a34", "a35", "a36", "a37", "a38", "a39", "a40", "a41", "a42", "a43", "a44", "a45", "a46", "a47", "a48", "a49", "a50", "a51", "a52", "a53", "a54", "a55", "a56", "a57", "a58", "a59", "a60", "a61", "a62", "a63", "a64", "a65", "a66", "a67", "a68", "a69", "a70", "a71", "a72", "a73", "a74", "a75", "a76", "a77", "a78", "a79", "a80", "a81", "a82", "a83", "a84", "a85", "a86", "a87", "a88", "a89", "a90", "a91", "a92", "a93", "a94", "a95", "a96", "a97", "a98", "a99", "a100", "a101", "a102", "a103", "a104", "a105", "a106", "a107", "a108", "a109", "a110", "a111", "a112", "a113", "a114", "a115", "a116", "a117", "a118", "a119", "a120", "a121", "a122", "a123", "a124", "a125", "a126", "a127", "a128", "a129", "a130", "a131", "a132", "a133", "a134", "a135", "a136", "a137", "a138", "a139", "a140", "a141", "a142", "a143", "a144", "a145", "a146", "a147", "a148", "a149", "a150", "a151", "a152", "a153", "a154", "a155", "a156", "a157", "a158", "a159", "a160", "a161", "a162", "a163", "a164", "a165", "a166", "a167", "a168", "a169", "a170", "a171", "a172", "a173", "a174", "a175", "a176", "a177", "a178", "a179", "a180", "a181", "a182", "a183", "a184", "a185", "a186", "a187", "a188", "a189", "a190", "a191", "a192", "a193", "a194", "a195", "a196", "a197", "a198", "a199", "a200", "a201", "a202", "a203", "a204", "a205", "a206", "a207", "a208", "a209", "a210", "a211", "a212", "a213", "a214", "a215", "a216", "a217", "a218", "a219", "a220", "a221", "a222", "a223", "s86", "s87", "s88", "s89", "s90", "s91", "s92", "s93", "s94", "s95", "vcc", "scc", "memory"

#define ASM_P3W_03_TEXT \
"s_add_u32 s86, %[wa], 0\n\t" \
"buffer_load_dwordx4 a[0:3], %[vw], %[rw], s86 offen offset:0 sc1\n\t" \
"buffer_load_dwordx4 a[4:7], %[vw], %[rw], s86 offen offset:1024 sc1\n\t" \
"s_add_u32 s86, %[wa], 2048\n\t" \
"buffer_load_dwordx4 a[8:11], %[vw], %[rw], s86 offen offset:0 sc1\n\t" \
"buffer_load_dwordx4 a[12:15], %[vw], %[rw], s86 offen offset:1024 sc1\n\t" \
"s_add_u32 s86, %[wa], 57344\n\t" \
"buffer_load_dwordx4 a[16:19], %[vw], %[rw], s86 offen offset:0 sc1\n\t" \
"buffer_load_dwordx4 a[20:23], %[vw], %[rw], s86 offen offset:1024 sc1\n\t" \
"s_add_u32 s86, %[wa], 59392\n\t" \
"buffer_load_dwordx4 a[24:27], %[vw], %[rw], s86 offen offset:0 sc1\n\t" \
"buffer_load_dwordx4 a[28:31], %[vw], %[rw], s86 offen offset:1024 sc1\n\t" \
"s_add_u32 s86, %[wa], 114688\n\t" \
"buffer_load_dwordx4 a[32:35], %[vw], %[rw], s86 offen offset:0 sc1\n\t" \
"buffer_load_dwordx4 a[36:39], %[vw], %[rw], s86 offen offset:1024 sc1\n\t" \
"s_add_u32 s86, %[wa], 116736\n\t" \
"buffer_load_dwordx4 a[40:43], %[vw], %[rw], s86 offen offset:0 sc1\n\t" \
"buffer_load_dwordx4 a[44:47], %[vw], %[rw], s86 offen offset:1024 sc1\n\t" \
"s_add_u32 s86, %[wb], 0\n\t" \
"buffer_load_dwordx4 a[48:51], %[vw], %[rw], s86 offen offset:0 sc1\n\t" \
"buffer_load_dwordx4 a[52:55], %[vw], %[rw], s86 offen offset:1024 sc1\n\t" \
"s_add_u32 s86, %[wa], 4096\n\t" \
"buffer_load_dwordx4 a[56:59], %[vw], %[rw], s86 offen offset:0 sc1\n\t" \
"buffer_load_dwordx4 a[60:63], %[vw], %[rw], s86 offen offset:1024 sc1\n\t" \
"s_add_u32 s86, %[wa], 6144\n\t" \
"buffer_load_dwordx4 a[64:67], %[vw], %[rw], s86 offen offset:0 sc1\n\t" \
"buffer_load_dwordx4 a[68:71], %[vw], %[rw], s86 offen offset:1024 sc1\n\t" \
"s_add_u32 s86, %[wa], 61440\n\t" \
"buffer_load_dwordx4 a[72:75], %[vw], %[rw], s86 offen offset:0 sc1\n\t" \
"buffer_load_dwordx4 a[76:79], %[vw], %[rw], s86 offen offset:1024 sc1\n\t" \
"s_add_u32 s86, %[wa], 63488\n\t" \
"buffer_load_dwordx4 a[80:83], %[vw], %[rw], s86 offen offset:0 sc1\n\t" \
"buffer_load_dwordx4 a[84:87], %[vw], %[rw], s86 offen offset:1024 sc1\n\t" \
"s_add_u32 s86, %[wa], 118784\n\t" \
"buffer_load_dwordx4 a[88:91], %[vw], %[rw], s86 offen offset:0 sc1\n\t" \
"buffer_load_dwordx4 a[92:95], %[vw], %[rw], s86 offen offset:1024 sc1\n\t" \
"s_add_u32 s86, %[wa], 120832\n\t" \
"buffer_load_dwordx4 a[96:99], %[vw], %[rw], s86 offen offset:0 sc1\n\t" \
"buffer_load_dwordx4 a[100:103], %[vw], %[rw], s86 offen offset:1024 sc1\n\t" \
"s_add_u32 s86, %[wb], 2048\n\t" \
"buffer_load_dwordx4 a[104:107], %[vw], %[rw], s86 offen offset:0 sc1\n\t" \
"buffer_load_dwordx4 a[108:111], %[vw], %[rw], s86 offen offset:1024 sc1\n\t" \
"s_add_u32 s86, %[wa], 8192\n\t" \
"buffer_load_dwordx4 a[112:115], %[vw], %[rw], s86 offen offset:0 sc1\n\t" \
"buffer_load_dwordx4 a[116:119], %[vw], %[rw], s86 offen offset:1024 sc1\n\t" \
"s_add_u32 s86, %[wa], 10240\n\t" \
"buffer_load_dwordx4 a[120:123], %[vw], %[rw], s86 offen offset:0 sc1\n\t" \
"buffer_load_dwordx4 a[124:127], %[vw], %[rw], s86 offen offset:1024 sc1\n\t" \
"s_add_u32 s86, %[wa], 65536\n\t" \
"buffer_load_dwordx4 a[128:131], %[vw], %[rw], s86 offen offset:0 sc1\n\t" \
"buffer_load_dwordx4 a[132:135], %[vw], %[rw], s86 offen offset:1024 sc1\n\t" \
"s_add_u32 s86, %[wa], 67584\n\t" \
"buffer_load_dwordx4 a[136:139], %[vw], %[rw], s86 offen offset:0 sc1\n\t" \
"buffer_load_dwordx4 a[140:143], %[vw], %[rw], s86 offen offset:1024 sc1\n\t" \
"s_add_u32 s86, %[wa], 122880\n\t" \
"buffer_load_dwordx4 a[144:147], %[vw], %[rw], s86 offen offset:0 sc1\n\t" \
"buffer_load_dwordx4 a[148:151], %[vw], %[rw], s86 offen offset:1024 sc1\n\t" \
"s_add_u32 s86, %[wa], 124928\n\t" \
"buffer_load_dwordx4 a[152:155], %[vw], %[rw], s86 offen offset:0 sc1\n\t" \
"buffer_load_dwordx4 a[156:159], %[vw], %[rw], s86 offen offset:1024 sc1\n\t" \
"s_add_u32 s86, %[wb], 4096\n\t" \
"buffer_load_dwordx4 a[160:163], %[vw], %[rw], s86 offen offset:0 sc1\n\t" \
"buffer_load_dwordx4 a[164:167], %[vw], %[rw], s86 offen offset:1024 sc1\n\t"

#define ASM_P3W_03_CLOB "a0", "a1", "a2", "a3", "a4", "a5", "a6", "a7", "a8", "a9", "a10", "a11", "a12", "a13", "a14", "a15", "a16", "a17", "a18", "a19", "a20", "a21", "a22", "a23", "a24", "a25", "a26", "a27", "a28", "a29", "a30", "a31", "a32", "a33", "a34", "a35", "a36", "a37", "a38", "a39", "a40", "a41", "a42", "a43", "a44", "a45", "a46", "a47", "a48", "a49", "a50", "a51", "a52", "a53", "a54", "a55", "a56", "a57", "a58", "a59", "a60", "a61", "a62", "a63", "a64", "a65", "a66", "a67", "a68", "a69", "a70", "a71", "a72", "a73", "a74", "a75", "a76", "a77", "a78", "a79", "a80", "a81", "a82", "a83", "a84", "a85", "a86", "a87", "a88", "a89", "a90", "a91", "a92", "a93", "a94", "a95", "a96", "a97", "a98", "a99", "a100", "a101", "a102", "a103", "a104", "a105", "a106", "a107", "a108", "a109", "a110", "a111", "a112", "a113", "a114", "a115", "a116", "a117", "a118", "a119", "a120", "a121", "a122", "a123", "a124", "a125", "a126", "a127", "a128", "a129", "a130", "a131", "a132", "a133", "a134", "a135", "a136", "a137", "a138", "a139", "a140", "a141", "a142", "a143", "a144", "a145", "a146", "a147", "a148", "a149", "a150", "a151", "a152", "a153", "a154", "a155", "a156", "a157", "a158", "a159", "a160", "a161", "a162", "a163", "a164", "a165", "a166", "a167", "s86", "s87", "s88", "s89", "s90", "s91", "s92", "s93", "s94", "s95", "vcc", "scc", "memory"

#define ASM_P3W_14_TEXT \
"s_add_u32 s86, %[wa], 0\n\t" \
"buffer_load_dwordx4 a[0:3], %[vw], %[rw], s86 offen offset:0 sc1\n\t" \
"buffer_load_dwordx4 a[4:7], %[vw], %[rw], s86 offen offset:1024 sc1\n\t" \
"s_add_u32 s86, %[wa], 2048\n\t" \
"buffer_load_dwordx4 a[8:11], %[vw], %[rw], s86 offen offset:0 sc1\n\t" \
"buffer_load_dwordx4 a[12:15], %[vw], %[rw], s86 offen offset:1024 sc1\n\t" \
"s_add_u32 s86, %[wa], 4096\n\t" \
"buffer_load_dwordx4 a[16:19], %[vw], %[rw], s86 offen offset:0 sc1\n\t" \
"buffer_load_dwordx4 a[20:23], %[vw], %[rw], s86 offen offset:1024 sc1\n\t" \
"s_add_u32 s86, %[wa], 6144\n\t" \
"buffer_load_dwordx4 a[24:27], %[vw], %[rw], s86 offen offset:0 sc1\n\t" \
"buffer_load_dwordx4 a[28:31], %[vw], %[rw], s86 offen offset:1024 sc1\n\t" \
"s_add_u32 s86, %[wa], 8192\n\t" \
"buffer_load_dwordx4 a[32:35], %[vw], %[rw], s86 offen offset:0 sc1\n\t" \
"buffer_load_dwordx4 a[36:39], %[vw], %[rw], s86 offen offset:1024 sc1\n\t" \
"s_add_u32 s86, %[wa], 10240\n\t" \
"buffer_load_dwordx4 a[40:43], %[vw], %[rw], s86 offen offset:0 sc1\n\t" \
"buffer_load_dwordx4 a[44:47], %[vw], %[rw], s86 offen offset:1024 sc1\n\t" \
"s_add_u32 s86, %[wa], 12288\n\t" \
"buffer_load_dwordx4 a[48:51], %[vw], %[rw], s86 offen offset:0 sc1\n\t" \
"buffer_load_dwordx4 a[52:55], %[vw], %[rw], s86 offen offset:1024 sc1\n\t" \
"s_add_u32 s86, %[wa], 229376\n\t" \
"buffer_load_dwordx4 a[56:59], %[vw], %[rw], s86 offen offset:0 sc1\n\t" \
"buffer_load_dwordx4 a[60:63], %[vw], %[rw], s86 offen offset:1024 sc1\n\t" \
"s_add_u32 s86, %[wa], 231424\n\t" \
"buffer_load_dwordx4 a[64:67], %[vw], %[rw], s86 offen offset:0 sc1\n\t" \
"buffer_load_dwordx4 a[68:71], %[vw], %[rw], s86 offen offset:1024 sc1\n\t" \
"s_add_u32 s86, %[wa], 233472\n\t" \
"buffer_load_dwordx4 a[72:75], %[vw], %[rw], s86 offen offset:0 sc1\n\t" \
"buffer_load_dwordx4 a[76:79], %[vw], %[rw], s86 offen offset:1024 sc1\n\t" \
"s_add_u32 s86, %[wa], 235520\n\t" \
"buffer_load_dwordx4 a[80:83], %[vw], %[rw], s86 offen offset:0 sc1\n\t" \
"buffer_load_dwordx4 a[84:87], %[vw], %[rw], s86 offen offset:1024 sc1\n\t" \
"s_add_u32 s86, %[wa], 237568\n\t" \
"buffer_load_dwordx4 a[88:91], %[vw], %[rw], s86 offen offset:0 sc1\n\t" \
"buffer_load_dwordx4 a[92:95], %[vw], %[rw], s86 offen offset:1024 sc1\n\t" \
"s_add_u32 s86, %[wa], 239616\n\t" \
"buffer_load_dwordx4 a[96:99], %[vw], %[rw], s86 offen offset:0 sc1\n\t" \
"buffer_load_dwordx4 a[100:103], %[vw], %[rw], s86 offen offset:1024 sc1\n\t" \
"s_add_u32 s86, %[wa], 241664\n\t" \
"buffer_load_dwordx4 a[104:107], %[vw], %[rw], s86 offen offset:0 sc1\n\t" \
"buffer_load_dwordx4 a[108:111], %[vw], %[rw], s86 offen offset:1024 sc1\n\t" \
"s_add_u32 s86, %[wa], 458752\n\t" \
"buffer_load_dwordx4 a[112:115], %[vw], %[rw], s86 offen offset:0 sc1\n\t" \
"buffer_load_dwordx4 a[116:119], %[vw], %[rw], s86 offen offset:1024 sc1\n\t" \
"s_add_u32 s86, %[wa], 460800\n\t" \
"buffer_load_dwordx4 a[120:123], %[vw], %[rw], s86 offen offset:0 sc1\n\t" \
"buffer_load_dwordx4 a[124:127], %[vw], %[rw], s86 offen offset:1024 sc1\n\t" \
"s_add_u32 s86, %[wa], 462848\n\t" \
"buffer_load_dwordx4 a[128:131], %[vw], %[rw], s86 offen offset:0 sc1\n\t" \
"buffer_load_dwordx4 a[132:135], %[vw], %[rw], s86 offen offset:1024 sc1\n\t" \
"s_add_u32 s86, %[wa], 464896\n\t" \
"buffer_load_dwordx4 a[136:139], %[vw], %[rw], s86 offen offset:0 sc1\n\t" \
"buffer_load_dwordx4 a[140:143], %[vw], %[rw], s86 offen offset:1024 sc1\n\t" \
"s_add_u32 s86, %[wa], 466944\n\t" \
"buffer_load_dwordx4 a[144:147], %[vw], %[rw], s86 offen offset:0 sc1\n\t" \
"buffer_load_dwordx4 a[148:151], %[vw], %[rw], s86 offen offset:1024 sc1\n\t" \
"s_add_u32 s86, %[wa], 468992\n\t" \
"buffer_load_dwordx4 a[152:155], %[vw], %[rw], s86 offen offset:0 sc1\n\t" \
"buffer_load_dwordx4 a[156:159], %[vw], %[rw], s86 offen offset:1024 sc1\n\t" \
"s_add_u32 s86, %[wa], 471040\n\t" \
"buffer_load_dwordx4 a[160:163], %[vw], %[rw], s86 offen offset:0 sc1\n\t" \
"buffer_load_dwordx4 a[164:167], %[vw], %[rw], s86 offen offset:1024 sc1\n\t" \
"s_add_u32 s86, %[wa], 688128\n\t" \
"buffer_load_dwordx4 a[168:171], %[vw], %[rw], s86 offen offset:0 sc1\n\t" \
"buffer_load_dwordx4 a[172:175], %[vw], %[rw], s86 offen offset:1024 sc1\n\t" \
"s_add_u32 s86, %[wa], 690176\n\t" \
"buffer_load_dwordx4 a[176:179], %[vw], %[rw], s86 offen offset:0 sc1\n\t" \
"buffer_load_dwordx4 a[180:183], %[vw], %[rw], s86 offen offset:1024 sc1\n\t" \
"s_add_u32 s86, %[wa], 692224\n\t" \
"buffer_load_dwordx4 a[184:187], %[vw], %[rw], s86 offen offset:0 sc1\n\t" \
"buffer_load_dwordx4 a[188:191], %[vw], %[rw], s86 offen offset:1024 sc1\n\t" \
"s_add_u32 s86, %[wa], 694272\n\t" \
"buffer_load_dwordx4 a[192:195], %[vw], %[rw], s86 offen offset:0 sc1\n\t" \
"buffer_load_dwordx4 a[196:199], %[vw], %[rw], s86 offen offset:1024 sc1\n\t" \
"s_add_u32 s86, %[wa], 696320\n\t" \
"buffer_load_dwordx4 a[200:203], %[vw], %[rw], s86 offen offset:0 sc1\n\t" \
"buffer_load_dwordx4 a[204:207], %[vw], %[rw], s86 offen offset:1024 sc1\n\t" \
"s_add_u32 s86, %[wa], 698368\n\t" \
"buffer_load_dwordx4 a[208:211], %[vw], %[rw], s86 offen offset:0 sc1\n\t" \
"buffer_load_dwordx4 a[212:215], %[vw], %[rw], s86 offen offset:1024 sc1\n\t" \
"s_add_u32 s86, %[wa], 700416\n\t" \
"buffer_load_dwordx4 a[216:219], %[vw], %[rw], s86 offen offset:0 sc1\n\t" \
"buffer_load_dwordx4 a[220:223], %[vw], %[rw], s86 offen offset:1024 sc1\n\t"

#define ASM_P3W_14_CLOB "a0", "a1", "a2", "a3", "a4", "a5", "a6", "a7", "a8", "a9", "a10", "a11", "a12", "a13", "a14", "a15", "a16", "a17", "a18", "a19", "a20", "a21", "a22", "a23", "a24", "a25", "a26", "a27", "a28", "a29", "a30", "a31", "a32", "a33", "a34", "a35", "a36", "a37", "a38", "a39", "a40", "a41", "a42", "a43", "a44", "a45", "a46", "a47", "a48", "a49", "a50", "a51", "a52", "a53", "a54", "a55", "a56", "a57", "a58", "a59", "a60", "a61", "a62", "a63", "a64", "a65", "a66", "a67", "a68", "a69", "a70", "a71", "a72", "a73", "a74", "a75", "a76", "a77", "a78", "a79", "a80", "a81", "a82", "a83", "a84", "a85", "a86", "a87", "a88", "a89", "a90", "a91", "a92", "a93", "a94", "a95", "a96", "a97", "a98", "a99", "a100", "a101", "a102", "a103", "a104", "a105", "a106", "a107", "a108", "a109", "a110", "a111", "a112", "a113", "a114", "a115", "a116", "a117", "a118", "a119", "a120", "a121", "a122", "a123", "a124", "a125", "a126", "a127", "a128", "a129", "a130", "a131", "a132", "a133", "a134", "a135", "a136", "a137", "a138", "a139", "a140", "a141", "a142", "a143", "a144", "a145", "a146", "a147", "a148", "a149", "a150", "a151", "a152", "a153", "a154", "a155", "a156", "a157", "a158", "a159", "a160", "a161", "a162", "a163", "a164", "a165", "a166", "a167", "a168", "a169", "a170", "a171", "a172", "a173", "a174", "a175", "a176", "a177", "a178", "a179", "a180", "a181", "a182", "a183", "a184", "a185", "a186", "a187", "a188", "a189", "a190", "a191", "a192", "a193", "a194", "a195", "a196", "a197", "a198", "a199", "a200", "a201", "a202", "a203", "a204", "a205", "a206", "a207", "a208", "a209", "a210", "a211", "a212", "a213", "a214", "a215", "a216", "a217", "a218", "a219", "a220", "a221", "a222", "a223", "s86", "s87", "s88", "s89", "s90", "s91", "s92", "s93", "s94", "s95", "vcc", "scc", "memory"

#define ASM_P3W_13_TEXT \
"s_add_u32 s86, %[wa], 0\n\t" \
"buffer_load_dwordx4 a[0:3], %[vw], %[rw], s86 offen offset:0 sc1\n\t" \
"buffer_load_dwordx4 a[4:7], %[vw], %[rw], s86 offen offset:1024 sc1\n\t" \
"s_add_u32 s86, %[wa], 2048\n\t" \
"buffer_load_dwordx4 a[8:11], %[vw], %[rw], s86 offen offset:0 sc1\n\t" \
"buffer_load_dwordx4 a[12:15], %[vw], %[rw], s86 offen offset:1024 sc1\n\t" \
"s_add_u32 s86, %[wa], 4096\n\t" \
"buffer_load_dwordx4 a[16:19], %[vw], %[rw], s86 offen offset:0 sc1\n\t" \
"buffer_load_dwordx4 a[20:23], %[vw], %[rw], s86 offen offset:1024 sc1\n\t" \
"s_add_u32 s86, %[wa], 6144\n\t" \
"buffer_load_dwordx4 a[24:27], %[vw], %[rw], s86 offen offset:0 sc1\n\t" \
"buffer_load_dwordx4 a[28:31], %[vw], %[rw], s86 offen offset:1024 sc1\n\t" \
"s_add_u32 s86, %[wa], 8192\n\t" \
"buffer_load_dwordx4 a[32:35], %[vw], %[rw], s86 offen offset:0 sc1\n\t" \
"buffer_load_dwordx4 a[36:39], %[vw], %[rw], s86 offen offset:1024 sc1\n\t" \
"s_add_u32 s86, %[wa], 10240\n\t" \
"buffer_load_dwordx4 a[40:43], %[vw], %[rw], s86 offen offset:0 sc1\n\t" \
"buffer_load_dwordx4 a[44:47], %[vw], %[rw], s86 offen offset:1024 sc1\n\t" \
"s_add_u32 s86, %[wa], 12288\n\t" \
"buffer_load_dwordx4 a[48:51], %[vw], %[rw], s86 offen offset:0 sc1\n\t" \
"buffer_load_dwordx4 a[52:55], %[vw], %[rw], s86 offen offset:1024 sc1\n\t" \
"s_add_u32 s86, %[wa], 229376\n\t" \
"buffer_load_dwordx4 a[56:59], %[vw], %[rw], s86 offen offset:0 sc1\n\t" \
"buffer_load_dwordx4 a[60:63], %[vw], %[rw], s86 offen offset:1024 sc1\n\t" \
"s_add_u32 s86, %[wa], 231424\n\t" \
"buffer_load_dwordx4 a[64:67], %[vw], %[rw], s86 offen offset:0 sc1\n\t" \
"buffer_load_dwordx4 a[68:71], %[vw], %[rw], s86 offen offset:1024 sc1\n\t" \
"s_add_u32 s86, %[wa], 233472\n\t" \
"buffer_load_dwordx4 a[72:75], %[vw], %[rw], s86 offen offset:0 sc1\n\t" \
"buffer_load_dwordx4 a[76:79], %[vw], %[rw], s86 offen offset:1024 sc1\n\t" \
"s_add_u32 s86, %[wa], 235520\n\t" \
"buffer_load_dwordx4 a[80:83], %[vw], %[rw], s86 offen offset:0 sc1\n\t" \
"buffer_load_dwordx4 a[84:87], %[vw], %[rw], s86 offen offset:1024 sc1\n\t" \
"s_add_u32 s86, %[wa], 237568\n\t" \
"buffer_load_dwordx4 a[88:91], %[vw], %[rw], s86 offen offset:0 sc1\n\t" \
"buffer_load_dwordx4 a[92:95], %[vw], %[rw], s86 offen offset:1024 sc1\n\t" \
"s_add_u32 s86, %[wa], 239616\n\t" \
"buffer_load_dwordx4 a[96:99], %[vw], %[rw], s86 offen offset:0 sc1\n\t" \
"buffer_load_dwordx4 a[100:103], %[vw], %[rw], s86 offen offset:1024 sc1\n\t" \
"s_add_u32 s86, %[wa], 241664\n\t" \
"buffer_load_dwordx4 a[104:107], %[vw], %[rw], s86 offen offset:0 sc1\n\t" \
"buffer_load_dwordx4 a[108:111], %[vw], %[rw], s86 offen offset:1024 sc1\n\t" \
"s_add_u32 s86, %[wa], 458752\n\t" \
"buffer_load_dwordx4 a[112:115], %[vw], %[rw], s86 offen offset:0 sc1\n\t" \
"buffer_load_dwordx4 a[116:119], %[vw], %[rw], s86 offen offset:1024 sc1\n\t" \
"s_add_u32 s86, %[wa], 460800\n\t" \
"buffer_load_dwordx4 a[120:123], %[vw], %[rw], s86 offen offset:0 sc1\n\t" \
"buffer_load_dwordx4 a[124:127], %[vw], %[rw], s86 offen offset:1024 sc1\n\t" \
"s_add_u32 s86, %[wa], 462848\n\t" \
"buffer_load_dwordx4 a[128:131], %[vw], %[rw], s86 offen offset:0 sc1\n\t" \
"buffer_load_dwordx4 a[132:135], %[vw], %[rw], s86 offen offset:1024 sc1\n\t" \
"s_add_u32 s86, %[wa], 464896\n\t" \
"buffer_load_dwordx4 a[136:139], %[vw], %[rw], s86 offen offset:0 sc1\n\t" \
"buffer_load_dwordx4 a[140:143], %[vw], %[rw], s86 offen offset:1024 sc1\n\t" \
"s_add_u32 s86, %[wa], 466944\n\t" \
"buffer_load_dwordx4 a[144:147], %[vw], %[rw], s86 offen offset:0 sc1\n\t" \
"buffer_load_dwordx4 a[148:151], %[vw], %[rw], s86 offen offset:1024 sc1\n\t" \
"s_add_u32 s86, %[wa], 468992\n\t" \
"buffer_load_dwordx4 a[152:155], %[vw], %[rw], s86 offen offset:0 sc1\n\t" \
"buffer_load_dwordx4 a[156:159], %[vw], %[rw], s86 offen offset:1024 sc1\n\t" \
"s_add_u32 s86, %[wa], 471040\n\t" \
"buffer_load_dwordx4 a[160:163], %[vw], %[rw], s86 offen offset:0 sc1\n\t" \
"buffer_load_dwordx4 a[164:167], %[vw], %[rw], s86 offen offset:1024 sc1\n\t"

#define ASM_P3W_13_CLOB "a0", "a1", "a2", "a3", "a4", "a5", "a6", "a7", "a8", "a9", "a10", "a11", "a12", "a13", "a14", "a15", "a16", "a17", "a18", "a19", "a20", "a21", "a22", "a23", "a24", "a25", "a26", "a27", "a28", "a29", "a30", "a31", "a32", "a33", "a34", "a35", "a36", "a37", "a38", "a39", "a40", "a41", "a42", "a43", "a44", "a45", "a46", "a47", "a48", "a49", "a50", "a51", "a52", "a53", "a54", "a55", "a56", "a57", "a58", "a59", "a60", "a61", "a62", "a63", "a64", "a65", "a66", "a67", "a68", "a69", "a70", "a71", "a72", "a73", "a74", "a75", "a76", "a77", "a78", "a79", "a80", "a81", "a82", "a83", "a84", "a85", "a86", "a87", "a88", "a89", "a90", "a91", "a92", "a93", "a94", "a95", "a96", "a97", "a98", "a99", "a100", "a101", "a102", "a103", "a104", "a105", "a106", "a107", "a108", "a109", "a110", "a111", "a112", "a113", "a114", "a115", "a116", "a117", "a118", "a119", "a120", "a121", "a122", "a123", "a124", "a125", "a126", "a127", "a128", "a129", "a130", "a131", "a132", "a133", "a134", "a135", "a136", "a137", "a138", "a139", "a140", "a141", "a142", "a143", "a144", "a145", "a146", "a147", "a148", "a149", "a150", "a151", "a152", "a153", "a154", "a155", "a156", "a157", "a158", "a159", "a160", "a161", "a162", "a163", "a164", "a165", "a166", "a167", "s86", "s87", "s88", "s89", "s90", "s91", "s92", "s93", "s94", "s95", "vcc", "scc", "memory"
#define ASM_P1L2_TEXT \
"s_mov_b32 s90, 0\n\t" \
"buffer_load_dwordx4 a[0:3], %[voff], %[rx], s90 offen offset:0\n\t" \
"buffer_load_dwordx4 a[4:7], %[voff], %[rx], s90 offen offset:64\n\t" \
"buffer_load_dwordx4 a[8:11], %[voff], %[rx], s90 offen offset:128\n\t" \
"buffer_load_dwordx4 a[12:15], %[voff], %[rx], s90 offen offset:192\n\t" \
"buffer_load_dwordx4 a[16:19], %[voff], %[rx], s90 offen offset:256\n\t" \
"buffer_load_dwordx4 a[20:23], %[voff], %[rx], s90 offen offset:320\n\t" \
"buffer_load_dwordx4 a[24:27], %[voff], %[rx], s90 offen offset:384\n\t" \
"buffer_load_dwordx4 a[28:31], %[voff], %[rx], s90 offen offset:448\n\t" \
"s_mov_b32 s90, 1835008\n\t" \
"buffer_load_dwordx4 a[32:35], %[voff], %[rx], s90 offen offset:0\n\t" \
"buffer_load_dwordx4 a[36:39], %[voff], %[rx], s90 offen offset:64\n\t" \
"buffer_load_dwordx4 a[40:43], %[voff], %[rx], s90 offen offset:128\n\t" \
"buffer_load_dwordx4 a[44:47], %[voff], %[rx], s90 offen offset:192\n\t" \
"buffer_load_dwordx4 a[48:51], %[voff], %[rx], s90 offen offset:256\n\t" \
"buffer_load_dwordx4 a[52:55], %[voff], %[rx], s90 offen offset:320\n\t" \
"buffer_load_dwordx4 a[56:59], %[voff], %[rx], s90 offen offset:384\n\t" \
"buffer_load_dwordx4 a[60:63], %[voff], %[rx], s90 offen offset:448\n\t" \
"s_mov_b32 s90, 131072\n\t" \
"buffer_load_dwordx4 a[64:67], %[voff], %[rx], s90 offen offset:0\n\t" \
"buffer_load_dwordx4 a[68:71], %[voff], %[rx], s90 offen offset:64\n\t" \
"buffer_load_dwordx4 a[72:75], %[voff], %[rx], s90 offen offset:128\n\t" \
"buffer_load_dwordx4 a[76:79], %[voff], %[rx], s90 offen offset:192\n\t" \
"buffer_load_dwordx4 a[80:83], %[voff], %[rx], s90 offen offset:256\n\t" \
"buffer_load_dwordx4 a[84:87], %[voff], %[rx], s90 offen offset:320\n\t" \
"buffer_load_dwordx4 a[88:91], %[voff], %[rx], s90 offen offset:384\n\t" \
"buffer_load_dwordx4 a[92:95], %[voff], %[rx], s90 offen offset:448\n\t" \
"s_mov_b32 s90, 1966080\n\t" \
"buffer_load_dwordx4 a[96:99], %[voff], %[rx], s90 offen offset:0\n\t" \
"buffer_load_dwordx4 a[100:103], %[voff], %[rx], s90 offen offset:64\n\t" \
"buffer_load_dwordx4 a[104:107], %[voff], %[rx], s90 offen offset:128\n\t" \
"buffer_load_dwordx4 a[108:111], %[voff], %[rx], s90 offen offset:192\n\t" \
"buffer_load_dwordx4 a[112:115], %[voff], %[rx], s90 offen offset:256\n\t" \
"buffer_load_dwordx4 a[116:119], %[voff], %[rx], s90 offen offset:320\n\t" \
"buffer_load_dwordx4 a[120:123], %[voff], %[rx], s90 offen offset:384\n\t" \
"buffer_load_dwordx4 a[124:127], %[voff], %[rx], s90 offen offset:448\n\t" \
"s_waitcnt vmcnt(32)\n\t" \
"s_barrier\n\t"

#define ASM_P1L2_CLOB "a0", "a1", "a2", "a3", "a4", "a5", "a6", "a7", "a8", "a9", "a10", "a11", "a12", "a13", "a14", "a15", "a16", "a17", "a18", "a19", "a20", "a21", "a22", "a23", "a24", "a25", "a26", "a27", "a28", "a29", "a30", "a31", "a32", "a33", "a34", "a35", "a36", "a37", "a38", "a39", "a40", "a41", "a42", "a43", "a44", "a45", "a46", "a47", "a48", "a49", "a50", "a51", "a52", "a53", "a54", "a55", "a56", "a57", "a58", "a59", "a60", "a61", "a62", "a63", "a64", "a65", "a66", "a67", "a68", "a69", "a70", "a71", "a72", "a73", "a74", "a75", "a76", "a77", "a78", "a79", "a80", "a81", "a82", "a83", "a84", "a85", "a86", "a87", "a88", "a89", "a90", "a91", "a92", "a93", "a94", "a95", "a96", "a97", "a98", "a99", "a100", "a101", "a102", "a103", "a104", "a105", "a106", "a107", "a108", "a109", "a110", "a111", "a112", "a113", "a114", "a115", "a116", "a117", "a118", "a119", "a120", "a121", "a122", "a123", "a124", "a125", "a126", "a127", "s90", "s91", "scc", "memory"

#define ASM_P1M20_TEXT \
"ds_read_b128 v[64:67], %[lz0] offset:0\n\t" \
"ds_read_b128 v[68:71], %[lz1] offset:0\n\t" \
"ds_read_b128 v[72:75], %[lz2] offset:0\n\t" \
"ds_read_b128 v[76:79], %[lz3] offset:0\n\t" \
"ds_read_b128 v[80:83], %[lz0] offset:256\n\t" \
"ds_read_b128 v[84:87], %[lz1] offset:256\n\t" \
"ds_read_b128 v[88:91], %[lz2] offset:256\n\t" \
"ds_read_b128 v[92:95], %[lz3] offset:256\n\t" \
"s_waitcnt vmcnt(16)\n\t" \
"s_waitcnt lgkmcnt(0)\n\t" \
"v_mfma_f32_16x16x32_bf16 v[128:131], a[0:3], v[64:67], 0\n\t" \
"ds_read_b128 v[96:99], %[lz0] offset:8192\n\t" \
"v_mfma_f32_16x16x32_bf16 v[132:135], a[32:35], v[64:67], 0\n\t" \
"ds_read_b128 v[100:103], %[lz1] offset:8192\n\t" \
"v_mfma_f32_16x16x32_bf16 v[128:131], a[4:7], v[68:71], v[128:131]\n\t" \
"ds_read_b128 v[104:107], %[lz2] offset:8192\n\t" \
"v_mfma_f32_16x16x32_bf16 v[132:135], a[36:39], v[68:71], v[132:135]\n\t" \
"ds_read_b128 v[108:111], %[lz3] offset:8192\n\t" \
"v_mfma_f32_16x16x32_bf16 v[128:131], a[8:11], v[72:75], v[128:131]\n\t" \
"ds_read_b128 v[112:115], %[lz0] offset:8448\n\t" \
"v_mfma_f32_16x16x32_bf16 v[132:135], a[40:43], v[72:75], v[132:135]\n\t" \
"ds_read_b128 v[116:119], %[lz1] offset:8448\n\t" \
"v_mfma_f32_16x16x32_bf16 v[128:131], a[12:15], v[76:79], v[128:131]\n\t" \
"ds_read_b128 v[120:123], %[lz2] offset:8448\n\t" \
"v_mfma_f32_16x16x32_bf16 v[132:135], a[44:47], v[76:79], v[132:135]\n\t" \
"ds_read_b128 v[124:127], %[lz3] offset:8448\n\t" \
"v_mfma_f32_16x16x32_bf16 v[128:131], a[16:19], v[80:83], v[128:131]\n\t" \
"v_mfma_f32_16x16x32_bf16 v[132:135], a[48:51], v[80:83], v[132:135]\n\t" \
"v_mfma_f32_16x16x32_bf16 v[128:131], a[20:23], v[84:87], v[128:131]\n\t" \
"v_mfma_f32_16x16x32_bf16 v[132:135], a[52:55], v[84:87], v[132:135]\n\t" \
"v_mfma_f32_16x16x32_bf16 v[128:131], a[24:27], v[88:91], v[128:131]\n\t" \
"v_mfma_f32_16x16x32_bf16 v[132:135], a[56:59], v[88:91], v[132:135]\n\t" \
"v_mfma_f32_16x16x32_bf16 v[128:131], a[28:31], v[92:95], v[128:131]\n\t" \
"v_mfma_f32_16x16x32_bf16 v[132:135], a[60:63], v[92:95], v[132:135]\n\t" \
"s_waitcnt lgkmcnt(0)\n\t" \
"v_mfma_f32_16x16x32_bf16 v[136:139], a[0:3], v[96:99], 0\n\t" \
"ds_read_b128 v[64:67], %[lz0] offset:16384\n\t" \
"v_mfma_f32_16x16x32_bf16 v[140:143], a[32:35], v[96:99], 0\n\t" \
"ds_read_b128 v[68:71], %[lz1] offset:16384\n\t" \
"v_mfma_f32_16x16x32_bf16 v[136:139], a[4:7], v[100:103], v[136:139]\n\t" \
"ds_read_b128 v[72:75], %[lz2] offset:16384\n\t" \
"s_mov_b32 s91, %[sb0]\n\t" \
"v_mfma_f32_16x16x32_bf16 v[140:143], a[36:39], v[100:103], v[140:143]\n\t" \
"ds_read_b128 v[76:79], %[lz3] offset:16384\n\t" \
"v_mul_f32 v128, %[scale], v128\n\t" \
"v_mfma_f32_16x16x32_bf16 v[136:139], a[8:11], v[104:107], v[136:139]\n\t" \
"ds_read_b128 v[80:83], %[lz0] offset:16640\n\t" \
"v_mul_f32 v129, %[scale], v129\n\t" \
"v_mfma_f32_16x16x32_bf16 v[140:143], a[40:43], v[104:107], v[140:143]\n\t" \
"ds_read_b128 v[84:87], %[lz1] offset:16640\n\t" \
"v_mul_f32 v130, %[scale], v130\n\t" \
"v_mfma_f32_16x16x32_bf16 v[136:139], a[12:15], v[108:111], v[136:139]\n\t" \
"ds_read_b128 v[88:91], %[lz2] offset:16640\n\t" \
"v_mul_f32 v131, %[scale], v131\n\t" \
"v_mfma_f32_16x16x32_bf16 v[140:143], a[44:47], v[108:111], v[140:143]\n\t" \
"ds_read_b128 v[92:95], %[lz3] offset:16640\n\t" \
"v_mul_f32 v132, %[scale], v132\n\t" \
"v_mfma_f32_16x16x32_bf16 v[136:139], a[16:19], v[112:115], v[136:139]\n\t" \
"v_mul_f32 v133, %[scale], v133\n\t" \
"v_mul_f32 v134, %[scale], v134\n\t" \
"v_mfma_f32_16x16x32_bf16 v[140:143], a[48:51], v[112:115], v[140:143]\n\t" \
"v_mul_f32 v135, %[scale], v135\n\t" \
"v_cvt_pk_bf16_f32 v144, v128, v129\n\t" \
"v_mfma_f32_16x16x32_bf16 v[136:139], a[20:23], v[116:119], v[136:139]\n\t" \
"v_cvt_pk_bf16_f32 v146, v130, v131\n\t" \
"v_cvt_pk_bf16_f32 v145, v132, v133\n\t" \
"v_mfma_f32_16x16x32_bf16 v[140:143], a[52:55], v[116:119], v[140:143]\n\t" \
"v_cvt_pk_bf16_f32 v147, v134, v135\n\t" \
"buffer_store_dwordx4 v[144:147], %[vst], %[rw], s91 offen sc1\n\t" \
"s_add_u32 s91, s91, %[ss0]\n\t" \
"v_mfma_f32_16x16x32_bf16 v[136:139], a[24:27], v[120:123], v[136:139]\n\t" \
"v_mfma_f32_16x16x32_bf16 v[140:143], a[56:59], v[120:123], v[140:143]\n\t" \
"v_mfma_f32_16x16x32_bf16 v[136:139], a[28:31], v[124:127], v[136:139]\n\t" \
"v_mfma_f32_16x16x32_bf16 v[140:143], a[60:63], v[124:127], v[140:143]\n\t" \
"s_waitcnt lgkmcnt(0)\n\t" \
"v_mfma_f32_16x16x32_bf16 v[128:131], a[0:3], v[64:67], 0\n\t" \
"ds_read_b128 v[96:99], %[lz0] offset:24576\n\t" \
"v_mfma_f32_16x16x32_bf16 v[132:135], a[32:35], v[64:67], 0\n\t" \
"ds_read_b128 v[100:103], %[lz1] offset:24576\n\t" \
"v_mfma_f32_16x16x32_bf16 v[128:131], a[4:7], v[68:71], v[128:131]\n\t" \
"ds_read_b128 v[104:107], %[lz2] offset:24576\n\t" \
"v_mul_f32 v136, %[scale], v136\n\t" \
"v_mfma_f32_16x16x32_bf16 v[132:135], a[36:39], v[68:71], v[132:135]\n\t" \
"ds_read_b128 v[108:111], %[lz3] offset:24576\n\t" \
"v_mul_f32 v137, %[scale], v137\n\t" \
"v_mfma_f32_16x16x32_bf16 v[128:131], a[8:11], v[72:75], v[128:131]\n\t" \
"ds_read_b128 v[112:115], %[lz0] offset:24832\n\t" \
"v_mul_f32 v138, %[scale], v138\n\t" \
"v_mfma_f32_16x16x32_bf16 v[132:135], a[40:43], v[72:75], v[132:135]\n\t" \
"ds_read_b128 v[116:119], %[lz1] offset:24832\n\t" \
"v_mul_f32 v139, %[scale], v139\n\t" \
"v_mfma_f32_16x16x32_bf16 v[128:131], a[12:15], v[76:79], v[128:131]\n\t" \
"ds_read_b128 v[120:123], %[lz2] offset:24832\n\t" \
"v_mul_f32 v140, %[scale], v140\n\t" \
"v_mfma_f32_16x16x32_bf16 v[132:135], a[44:47], v[76:79], v[132:135]\n\t" \
"ds_read_b128 v[124:127], %[lz3] offset:24832\n\t" \
"v_mul_f32 v141, %[scale], v141\n\t" \
"v_mfma_f32_16x16x32_bf16 v[128:131], a[16:19], v[80:83], v[128:131]\n\t" \
"v_mul_f32 v142, %[scale], v142\n\t" \
"v_mul_f32 v143, %[scale], v143\n\t" \
"v_mfma_f32_16x16x32_bf16 v[132:135], a[48:51], v[80:83], v[132:135]\n\t" \
"v_cvt_pk_bf16_f32 v148, v136, v137\n\t" \
"v_cvt_pk_bf16_f32 v150, v138, v139\n\t" \
"v_mfma_f32_16x16x32_bf16 v[128:131], a[20:23], v[84:87], v[128:131]\n\t" \
"v_cvt_pk_bf16_f32 v149, v140, v141\n\t" \
"v_cvt_pk_bf16_f32 v151, v142, v143\n\t" \
"v_mfma_f32_16x16x32_bf16 v[132:135], a[52:55], v[84:87], v[132:135]\n\t" \
"buffer_store_dwordx4 v[148:151], %[vst], %[rw], s91 offen sc1\n\t" \
"s_add_u32 s91, s91, %[ss0]\n\t" \
"v_mfma_f32_16x16x32_bf16 v[128:131], a[24:27], v[88:91], v[128:131]\n\t" \
"v_mfma_f32_16x16x32_bf16 v[132:135], a[56:59], v[88:91], v[132:135]\n\t" \
"v_mfma_f32_16x16x32_bf16 v[128:131], a[28:31], v[92:95], v[128:131]\n\t" \
"v_mfma_f32_16x16x32_bf16 v[132:135], a[60:63], v[92:95], v[132:135]\n\t" \
"s_waitcnt lgkmcnt(0)\n\t" \
"v_mfma_f32_16x16x32_bf16 v[136:139], a[0:3], v[96:99], 0\n\t" \
"ds_read_b128 v[64:67], %[lz0] offset:32768\n\t" \
"v_mfma_f32_16x16x32_bf16 v[140:143], a[32:35], v[96:99], 0\n\t" \
"ds_read_b128 v[68:71], %[lz1] offset:32768\n\t" \
"v_mfma_f32_16x16x32_bf16 v[136:139], a[4:7], v[100:103], v[136:139]\n\t" \
"ds_read_b128 v[72:75], %[lz2] offset:32768\n\t" \
"v_mul_f32 v128, %[scale], v128\n\t" \
"v_mfma_f32_16x16x32_bf16 v[140:143], a[36:39], v[100:103], v[140:143]\n\t" \
"ds_read_b128 v[76:79], %[lz3] offset:32768\n\t" \
"v_mul_f32 v129, %[scale], v129\n\t" \
"v_mfma_f32_16x16x32_bf16 v[136:139], a[8:11], v[104:107], v[136:139]\n\t" \
"ds_read_b128 v[80:83], %[lz0] offset:33024\n\t" \
"v_mul_f32 v130, %[scale], v130\n\t" \
"v_mfma_f32_16x16x32_bf16 v[140:143], a[40:43], v[104:107], v[140:143]\n\t" \
"ds_read_b128 v[84:87], %[lz1] offset:33024\n\t" \
"v_mul_f32 v131, %[scale], v131\n\t" \
"v_mfma_f32_16x16x32_bf16 v[136:139], a[12:15], v[108:111], v[136:139]\n\t" \
"ds_read_b128 v[88:91], %[lz2] offset:33024\n\t" \
"v_mul_f32 v132, %[scale], v132\n\t" \
"v_mfma_f32_16x16x32_bf16 v[140:143], a[44:47], v[108:111], v[140:143]\n\t" \
"ds_read_b128 v[92:95], %[lz3] offset:33024\n\t" \
"v_mul_f32 v133, %[scale], v133\n\t" \
"v_mfma_f32_16x16x32_bf16 v[136:139], a[16:19], v[112:115], v[136:139]\n\t" \
"v_mul_f32 v134, %[scale], v134\n\t" \
"v_mul_f32 v135, %[scale], v135\n\t" \
"v_mfma_f32_16x16x32_bf16 v[140:143], a[48:51], v[112:115], v[140:143]\n\t" \
"v_cvt_pk_bf16_f32 v144, v128, v129\n\t" \
"v_cvt_pk_bf16_f32 v146, v130, v131\n\t" \
"v_mfma_f32_16x16x32_bf16 v[136:139], a[20:23], v[116:119], v[136:139]\n\t" \
"v_cvt_pk_bf16_f32 v145, v132, v133\n\t" \
"v_cvt_pk_bf16_f32 v147, v134, v135\n\t" \
"v_mfma_f32_16x16x32_bf16 v[140:143], a[52:55], v[116:119], v[140:143]\n\t" \
"buffer_store_dwordx4 v[144:147], %[vst], %[rw], s91 offen sc1\n\t" \
"s_add_u32 s91, s91, %[ss0]\n\t" \
"v_mfma_f32_16x16x32_bf16 v[136:139], a[24:27], v[120:123], v[136:139]\n\t" \
"v_mfma_f32_16x16x32_bf16 v[140:143], a[56:59], v[120:123], v[140:143]\n\t" \
"v_mfma_f32_16x16x32_bf16 v[136:139], a[28:31], v[124:127], v[136:139]\n\t" \
"v_mfma_f32_16x16x32_bf16 v[140:143], a[60:63], v[124:127], v[140:143]\n\t" \
"s_waitcnt lgkmcnt(0)\n\t" \
"v_mfma_f32_16x16x32_bf16 v[128:131], a[0:3], v[64:67], 0\n\t" \
"ds_read_b128 v[96:99], %[lz0] offset:40960\n\t" \
"v_mfma_f32_16x16x32_bf16 v[132:135], a[32:35], v[64:67], 0\n\t" \
"ds_read_b128 v[100:103], %[lz1] offset:40960\n\t" \
"v_mfma_f32_16x16x32_bf16 v[128:131], a[4:7], v[68:71], v[128:131]\n\t" \
"ds_read_b128 v[104:107], %[lz2] offset:40960\n\t" \
"v_mul_f32 v136, %[scale], v136\n\t" \
"v_mfma_f32_16x16x32_bf16 v[132:135], a[36:39], v[68:71], v[132:135]\n\t" \
"ds_read_b128 v[108:111], %[lz3] offset:40960\n\t" \
"v_mul_f32 v137, %[scale], v137\n\t" \
"v_mfma_f32_16x16x32_bf16 v[128:131], a[8:11], v[72:75], v[128:131]\n\t" \
"ds_read_b128 v[112:115], %[lz0] offset:41216\n\t" \
"v_mul_f32 v138, %[scale], v138\n\t" \
"v_mfma_f32_16x16x32_bf16 v[132:135], a[40:43], v[72:75], v[132:135]\n\t" \
"ds_read_b128 v[116:119], %[lz1] offset:41216\n\t" \
"v_mul_f32 v139, %[scale], v139\n\t" \
"v_mfma_f32_16x16x32_bf16 v[128:131], a[12:15], v[76:79], v[128:131]\n\t" \
"ds_read_b128 v[120:123], %[lz2] offset:41216\n\t" \
"v_mul_f32 v140, %[scale], v140\n\t" \
"v_mfma_f32_16x16x32_bf16 v[132:135], a[44:47], v[76:79], v[132:135]\n\t" \
"ds_read_b128 v[124:127], %[lz3] offset:41216\n\t" \
"v_mul_f32 v141, %[scale], v141\n\t" \
"v_mfma_f32_16x16x32_bf16 v[128:131], a[16:19], v[80:83], v[128:131]\n\t" \
"v_mul_f32 v142, %[scale], v142\n\t" \
"v_mul_f32 v143, %[scale], v143\n\t" \
"v_mfma_f32_16x16x32_bf16 v[132:135], a[48:51], v[80:83], v[132:135]\n\t" \
"v_cvt_pk_bf16_f32 v148, v136, v137\n\t" \
"v_cvt_pk_bf16_f32 v150, v138, v139\n\t" \
"v_mfma_f32_16x16x32_bf16 v[128:131], a[20:23], v[84:87], v[128:131]\n\t" \
"v_cvt_pk_bf16_f32 v149, v140, v141\n\t" \
"v_cvt_pk_bf16_f32 v151, v142, v143\n\t" \
"v_mfma_f32_16x16x32_bf16 v[132:135], a[52:55], v[84:87], v[132:135]\n\t" \
"buffer_store_dwordx4 v[148:151], %[vst], %[rw], s91 offen sc1\n\t" \
"s_add_u32 s91, s91, %[ss0]\n\t" \
"v_mfma_f32_16x16x32_bf16 v[128:131], a[24:27], v[88:91], v[128:131]\n\t" \
"v_mfma_f32_16x16x32_bf16 v[132:135], a[56:59], v[88:91], v[132:135]\n\t" \
"v_mfma_f32_16x16x32_bf16 v[128:131], a[28:31], v[92:95], v[128:131]\n\t" \
"v_mfma_f32_16x16x32_bf16 v[132:135], a[60:63], v[92:95], v[132:135]\n\t" \
"s_waitcnt lgkmcnt(0)\n\t" \
"v_mfma_f32_16x16x32_bf16 v[136:139], a[0:3], v[96:99], 0\n\t" \
"ds_read_b128 v[64:67], %[lz0] offset:49152\n\t" \
"v_mfma_f32_16x16x32_bf16 v[140:143], a[32:35], v[96:99], 0\n\t" \
"ds_read_b128 v[68:71], %[lz1] offset:49152\n\t" \
"v_mfma_f32_16x16x32_bf16 v[136:139], a[4:7], v[100:103], v[136:139]\n\t" \
"ds_read_b128 v[72:75], %[lz2] offset:49152\n\t" \
"v_mul_f32 v128, %[scale], v128\n\t" \
"v_mfma_f32_16x16x32_bf16 v[140:143], a[36:39], v[100:103], v[140:143]\n\t" \
"ds_read_b128 v[76:79], %[lz3] offset:49152\n\t" \
"v_mul_f32 v129, %[scale], v129\n\t" \
"v_mfma_f32_16x16x32_bf16 v[136:139], a[8:11], v[104:107], v[136:139]\n\t" \
"ds_read_b128 v[80:83], %[lz0] offset:49408\n\t" \
"v_mul_f32 v130, %[scale], v130\n\t" \
"v_mfma_f32_16x16x32_bf16 v[140:143], a[40:43], v[104:107], v[140:143]\n\t" \
"ds_read_b128 v[84:87], %[lz1] offset:49408\n\t" \
"v_mul_f32 v131, %[scale], v131\n\t" \
"v_mfma_f32_16x16x32_bf16 v[136:139], a[12:15], v[108:111], v[136:139]\n\t" \
"ds_read_b128 v[88:91], %[lz2] offset:49408\n\t" \
"v_mul_f32 v132, %[scale], v132\n\t" \
"v_mfma_f32_16x16x32_bf16 v[140:143], a[44:47], v[108:111], v[140:143]\n\t" \
"ds_read_b128 v[92:95], %[lz3] offset:49408\n\t" \
"v_mul_f32 v133, %[scale], v133\n\t" \
"v_mfma_f32_16x16x32_bf16 v[136:139], a[16:19], v[112:115], v[136:139]\n\t" \
"v_mul_f32 v134, %[scale], v134\n\t" \
"v_mul_f32 v135, %[scale], v135\n\t" \
"v_mfma_f32_16x16x32_bf16 v[140:143], a[48:51], v[112:115], v[140:143]\n\t" \
"v_cvt_pk_bf16_f32 v144, v128, v129\n\t" \
"v_cvt_pk_bf16_f32 v146, v130, v131\n\t" \
"v_mfma_f32_16x16x32_bf16 v[136:139], a[20:23], v[116:119], v[136:139]\n\t" \
"v_cvt_pk_bf16_f32 v145, v132, v133\n\t" \
"v_cvt_pk_bf16_f32 v147, v134, v135\n\t" \
"v_mfma_f32_16x16x32_bf16 v[140:143], a[52:55], v[116:119], v[140:143]\n\t" \
"buffer_store_dwordx4 v[144:147], %[vst], %[rw], s91 offen sc1\n\t" \
"s_add_u32 s91, s91, %[ss0]\n\t" \
"v_mfma_f32_16x16x32_bf16 v[136:139], a[24:27], v[120:123], v[136:139]\n\t" \
"v_mfma_f32_16x16x32_bf16 v[140:143], a[56:59], v[120:123], v[140:143]\n\t" \
"v_mfma_f32_16x16x32_bf16 v[136:139], a[28:31], v[124:127], v[136:139]\n\t" \
"v_mfma_f32_16x16x32_bf16 v[140:143], a[60:63], v[124:127], v[140:143]\n\t" \
"s_waitcnt lgkmcnt(0)\n\t" \
"v_mfma_f32_16x16x32_bf16 v[128:131], a[0:3], v[64:67], 0\n\t" \
"ds_read_b128 v[96:99], %[lz0] offset:0\n\t" \
"v_mfma_f32_16x16x32_bf16 v[132:135], a[32:35], v[64:67], 0\n\t" \
"ds_read_b128 v[100:103], %[lz1] offset:0\n\t" \
"v_mfma_f32_16x16x32_bf16 v[128:131], a[4:7], v[68:71], v[128:131]\n\t" \
"ds_read_b128 v[104:107], %[lz2] offset:0\n\t" \
"v_mul_f32 v136, %[scale], v136\n\t" \
"v_mfma_f32_16x16x32_bf16 v[132:135], a[36:39], v[68:71], v[132:135]\n\t" \
"ds_read_b128 v[108:111], %[lz3] offset:0\n\t" \
"v_mul_f32 v137, %[scale], v137\n\t" \
"v_mfma_f32_16x16x32_bf16 v[128:131], a[8:11], v[72:75], v[128:131]\n\t" \
"ds_read_b128 v[112:115], %[lz0] offset:256\n\t" \
"v_mul_f32 v138, %[scale], v138\n\t" \
"v_mfma_f32_16x16x32_bf16 v[132:135], a[40:43], v[72:75], v[132:135]\n\t" \
"ds_read_b128 v[116:119], %[lz1] offset:256\n\t" \
"v_mul_f32 v139, %[scale], v139\n\t" \
"v_mfma_f32_16x16x32_bf16 v[128:131], a[12:15], v[76:79], v[128:131]\n\t" \
"ds_read_b128 v[120:123], %[lz2] offset:256\n\t" \
"v_mul_f32 v140, %[scale], v140\n\t" \
"v_mfma_f32_16x16x32_bf16 v[132:135], a[44:47], v[76:79], v[132:135]\n\t" \
"ds_read_b128 v[124:127], %[lz3] offset:256\n\t" \
"v_mul_f32 v141, %[scale], v141\n\t" \
"v_mfma_f32_16x16x32_bf16 v[128:131], a[16:19], v[80:83], v[128:131]\n\t" \
"v_mul_f32 v142, %[scale], v142\n\t" \
"v_mul_f32 v143, %[scale], v143\n\t" \
"v_mfma_f32_16x16x32_bf16 v[132:135], a[48:51], v[80:83], v[132:135]\n\t" \
"v_cvt_pk_bf16_f32 v148, v136, v137\n\t" \
"v_cvt_pk_bf16_f32 v150, v138, v139\n\t" \
"v_mfma_f32_16x16x32_bf16 v[128:131], a[20:23], v[84:87], v[128:131]\n\t" \
"v_cvt_pk_bf16_f32 v149, v140, v141\n\t" \
"v_cvt_pk_bf16_f32 v151, v142, v143\n\t" \
"v_mfma_f32_16x16x32_bf16 v[132:135], a[52:55], v[84:87], v[132:135]\n\t" \
"buffer_store_dwordx4 v[148:151], %[vst], %[rw], s91 offen sc1\n\t" \
"s_add_u32 s91, s91, %[ss0]\n\t" \
"v_mfma_f32_16x16x32_bf16 v[128:131], a[24:27], v[88:91], v[128:131]\n\t" \
"v_mfma_f32_16x16x32_bf16 v[132:135], a[56:59], v[88:91], v[132:135]\n\t" \
"v_mfma_f32_16x16x32_bf16 v[128:131], a[28:31], v[92:95], v[128:131]\n\t" \
"v_mfma_f32_16x16x32_bf16 v[132:135], a[60:63], v[92:95], v[132:135]\n\t" \
"s_waitcnt vmcnt(6)\n\t" \
"s_waitcnt lgkmcnt(0)\n\t" \
"v_mfma_f32_16x16x32_bf16 v[136:139], a[64:67], v[96:99], 0\n\t" \
"ds_read_b128 v[64:67], %[lz0] offset:8192\n\t" \
"v_mfma_f32_16x16x32_bf16 v[140:143], a[96:99], v[96:99], 0\n\t" \
"ds_read_b128 v[68:71], %[lz1] offset:8192\n\t" \
"v_mfma_f32_16x16x32_bf16 v[136:139], a[68:71], v[100:103], v[136:139]\n\t" \
"ds_read_b128 v[72:75], %[lz2] offset:8192\n\t" \
"v_mul_f32 v128, %[scale], v128\n\t" \
"v_mfma_f32_16x16x32_bf16 v[140:143], a[100:103], v[100:103], v[140:143]\n\t" \
"ds_read_b128 v[76:79], %[lz3] offset:8192\n\t" \
"v_mul_f32 v129, %[scale], v129\n\t" \
"v_mfma_f32_16x16x32_bf16 v[136:139], a[72:75], v[104:107], v[136:139]\n\t" \
"ds_read_b128 v[80:83], %[lz0] offset:8448\n\t" \
"v_mul_f32 v130, %[scale], v130\n\t" \
"v_mfma_f32_16x16x32_bf16 v[140:143], a[104:107], v[104:107], v[140:143]\n\t" \
"ds_read_b128 v[84:87], %[lz1] offset:8448\n\t" \
"v_mul_f32 v131, %[scale], v131\n\t" \
"v_mfma_f32_16x16x32_bf16 v[136:139], a[76:79], v[108:111], v[136:139]\n\t" \
"ds_read_b128 v[88:91], %[lz2] offset:8448\n\t" \
"v_mul_f32 v132, %[scale], v132\n\t" \
"v_mfma_f32_16x16x32_bf16 v[140:143], a[108:111], v[108:111], v[140:143]\n\t" \
"ds_read_b128 v[92:95], %[lz3] offset:8448\n\t" \
"v_mul_f32 v133, %[scale], v133\n\t" \
"v_mfma_f32_16x16x32_bf16 v[136:139], a[80:83], v[112:115], v[136:139]\n\t" \
"v_mul_f32 v134, %[scale], v134\n\t" \
"v_mul_f32 v135, %[scale], v135\n\t" \
"v_mfma_f32_16x16x32_bf16 v[140:143], a[112:115], v[112:115], v[140:143]\n\t" \
"v_cvt_pk_bf16_f32 v144, v128, v129\n\t" \
"v_cvt_pk_bf16_f32 v146, v130, v131\n\t" \
"v_mfma_f32_16x16x32_bf16 v[136:139], a[84:87], v[116:119], v[136:139]\n\t" \
"v_cvt_pk_bf16_f32 v145, v132, v133\n\t" \
"v_cvt_pk_bf16_f32 v147, v134, v135\n\t" \
"v_mfma_f32_16x16x32_bf16 v[140:143], a[116:119], v[116:119], v[140:143]\n\t" \
"buffer_store_dwordx4 v[144:147], %[vst], %[rw], s91 offen sc1\n\t" \
"s_add_u32 s91, s91, %[ss0]\n\t" \
"v_mfma_f32_16x16x32_bf16 v[136:139], a[88:91], v[120:123], v[136:139]\n\t" \
"v_mfma_f32_16x16x32_bf16 v[140:143], a[120:123], v[120:123], v[140:143]\n\t" \
"v_mfma_f32_16x16x32_bf16 v[136:139], a[92:95], v[124:127], v[136:139]\n\t" \
"v_mfma_f32_16x16x32_bf16 v[140:143], a[124:127], v[124:127], v[140:143]\n\t" \
"s_waitcnt lgkmcnt(0)\n\t" \
"v_mfma_f32_16x16x32_bf16 v[128:131], a[64:67], v[64:67], 0\n\t" \
"ds_read_b128 v[96:99], %[lz0] offset:16384\n\t" \
"v_mfma_f32_16x16x32_bf16 v[132:135], a[96:99], v[64:67], 0\n\t" \
"ds_read_b128 v[100:103], %[lz1] offset:16384\n\t" \
"v_mfma_f32_16x16x32_bf16 v[128:131], a[68:71], v[68:71], v[128:131]\n\t" \
"ds_read_b128 v[104:107], %[lz2] offset:16384\n\t" \
"s_mov_b32 s91, %[sb1]\n\t" \
"v_mfma_f32_16x16x32_bf16 v[132:135], a[100:103], v[68:71], v[132:135]\n\t" \
"ds_read_b128 v[108:111], %[lz3] offset:16384\n\t" \
"v_mul_f32 v136, %[scale], v136\n\t" \
"v_mfma_f32_16x16x32_bf16 v[128:131], a[72:75], v[72:75], v[128:131]\n\t" \
"ds_read_b128 v[112:115], %[lz0] offset:16640\n\t" \
"v_mul_f32 v137, %[scale], v137\n\t" \
"v_mfma_f32_16x16x32_bf16 v[132:135], a[104:107], v[72:75], v[132:135]\n\t" \
"ds_read_b128 v[116:119], %[lz1] offset:16640\n\t" \
"v_mul_f32 v138, %[scale], v138\n\t" \
"v_mfma_f32_16x16x32_bf16 v[128:131], a[76:79], v[76:79], v[128:131]\n\t" \
"ds_read_b128 v[120:123], %[lz2] offset:16640\n\t" \
"v_mul_f32 v139, %[scale], v139\n\t" \
"v_mfma_f32_16x16x32_bf16 v[132:135], a[108:111], v[76:79], v[132:135]\n\t" \
"ds_read_b128 v[124:127], %[lz3] offset:16640\n\t" \
"v_mul_f32 v140, %[scale], v140\n\t" \
"v_mfma_f32_16x16x32_bf16 v[128:131], a[80:83], v[80:83], v[128:131]\n\t" \
"v_mul_f32 v141, %[scale], v141\n\t" \
"v_mul_f32 v142, %[scale], v142\n\t" \
"v_mfma_f32_16x16x32_bf16 v[132:135], a[112:115], v[80:83], v[132:135]\n\t" \
"v_mul_f32 v143, %[scale], v143\n\t" \
"v_cvt_pk_bf16_f32 v148, v136, v137\n\t" \
"v_mfma_f32_16x16x32_bf16 v[128:131], a[84:87], v[84:87], v[128:131]\n\t" \
"v_cvt_pk_bf16_f32 v150, v138, v139\n\t" \
"v_cvt_pk_bf16_f32 v149, v140, v141\n\t" \
"v_mfma_f32_16x16x32_bf16 v[132:135], a[116:119], v[84:87], v[132:135]\n\t" \
"v_cvt_pk_bf16_f32 v151, v142, v143\n\t" \
"buffer_store_dwordx4 v[148:151], %[vst], %[rw], s91 offen sc1\n\t" \
"s_add_u32 s91, s91, %[ss1]\n\t" \
"v_mfma_f32_16x16x32_bf16 v[128:131], a[88:91], v[88:91], v[128:131]\n\t" \
"v_mfma_f32_16x16x32_bf16 v[132:135], a[120:123], v[88:91], v[132:135]\n\t" \
"v_mfma_f32_16x16x32_bf16 v[128:131], a[92:95], v[92:95], v[128:131]\n\t" \
"v_mfma_f32_16x16x32_bf16 v[132:135], a[124:127], v[92:95], v[132:135]\n\t" \
"s_waitcnt lgkmcnt(0)\n\t" \
"v_mfma_f32_16x16x32_bf16 v[136:139], a[64:67], v[96:99], 0\n\t" \
"ds_read_b128 v[64:67], %[lz0] offset:24576\n\t" \
"v_mfma_f32_16x16x32_bf16 v[140:143], a[96:99], v[96:99], 0\n\t" \
"ds_read_b128 v[68:71], %[lz1] offset:24576\n\t" \
"v_mfma_f32_16x16x32_bf16 v[136:139], a[68:71], v[100:103], v[136:139]\n\t" \
"ds_read_b128 v[72:75], %[lz2] offset:24576\n\t" \
"v_mul_f32 v128, %[scale], v128\n\t" \
"v_mfma_f32_16x16x32_bf16 v[140:143], a[100:103], v[100:103], v[140:143]\n\t" \
"ds_read_b128 v[76:79], %[lz3] offset:24576\n\t" \
"v_mul_f32 v129, %[scale], v129\n\t" \
"v_mfma_f32_16x16x32_bf16 v[136:139], a[72:75], v[104:107], v[136:139]\n\t" \
"ds_read_b128 v[80:83], %[lz0] offset:24832\n\t" \
"v_mul_f32 v130, %[scale], v130\n\t" \
"v_mfma_f32_16x16x32_bf16 v[140:143], a[104:107], v[104:107], v[140:143]\n\t" \
"ds_read_b128 v[84:87], %[lz1] offset:24832\n\t" \
"v_mul_f32 v131, %[scale], v131\n\t" \
"v_mfma_f32_16x16x32_bf16 v[136:139], a[76:79], v[108:111], v[136:139]\n\t" \
"ds_read_b128 v[88:91], %[lz2] offset:24832\n\t" \
"v_mul_f32 v132, %[scale], v132\n\t" \
"v_mfma_f32_16x16x32_bf16 v[140:143], a[108:111], v[108:111], v[140:143]\n\t" \
"ds_read_b128 v[92:95], %[lz3] offset:24832\n\t" \
"v_mul_f32 v133, %[scale], v133\n\t" \
"v_mfma_f32_16x16x32_bf16 v[136:139], a[80:83], v[112:115], v[136:139]\n\t" \
"v_mul_f32 v134, %[scale], v134\n\t" \
"v_mul_f32 v135, %[scale], v135\n\t" \
"v_mfma_f32_16x16x32_bf16 v[140:143], a[112:115], v[112:115], v[140:143]\n\t" \
"v_cvt_pk_bf16_f32 v144, v128, v129\n\t" \
"v_cvt_pk_bf16_f32 v146, v130, v131\n\t" \
"v_mfma_f32_16x16x32_bf16 v[136:139], a[84:87], v[116:119], v[136:139]\n\t" \
"v_cvt_pk_bf16_f32 v145, v132, v133\n\t" \
"v_cvt_pk_bf16_f32 v147, v134, v135\n\t" \
"v_mfma_f32_16x16x32_bf16 v[140:143], a[116:119], v[116:119], v[140:143]\n\t" \
"buffer_store_dwordx4 v[144:147], %[vst], %[rw], s91 offen sc1\n\t" \
"s_add_u32 s91, s91, %[ss1]\n\t" \
"v_mfma_f32_16x16x32_bf16 v[136:139], a[88:91], v[120:123], v[136:139]\n\t" \
"v_mfma_f32_16x16x32_bf16 v[140:143], a[120:123], v[120:123], v[140:143]\n\t" \
"v_mfma_f32_16x16x32_bf16 v[136:139], a[92:95], v[124:127], v[136:139]\n\t" \
"v_mfma_f32_16x16x32_bf16 v[140:143], a[124:127], v[124:127], v[140:143]\n\t" \
"s_waitcnt lgkmcnt(0)\n\t" \
"v_mfma_f32_16x16x32_bf16 v[128:131], a[64:67], v[64:67], 0\n\t" \
"ds_read_b128 v[96:99], %[lz0] offset:32768\n\t" \
"v_mfma_f32_16x16x32_bf16 v[132:135], a[96:99], v[64:67], 0\n\t" \
"ds_read_b128 v[100:103], %[lz1] offset:32768\n\t" \
"v_mfma_f32_16x16x32_bf16 v[128:131], a[68:71], v[68:71], v[128:131]\n\t" \
"ds_read_b128 v[104:107], %[lz2] offset:32768\n\t" \
"v_mul_f32 v136, %[scale], v136\n\t" \
"v_mfma_f32_16x16x32_bf16 v[132:135], a[100:103], v[68:71], v[132:135]\n\t" \
"ds_read_b128 v[108:111], %[lz3] offset:32768\n\t" \
"v_mul_f32 v137, %[scale], v137\n\t" \
"v_mfma_f32_16x16x32_bf16 v[128:131], a[72:75], v[72:75], v[128:131]\n\t" \
"ds_read_b128 v[112:115], %[lz0] offset:33024\n\t" \
"v_mul_f32 v138, %[scale], v138\n\t" \
"v_mfma_f32_16x16x32_bf16 v[132:135], a[104:107], v[72:75], v[132:135]\n\t" \
"ds_read_b128 v[116:119], %[lz1] offset:33024\n\t" \
"v_mul_f32 v139, %[scale], v139\n\t" \
"v_mfma_f32_16x16x32_bf16 v[128:131], a[76:79], v[76:79], v[128:131]\n\t" \
"ds_read_b128 v[120:123], %[lz2] offset:33024\n\t" \
"v_mul_f32 v140, %[scale], v140\n\t" \
"v_mfma_f32_16x16x32_bf16 v[132:135], a[108:111], v[76:79], v[132:135]\n\t" \
"ds_read_b128 v[124:127], %[lz3] offset:33024\n\t" \
"v_mul_f32 v141, %[scale], v141\n\t" \
"v_mfma_f32_16x16x32_bf16 v[128:131], a[80:83], v[80:83], v[128:131]\n\t" \
"v_mul_f32 v142, %[scale], v142\n\t" \
"v_mul_f32 v143, %[scale], v143\n\t" \
"v_mfma_f32_16x16x32_bf16 v[132:135], a[112:115], v[80:83], v[132:135]\n\t" \
"v_cvt_pk_bf16_f32 v148, v136, v137\n\t" \
"v_cvt_pk_bf16_f32 v150, v138, v139\n\t" \
"v_mfma_f32_16x16x32_bf16 v[128:131], a[84:87], v[84:87], v[128:131]\n\t" \
"v_cvt_pk_bf16_f32 v149, v140, v141\n\t" \
"v_cvt_pk_bf16_f32 v151, v142, v143\n\t" \
"v_mfma_f32_16x16x32_bf16 v[132:135], a[116:119], v[84:87], v[132:135]\n\t" \
"buffer_store_dwordx4 v[148:151], %[vst], %[rw], s91 offen sc1\n\t" \
"s_add_u32 s91, s91, %[ss1]\n\t" \
"v_mfma_f32_16x16x32_bf16 v[128:131], a[88:91], v[88:91], v[128:131]\n\t" \
"v_mfma_f32_16x16x32_bf16 v[132:135], a[120:123], v[88:91], v[132:135]\n\t" \
"v_mfma_f32_16x16x32_bf16 v[128:131], a[92:95], v[92:95], v[128:131]\n\t" \
"v_mfma_f32_16x16x32_bf16 v[132:135], a[124:127], v[92:95], v[132:135]\n\t" \
"s_waitcnt lgkmcnt(0)\n\t" \
"v_mfma_f32_16x16x32_bf16 v[136:139], a[64:67], v[96:99], 0\n\t" \
"ds_read_b128 v[64:67], %[lz0] offset:40960\n\t" \
"v_mfma_f32_16x16x32_bf16 v[140:143], a[96:99], v[96:99], 0\n\t" \
"ds_read_b128 v[68:71], %[lz1] offset:40960\n\t" \
"v_mfma_f32_16x16x32_bf16 v[136:139], a[68:71], v[100:103], v[136:139]\n\t" \
"ds_read_b128 v[72:75], %[lz2] offset:40960\n\t" \
"v_mul_f32 v128, %[scale], v128\n\t" \
"v_mfma_f32_16x16x32_bf16 v[140:143], a[100:103], v[100:103], v[140:143]\n\t" \
"ds_read_b128 v[76:79], %[lz3] offset:40960\n\t" \
"v_mul_f32 v129, %[scale], v129\n\t" \
"v_mfma_f32_16x16x32_bf16 v[136:139], a[72:75], v[104:107], v[136:139]\n\t" \
"ds_read_b128 v[80:83], %[lz0] offset:41216\n\t" \
"v_mul_f32 v130, %[scale], v130\n\t" \
"v_mfma_f32_16x16x32_bf16 v[140:143], a[104:107], v[104:107], v[140:143]\n\t" \
"ds_read_b128 v[84:87], %[lz1] offset:41216\n\t" \
"v_mul_f32 v131, %[scale], v131\n\t" \
"v_mfma_f32_16x16x32_bf16 v[136:139], a[76:79], v[108:111], v[136:139]\n\t" \
"ds_read_b128 v[88:91], %[lz2] offset:41216\n\t" \
"v_mul_f32 v132, %[scale], v132\n\t" \
"v_mfma_f32_16x16x32_bf16 v[140:143], a[108:111], v[108:111], v[140:143]\n\t" \
"ds_read_b128 v[92:95], %[lz3] offset:41216\n\t" \
"v_mul_f32 v133, %[scale], v133\n\t" \
"v_mfma_f32_16x16x32_bf16 v[136:139], a[80:83], v[112:115], v[136:139]\n\t" \
"v_mul_f32 v134, %[scale], v134\n\t" \
"v_mul_f32 v135, %[scale], v135\n\t" \
"v_mfma_f32_16x16x32_bf16 v[140:143], a[112:115], v[112:115], v[140:143]\n\t" \
"v_cvt_pk_bf16_f32 v144, v128, v129\n\t" \
"v_cvt_pk_bf16_f32 v146, v130, v131\n\t" \
"v_mfma_f32_16x16x32_bf16 v[136:139], a[84:87], v[116:119], v[136:139]\n\t" \
"v_cvt_pk_bf16_f32 v145, v132, v133\n\t" \
"v_cvt_pk_bf16_f32 v147, v134, v135\n\t" \
"v_mfma_f32_16x16x32_bf16 v[140:143], a[116:119], v[116:119], v[140:143]\n\t" \
"buffer_store_dwordx4 v[144:147], %[vst], %[rw], s91 offen sc1\n\t" \
"s_add_u32 s91, s91, %[ss1]\n\t" \
"v_mfma_f32_16x16x32_bf16 v[136:139], a[88:91], v[120:123], v[136:139]\n\t" \
"v_mfma_f32_16x16x32_bf16 v[140:143], a[120:123], v[120:123], v[140:143]\n\t" \
"v_mfma_f32_16x16x32_bf16 v[136:139], a[92:95], v[124:127], v[136:139]\n\t" \
"v_mfma_f32_16x16x32_bf16 v[140:143], a[124:127], v[124:127], v[140:143]\n\t" \
"s_waitcnt lgkmcnt(0)\n\t" \
"v_mfma_f32_16x16x32_bf16 v[128:131], a[64:67], v[64:67], 0\n\t" \
"ds_read_b128 v[96:99], %[lz0] offset:49152\n\t" \
"v_mfma_f32_16x16x32_bf16 v[132:135], a[96:99], v[64:67], 0\n\t" \
"ds_read_b128 v[100:103], %[lz1] offset:49152\n\t" \
"v_mfma_f32_16x16x32_bf16 v[128:131], a[68:71], v[68:71], v[128:131]\n\t" \
"ds_read_b128 v[104:107], %[lz2] offset:49152\n\t" \
"v_mul_f32 v136, %[scale], v136\n\t" \
"v_mfma_f32_16x16x32_bf16 v[132:135], a[100:103], v[68:71], v[132:135]\n\t" \
"ds_read_b128 v[108:111], %[lz3] offset:49152\n\t" \
"v_mul_f32 v137, %[scale], v137\n\t" \
"v_mfma_f32_16x16x32_bf16 v[128:131], a[72:75], v[72:75], v[128:131]\n\t" \
"ds_read_b128 v[112:115], %[lz0] offset:49408\n\t" \
"v_mul_f32 v138, %[scale], v138\n\t" \
"v_mfma_f32_16x16x32_bf16 v[132:135], a[104:107], v[72:75], v[132:135]\n\t" \
"ds_read_b128 v[116:119], %[lz1] offset:49408\n\t" \
"v_mul_f32 v139, %[scale], v139\n\t" \
"v_mfma_f32_16x16x32_bf16 v[128:131], a[76:79], v[76:79], v[128:131]\n\t" \
"ds_read_b128 v[120:123], %[lz2] offset:49408\n\t" \
"v_mul_f32 v140, %[scale], v140\n\t" \
"v_mfma_f32_16x16x32_bf16 v[132:135], a[108:111], v[76:79], v[132:135]\n\t" \
"ds_read_b128 v[124:127], %[lz3] offset:49408\n\t" \
"v_mul_f32 v141, %[scale], v141\n\t" \
"v_mfma_f32_16x16x32_bf16 v[128:131], a[80:83], v[80:83], v[128:131]\n\t" \
"v_mul_f32 v142, %[scale], v142\n\t" \
"v_mul_f32 v143, %[scale], v143\n\t" \
"v_mfma_f32_16x16x32_bf16 v[132:135], a[112:115], v[80:83], v[132:135]\n\t" \
"v_cvt_pk_bf16_f32 v148, v136, v137\n\t" \
"v_cvt_pk_bf16_f32 v150, v138, v139\n\t" \
"v_mfma_f32_16x16x32_bf16 v[128:131], a[84:87], v[84:87], v[128:131]\n\t" \
"v_cvt_pk_bf16_f32 v149, v140, v141\n\t" \
"v_cvt_pk_bf16_f32 v151, v142, v143\n\t" \
"v_mfma_f32_16x16x32_bf16 v[132:135], a[116:119], v[84:87], v[132:135]\n\t" \
"buffer_store_dwordx4 v[148:151], %[vst], %[rw], s91 offen sc1\n\t" \
"s_add_u32 s91, s91, %[ss1]\n\t" \
"v_mfma_f32_16x16x32_bf16 v[128:131], a[88:91], v[88:91], v[128:131]\n\t" \
"v_mfma_f32_16x16x32_bf16 v[132:135], a[120:123], v[88:91], v[132:135]\n\t" \
"v_mfma_f32_16x16x32_bf16 v[128:131], a[92:95], v[92:95], v[128:131]\n\t" \
"v_mfma_f32_16x16x32_bf16 v[132:135], a[124:127], v[92:95], v[132:135]\n\t" \
"s_waitcnt lgkmcnt(0)\n\t" \
"v_mfma_f32_16x16x32_bf16 v[136:139], a[64:67], v[96:99], 0\n\t" \
"v_mfma_f32_16x16x32_bf16 v[140:143], a[96:99], v[96:99], 0\n\t" \
"v_mfma_f32_16x16x32_bf16 v[136:139], a[68:71], v[100:103], v[136:139]\n\t" \
"s_nop 2\n\t" \
"v_mul_f32 v128, %[scale], v128\n\t" \
"v_mfma_f32_16x16x32_bf16 v[140:143], a[100:103], v[100:103], v[140:143]\n\t" \
"v_mul_f32 v129, %[scale], v129\n\t" \
"v_mfma_f32_16x16x32_bf16 v[136:139], a[72:75], v[104:107], v[136:139]\n\t" \
"v_mul_f32 v130, %[scale], v130\n\t" \
"v_mfma_f32_16x16x32_bf16 v[140:143], a[104:107], v[104:107], v[140:143]\n\t" \
"v_mul_f32 v131, %[scale], v131\n\t" \
"v_mfma_f32_16x16x32_bf16 v[136:139], a[76:79], v[108:111], v[136:139]\n\t" \
"v_mul_f32 v132, %[scale], v132\n\t" \
"v_mfma_f32_16x16x32_bf16 v[140:143], a[108:111], v[108:111], v[140:143]\n\t" \
"v_mul_f32 v133, %[scale], v133\n\t" \
"v_mfma_f32_16x16x32_bf16 v[136:139], a[80:83], v[112:115], v[136:139]\n\t" \
"v_mul_f32 v134, %[scale], v134\n\t" \
"v_mul_f32 v135, %[scale], v135\n\t" \
"v_mfma_f32_16x16x32_bf16 v[140:143], a[112:115], v[112:115], v[140:143]\n\t" \
"v_cvt_pk_bf16_f32 v144, v128, v129\n\t" \
"v_cvt_pk_bf16_f32 v146, v130, v131\n\t" \
"v_mfma_f32_16x16x32_bf16 v[136:139], a[84:87], v[116:119], v[136:139]\n\t" \
"v_cvt_pk_bf16_f32 v145, v132, v133\n\t" \
"v_cvt_pk_bf16_f32 v147, v134, v135\n\t" \
"v_mfma_f32_16x16x32_bf16 v[140:143], a[116:119], v[116:119], v[140:143]\n\t" \
"buffer_store_dwordx4 v[144:147], %[vst], %[rw], s91 offen sc1\n\t" \
"s_add_u32 s91, s91, %[ss1]\n\t" \
"v_mfma_f32_16x16x32_bf16 v[136:139], a[88:91], v[120:123], v[136:139]\n\t" \
"v_mfma_f32_16x16x32_bf16 v[140:143], a[120:123], v[120:123], v[140:143]\n\t" \
"v_mfma_f32_16x16x32_bf16 v[136:139], a[92:95], v[124:127], v[136:139]\n\t" \
"v_mfma_f32_16x16x32_bf16 v[140:143], a[124:127], v[124:127], v[140:143]\n\t" \
"s_nop 6\n\t" \
"v_mul_f32 v136, %[scale], v136\n\t" \
"v_mul_f32 v137, %[scale], v137\n\t" \
"v_mul_f32 v138, %[scale], v138\n\t" \
"v_mul_f32 v139, %[scale], v139\n\t" \
"v_mul_f32 v140, %[scale], v140\n\t" \
"v_mul_f32 v141, %[scale], v141\n\t" \
"v_mul_f32 v142, %[scale], v142\n\t" \
"v_mul_f32 v143, %[scale], v143\n\t" \
"v_cvt_pk_bf16_f32 v148, v136, v137\n\t" \
"v_cvt_pk_bf16_f32 v150, v138, v139\n\t" \
"v_cvt_pk_bf16_f32 v149, v140, v141\n\t" \
"v_cvt_pk_bf16_f32 v151, v142, v143\n\t" \
"buffer_store_dwordx4 v[148:151], %[vst], %[rw], s91 offen sc1\n\t" \
"s_add_u32 s91, s91, %[ss1]\n\t" \
"s_waitcnt vmcnt(0) lgkmcnt(0)\n\t" \
"s_barrier\n\t"

#define ASM_P1M20_CLOB "v64", "v65", "v66", "v67", "v68", "v69", "v70", "v71", "v72", "v73", "v74", "v75", "v76", "v77", "v78", "v79", "v80", "v81", "v82", "v83", "v84", "v85", "v86", "v87", "v88", "v89", "v90", "v91", "v92", "v93", "v94", "v95", "v96", "v97", "v98", "v99", "v100", "v101", "v102", "v103", "v104", "v105", "v106", "v107", "v108", "v109", "v110", "v111", "v112", "v113", "v114", "v115", "v116", "v117", "v118", "v119", "v120", "v121", "v122", "v123", "v124", "v125", "v126", "v127", "v128", "v129", "v130", "v131", "v132", "v133", "v134", "v135", "v136", "v137", "v138", "v139", "v140", "v141", "v142", "v143", "v144", "v145", "v146", "v147", "v148", "v149", "v150", "v151", "a0", "a1", "a2", "a3", "a4", "a5", "a6", "a7", "a8", "a9", "a10", "a11", "a12", "a13", "a14", "a15", "a16", "a17", "a18", "a19", "a20", "a21", "a22", "a23", "a24", "a25", "a26", "a27", "a28", "a29", "a30", "a31", "a32", "a33", "a34", "a35", "a36", "a37", "a38", "a39", "a40", "a41", "a42", "a43", "a44", "a45", "a46", "a47", "a48", "a49", "a50", "a51", "a52", "a53", "a54", "a55", "a56", "a57", "a58", "a59", "a60", "a61", "a62", "a63", "a64", "a65", "a66", "a67", "a68", "a69", "a70", "a71", "a72", "a73", "a74", "a75", "a76", "a77", "a78", "a79", "a80", "a81", "a82", "a83", "a84", "a85", "a86", "a87", "a88", "a89", "a90", "a91", "a92", "a93", "a94", "a95", "a96", "a97", "a98", "a99", "a100", "a101", "a102", "a103", "a104", "a105", "a106", "a107", "a108", "a109", "a110", "a111", "a112", "a113", "a114", "a115", "a116", "a117", "a118", "a119", "a120", "a121", "a122", "a123", "a124", "a125", "a126", "a127", "s90", "s91", "scc", "memory"

#define ASM_P1M21_TEXT \
"ds_read_b64_tr_b16 v[64:65], %[lz0] offset:0\n\t" \
"ds_read_b64_tr_b16 v[66:67], %[lz0] offset:896\n\t" \
"ds_read_b64_tr_b16 v[68:69], %[lz0] offset:7168\n\t" \
"ds_read_b64_tr_b16 v[70:71], %[lz0] offset:8064\n\t" \
"ds_read_b64_tr_b16 v[72:73], %[lz0] offset:14336\n\t" \
"ds_read_b64_tr_b16 v[74:75], %[lz0] offset:15232\n\t" \
"ds_read_b64_tr_b16 v[76:77], %[lz0] offset:21504\n\t" \
"ds_read_b64_tr_b16 v[78:79], %[lz0] offset:22400\n\t" \
"ds_read_b64_tr_b16 v[80:81], %[lz0] offset:28672\n\t" \
"ds_read_b64_tr_b16 v[82:83], %[lz0] offset:29568\n\t" \
"ds_read_b64_tr_b16 v[84:85], %[lz0] offset:35840\n\t" \
"ds_read_b64_tr_b16 v[86:87], %[lz0] offset:36736\n\t" \
"ds_read_b64_tr_b16 v[88:89], %[lz0] offset:43008\n\t" \
"ds_read_b64_tr_b16 v[90:91], %[lz0] offset:43904\n\t" \
"ds_read_b64_tr_b16 v[92:93], %[lz0] offset:50176\n\t" \
"ds_read_b64_tr_b16 v[94:95], %[lz0] offset:51072\n\t" \
"s_waitcnt vmcnt(16)\n\t" \
"s_waitcnt lgkmcnt(0)\n\t" \
"v_mfma_f32_16x16x32_bf16 v[128:131], a[0:3], v[64:67], 0\n\t" \
"ds_read_b64_tr_b16 v[96:97], %[lz0] offset:32\n\t" \
"ds_read_b64_tr_b16 v[98:99], %[lz0] offset:928\n\t" \
"v_mfma_f32_16x16x32_bf16 v[132:135], a[32:35], v[64:67], 0\n\t" \
"ds_read_b64_tr_b16 v[100:101], %[lz0] offset:7200\n\t" \
"ds_read_b64_tr_b16 v[102:103], %[lz0] offset:8096\n\t" \
"v_mfma_f32_16x16x32_bf16 v[128:131], a[4:7], v[68:71], v[128:131]\n\t" \
"ds_read_b64_tr_b16 v[104:105], %[lz0] offset:14368\n\t" \
"ds_read_b64_tr_b16 v[106:107], %[lz0] offset:15264\n\t" \
"v_mfma_f32_16x16x32_bf16 v[132:135], a[36:39], v[68:71], v[132:135]\n\t" \
"ds_read_b64_tr_b16 v[108:109], %[lz0] offset:21536\n\t" \
"ds_read_b64_tr_b16 v[110:111], %[lz0] offset:22432\n\t" \
"v_mfma_f32_16x16x32_bf16 v[128:131], a[8:11], v[72:75], v[128:131]\n\t" \
"ds_read_b64_tr_b16 v[112:113], %[lz0] offset:28704\n\t" \
"ds_read_b64_tr_b16 v[114:115], %[lz0] offset:29600\n\t" \
"v_mfma_f32_16x16x32_bf16 v[132:135], a[40:43], v[72:75], v[132:135]\n\t" \
"ds_read_b64_tr_b16 v[116:117], %[lz0] offset:35872\n\t" \
"ds_read_b64_tr_b16 v[118:119], %[lz0] offset:36768\n\t" \
"v_mfma_f32_16x16x32_bf16 v[128:131], a[12:15], v[76:79], v[128:131]\n\t" \
"ds_read_b64_tr_b16 v[120:121], %[lz0] offset:43040\n\t" \
"ds_read_b64_tr_b16 v[122:123], %[lz0] offset:43936\n\t" \
"v_mfma_f32_16x16x32_bf16 v[132:135], a[44:47], v[76:79], v[132:135]\n\t" \
"ds_read_b64_tr_b16 v[124:125], %[lz0] offset:50208\n\t" \
"ds_read_b64_tr_b16 v[126:127], %[lz0] offset:51104\n\t" \
"v_mfma_f32_16x16x32_bf16 v[128:131], a[16:19], v[80:83], v[128:131]\n\t" \
"v_mfma_f32_16x16x32_bf16 v[132:135], a[48:51], v[80:83], v[132:135]\n\t" \
"v_mfma_f32_16x16x32_bf16 v[128:131], a[20:23], v[84:87], v[128:131]\n\t" \
"v_mfma_f32_16x16x32_bf16 v[132:135], a[52:55], v[84:87], v[132:135]\n\t" \
"v_mfma_f32_16x16x32_bf16 v[128:131], a[24:27], v[88:91], v[128:131]\n\t" \
"v_mfma_f32_16x16x32_bf16 v[132:135], a[56:59], v[88:91], v[132:135]\n\t" \
"v_mfma_f32_16x16x32_bf16 v[128:131], a[28:31], v[92:95], v[128:131]\n\t" \
"v_mfma_f32_16x16x32_bf16 v[132:135], a[60:63], v[92:95], v[132:135]\n\t" \
"s_waitcnt lgkmcnt(0)\n\t" \
"v_mfma_f32_16x16x32_bf16 v[136:139], a[0:3], v[96:99], 0\n\t" \
"ds_read_b64_tr_b16 v[64:65], %[lz0] offset:64\n\t" \
"ds_read_b64_tr_b16 v[66:67], %[lz0] offset:960\n\t" \
"v_mfma_f32_16x16x32_bf16 v[140:143], a[32:35], v[96:99], 0\n\t" \
"ds_read_b64_tr_b16 v[68:69], %[lz0] offset:7232\n\t" \
"ds_read_b64_tr_b16 v[70:71], %[lz0] offset:8128\n\t" \
"v_mfma_f32_16x16x32_bf16 v[136:139], a[4:7], v[100:103], v[136:139]\n\t" \
"ds_read_b64_tr_b16 v[72:73], %[lz0] offset:14400\n\t" \
"ds_read_b64_tr_b16 v[74:75], %[lz0] offset:15296\n\t" \
"s_mov_b32 s91, %[sb0]\n\t" \
"v_mfma_f32_16x16x32_bf16 v[140:143], a[36:39], v[100:103], v[140:143]\n\t" \
"ds_read_b64_tr_b16 v[76:77], %[lz0] offset:21568\n\t" \
"ds_read_b64_tr_b16 v[78:79], %[lz0] offset:22464\n\t" \
"v_mul_f32 v128, %[scale], v128\n\t" \
"v_mfma_f32_16x16x32_bf16 v[136:139], a[8:11], v[104:107], v[136:139]\n\t" \
"ds_read_b64_tr_b16 v[80:81], %[lz0] offset:28736\n\t" \
"ds_read_b64_tr_b16 v[82:83], %[lz0] offset:29632\n\t" \
"v_mul_f32 v129, %[scale], v129\n\t" \
"v_mfma_f32_16x16x32_bf16 v[140:143], a[40:43], v[104:107], v[140:143]\n\t" \
"ds_read_b64_tr_b16 v[84:85], %[lz0] offset:35904\n\t" \
"ds_read_b64_tr_b16 v[86:87], %[lz0] offset:36800\n\t" \
"v_mul_f32 v130, %[scale], v130\n\t" \
"v_mfma_f32_16x16x32_bf16 v[136:139], a[12:15], v[108:111], v[136:139]\n\t" \
"ds_read_b64_tr_b16 v[88:89], %[lz0] offset:43072\n\t" \
"ds_read_b64_tr_b16 v[90:91], %[lz0] offset:43968\n\t" \
"v_mul_f32 v131, %[scale], v131\n\t" \
"v_mfma_f32_16x16x32_bf16 v[140:143], a[44:47], v[108:111], v[140:143]\n\t" \
"ds_read_b64_tr_b16 v[92:93], %[lz0] offset:50240\n\t" \
"ds_read_b64_tr_b16 v[94:95], %[lz0] offset:51136\n\t" \
"v_mul_f32 v132, %[scale], v132\n\t" \
"v_mfma_f32_16x16x32_bf16 v[136:139], a[16:19], v[112:115], v[136:139]\n\t" \
"v_mul_f32 v133, %[scale], v133\n\t" \
"v_mul_f32 v134, %[scale], v134\n\t" \
"v_mfma_f32_16x16x32_bf16 v[140:143], a[48:51], v[112:115], v[140:143]\n\t" \
"v_mul_f32 v135, %[scale], v135\n\t" \
"v_cvt_pk_bf16_f32 v144, v128, v129\n\t" \
"v_mfma_f32_16x16x32_bf16 v[136:139], a[20:23], v[116:119], v[136:139]\n\t" \
"v_cvt_pk_bf16_f32 v146, v130, v131\n\t" \
"v_cvt_pk_bf16_f32 v145, v132, v133\n\t" \
"v_mfma_f32_16x16x32_bf16 v[140:143], a[52:55], v[116:119], v[140:143]\n\t" \
"v_cvt_pk_bf16_f32 v147, v134, v135\n\t" \
"buffer_store_dwordx4 v[144:147], %[vst], %[rw], s91 offen sc1\n\t" \
"s_add_u32 s91, s91, %[ss0]\n\t" \
"v_mfma_f32_16x16x32_bf16 v[136:139], a[24:27], v[120:123], v[136:139]\n\t" \
"v_mfma_f32_16x16x32_bf16 v[140:143], a[56:59], v[120:123], v[140:143]\n\t" \
"v_mfma_f32_16x16x32_bf16 v[136:139], a[28:31], v[124:127], v[136:139]\n\t" \
"v_mfma_f32_16x16x32_bf16 v[140:143], a[60:63], v[124:127], v[140:143]\n\t" \
"s_waitcnt lgkmcnt(0)\n\t" \
"v_mfma_f32_16x16x32_bf16 v[128:131], a[0:3], v[64:67], 0\n\t" \
"ds_read_b64_tr_b16 v[96:97], %[lz0] offset:96\n\t" \
"ds_read_b64_tr_b16 v[98:99], %[lz0] offset:992\n\t" \
"v_mfma_f32_16x16x32_bf16 v[132:135], a[32:35], v[64:67], 0\n\t" \
"ds_read_b64_tr_b16 v[100:101], %[lz0] offset:7264\n\t" \
"ds_read_b64_tr_b16 v[102:103], %[lz0] offset:8160\n\t" \
"v_mfma_f32_16x16x32_bf16 v[128:131], a[4:7], v[68:71], v[128:131]\n\t" \
"ds_read_b64_tr_b16 v[104:105], %[lz0] offset:14432\n\t" \
"ds_read_b64_tr_b16 v[106:107], %[lz0] offset:15328\n\t" \
"v_mul_f32 v136, %[scale], v136\n\t" \
"v_mfma_f32_16x16x32_bf16 v[132:135], a[36:39], v[68:71], v[132:135]\n\t" \
"ds_read_b64_tr_b16 v[108:109], %[lz0] offset:21600\n\t" \
"ds_read_b64_tr_b16 v[110:111], %[lz0] offset:22496\n\t" \
"v_mul_f32 v137, %[scale], v137\n\t" \
"v_mfma_f32_16x16x32_bf16 v[128:131], a[8:11], v[72:75], v[128:131]\n\t" \
"ds_read_b64_tr_b16 v[112:113], %[lz0] offset:28768\n\t" \
"ds_read_b64_tr_b16 v[114:115], %[lz0] offset:29664\n\t" \
"v_mul_f32 v138, %[scale], v138\n\t" \
"v_mfma_f32_16x16x32_bf16 v[132:135], a[40:43], v[72:75], v[132:135]\n\t" \
"ds_read_b64_tr_b16 v[116:117], %[lz0] offset:35936\n\t" \
"ds_read_b64_tr_b16 v[118:119], %[lz0] offset:36832\n\t" \
"v_mul_f32 v139, %[scale], v139\n\t" \
"v_mfma_f32_16x16x32_bf16 v[128:131], a[12:15], v[76:79], v[128:131]\n\t" \
"ds_read_b64_tr_b16 v[120:121], %[lz0] offset:43104\n\t" \
"ds_read_b64_tr_b16 v[122:123], %[lz0] offset:44000\n\t" \
"v_mul_f32 v140, %[scale], v140\n\t" \
"v_mfma_f32_16x16x32_bf16 v[132:135], a[44:47], v[76:79], v[132:135]\n\t" \
"ds_read_b64_tr_b16 v[124:125], %[lz0] offset:50272\n\t" \
"ds_read_b64_tr_b16 v[126:127], %[lz0] offset:51168\n\t" \
"v_mul_f32 v141, %[scale], v141\n\t" \
"v_mfma_f32_16x16x32_bf16 v[128:131], a[16:19], v[80:83], v[128:131]\n\t" \
"v_mul_f32 v142, %[scale], v142\n\t" \
"v_mul_f32 v143, %[scale], v143\n\t" \
"v_mfma_f32_16x16x32_bf16 v[132:135], a[48:51], v[80:83], v[132:135]\n\t" \
"v_cvt_pk_bf16_f32 v148, v136, v137\n\t" \
"v_cvt_pk_bf16_f32 v150, v138, v139\n\t" \
"v_mfma_f32_16x16x32_bf16 v[128:131], a[20:23], v[84:87], v[128:131]\n\t" \
"v_cvt_pk_bf16_f32 v149, v140, v141\n\t" \
"v_cvt_pk_bf16_f32 v151, v142, v143\n\t" \
"v_mfma_f32_16x16x32_bf16 v[132:135], a[52:55], v[84:87], v[132:135]\n\t" \
"buffer_store_dwordx4 v[148:151], %[vst], %[rw], s91 offen sc1\n\t" \
"s_add_u32 s91, s91, %[ss0]\n\t" \
"v_mfma_f32_16x16x32_bf16 v[128:131], a[24:27], v[88:91], v[128:131]\n\t" \
"v_mfma_f32_16x16x32_bf16 v[132:135], a[56:59], v[88:91], v[132:135]\n\t" \
"v_mfma_f32_16x16x32_bf16 v[128:131], a[28:31], v[92:95], v[128:131]\n\t" \
"v_mfma_f32_16x16x32_bf16 v[132:135], a[60:63], v[92:95], v[132:135]\n\t" \
"s_waitcnt lgkmcnt(0)\n\t" \
"v_mfma_f32_16x16x32_bf16 v[136:139], a[0:3], v[96:99], 0\n\t" \
"ds_read_b64_tr_b16 v[64:65], %[lz0] offset:128\n\t" \
"ds_read_b64_tr_b16 v[66:67], %[lz0] offset:1024\n\t" \
"v_mfma_f32_16x16x32_bf16 v[140:143], a[32:35], v[96:99], 0\n\t" \
"ds_read_b64_tr_b16 v[68:69], %[lz0] offset:7296\n\t" \
"ds_read_b64_tr_b16 v[70:71], %[lz0] offset:8192\n\t" \
"v_mfma_f32_16x16x32_bf16 v[136:139], a[4:7], v[100:103], v[136:139]\n\t" \
"ds_read_b64_tr_b16 v[72:73], %[lz0] offset:14464\n\t" \
"ds_read_b64_tr_b16 v[74:75], %[lz0] offset:15360\n\t" \
"v_mul_f32 v128, %[scale], v128\n\t" \
"v_mfma_f32_16x16x32_bf16 v[140:143], a[36:39], v[100:103], v[140:143]\n\t" \
"ds_read_b64_tr_b16 v[76:77], %[lz0] offset:21632\n\t" \
"ds_read_b64_tr_b16 v[78:79], %[lz0] offset:22528\n\t" \
"v_mul_f32 v129, %[scale], v129\n\t" \
"v_mfma_f32_16x16x32_bf16 v[136:139], a[8:11], v[104:107], v[136:139]\n\t" \
"ds_read_b64_tr_b16 v[80:81], %[lz0] offset:28800\n\t" \
"ds_read_b64_tr_b16 v[82:83], %[lz0] offset:29696\n\t" \
"v_mul_f32 v130, %[scale], v130\n\t" \
"v_mfma_f32_16x16x32_bf16 v[140:143], a[40:43], v[104:107], v[140:143]\n\t" \
"ds_read_b64_tr_b16 v[84:85], %[lz0] offset:35968\n\t" \
"ds_read_b64_tr_b16 v[86:87], %[lz0] offset:36864\n\t" \
"v_mul_f32 v131, %[scale], v131\n\t" \
"v_mfma_f32_16x16x32_bf16 v[136:139], a[12:15], v[108:111], v[136:139]\n\t" \
"ds_read_b64_tr_b16 v[88:89], %[lz0] offset:43136\n\t" \
"ds_read_b64_tr_b16 v[90:91], %[lz0] offset:44032\n\t" \
"v_mul_f32 v132, %[scale], v132\n\t" \
"v_mfma_f32_16x16x32_bf16 v[140:143], a[44:47], v[108:111], v[140:143]\n\t" \
"ds_read_b64_tr_b16 v[92:93], %[lz0] offset:50304\n\t" \
"ds_read_b64_tr_b16 v[94:95], %[lz0] offset:51200\n\t" \
"v_mul_f32 v133, %[scale], v133\n\t" \
"v_mfma_f32_16x16x32_bf16 v[136:139], a[16:19], v[112:115], v[136:139]\n\t" \
"v_mul_f32 v134, %[scale], v134\n\t" \
"v_mul_f32 v135, %[scale], v135\n\t" \
"v_mfma_f32_16x16x32_bf16 v[140:143], a[48:51], v[112:115], v[140:143]\n\t" \
"v_cvt_pk_bf16_f32 v144, v128, v129\n\t" \
"v_cvt_pk_bf16_f32 v146, v130, v131\n\t" \
"v_mfma_f32_16x16x32_bf16 v[136:139], a[20:23], v[116:119], v[136:139]\n\t" \
"v_cvt_pk_bf16_f32 v145, v132, v133\n\t" \
"v_cvt_pk_bf16_f32 v147, v134, v135\n\t" \
"v_mfma_f32_16x16x32_bf16 v[140:143], a[52:55], v[116:119], v[140:143]\n\t" \
"buffer_store_dwordx4 v[144:147], %[vst], %[rw], s91 offen sc1\n\t" \
"s_add_u32 s91, s91, %[ss0]\n\t" \
"v_mfma_f32_16x16x32_bf16 v[136:139], a[24:27], v[120:123], v[136:139]\n\t" \
"v_mfma_f32_16x16x32_bf16 v[140:143], a[56:59], v[120:123], v[140:143]\n\t" \
"v_mfma_f32_16x16x32_bf16 v[136:139], a[28:31], v[124:127], v[136:139]\n\t" \
"v_mfma_f32_16x16x32_bf16 v[140:143], a[60:63], v[124:127], v[140:143]\n\t" \
"s_waitcnt lgkmcnt(0)\n\t" \
"v_mfma_f32_16x16x32_bf16 v[128:131], a[0:3], v[64:67], 0\n\t" \
"ds_read_b64_tr_b16 v[96:97], %[lz0] offset:160\n\t" \
"ds_read_b64_tr_b16 v[98:99], %[lz0] offset:1056\n\t" \
"v_mfma_f32_16x16x32_bf16 v[132:135], a[32:35], v[64:67], 0\n\t" \
"ds_read_b64_tr_b16 v[100:101], %[lz0] offset:7328\n\t" \
"ds_read_b64_tr_b16 v[102:103], %[lz0] offset:8224\n\t" \
"v_mfma_f32_16x16x32_bf16 v[128:131], a[4:7], v[68:71], v[128:131]\n\t" \
"ds_read_b64_tr_b16 v[104:105], %[lz0] offset:14496\n\t" \
"ds_read_b64_tr_b16 v[106:107], %[lz0] offset:15392\n\t" \
"v_mul_f32 v136, %[scale], v136\n\t" \
"v_mfma_f32_16x16x32_bf16 v[132:135], a[36:39], v[68:71], v[132:135]\n\t" \
"ds_read_b64_tr_b16 v[108:109], %[lz0] offset:21664\n\t" \
"ds_read_b64_tr_b16 v[110:111], %[lz0] offset:22560\n\t" \
"v_mul_f32 v137, %[scale], v137\n\t" \
"v_mfma_f32_16x16x32_bf16 v[128:131], a[8:11], v[72:75], v[128:131]\n\t" \
"ds_read_b64_tr_b16 v[112:113], %[lz0] offset:28832\n\t" \
"ds_read_b64_tr_b16 v[114:115], %[lz0] offset:29728\n\t" \
"v_mul_f32 v138, %[scale], v138\n\t" \
"v_mfma_f32_16x16x32_bf16 v[132:135], a[40:43], v[72:75], v[132:135]\n\t" \
"ds_read_b64_tr_b16 v[116:117], %[lz0] offset:36000\n\t" \
"ds_read_b64_tr_b16 v[118:119], %[lz0] offset:36896\n\t" \
"v_mul_f32 v139, %[scale], v139\n\t" \
"v_mfma_f32_16x16x32_bf16 v[128:131], a[12:15], v[76:79], v[128:131]\n\t" \
"ds_read_b64_tr_b16 v[120:121], %[lz0] offset:43168\n\t" \
"ds_read_b64_tr_b16 v[122:123], %[lz0] offset:44064\n\t" \
"v_mul_f32 v140, %[scale], v140\n\t" \
"v_mfma_f32_16x16x32_bf16 v[132:135], a[44:47], v[76:79], v[132:135]\n\t" \
"ds_read_b64_tr_b16 v[124:125], %[lz0] offset:50336\n\t" \
"ds_read_b64_tr_b16 v[126:127], %[lz0] offset:51232\n\t" \
"v_mul_f32 v141, %[scale], v141\n\t" \
"v_mfma_f32_16x16x32_bf16 v[128:131], a[16:19], v[80:83], v[128:131]\n\t" \
"v_mul_f32 v142, %[scale], v142\n\t" \
"v_mul_f32 v143, %[scale], v143\n\t" \
"v_mfma_f32_16x16x32_bf16 v[132:135], a[48:51], v[80:83], v[132:135]\n\t" \
"v_cvt_pk_bf16_f32 v148, v136, v137\n\t" \
"v_cvt_pk_bf16_f32 v150, v138, v139\n\t" \
"v_mfma_f32_16x16x32_bf16 v[128:131], a[20:23], v[84:87], v[128:131]\n\t" \
"v_cvt_pk_bf16_f32 v149, v140, v141\n\t" \
"v_cvt_pk_bf16_f32 v151, v142, v143\n\t" \
"v_mfma_f32_16x16x32_bf16 v[132:135], a[52:55], v[84:87], v[132:135]\n\t" \
"buffer_store_dwordx4 v[148:151], %[vst], %[rw], s91 offen sc1\n\t" \
"s_add_u32 s91, s91, %[ss0]\n\t" \
"v_mfma_f32_16x16x32_bf16 v[128:131], a[24:27], v[88:91], v[128:131]\n\t" \
"v_mfma_f32_16x16x32_bf16 v[132:135], a[56:59], v[88:91], v[132:135]\n\t" \
"v_mfma_f32_16x16x32_bf16 v[128:131], a[28:31], v[92:95], v[128:131]\n\t" \
"v_mfma_f32_16x16x32_bf16 v[132:135], a[60:63], v[92:95], v[132:135]\n\t" \
"s_waitcnt lgkmcnt(0)\n\t" \
"v_mfma_f32_16x16x32_bf16 v[136:139], a[0:3], v[96:99], 0\n\t" \
"ds_read_b64_tr_b16 v[64:65], %[lz0] offset:192\n\t" \
"ds_read_b64_tr_b16 v[66:67], %[lz0] offset:1088\n\t" \
"v_mfma_f32_16x16x32_bf16 v[140:143], a[32:35], v[96:99], 0\n\t" \
"ds_read_b64_tr_b16 v[68:69], %[lz0] offset:7360\n\t" \
"ds_read_b64_tr_b16 v[70:71], %[lz0] offset:8256\n\t" \
"v_mfma_f32_16x16x32_bf16 v[136:139], a[4:7], v[100:103], v[136:139]\n\t" \
"ds_read_b64_tr_b16 v[72:73], %[lz0] offset:14528\n\t" \
"ds_read_b64_tr_b16 v[74:75], %[lz0] offset:15424\n\t" \
"v_mul_f32 v128, %[scale], v128\n\t" \
"v_mfma_f32_16x16x32_bf16 v[140:143], a[36:39], v[100:103], v[140:143]\n\t" \
"ds_read_b64_tr_b16 v[76:77], %[lz0] offset:21696\n\t" \
"ds_read_b64_tr_b16 v[78:79], %[lz0] offset:22592\n\t" \
"v_mul_f32 v129, %[scale], v129\n\t" \
"v_mfma_f32_16x16x32_bf16 v[136:139], a[8:11], v[104:107], v[136:139]\n\t" \
"ds_read_b64_tr_b16 v[80:81], %[lz0] offset:28864\n\t" \
"ds_read_b64_tr_b16 v[82:83], %[lz0] offset:29760\n\t" \
"v_mul_f32 v130, %[scale], v130\n\t" \
"v_mfma_f32_16x16x32_bf16 v[140:143], a[40:43], v[104:107], v[140:143]\n\t" \
"ds_read_b64_tr_b16 v[84:85], %[lz0] offset:36032\n\t" \
"ds_read_b64_tr_b16 v[86:87], %[lz0] offset:36928\n\t" \
"v_mul_f32 v131, %[scale], v131\n\t" \
"v_mfma_f32_16x16x32_bf16 v[136:139], a[12:15], v[108:111], v[136:139]\n\t" \
"ds_read_b64_tr_b16 v[88:89], %[lz0] offset:43200\n\t" \
"ds_read_b64_tr_b16 v[90:91], %[lz0] offset:44096\n\t" \
"v_mul_f32 v132, %[scale], v132\n\t" \
"v_mfma_f32_16x16x32_bf16 v[140:143], a[44:47], v[108:111], v[140:143]\n\t" \
"ds_read_b64_tr_b16 v[92:93], %[lz0] offset:50368\n\t" \
"ds_read_b64_tr_b16 v[94:95], %[lz0] offset:51264\n\t" \
"v_mul_f32 v133, %[scale], v133\n\t" \
"v_mfma_f32_16x16x32_bf16 v[136:139], a[16:19], v[112:115], v[136:139]\n\t" \
"v_mul_f32 v134, %[scale], v134\n\t" \
"v_mul_f32 v135, %[scale], v135\n\t" \
"v_mfma_f32_16x16x32_bf16 v[140:143], a[48:51], v[112:115], v[140:143]\n\t" \
"v_cvt_pk_bf16_f32 v144, v128, v129\n\t" \
"v_cvt_pk_bf16_f32 v146, v130, v131\n\t" \
"v_mfma_f32_16x16x32_bf16 v[136:139], a[20:23], v[116:119], v[136:139]\n\t" \
"v_cvt_pk_bf16_f32 v145, v132, v133\n\t" \
"v_cvt_pk_bf16_f32 v147, v134, v135\n\t" \
"v_mfma_f32_16x16x32_bf16 v[140:143], a[52:55], v[116:119], v[140:143]\n\t" \
"buffer_store_dwordx4 v[144:147], %[vst], %[rw], s91 offen sc1\n\t" \
"s_add_u32 s91, s91, %[ss0]\n\t" \
"v_mfma_f32_16x16x32_bf16 v[136:139], a[24:27], v[120:123], v[136:139]\n\t" \
"v_mfma_f32_16x16x32_bf16 v[140:143], a[56:59], v[120:123], v[140:143]\n\t" \
"v_mfma_f32_16x16x32_bf16 v[136:139], a[28:31], v[124:127], v[136:139]\n\t" \
"v_mfma_f32_16x16x32_bf16 v[140:143], a[60:63], v[124:127], v[140:143]\n\t" \
"s_waitcnt lgkmcnt(0)\n\t" \
"v_mfma_f32_16x16x32_bf16 v[128:131], a[0:3], v[64:67], 0\n\t" \
"ds_read_b64_tr_b16 v[96:97], %[lz0] offset:0\n\t" \
"ds_read_b64_tr_b16 v[98:99], %[lz0] offset:896\n\t" \
"v_mfma_f32_16x16x32_bf16 v[132:135], a[32:35], v[64:67], 0\n\t" \
"ds_read_b64_tr_b16 v[100:101], %[lz0] offset:7168\n\t" \
"ds_read_b64_tr_b16 v[102:103], %[lz0] offset:8064\n\t" \
"v_mfma_f32_16x16x32_bf16 v[128:131], a[4:7], v[68:71], v[128:131]\n\t" \
"ds_read_b64_tr_b16 v[104:105], %[lz0] offset:14336\n\t" \
"ds_read_b64_tr_b16 v[106:107], %[lz0] offset:15232\n\t" \
"v_mul_f32 v136, %[scale], v136\n\t" \
"v_mfma_f32_16x16x32_bf16 v[132:135], a[36:39], v[68:71], v[132:135]\n\t" \
"ds_read_b64_tr_b16 v[108:109], %[lz0] offset:21504\n\t" \
"ds_read_b64_tr_b16 v[110:111], %[lz0] offset:22400\n\t" \
"v_mul_f32 v137, %[scale], v137\n\t" \
"v_mfma_f32_16x16x32_bf16 v[128:131], a[8:11], v[72:75], v[128:131]\n\t" \
"ds_read_b64_tr_b16 v[112:113], %[lz0] offset:28672\n\t" \
"ds_read_b64_tr_b16 v[114:115], %[lz0] offset:29568\n\t" \
"v_mul_f32 v138, %[scale], v138\n\t" \
"v_mfma_f32_16x16x32_bf16 v[132:135], a[40:43], v[72:75], v[132:135]\n\t" \
"ds_read_b64_tr_b16 v[116:117], %[lz0] offset:35840\n\t" \
"ds_read_b64_tr_b16 v[118:119], %[lz0] offset:36736\n\t" \
"v_mul_f32 v139, %[scale], v139\n\t" \
"v_mfma_f32_16x16x32_bf16 v[128:131], a[12:15], v[76:79], v[128:131]\n\t" \
"ds_read_b64_tr_b16 v[120:121], %[lz0] offset:43008\n\t" \
"ds_read_b64_tr_b16 v[122:123], %[lz0] offset:43904\n\t" \
"v_mul_f32 v140, %[scale], v140\n\t" \
"v_mfma_f32_16x16x32_bf16 v[132:135], a[44:47], v[76:79], v[132:135]\n\t" \
"ds_read_b64_tr_b16 v[124:125], %[lz0] offset:50176\n\t" \
"ds_read_b64_tr_b16 v[126:127], %[lz0] offset:51072\n\t" \
"v_mul_f32 v141, %[scale], v141\n\t" \
"v_mfma_f32_16x16x32_bf16 v[128:131], a[16:19], v[80:83], v[128:131]\n\t" \
"v_mul_f32 v142, %[scale], v142\n\t" \
"v_mul_f32 v143, %[scale], v143\n\t" \
"v_mfma_f32_16x16x32_bf16 v[132:135], a[48:51], v[80:83], v[132:135]\n\t" \
"v_cvt_pk_bf16_f32 v148, v136, v137\n\t" \
"v_cvt_pk_bf16_f32 v150, v138, v139\n\t" \
"v_mfma_f32_16x16x32_bf16 v[128:131], a[20:23], v[84:87], v[128:131]\n\t" \
"v_cvt_pk_bf16_f32 v149, v140, v141\n\t" \
"v_cvt_pk_bf16_f32 v151, v142, v143\n\t" \
"v_mfma_f32_16x16x32_bf16 v[132:135], a[52:55], v[84:87], v[132:135]\n\t" \
"buffer_store_dwordx4 v[148:151], %[vst], %[rw], s91 offen sc1\n\t" \
"s_add_u32 s91, s91, %[ss0]\n\t" \
"v_mfma_f32_16x16x32_bf16 v[128:131], a[24:27], v[88:91], v[128:131]\n\t" \
"v_mfma_f32_16x16x32_bf16 v[132:135], a[56:59], v[88:91], v[132:135]\n\t" \
"v_mfma_f32_16x16x32_bf16 v[128:131], a[28:31], v[92:95], v[128:131]\n\t" \
"v_mfma_f32_16x16x32_bf16 v[132:135], a[60:63], v[92:95], v[132:135]\n\t" \
"s_waitcnt vmcnt(6)\n\t" \
"s_waitcnt lgkmcnt(0)\n\t" \
"v_mfma_f32_16x16x32_bf16 v[136:139], a[64:67], v[96:99], 0\n\t" \
"ds_read_b64_tr_b16 v[64:65], %[lz0] offset:32\n\t" \
"ds_read_b64_tr_b16 v[66:67], %[lz0] offset:928\n\t" \
"v_mfma_f32_16x16x32_bf16 v[140:143], a[96:99], v[96:99], 0\n\t" \
"ds_read_b64_tr_b16 v[68:69], %[lz0] offset:7200\n\t" \
"ds_read_b64_tr_b16 v[70:71], %[lz0] offset:8096\n\t" \
"v_mfma_f32_16x16x32_bf16 v[136:139], a[68:71], v[100:103], v[136:139]\n\t" \
"ds_read_b64_tr_b16 v[72:73], %[lz0] offset:14368\n\t" \
"ds_read_b64_tr_b16 v[74:75], %[lz0] offset:15264\n\t" \
"v_mul_f32 v128, %[scale], v128\n\t" \
"v_mfma_f32_16x16x32_bf16 v[140:143], a[100:103], v[100:103], v[140:143]\n\t" \
"ds_read_b64_tr_b16 v[76:77], %[lz0] offset:21536\n\t" \
"ds_read_b64_tr_b16 v[78:79], %[lz0] offset:22432\n\t" \
"v_mul_f32 v129, %[scale], v129\n\t" \
"v_mfma_f32_16x16x32_bf16 v[136:139], a[72:75], v[104:107], v[136:139]\n\t" \
"ds_read_b64_tr_b16 v[80:81], %[lz0] offset:28704\n\t" \
"ds_read_b64_tr_b16 v[82:83], %[lz0] offset:29600\n\t" \
"v_mul_f32 v130, %[scale], v130\n\t" \
"v_mfma_f32_16x16x32_bf16 v[140:143], a[104:107], v[104:107], v[140:143]\n\t" \
"ds_read_b64_tr_b16 v[84:85], %[lz0] offset:35872\n\t" \
"ds_read_b64_tr_b16 v[86:87], %[lz0] offset:36768\n\t" \
"v_mul_f32 v131, %[scale], v131\n\t" \
"v_mfma_f32_16x16x32_bf16 v[136:139], a[76:79], v[108:111], v[136:139]\n\t" \
"ds_read_b64_tr_b16 v[88:89], %[lz0] offset:43040\n\t" \
"ds_read_b64_tr_b16 v[90:91], %[lz0] offset:43936\n\t" \
"v_mul_f32 v132, %[scale], v132\n\t" \
"v_mfma_f32_16x16x32_bf16 v[140:143], a[108:111], v[108:111], v[140:143]\n\t" \
"ds_read_b64_tr_b16 v[92:93], %[lz0] offset:50208\n\t" \
"ds_read_b64_tr_b16 v[94:95], %[lz0] offset:51104\n\t" \
"v_mul_f32 v133, %[scale], v133\n\t" \
"v_mfma_f32_16x16x32_bf16 v[136:139], a[80:83], v[112:115], v[136:139]\n\t" \
"v_mul_f32 v134, %[scale], v134\n\t" \
"v_mul_f32 v135, %[scale], v135\n\t" \
"v_mfma_f32_16x16x32_bf16 v[140:143], a[112:115], v[112:115], v[140:143]\n\t" \
"v_cvt_pk_bf16_f32 v144, v128, v129\n\t" \
"v_cvt_pk_bf16_f32 v146, v130, v131\n\t" \
"v_mfma_f32_16x16x32_bf16 v[136:139], a[84:87], v[116:119], v[136:139]\n\t" \
"v_cvt_pk_bf16_f32 v145, v132, v133\n\t" \
"v_cvt_pk_bf16_f32 v147, v134, v135\n\t" \
"v_mfma_f32_16x16x32_bf16 v[140:143], a[116:119], v[116:119], v[140:143]\n\t" \
"buffer_store_dwordx4 v[144:147], %[vst], %[rw], s91 offen sc1\n\t" \
"s_add_u32 s91, s91, %[ss0]\n\t" \
"v_mfma_f32_16x16x32_bf16 v[136:139], a[88:91], v[120:123], v[136:139]\n\t" \
"v_mfma_f32_16x16x32_bf16 v[140:143], a[120:123], v[120:123], v[140:143]\n\t" \
"v_mfma_f32_16x16x32_bf16 v[136:139], a[92:95], v[124:127], v[136:139]\n\t" \
"v_mfma_f32_16x16x32_bf16 v[140:143], a[124:127], v[124:127], v[140:143]\n\t" \
"s_waitcnt lgkmcnt(0)\n\t" \
"v_mfma_f32_16x16x32_bf16 v[128:131], a[64:67], v[64:67], 0\n\t" \
"ds_read_b64_tr_b16 v[96:97], %[lz0] offset:64\n\t" \
"ds_read_b64_tr_b16 v[98:99], %[lz0] offset:960\n\t" \
"v_mfma_f32_16x16x32_bf16 v[132:135], a[96:99], v[64:67], 0\n\t" \
"ds_read_b64_tr_b16 v[100:101], %[lz0] offset:7232\n\t" \
"ds_read_b64_tr_b16 v[102:103], %[lz0] offset:8128\n\t" \
"v_mfma_f32_16x16x32_bf16 v[128:131], a[68:71], v[68:71], v[128:131]\n\t" \
"ds_read_b64_tr_b16 v[104:105], %[lz0] offset:14400\n\t" \
"ds_read_b64_tr_b16 v[106:107], %[lz0] offset:15296\n\t" \
"s_mov_b32 s91, %[sb1]\n\t" \
"v_mfma_f32_16x16x32_bf16 v[132:135], a[100:103], v[68:71], v[132:135]\n\t" \
"ds_read_b64_tr_b16 v[108:109], %[lz0] offset:21568\n\t" \
"ds_read_b64_tr_b16 v[110:111], %[lz0] offset:22464\n\t" \
"v_mul_f32 v136, %[scale], v136\n\t" \
"v_mfma_f32_16x16x32_bf16 v[128:131], a[72:75], v[72:75], v[128:131]\n\t" \
"ds_read_b64_tr_b16 v[112:113], %[lz0] offset:28736\n\t" \
"ds_read_b64_tr_b16 v[114:115], %[lz0] offset:29632\n\t" \
"v_mul_f32 v137, %[scale], v137\n\t" \
"v_mfma_f32_16x16x32_bf16 v[132:135], a[104:107], v[72:75], v[132:135]\n\t" \
"ds_read_b64_tr_b16 v[116:117], %[lz0] offset:35904\n\t" \
"ds_read_b64_tr_b16 v[118:119], %[lz0] offset:36800\n\t" \
"v_mul_f32 v138, %[scale], v138\n\t" \
"v_mfma_f32_16x16x32_bf16 v[128:131], a[76:79], v[76:79], v[128:131]\n\t" \
"ds_read_b64_tr_b16 v[120:121], %[lz0] offset:43072\n\t" \
"ds_read_b64_tr_b16 v[122:123], %[lz0] offset:43968\n\t" \
"v_mul_f32 v139, %[scale], v139\n\t" \
"v_mfma_f32_16x16x32_bf16 v[132:135], a[108:111], v[76:79], v[132:135]\n\t" \
"ds_read_b64_tr_b16 v[124:125], %[lz0] offset:50240\n\t" \
"ds_read_b64_tr_b16 v[126:127], %[lz0] offset:51136\n\t" \
"v_mul_f32 v140, %[scale], v140\n\t" \
"v_mfma_f32_16x16x32_bf16 v[128:131], a[80:83], v[80:83], v[128:131]\n\t" \
"v_mul_f32 v141, %[scale], v141\n\t" \
"v_mul_f32 v142, %[scale], v142\n\t" \
"v_mfma_f32_16x16x32_bf16 v[132:135], a[112:115], v[80:83], v[132:135]\n\t" \
"v_mul_f32 v143, %[scale], v143\n\t" \
"v_cvt_pk_bf16_f32 v148, v136, v137\n\t" \
"v_mfma_f32_16x16x32_bf16 v[128:131], a[84:87], v[84:87], v[128:131]\n\t" \
"v_cvt_pk_bf16_f32 v150, v138, v139\n\t" \
"v_cvt_pk_bf16_f32 v149, v140, v141\n\t" \
"v_mfma_f32_16x16x32_bf16 v[132:135], a[116:119], v[84:87], v[132:135]\n\t" \
"v_cvt_pk_bf16_f32 v151, v142, v143\n\t" \
"buffer_store_dwordx4 v[148:151], %[vst], %[rw], s91 offen sc1\n\t" \
"s_add_u32 s91, s91, %[ss1]\n\t" \
"v_mfma_f32_16x16x32_bf16 v[128:131], a[88:91], v[88:91], v[128:131]\n\t" \
"v_mfma_f32_16x16x32_bf16 v[132:135], a[120:123], v[88:91], v[132:135]\n\t" \
"v_mfma_f32_16x16x32_bf16 v[128:131], a[92:95], v[92:95], v[128:131]\n\t" \
"v_mfma_f32_16x16x32_bf16 v[132:135], a[124:127], v[92:95], v[132:135]\n\t" \
"s_waitcnt lgkmcnt(0)\n\t" \
"v_mfma_f32_16x16x32_bf16 v[136:139], a[64:67], v[96:99], 0\n\t" \
"ds_read_b64_tr_b16 v[64:65], %[lz0] offset:96\n\t" \
"ds_read_b64_tr_b16 v[66:67], %[lz0] offset:992\n\t" \
"v_mfma_f32_16x16x32_bf16 v[140:143], a[96:99], v[96:99], 0\n\t" \
"ds_read_b64_tr_b16 v[68:69], %[lz0] offset:7264\n\t" \
"ds_read_b64_tr_b16 v[70:71], %[lz0] offset:8160\n\t" \
"v_mfma_f32_16x16x32_bf16 v[136:139], a[68:71], v[100:103], v[136:139]\n\t" \
"ds_read_b64_tr_b16 v[72:73], %[lz0] offset:14432\n\t" \
"ds_read_b64_tr_b16 v[74:75], %[lz0] offset:15328\n\t" \
"v_mul_f32 v128, %[scale], v128\n\t" \
"v_mfma_f32_16x16x32_bf16 v[140:143], a[100:103], v[100:103], v[140:143]\n\t" \
"ds_read_b64_tr_b16 v[76:77], %[lz0] offset:21600\n\t" \
"ds_read_b64_tr_b16 v[78:79], %[lz0] offset:22496\n\t" \
"v_mul_f32 v129, %[scale], v129\n\t" \
"v_mfma_f32_16x16x32_bf16 v[136:139], a[72:75], v[104:107], v[136:139]\n\t" \
"ds_read_b64_tr_b16 v[80:81], %[lz0] offset:28768\n\t" \
"ds_read_b64_tr_b16 v[82:83], %[lz0] offset:29664\n\t" \
"v_mul_f32 v130, %[scale], v130\n\t" \
"v_mfma_f32_16x16x32_bf16 v[140:143], a[104:107], v[104:107], v[140:143]\n\t" \
"ds_read_b64_tr_b16 v[84:85], %[lz0] offset:35936\n\t" \
"ds_read_b64_tr_b16 v[86:87], %[lz0] offset:36832\n\t" \
"v_mul_f32 v131, %[scale], v131\n\t" \
"v_mfma_f32_16x16x32_bf16 v[136:139], a[76:79], v[108:111], v[136:139]\n\t" \
"ds_read_b64_tr_b16 v[88:89], %[lz0] offset:43104\n\t" \
"ds_read_b64_tr_b16 v[90:91], %[lz0] offset:44000\n\t" \
"v_mul_f32 v132, %[scale], v132\n\t" \
"v_mfma_f32_16x16x32_bf16 v[140:143], a[108:111], v[108:111], v[140:143]\n\t" \
"ds_read_b64_tr_b16 v[92:93], %[lz0] offset:50272\n\t" \
"ds_read_b64_tr_b16 v[94:95], %[lz0] offset:51168\n\t" \
"v_mul_f32 v133, %[scale], v133\n\t" \
"v_mfma_f32_16x16x32_bf16 v[136:139], a[80:83], v[112:115], v[136:139]\n\t" \
"v_mul_f32 v134, %[scale], v134\n\t" \
"v_mul_f32 v135, %[scale], v135\n\t" \
"v_mfma_f32_16x16x32_bf16 v[140:143], a[112:115], v[112:115], v[140:143]\n\t" \
"v_cvt_pk_bf16_f32 v144, v128, v129\n\t" \
"v_cvt_pk_bf16_f32 v146, v130, v131\n\t" \
"v_mfma_f32_16x16x32_bf16 v[136:139], a[84:87], v[116:119], v[136:139]\n\t" \
"v_cvt_pk_bf16_f32 v145, v132, v133\n\t" \
"v_cvt_pk_bf16_f32 v147, v134, v135\n\t" \
"v_mfma_f32_16x16x32_bf16 v[140:143], a[116:119], v[116:119], v[140:143]\n\t" \
"buffer_store_dwordx4 v[144:147], %[vst], %[rw], s91 offen sc1\n\t" \
"s_add_u32 s91, s91, %[ss1]\n\t" \
"v_mfma_f32_16x16x32_bf16 v[136:139], a[88:91], v[120:123], v[136:139]\n\t" \
"v_mfma_f32_16x16x32_bf16 v[140:143], a[120:123], v[120:123], v[140:143]\n\t" \
"v_mfma_f32_16x16x32_bf16 v[136:139], a[92:95], v[124:127], v[136:139]\n\t" \
"v_mfma_f32_16x16x32_bf16 v[140:143], a[124:127], v[124:127], v[140:143]\n\t" \
"s_waitcnt lgkmcnt(0)\n\t" \
"v_mfma_f32_16x16x32_bf16 v[128:131], a[64:67], v[64:67], 0\n\t" \
"ds_read_b64_tr_b16 v[96:97], %[lz0] offset:128\n\t" \
"ds_read_b64_tr_b16 v[98:99], %[lz0] offset:1024\n\t" \
"v_mfma_f32_16x16x32_bf16 v[132:135], a[96:99], v[64:67], 0\n\t" \
"ds_read_b64_tr_b16 v[100:101], %[lz0] offset:7296\n\t" \
"ds_read_b64_tr_b16 v[102:103], %[lz0] offset:8192\n\t" \
"v_mfma_f32_16x16x32_bf16 v[128:131], a[68:71], v[68:71], v[128:131]\n\t" \
"ds_read_b64_tr_b16 v[104:105], %[lz0] offset:14464\n\t" \
"ds_read_b64_tr_b16 v[106:107], %[lz0] offset:15360\n\t" \
"v_mul_f32 v136, %[scale], v136\n\t" \
"v_mfma_f32_16x16x32_bf16 v[132:135], a[100:103], v[68:71], v[132:135]\n\t" \
"ds_read_b64_tr_b16 v[108:109], %[lz0] offset:21632\n\t" \
"ds_read_b64_tr_b16 v[110:111], %[lz0] offset:22528\n\t" \
"v_mul_f32 v137, %[scale], v137\n\t" \
"v_mfma_f32_16x16x32_bf16 v[128:131], a[72:75], v[72:75], v[128:131]\n\t" \
"ds_read_b64_tr_b16 v[112:113], %[lz0] offset:28800\n\t" \
"ds_read_b64_tr_b16 v[114:115], %[lz0] offset:29696\n\t" \
"v_mul_f32 v138, %[scale], v138\n\t" \
"v_mfma_f32_16x16x32_bf16 v[132:135], a[104:107], v[72:75], v[132:135]\n\t" \
"ds_read_b64_tr_b16 v[116:117], %[lz0] offset:35968\n\t" \
"ds_read_b64_tr_b16 v[118:119], %[lz0] offset:36864\n\t" \
"v_mul_f32 v139, %[scale], v139\n\t" \
"v_mfma_f32_16x16x32_bf16 v[128:131], a[76:79], v[76:79], v[128:131]\n\t" \
"ds_read_b64_tr_b16 v[120:121], %[lz0] offset:43136\n\t" \
"ds_read_b64_tr_b16 v[122:123], %[lz0] offset:44032\n\t" \
"v_mul_f32 v140, %[scale], v140\n\t" \
"v_mfma_f32_16x16x32_bf16 v[132:135], a[108:111], v[76:79], v[132:135]\n\t" \
"ds_read_b64_tr_b16 v[124:125], %[lz0] offset:50304\n\t" \
"ds_read_b64_tr_b16 v[126:127], %[lz0] offset:51200\n\t" \
"v_mul_f32 v141, %[scale], v141\n\t" \
"v_mfma_f32_16x16x32_bf16 v[128:131], a[80:83], v[80:83], v[128:131]\n\t" \
"v_mul_f32 v142, %[scale], v142\n\t" \
"v_mul_f32 v143, %[scale], v143\n\t" \
"v_mfma_f32_16x16x32_bf16 v[132:135], a[112:115], v[80:83], v[132:135]\n\t" \
"v_cvt_pk_bf16_f32 v148, v136, v137\n\t" \
"v_cvt_pk_bf16_f32 v150, v138, v139\n\t" \
"v_mfma_f32_16x16x32_bf16 v[128:131], a[84:87], v[84:87], v[128:131]\n\t" \
"v_cvt_pk_bf16_f32 v149, v140, v141\n\t" \
"v_cvt_pk_bf16_f32 v151, v142, v143\n\t" \
"v_mfma_f32_16x16x32_bf16 v[132:135], a[116:119], v[84:87], v[132:135]\n\t" \
"buffer_store_dwordx4 v[148:151], %[vst], %[rw], s91 offen sc1\n\t" \
"s_add_u32 s91, s91, %[ss1]\n\t" \
"v_mfma_f32_16x16x32_bf16 v[128:131], a[88:91], v[88:91], v[128:131]\n\t" \
"v_mfma_f32_16x16x32_bf16 v[132:135], a[120:123], v[88:91], v[132:135]\n\t" \
"v_mfma_f32_16x16x32_bf16 v[128:131], a[92:95], v[92:95], v[128:131]\n\t" \
"v_mfma_f32_16x16x32_bf16 v[132:135], a[124:127], v[92:95], v[132:135]\n\t" \
"s_waitcnt lgkmcnt(0)\n\t" \
"v_mfma_f32_16x16x32_bf16 v[136:139], a[64:67], v[96:99], 0\n\t" \
"ds_read_b64_tr_b16 v[64:65], %[lz0] offset:160\n\t" \
"ds_read_b64_tr_b16 v[66:67], %[lz0] offset:1056\n\t" \
"v_mfma_f32_16x16x32_bf16 v[140:143], a[96:99], v[96:99], 0\n\t" \
"ds_read_b64_tr_b16 v[68:69], %[lz0] offset:7328\n\t" \
"ds_read_b64_tr_b16 v[70:71], %[lz0] offset:8224\n\t" \
"v_mfma_f32_16x16x32_bf16 v[136:139], a[68:71], v[100:103], v[136:139]\n\t" \
"ds_read_b64_tr_b16 v[72:73], %[lz0] offset:14496\n\t" \
"ds_read_b64_tr_b16 v[74:75], %[lz0] offset:15392\n\t" \
"v_mul_f32 v128, %[scale], v128\n\t" \
"v_mfma_f32_16x16x32_bf16 v[140:143], a[100:103], v[100:103], v[140:143]\n\t" \
"ds_read_b64_tr_b16 v[76:77], %[lz0] offset:21664\n\t" \
"ds_read_b64_tr_b16 v[78:79], %[lz0] offset:22560\n\t" \
"v_mul_f32 v129, %[scale], v129\n\t" \
"v_mfma_f32_16x16x32_bf16 v[136:139], a[72:75], v[104:107], v[136:139]\n\t" \
"ds_read_b64_tr_b16 v[80:81], %[lz0] offset:28832\n\t" \
"ds_read_b64_tr_b16 v[82:83], %[lz0] offset:29728\n\t" \
"v_mul_f32 v130, %[scale], v130\n\t" \
"v_mfma_f32_16x16x32_bf16 v[140:143], a[104:107], v[104:107], v[140:143]\n\t" \
"ds_read_b64_tr_b16 v[84:85], %[lz0] offset:36000\n\t" \
"ds_read_b64_tr_b16 v[86:87], %[lz0] offset:36896\n\t" \
"v_mul_f32 v131, %[scale], v131\n\t" \
"v_mfma_f32_16x16x32_bf16 v[136:139], a[76:79], v[108:111], v[136:139]\n\t" \
"ds_read_b64_tr_b16 v[88:89], %[lz0] offset:43168\n\t" \
"ds_read_b64_tr_b16 v[90:91], %[lz0] offset:44064\n\t" \
"v_mul_f32 v132, %[scale], v132\n\t" \
"v_mfma_f32_16x16x32_bf16 v[140:143], a[108:111], v[108:111], v[140:143]\n\t" \
"ds_read_b64_tr_b16 v[92:93], %[lz0] offset:50336\n\t" \
"ds_read_b64_tr_b16 v[94:95], %[lz0] offset:51232\n\t" \
"v_mul_f32 v133, %[scale], v133\n\t" \
"v_mfma_f32_16x16x32_bf16 v[136:139], a[80:83], v[112:115], v[136:139]\n\t" \
"v_mul_f32 v134, %[scale], v134\n\t" \
"v_mul_f32 v135, %[scale], v135\n\t" \
"v_mfma_f32_16x16x32_bf16 v[140:143], a[112:115], v[112:115], v[140:143]\n\t" \
"v_cvt_pk_bf16_f32 v144, v128, v129\n\t" \
"v_cvt_pk_bf16_f32 v146, v130, v131\n\t" \
"v_mfma_f32_16x16x32_bf16 v[136:139], a[84:87], v[116:119], v[136:139]\n\t" \
"v_cvt_pk_bf16_f32 v145, v132, v133\n\t" \
"v_cvt_pk_bf16_f32 v147, v134, v135\n\t" \
"v_mfma_f32_16x16x32_bf16 v[140:143], a[116:119], v[116:119], v[140:143]\n\t" \
"buffer_store_dwordx4 v[144:147], %[vst], %[rw], s91 offen sc1\n\t" \
"s_add_u32 s91, s91, %[ss1]\n\t" \
"v_mfma_f32_16x16x32_bf16 v[136:139], a[88:91], v[120:123], v[136:139]\n\t" \
"v_mfma_f32_16x16x32_bf16 v[140:143], a[120:123], v[120:123], v[140:143]\n\t" \
"v_mfma_f32_16x16x32_bf16 v[136:139], a[92:95], v[124:127], v[136:139]\n\t" \
"v_mfma_f32_16x16x32_bf16 v[140:143], a[124:127], v[124:127], v[140:143]\n\t" \
"s_waitcnt lgkmcnt(0)\n\t" \
"v_mfma_f32_16x16x32_bf16 v[128:131], a[64:67], v[64:67], 0\n\t" \
"ds_read_b64_tr_b16 v[96:97], %[lz0] offset:192\n\t" \
"ds_read_b64_tr_b16 v[98:99], %[lz0] offset:1088\n\t" \
"v_mfma_f32_16x16x32_bf16 v[132:135], a[96:99], v[64:67], 0\n\t" \
"ds_read_b64_tr_b16 v[100:101], %[lz0] offset:7360\n\t" \
"ds_read_b64_tr_b16 v[102:103], %[lz0] offset:8256\n\t" \
"v_mfma_f32_16x16x32_bf16 v[128:131], a[68:71], v[68:71], v[128:131]\n\t" \
"ds_read_b64_tr_b16 v[104:105], %[lz0] offset:14528\n\t" \
"ds_read_b64_tr_b16 v[106:107], %[lz0] offset:15424\n\t" \
"v_mul_f32 v136, %[scale], v136\n\t" \
"v_mfma_f32_16x16x32_bf16 v[132:135], a[100:103], v[68:71], v[132:135]\n\t" \
"ds_read_b64_tr_b16 v[108:109], %[lz0] offset:21696\n\t" \
"ds_read_b64_tr_b16 v[110:111], %[lz0] offset:22592\n\t" \
"v_mul_f32 v137, %[scale], v137\n\t" \
"v_mfma_f32_16x16x32_bf16 v[128:131], a[72:75], v[72:75], v[128:131]\n\t" \
"ds_read_b64_tr_b16 v[112:113], %[lz0] offset:28864\n\t" \
"ds_read_b64_tr_b16 v[114:115], %[lz0] offset:29760\n\t" \
"v_mul_f32 v138, %[scale], v138\n\t" \
"v_mfma_f32_16x16x32_bf16 v[132:135], a[104:107], v[72:75], v[132:135]\n\t" \
"ds_read_b64_tr_b16 v[116:117], %[lz0] offset:36032\n\t" \
"ds_read_b64_tr_b16 v[118:119], %[lz0] offset:36928\n\t" \
"v_mul_f32 v139, %[scale], v139\n\t" \
"v_mfma_f32_16x16x32_bf16 v[128:131], a[76:79], v[76:79], v[128:131]\n\t" \
"ds_read_b64_tr_b16 v[120:121], %[lz0] offset:43200\n\t" \
"ds_read_b64_tr_b16 v[122:123], %[lz0] offset:44096\n\t" \
"v_mul_f32 v140, %[scale], v140\n\t" \
"v_mfma_f32_16x16x32_bf16 v[132:135], a[108:111], v[76:79], v[132:135]\n\t" \
"ds_read_b64_tr_b16 v[124:125], %[lz0] offset:50368\n\t" \
"ds_read_b64_tr_b16 v[126:127], %[lz0] offset:51264\n\t" \
"v_mul_f32 v141, %[scale], v141\n\t" \
"v_mfma_f32_16x16x32_bf16 v[128:131], a[80:83], v[80:83], v[128:131]\n\t" \
"v_mul_f32 v142, %[scale], v142\n\t" \
"v_mul_f32 v143, %[scale], v143\n\t" \
"v_mfma_f32_16x16x32_bf16 v[132:135], a[112:115], v[80:83], v[132:135]\n\t" \
"v_cvt_pk_bf16_f32 v148, v136, v137\n\t" \
"v_cvt_pk_bf16_f32 v150, v138, v139\n\t" \
"v_mfma_f32_16x16x32_bf16 v[128:131], a[84:87], v[84:87], v[128:131]\n\t" \
"v_cvt_pk_bf16_f32 v149, v140, v141\n\t" \
"v_cvt_pk_bf16_f32 v151, v142, v143\n\t" \
"v_mfma_f32_16x16x32_bf16 v[132:135], a[116:119], v[84:87], v[132:135]\n\t" \
"buffer_store_dwordx4 v[148:151], %[vst], %[rw], s91 offen sc1\n\t" \
"s_add_u32 s91, s91, %[ss1]\n\t" \
"v_mfma_f32_16x16x32_bf16 v[128:131], a[88:91], v[88:91], v[128:131]\n\t" \
"v_mfma_f32_16x16x32_bf16 v[132:135], a[120:123], v[88:91], v[132:135]\n\t" \
"v_mfma_f32_16x16x32_bf16 v[128:131], a[92:95], v[92:95], v[128:131]\n\t" \
"v_mfma_f32_16x16x32_bf16 v[132:135], a[124:127], v[92:95], v[132:135]\n\t" \
"s_waitcnt lgkmcnt(0)\n\t" \
"v_mfma_f32_16x16x32_bf16 v[136:139], a[64:67], v[96:99], 0\n\t" \
"v_mfma_f32_16x16x32_bf16 v[140:143], a[96:99], v[96:99], 0\n\t" \
"v_mfma_f32_16x16x32_bf16 v[136:139], a[68:71], v[100:103], v[136:139]\n\t" \
"s_nop 2\n\t" \
"v_mul_f32 v128, %[scale], v128\n\t" \
"v_mfma_f32_16x16x32_bf16 v[140:143], a[100:103], v[100:103], v[140:143]\n\t" \
"v_mul_f32 v129, %[scale], v129\n\t" \
"v_mfma_f32_16x16x32_bf16 v[136:139], a[72:75], v[104:107], v[136:139]\n\t" \
"v_mul_f32 v130, %[scale], v130\n\t" \
"v_mfma_f32_16x16x32_bf16 v[140:143], a[104:107], v[104:107], v[140:143]\n\t" \
"v_mul_f32 v131, %[scale], v131\n\t" \
"v_mfma_f32_16x16x32_bf16 v[136:139], a[76:79], v[108:111], v[136:139]\n\t" \
"v_mul_f32 v132, %[scale], v132\n\t" \
"v_mfma_f32_16x16x32_bf16 v[140:143], a[108:111], v[108:111], v[140:143]\n\t" \
"v_mul_f32 v133, %[scale], v133\n\t" \
"v_mfma_f32_16x16x32_bf16 v[136:139], a[80:83], v[112:115], v[136:139]\n\t" \
"v_mul_f32 v134, %[scale], v134\n\t" \
"v_mul_f32 v135, %[scale], v135\n\t" \
"v_mfma_f32_16x16x32_bf16 v[140:143], a[112:115], v[112:115], v[140:143]\n\t" \
"v_cvt_pk_bf16_f32 v144, v128, v129\n\t" \
"v_cvt_pk_bf16_f32 v146, v130, v131\n\t" \
"v_mfma_f32_16x16x32_bf16 v[136:139], a[84:87], v[116:119], v[136:139]\n\t" \
"v_cvt_pk_bf16_f32 v145, v132, v133\n\t" \
"v_cvt_pk_bf16_f32 v147, v134, v135\n\t" \
"v_mfma_f32_16x16x32_bf16 v[140:143], a[116:119], v[116:119], v[140:143]\n\t" \
"buffer_store_dwordx4 v[144:147], %[vst], %[rw], s91 offen sc1\n\t" \
"s_add_u32 s91, s91, %[ss1]\n\t" \
"v_mfma_f32_16x16x32_bf16 v[136:139], a[88:91], v[120:123], v[136:139]\n\t" \
"v_mfma_f32_16x16x32_bf16 v[140:143], a[120:123], v[120:123], v[140:143]\n\t" \
"v_mfma_f32_16x16x32_bf16 v[136:139], a[92:95], v[124:127], v[136:139]\n\t" \
"v_mfma_f32_16x16x32_bf16 v[140:143], a[124:127], v[124:127], v[140:143]\n\t" \
"s_nop 6\n\t" \
"v_mul_f32 v136, %[scale], v136\n\t" \
"v_mul_f32 v137, %[scale], v137\n\t" \
"v_mul_f32 v138, %[scale], v138\n\t" \
"v_mul_f32 v139, %[scale], v139\n\t" \
"v_mul_f32 v140, %[scale], v140\n\t" \
"v_mul_f32 v141, %[scale], v141\n\t" \
"v_mul_f32 v142, %[scale], v142\n\t" \
"v_mul_f32 v143, %[scale], v143\n\t" \
"v_cvt_pk_bf16_f32 v148, v136, v137\n\t" \
"v_cvt_pk_bf16_f32 v150, v138, v139\n\t" \
"v_cvt_pk_bf16_f32 v149, v140, v141\n\t" \
"v_cvt_pk_bf16_f32 v151, v142, v143\n\t" \
"buffer_store_dwordx4 v[148:151], %[vst], %[rw], s91 offen sc1\n\t" \
"s_add_u32 s91, s91, %[ss1]\n\t" \
"s_waitcnt vmcnt(0) lgkmcnt(0)\n\t" \
"s_barrier\n\t"

#define ASM_P1M21_CLOB "v64", "v65", "v66", "v67", "v68", "v69", "v70", "v71", "v72", "v73", "v74", "v75", "v76", "v77", "v78", "v79", "v80", "v81", "v82", "v83", "v84", "v85", "v86", "v87", "v88", "v89", "v90", "v91", "v92", "v93", "v94", "v95", "v96", "v97", "v98", "v99", "v100", "v101", "v102", "v103", "v104", "v105", "v106", "v107", "v108", "v109", "v110", "v111", "v112", "v113", "v114", "v115", "v116", "v117", "v118", "v119", "v120", "v121", "v122", "v123", "v124", "v125", "v126", "v127", "v128", "v129", "v130", "v131", "v132", "v133", "v134", "v135", "v136", "v137", "v138", "v139", "v140", "v141", "v142", "v143", "v144", "v145", "v146", "v147", "v148", "v149", "v150", "v151", "a0", "a1", "a2", "a3", "a4", "a5", "a6", "a7", "a8", "a9", "a10", "a11", "a12", "a13", "a14", "a15", "a16", "a17", "a18", "a19", "a20", "a21", "a22", "a23", "a24", "a25", "a26", "a27", "a28", "a29", "a30", "a31", "a32", "a33", "a34", "a35", "a36", "a37", "a38", "a39", "a40", "a41", "a42", "a43", "a44", "a45", "a46", "a47", "a48", "a49", "a50", "a51", "a52", "a53", "a54", "a55", "a56", "a57", "a58", "a59", "a60", "a61", "a62", "a63", "a64", "a65", "a66", "a67", "a68", "a69", "a70", "a71", "a72", "a73", "a74", "a75", "a76", "a77", "a78", "a79", "a80", "a81", "a82", "a83", "a84", "a85", "a86", "a87", "a88", "a89", "a90", "a91", "a92", "a93", "a94", "a95", "a96", "a97", "a98", "a99", "a100", "a101", "a102", "a103", "a104", "a105", "a106", "a107", "a108", "a109", "a110", "a111", "a112", "a113", "a114", "a115", "a116", "a117", "a118", "a119", "a120", "a121", "a122", "a123", "a124", "a125", "a126", "a127", "s90", "s91", "scc", "memory"

#define ASM_P1L1_TEXT \
"s_mov_b32 s90, 0\n\t" \
"buffer_load_dwordx4 a[0:3], %[voff], %[rx], s90 offen offset:0\n\t" \
"buffer_load_dwordx4 a[4:7], %[voff], %[rx], s90 offen offset:64\n\t" \
"buffer_load_dwordx4 a[8:11], %[voff], %[rx], s90 offen offset:128\n\t" \
"buffer_load_dwordx4 a[12:15], %[voff], %[rx], s90 offen offset:192\n\t" \
"buffer_load_dwordx4 a[16:19], %[voff], %[rx], s90 offen offset:256\n\t" \
"buffer_load_dwordx4 a[20:23], %[voff], %[rx], s90 offen offset:320\n\t" \
"buffer_load_dwordx4 a[24:27], %[voff], %[rx], s90 offen offset:384\n\t" \
"buffer_load_dwordx4 a[28:31], %[voff], %[rx], s90 offen offset:448\n\t" \
"s_mov_b32 s90, 1835008\n\t" \
"buffer_load_dwordx4 a[32:35], %[voff], %[rx], s90 offen offset:0\n\t" \
"buffer_load_dwordx4 a[36:39], %[voff], %[rx], s90 offen offset:64\n\t" \
"buffer_load_dwordx4 a[40:43], %[voff], %[rx], s90 offen offset:128\n\t" \
"buffer_load_dwordx4 a[44:47], %[voff], %[rx], s90 offen offset:192\n\t" \
"buffer_load_dwordx4 a[48:51], %[voff], %[rx], s90 offen offset:256\n\t" \
"buffer_load_dwordx4 a[52:55], %[voff], %[rx], s90 offen offset:320\n\t" \
"buffer_load_dwordx4 a[56:59], %[voff], %[rx], s90 offen offset:384\n\t" \
"buffer_load_dwordx4 a[60:63], %[voff], %[rx], s90 offen offset:448\n\t" \
"s_waitcnt vmcnt(16)\n\t" \
"s_barrier\n\t"

#define ASM_P1L1_CLOB "a0", "a1", "a2", "a3", "a4", "a5", "a6", "a7", "a8", "a9", "a10", "a11", "a12", "a13", "a14", "a15", "a16", "a17", "a18", "a19", "a20", "a21", "a22", "a23", "a24", "a25", "a26", "a27", "a28", "a29", "a30", "a31", "a32", "a33", "a34", "a35", "a36", "a37", "a38", "a39", "a40", "a41", "a42", "a43", "a44", "a45", "a46", "a47", "a48", "a49", "a50", "a51", "a52", "a53", "a54", "a55", "a56", "a57", "a58", "a59", "a60", "a61", "a62", "a63", "s90", "s91", "scc", "memory"

#define ASM_P1M10_TEXT \
"ds_read_b128 v[64:67], %[lz0] offset:0\n\t" \
"ds_read_b128 v[68:71], %[lz1] offset:0\n\t" \
"ds_read_b128 v[72:75], %[lz2] offset:0\n\t" \
"ds_read_b128 v[76:79], %[lz3] offset:0\n\t" \
"ds_read_b128 v[80:83], %[lz0] offset:256\n\t" \
"ds_read_b128 v[84:87], %[lz1] offset:256\n\t" \
"ds_read_b128 v[88:91], %[lz2] offset:256\n\t" \
"ds_read_b128 v[92:95], %[lz3] offset:256\n\t" \
"s_waitcnt vmcnt(0)\n\t" \
"s_waitcnt lgkmcnt(0)\n\t" \
"v_mfma_f32_16x16x32_bf16 v[128:131], a[0:3], v[64:67], 0\n\t" \
"ds_read_b128 v[96:99], %[lz0] offset:8192\n\t" \
"v_mfma_f32_16x16x32_bf16 v[132:135], a[32:35], v[64:67], 0\n\t" \
"ds_read_b128 v[100:103], %[lz1] offset:8192\n\t" \
"v_mfma_f32_16x16x32_bf16 v[128:131], a[4:7], v[68:71], v[128:131]\n\t" \
"ds_read_b128 v[104:107], %[lz2] offset:8192\n\t" \
"v_mfma_f32_16x16x32_bf16 v[132:135], a[36:39], v[68:71], v[132:135]\n\t" \
"ds_read_b128 v[108:111], %[lz3] offset:8192\n\t" \
"v_mfma_f32_16x16x32_bf16 v[128:131], a[8:11], v[72:75], v[128:131]\n\t" \
"ds_read_b128 v[112:115], %[lz0] offset:8448\n\t" \
"v_mfma_f32_16x16x32_bf16 v[132:135], a[40:43], v[72:75], v[132:135]\n\t" \
"ds_read_b128 v[116:119], %[lz1] offset:8448\n\t" \
"v_mfma_f32_16x16x32_bf16 v[128:131], a[12:15], v[76:79], v[128:131]\n\t" \
"ds_read_b128 v[120:123], %[lz2] offset:8448\n\t" \
"v_mfma_f32_16x16x32_bf16 v[132:135], a[44:47], v[76:79], v[132:135]\n\t" \
"ds_read_b128 v[124:127], %[lz3] offset:8448\n\t" \
"v_mfma_f32_16x16x32_bf16 v[128:131], a[16:19], v[80:83], v[128:131]\n\t" \
"v_mfma_f32_16x16x32_bf16 v[132:135], a[48:51], v[80:83], v[132:135]\n\t" \
"v_mfma_f32_16x16x32_bf16 v[128:131], a[20:23], v[84:87], v[128:131]\n\t" \
"v_mfma_f32_16x16x32_bf16 v[132:135], a[52:55], v[84:87], v[132:135]\n\t" \
"v_mfma_f32_16x16x32_bf16 v[128:131], a[24:27], v[88:91], v[128:131]\n\t" \
"v_mfma_f32_16x16x32_bf16 v[132:135], a[56:59], v[88:91], v[132:135]\n\t" \
"v_mfma_f32_16x16x32_bf16 v[128:131], a[28:31], v[92:95], v[128:131]\n\t" \
"v_mfma_f32_16x16x32_bf16 v[132:135], a[60:63], v[92:95], v[132:135]\n\t" \
"s_waitcnt lgkmcnt(0)\n\t" \
"v_mfma_f32_16x16x32_bf16 v[136:139], a[0:3], v[96:99], 0\n\t" \
"ds_read_b128 v[64:67], %[lz0] offset:16384\n\t" \
"v_mfma_f32_16x16x32_bf16 v[140:143], a[32:35], v[96:99], 0\n\t" \
"ds_read_b128 v[68:71], %[lz1] offset:16384\n\t" \
"v_mfma_f32_16x16x32_bf16 v[136:139], a[4:7], v[100:103], v[136:139]\n\t" \
"ds_read_b128 v[72:75], %[lz2] offset:16384\n\t" \
"s_mov_b32 s91, %[sb0]\n\t" \
"v_mfma_f32_16x16x32_bf16 v[140:143], a[36:39], v[100:103], v[140:143]\n\t" \
"ds_read_b128 v[76:79], %[lz3] offset:16384\n\t" \
"v_mul_f32 v128, %[scale], v128\n\t" \
"v_mfma_f32_16x16x32_bf16 v[136:139], a[8:11], v[104:107], v[136:139]\n\t" \
"ds_read_b128 v[80:83], %[lz0] offset:16640\n\t" \
"v_mul_f32 v129, %[scale], v129\n\t" \
"v_mfma_f32_16x16x32_bf16 v[140:143], a[40:43], v[104:107], v[140:143]\n\t" \
"ds_read_b128 v[84:87], %[lz1] offset:16640\n\t" \
"v_mul_f32 v130, %[scale], v130\n\t" \
"v_mfma_f32_16x16x32_bf16 v[136:139], a[12:15], v[108:111], v[136:139]\n\t" \
"ds_read_b128 v[88:91], %[lz2] offset:16640\n\t" \
"v_mul_f32 v131, %[scale], v131\n\t" \
"v_mfma_f32_16x16x32_bf16 v[140:143], a[44:47], v[108:111], v[140:143]\n\t" \
"ds_read_b128 v[92:95], %[lz3] offset:16640\n\t" \
"v_mul_f32 v132, %[scale], v132\n\t" \
"v_mfma_f32_16x16x32_bf16 v[136:139], a[16:19], v[112:115], v[136:139]\n\t" \
"v_mul_f32 v133, %[scale], v133\n\t" \
"v_mul_f32 v134, %[scale], v134\n\t" \
"v_mfma_f32_16x16x32_bf16 v[140:143], a[48:51], v[112:115], v[140:143]\n\t" \
"v_mul_f32 v135, %[scale], v135\n\t" \
"v_cvt_pk_bf16_f32 v144, v128, v129\n\t" \
"v_mfma_f32_16x16x32_bf16 v[136:139], a[20:23], v[116:119], v[136:139]\n\t" \
"v_cvt_pk_bf16_f32 v146, v130, v131\n\t" \
"v_cvt_pk_bf16_f32 v145, v132, v133\n\t" \
"v_mfma_f32_16x16x32_bf16 v[140:143], a[52:55], v[116:119], v[140:143]\n\t" \
"v_cvt_pk_bf16_f32 v147, v134, v135\n\t" \
"buffer_store_dwordx4 v[144:147], %[vst], %[rw], s91 offen sc1\n\t" \
"s_add_u32 s91, s91, %[ss0]\n\t" \
"v_mfma_f32_16x16x32_bf16 v[136:139], a[24:27], v[120:123], v[136:139]\n\t" \
"v_mfma_f32_16x16x32_bf16 v[140:143], a[56:59], v[120:123], v[140:143]\n\t" \
"v_mfma_f32_16x16x32_bf16 v[136:139], a[28:31], v[124:127], v[136:139]\n\t" \
"v_mfma_f32_16x16x32_bf16 v[140:143], a[60:63], v[124:127], v[140:143]\n\t" \
"s_waitcnt lgkmcnt(0)\n\t" \
"v_mfma_f32_16x16x32_bf16 v[128:131], a[0:3], v[64:67], 0\n\t" \
"ds_read_b128 v[96:99], %[lz0] offset:24576\n\t" \
"v_mfma_f32_16x16x32_bf16 v[132:135], a[32:35], v[64:67], 0\n\t" \
"ds_read_b128 v[100:103], %[lz1] offset:24576\n\t" \
"v_mfma_f32_16x16x32_bf16 v[128:131], a[4:7], v[68:71], v[128:131]\n\t" \
"ds_read_b128 v[104:107], %[lz2] offset:24576\n\t" \
"v_mul_f32 v136, %[scale], v136\n\t" \
"v_mfma_f32_16x16x32_bf16 v[132:135], a[36:39], v[68:71], v[132:135]\n\t" \
"ds_read_b128 v[108:111], %[lz3] offset:24576\n\t" \
"v_mul_f32 v137, %[scale], v137\n\t" \
"v_mfma_f32_16x16x32_bf16 v[128:131], a[8:11], v[72:75], v[128:131]\n\t" \
"ds_read_b128 v[112:115], %[lz0] offset:24832\n\t" \
"v_mul_f32 v138, %[scale], v138\n\t" \
"v_mfma_f32_16x16x32_bf16 v[132:135], a[40:43], v[72:75], v[132:135]\n\t" \
"ds_read_b128 v[116:119], %[lz1] offset:24832\n\t" \
"v_mul_f32 v139, %[scale], v139\n\t" \
"v_mfma_f32_16x16x32_bf16 v[128:131], a[12:15], v[76:79], v[128:131]\n\t" \
"ds_read_b128 v[120:123], %[lz2] offset:24832\n\t" \
"v_mul_f32 v140, %[scale], v140\n\t" \
"v_mfma_f32_16x16x32_bf16 v[132:135], a[44:47], v[76:79], v[132:135]\n\t" \
"ds_read_b128 v[124:127], %[lz3] offset:24832\n\t" \
"v_mul_f32 v141, %[scale], v141\n\t" \
"v_mfma_f32_16x16x32_bf16 v[128:131], a[16:19], v[80:83], v[128:131]\n\t" \
"v_mul_f32 v142, %[scale], v142\n\t" \
"v_mul_f32 v143, %[scale], v143\n\t" \
"v_mfma_f32_16x16x32_bf16 v[132:135], a[48:51], v[80:83], v[132:135]\n\t" \
"v_cvt_pk_bf16_f32 v148, v136, v137\n\t" \
"v_cvt_pk_bf16_f32 v150, v138, v139\n\t" \
"v_mfma_f32_16x16x32_bf16 v[128:131], a[20:23], v[84:87], v[128:131]\n\t" \
"v_cvt_pk_bf16_f32 v149, v140, v141\n\t" \
"v_cvt_pk_bf16_f32 v151, v142, v143\n\t" \
"v_mfma_f32_16x16x32_bf16 v[132:135], a[52:55], v[84:87], v[132:135]\n\t" \
"buffer_store_dwordx4 v[148:151], %[vst], %[rw], s91 offen sc1\n\t" \
"s_add_u32 s91, s91, %[ss0]\n\t" \
"v_mfma_f32_16x16x32_bf16 v[128:131], a[24:27], v[88:91], v[128:131]\n\t" \
"v_mfma_f32_16x16x32_bf16 v[132:135], a[56:59], v[88:91], v[132:135]\n\t" \
"v_mfma_f32_16x16x32_bf16 v[128:131], a[28:31], v[92:95], v[128:131]\n\t" \
"v_mfma_f32_16x16x32_bf16 v[132:135], a[60:63], v[92:95], v[132:135]\n\t" \
"s_waitcnt lgkmcnt(0)\n\t" \
"v_mfma_f32_16x16x32_bf16 v[136:139], a[0:3], v[96:99], 0\n\t" \
"ds_read_b128 v[64:67], %[lz0] offset:32768\n\t" \
"v_mfma_f32_16x16x32_bf16 v[140:143], a[32:35], v[96:99], 0\n\t" \
"ds_read_b128 v[68:71], %[lz1] offset:32768\n\t" \
"v_mfma_f32_16x16x32_bf16 v[136:139], a[4:7], v[100:103], v[136:139]\n\t" \
"ds_read_b128 v[72:75], %[lz2] offset:32768\n\t" \
"v_mul_f32 v128, %[scale], v128\n\t" \
"v_mfma_f32_16x16x32_bf16 v[140:143], a[36:39], v[100:103], v[140:143]\n\t" \
"ds_read_b128 v[76:79], %[lz3] offset:32768\n\t" \
"v_mul_f32 v129, %[scale], v129\n\t" \
"v_mfma_f32_16x16x32_bf16 v[136:139], a[8:11], v[104:107], v[136:139]\n\t" \
"ds_read_b128 v[80:83], %[lz0] offset:33024\n\t" \
"v_mul_f32 v130, %[scale], v130\n\t" \
"v_mfma_f32_16x16x32_bf16 v[140:143], a[40:43], v[104:107], v[140:143]\n\t" \
"ds_read_b128 v[84:87], %[lz1] offset:33024\n\t" \
"v_mul_f32 v131, %[scale], v131\n\t" \
"v_mfma_f32_16x16x32_bf16 v[136:139], a[12:15], v[108:111], v[136:139]\n\t" \
"ds_read_b128 v[88:91], %[lz2] offset:33024\n\t" \
"v_mul_f32 v132, %[scale], v132\n\t" \
"v_mfma_f32_16x16x32_bf16 v[140:143], a[44:47], v[108:111], v[140:143]\n\t" \
"ds_read_b128 v[92:95], %[lz3] offset:33024\n\t" \
"v_mul_f32 v133, %[scale], v133\n\t" \
"v_mfma_f32_16x16x32_bf16 v[136:139], a[16:19], v[112:115], v[136:139]\n\t" \
"v_mul_f32 v134, %[scale], v134\n\t" \
"v_mul_f32 v135, %[scale], v135\n\t" \
"v_mfma_f32_16x16x32_bf16 v[140:143], a[48:51], v[112:115], v[140:143]\n\t" \
"v_cvt_pk_bf16_f32 v144, v128, v129\n\t" \
"v_cvt_pk_bf16_f32 v146, v130, v131\n\t" \
"v_mfma_f32_16x16x32_bf16 v[136:139], a[20:23], v[116:119], v[136:139]\n\t" \
"v_cvt_pk_bf16_f32 v145, v132, v133\n\t" \
"v_cvt_pk_bf16_f32 v147, v134, v135\n\t" \
"v_mfma_f32_16x16x32_bf16 v[140:143], a[52:55], v[116:119], v[140:143]\n\t" \
"buffer_store_dwordx4 v[144:147], %[vst], %[rw], s91 offen sc1\n\t" \
"s_add_u32 s91, s91, %[ss0]\n\t" \
"v_mfma_f32_16x16x32_bf16 v[136:139], a[24:27], v[120:123], v[136:139]\n\t" \
"v_mfma_f32_16x16x32_bf16 v[140:143], a[56:59], v[120:123], v[140:143]\n\t" \
"v_mfma_f32_16x16x32_bf16 v[136:139], a[28:31], v[124:127], v[136:139]\n\t" \
"v_mfma_f32_16x16x32_bf16 v[140:143], a[60:63], v[124:127], v[140:143]\n\t" \
"s_waitcnt lgkmcnt(0)\n\t" \
"v_mfma_f32_16x16x32_bf16 v[128:131], a[0:3], v[64:67], 0\n\t" \
"ds_read_b128 v[96:99], %[lz0] offset:40960\n\t" \
"v_mfma_f32_16x16x32_bf16 v[132:135], a[32:35], v[64:67], 0\n\t" \
"ds_read_b128 v[100:103], %[lz1] offset:40960\n\t" \
"v_mfma_f32_16x16x32_bf16 v[128:131], a[4:7], v[68:71], v[128:131]\n\t" \
"ds_read_b128 v[104:107], %[lz2] offset:40960\n\t" \
"v_mul_f32 v136, %[scale], v136\n\t" \
"v_mfma_f32_16x16x32_bf16 v[132:135], a[36:39], v[68:71], v[132:135]\n\t" \
"ds_read_b128 v[108:111], %[lz3] offset:40960\n\t" \
"v_mul_f32 v137, %[scale], v137\n\t" \
"v_mfma_f32_16x16x32_bf16 v[128:131], a[8:11], v[72:75], v[128:131]\n\t" \
"ds_read_b128 v[112:115], %[lz0] offset:41216\n\t" \
"v_mul_f32 v138, %[scale], v138\n\t" \
"v_mfma_f32_16x16x32_bf16 v[132:135], a[40:43], v[72:75], v[132:135]\n\t" \
"ds_read_b128 v[116:119], %[lz1] offset:41216\n\t" \
"v_mul_f32 v139, %[scale], v139\n\t" \
"v_mfma_f32_16x16x32_bf16 v[128:131], a[12:15], v[76:79], v[128:131]\n\t" \
"ds_read_b128 v[120:123], %[lz2] offset:41216\n\t" \
"v_mul_f32 v140, %[scale], v140\n\t" \
"v_mfma_f32_16x16x32_bf16 v[132:135], a[44:47], v[76:79], v[132:135]\n\t" \
"ds_read_b128 v[124:127], %[lz3] offset:41216\n\t" \
"v_mul_f32 v141, %[scale], v141\n\t" \
"v_mfma_f32_16x16x32_bf16 v[128:131], a[16:19], v[80:83], v[128:131]\n\t" \
"v_mul_f32 v142, %[scale], v142\n\t" \
"v_mul_f32 v143, %[scale], v143\n\t" \
"v_mfma_f32_16x16x32_bf16 v[132:135], a[48:51], v[80:83], v[132:135]\n\t" \
"v_cvt_pk_bf16_f32 v148, v136, v137\n\t" \
"v_cvt_pk_bf16_f32 v150, v138, v139\n\t" \
"v_mfma_f32_16x16x32_bf16 v[128:131], a[20:23], v[84:87], v[128:131]\n\t" \
"v_cvt_pk_bf16_f32 v149, v140, v141\n\t" \
"v_cvt_pk_bf16_f32 v151, v142, v143\n\t" \
"v_mfma_f32_16x16x32_bf16 v[132:135], a[52:55], v[84:87], v[132:135]\n\t" \
"buffer_store_dwordx4 v[148:151], %[vst], %[rw], s91 offen sc1\n\t" \
"s_add_u32 s91, s91, %[ss0]\n\t" \
"v_mfma_f32_16x16x32_bf16 v[128:131], a[24:27], v[88:91], v[128:131]\n\t" \
"v_mfma_f32_16x16x32_bf16 v[132:135], a[56:59], v[88:91], v[132:135]\n\t" \
"v_mfma_f32_16x16x32_bf16 v[128:131], a[28:31], v[92:95], v[128:131]\n\t" \
"v_mfma_f32_16x16x32_bf16 v[132:135], a[60:63], v[92:95], v[132:135]\n\t" \
"s_waitcnt lgkmcnt(0)\n\t" \
"v_mfma_f32_16x16x32_bf16 v[136:139], a[0:3], v[96:99], 0\n\t" \
"ds_read_b128 v[64:67], %[lz0] offset:49152\n\t" \
"v_mfma_f32_16x16x32_bf16 v[140:143], a[32:35], v[96:99], 0\n\t" \
"ds_read_b128 v[68:71], %[lz1] offset:49152\n\t" \
"v_mfma_f32_16x16x32_bf16 v[136:139], a[4:7], v[100:103], v[136:139]\n\t" \
"ds_read_b128 v[72:75], %[lz2] offset:49152\n\t" \
"v_mul_f32 v128, %[scale], v128\n\t" \
"v_mfma_f32_16x16x32_bf16 v[140:143], a[36:39], v[100:103], v[140:143]\n\t" \
"ds_read_b128 v[76:79], %[lz3] offset:49152\n\t" \
"v_mul_f32 v129, %[scale], v129\n\t" \
"v_mfma_f32_16x16x32_bf16 v[136:139], a[8:11], v[104:107], v[136:139]\n\t" \
"ds_read_b128 v[80:83], %[lz0] offset:49408\n\t" \
"v_mul_f32 v130, %[scale], v130\n\t" \
"v_mfma_f32_16x16x32_bf16 v[140:143], a[40:43], v[104:107], v[140:143]\n\t" \
"ds_read_b128 v[84:87], %[lz1] offset:49408\n\t" \
"v_mul_f32 v131, %[scale], v131\n\t" \
"v_mfma_f32_16x16x32_bf16 v[136:139], a[12:15], v[108:111], v[136:139]\n\t" \
"ds_read_b128 v[88:91], %[lz2] offset:49408\n\t" \
"v_mul_f32 v132, %[scale], v132\n\t" \
"v_mfma_f32_16x16x32_bf16 v[140:143], a[44:47], v[108:111], v[140:143]\n\t" \
"ds_read_b128 v[92:95], %[lz3] offset:49408\n\t" \
"v_mul_f32 v133, %[scale], v133\n\t" \
"v_mfma_f32_16x16x32_bf16 v[136:139], a[16:19], v[112:115], v[136:139]\n\t" \
"v_mul_f32 v134, %[scale], v134\n\t" \
"v_mul_f32 v135, %[scale], v135\n\t" \
"v_mfma_f32_16x16x32_bf16 v[140:143], a[48:51], v[112:115], v[140:143]\n\t" \
"v_cvt_pk_bf16_f32 v144, v128, v129\n\t" \
"v_cvt_pk_bf16_f32 v146, v130, v131\n\t" \
"v_mfma_f32_16x16x32_bf16 v[136:139], a[20:23], v[116:119], v[136:139]\n\t" \
"v_cvt_pk_bf16_f32 v145, v132, v133\n\t" \
"v_cvt_pk_bf16_f32 v147, v134, v135\n\t" \
"v_mfma_f32_16x16x32_bf16 v[140:143], a[52:55], v[116:119], v[140:143]\n\t" \
"buffer_store_dwordx4 v[144:147], %[vst], %[rw], s91 offen sc1\n\t" \
"s_add_u32 s91, s91, %[ss0]\n\t" \
"v_mfma_f32_16x16x32_bf16 v[136:139], a[24:27], v[120:123], v[136:139]\n\t" \
"v_mfma_f32_16x16x32_bf16 v[140:143], a[56:59], v[120:123], v[140:143]\n\t" \
"v_mfma_f32_16x16x32_bf16 v[136:139], a[28:31], v[124:127], v[136:139]\n\t" \
"v_mfma_f32_16x16x32_bf16 v[140:143], a[60:63], v[124:127], v[140:143]\n\t" \
"s_waitcnt lgkmcnt(0)\n\t" \
"v_mfma_f32_16x16x32_bf16 v[128:131], a[0:3], v[64:67], 0\n\t" \
"v_mfma_f32_16x16x32_bf16 v[132:135], a[32:35], v[64:67], 0\n\t" \
"v_mfma_f32_16x16x32_bf16 v[128:131], a[4:7], v[68:71], v[128:131]\n\t" \
"s_nop 2\n\t" \
"v_mul_f32 v136, %[scale], v136\n\t" \
"v_mfma_f32_16x16x32_bf16 v[132:135], a[36:39], v[68:71], v[132:135]\n\t" \
"v_mul_f32 v137, %[scale], v137\n\t" \
"v_mfma_f32_16x16x32_bf16 v[128:131], a[8:11], v[72:75], v[128:131]\n\t" \
"v_mul_f32 v138, %[scale], v138\n\t" \
"v_mfma_f32_16x16x32_bf16 v[132:135], a[40:43], v[72:75], v[132:135]\n\t" \
"v_mul_f32 v139, %[scale], v139\n\t" \
"v_mfma_f32_16x16x32_bf16 v[128:131], a[12:15], v[76:79], v[128:131]\n\t" \
"v_mul_f32 v140, %[scale], v140\n\t" \
"v_mfma_f32_16x16x32_bf16 v[132:135], a[44:47], v[76:79], v[132:135]\n\t" \
"v_mul_f32 v141, %[scale], v141\n\t" \
"v_mfma_f32_16x16x32_bf16 v[128:131], a[16:19], v[80:83], v[128:131]\n\t" \
"v_mul_f32 v142, %[scale], v142\n\t" \
"v_mul_f32 v143, %[scale], v143\n\t" \
"v_mfma_f32_16x16x32_bf16 v[132:135], a[48:51], v[80:83], v[132:135]\n\t" \
"v_cvt_pk_bf16_f32 v148, v136, v137\n\t" \
"v_cvt_pk_bf16_f32 v150, v138, v139\n\t" \
"v_mfma_f32_16x16x32_bf16 v[128:131], a[20:23], v[84:87], v[128:131]\n\t" \
"v_cvt_pk_bf16_f32 v149, v140, v141\n\t" \
"v_cvt_pk_bf16_f32 v151, v142, v143\n\t" \
"v_mfma_f32_16x16x32_bf16 v[132:135], a[52:55], v[84:87], v[132:135]\n\t" \
"buffer_store_dwordx4 v[148:151], %[vst], %[rw], s91 offen sc1\n\t" \
"s_add_u32 s91, s91, %[ss0]\n\t" \
"v_mfma_f32_16x16x32_bf16 v[128:131], a[24:27], v[88:91], v[128:131]\n\t" \
"v_mfma_f32_16x16x32_bf16 v[132:135], a[56:59], v[88:91], v[132:135]\n\t" \
"v_mfma_f32_16x16x32_bf16 v[128:131], a[28:31], v[92:95], v[128:131]\n\t" \
"v_mfma_f32_16x16x32_bf16 v[132:135], a[60:63], v[92:95], v[132:135]\n\t" \
"s_nop 6\n\t" \
"v_mul_f32 v128, %[scale], v128\n\t" \
"v_mul_f32 v129, %[scale], v129\n\t" \
"v_mul_f32 v130, %[scale], v130\n\t" \
"v_mul_f32 v131, %[scale], v131\n\t" \
"v_mul_f32 v132, %[scale], v132\n\t" \
"v_mul_f32 v133, %[scale], v133\n\t" \
"v_mul_f32 v134, %[scale], v134\n\t" \
"v_mul_f32 v135, %[scale], v135\n\t" \
"v_cvt_pk_bf16_f32 v144, v128, v129\n\t" \
"v_cvt_pk_bf16_f32 v146, v130, v131\n\t" \
"v_cvt_pk_bf16_f32 v145, v132, v133\n\t" \
"v_cvt_pk_bf16_f32 v147, v134, v135\n\t" \
"buffer_store_dwordx4 v[144:147], %[vst], %[rw], s91 offen sc1\n\t" \
"s_add_u32 s91, s91, %[ss0]\n\t" \
"s_waitcnt vmcnt(0) lgkmcnt(0)\n\t" \
"s_barrier\n\t"

#define ASM_P1M10_CLOB "v64", "v65", "v66", "v67", "v68", "v69", "v70", "v71", "v72", "v73", "v74", "v75", "v76", "v77", "v78", "v79", "v80", "v81", "v82", "v83", "v84", "v85", "v86", "v87", "v88", "v89", "v90", "v91", "v92", "v93", "v94", "v95", "v96", "v97", "v98", "v99", "v100", "v101", "v102", "v103", "v104", "v105", "v106", "v107", "v108", "v109", "v110", "v111", "v112", "v113", "v114", "v115", "v116", "v117", "v118", "v119", "v120", "v121", "v122", "v123", "v124", "v125", "v126", "v127", "v128", "v129", "v130", "v131", "v132", "v133", "v134", "v135", "v136", "v137", "v138", "v139", "v140", "v141", "v142", "v143", "v144", "v145", "v146", "v147", "v148", "v149", "v150", "v151", "a0", "a1", "a2", "a3", "a4", "a5", "a6", "a7", "a8", "a9", "a10", "a11", "a12", "a13", "a14", "a15", "a16", "a17", "a18", "a19", "a20", "a21", "a22", "a23", "a24", "a25", "a26", "a27", "a28", "a29", "a30", "a31", "a32", "a33", "a34", "a35", "a36", "a37", "a38", "a39", "a40", "a41", "a42", "a43", "a44", "a45", "a46", "a47", "a48", "a49", "a50", "a51", "a52", "a53", "a54", "a55", "a56", "a57", "a58", "a59", "a60", "a61", "a62", "a63", "s90", "s91", "scc", "memory"

#define ASM_P1M11_TEXT \
"ds_read_b64_tr_b16 v[64:65], %[lz0] offset:0\n\t" \
"ds_read_b64_tr_b16 v[66:67], %[lz0] offset:896\n\t" \
"ds_read_b64_tr_b16 v[68:69], %[lz0] offset:7168\n\t" \
"ds_read_b64_tr_b16 v[70:71], %[lz0] offset:8064\n\t" \
"ds_read_b64_tr_b16 v[72:73], %[lz0] offset:14336\n\t" \
"ds_read_b64_tr_b16 v[74:75], %[lz0] offset:15232\n\t" \
"ds_read_b64_tr_b16 v[76:77], %[lz0] offset:21504\n\t" \
"ds_read_b64_tr_b16 v[78:79], %[lz0] offset:22400\n\t" \
"ds_read_b64_tr_b16 v[80:81], %[lz0] offset:28672\n\t" \
"ds_read_b64_tr_b16 v[82:83], %[lz0] offset:29568\n\t" \
"ds_read_b64_tr_b16 v[84:85], %[lz0] offset:35840\n\t" \
"ds_read_b64_tr_b16 v[86:87], %[lz0] offset:36736\n\t" \
"ds_read_b64_tr_b16 v[88:89], %[lz0] offset:43008\n\t" \
"ds_read_b64_tr_b16 v[90:91], %[lz0] offset:43904\n\t" \
"ds_read_b64_tr_b16 v[92:93], %[lz0] offset:50176\n\t" \
"ds_read_b64_tr_b16 v[94:95], %[lz0] offset:51072\n\t" \
"s_waitcnt vmcnt(0)\n\t" \
"s_waitcnt lgkmcnt(0)\n\t" \
"v_mfma_f32_16x16x32_bf16 v[128:131], a[0:3], v[64:67], 0\n\t" \
"ds_read_b64_tr_b16 v[96:97], %[lz0] offset:32\n\t" \
"ds_read_b64_tr_b16 v[98:99], %[lz0] offset:928\n\t" \
"v_mfma_f32_16x16x32_bf16 v[132:135], a[32:35], v[64:67], 0\n\t" \
"ds_read_b64_tr_b16 v[100:101], %[lz0] offset:7200\n\t" \
"ds_read_b64_tr_b16 v[102:103], %[lz0] offset:8096\n\t" \
"v_mfma_f32_16x16x32_bf16 v[128:131], a[4:7], v[68:71], v[128:131]\n\t" \
"ds_read_b64_tr_b16 v[104:105], %[lz0] offset:14368\n\t" \
"ds_read_b64_tr_b16 v[106:107], %[lz0] offset:15264\n\t" \
"v_mfma_f32_16x16x32_bf16 v[132:135], a[36:39], v[68:71], v[132:135]\n\t" \
"ds_read_b64_tr_b16 v[108:109], %[lz0] offset:21536\n\t" \
"ds_read_b64_tr_b16 v[110:111], %[lz0] offset:22432\n\t" \
"v_mfma_f32_16x16x32_bf16 v[128:131], a[8:11], v[72:75], v[128:131]\n\t" \
"ds_read_b64_tr_b16 v[112:113], %[lz0] offset:28704\n\t" \
"ds_read_b64_tr_b16 v[114:115], %[lz0] offset:29600\n\t" \
"v_mfma_f32_16x16x32_bf16 v[132:135], a[40:43], v[72:75], v[132:135]\n\t" \
"ds_read_b64_tr_b16 v[116:117], %[lz0] offset:35872\n\t" \
"ds_read_b64_tr_b16 v[118:119], %[lz0] offset:36768\n\t" \
"v_mfma_f32_16x16x32_bf16 v[128:131], a[12:15], v[76:79], v[128:131]\n\t" \
"ds_read_b64_tr_b16 v[120:121], %[lz0] offset:43040\n\t" \
"ds_read_b64_tr_b16 v[122:123], %[lz0] offset:43936\n\t" \
"v_mfma_f32_16x16x32_bf16 v[132:135], a[44:47], v[76:79], v[132:135]\n\t" \
"ds_read_b64_tr_b16 v[124:125], %[lz0] offset:50208\n\t" \
"ds_read_b64_tr_b16 v[126:127], %[lz0] offset:51104\n\t" \
"v_mfma_f32_16x16x32_bf16 v[128:131], a[16:19], v[80:83], v[128:131]\n\t" \
"v_mfma_f32_16x16x32_bf16 v[132:135], a[48:51], v[80:83], v[132:135]\n\t" \
"v_mfma_f32_16x16x32_bf16 v[128:131], a[20:23], v[84:87], v[128:131]\n\t" \
"v_mfma_f32_16x16x32_bf16 v[132:135], a[52:55], v[84:87], v[132:135]\n\t" \
"v_mfma_f32_16x16x32_bf16 v[128:131], a[24:27], v[88:91], v[128:131]\n\t" \
"v_mfma_f32_16x16x32_bf16 v[132:135], a[56:59], v[88:91], v[132:135]\n\t" \
"v_mfma_f32_16x16x32_bf16 v[128:131], a[28:31], v[92:95], v[128:131]\n\t" \
"v_mfma_f32_16x16x32_bf16 v[132:135], a[60:63], v[92:95], v[132:135]\n\t" \
"s_waitcnt lgkmcnt(0)\n\t" \
"v_mfma_f32_16x16x32_bf16 v[136:139], a[0:3], v[96:99], 0\n\t" \
"ds_read_b64_tr_b16 v[64:65], %[lz0] offset:64\n\t" \
"ds_read_b64_tr_b16 v[66:67], %[lz0] offset:960\n\t" \
"v_mfma_f32_16x16x32_bf16 v[140:143], a[32:35], v[96:99], 0\n\t" \
"ds_read_b64_tr_b16 v[68:69], %[lz0] offset:7232\n\t" \
"ds_read_b64_tr_b16 v[70:71], %[lz0] offset:8128\n\t" \
"v_mfma_f32_16x16x32_bf16 v[136:139], a[4:7], v[100:103], v[136:139]\n\t" \
"ds_read_b64_tr_b16 v[72:73], %[lz0] offset:14400\n\t" \
"ds_read_b64_tr_b16 v[74:75], %[lz0] offset:15296\n\t" \
"s_mov_b32 s91, %[sb0]\n\t" \
"v_mfma_f32_16x16x32_bf16 v[140:143], a[36:39], v[100:103], v[140:143]\n\t" \
"ds_read_b64_tr_b16 v[76:77], %[lz0] offset:21568\n\t" \
"ds_read_b64_tr_b16 v[78:79], %[lz0] offset:22464\n\t" \
"v_mul_f32 v128, %[scale], v128\n\t" \
"v_mfma_f32_16x16x32_bf16 v[136:139], a[8:11], v[104:107], v[136:139]\n\t" \
"ds_read_b64_tr_b16 v[80:81], %[lz0] offset:28736\n\t" \
"ds_read_b64_tr_b16 v[82:83], %[lz0] offset:29632\n\t" \
"v_mul_f32 v129, %[scale], v129\n\t" \
"v_mfma_f32_16x16x32_bf16 v[140:143], a[40:43], v[104:107], v[140:143]\n\t" \
"ds_read_b64_tr_b16 v[84:85], %[lz0] offset:35904\n\t" \
"ds_read_b64_tr_b16 v[86:87], %[lz0] offset:36800\n\t" \
"v_mul_f32 v130, %[scale], v130\n\t" \
"v_mfma_f32_16x16x32_bf16 v[136:139], a[12:15], v[108:111], v[136:139]\n\t" \
"ds_read_b64_tr_b16 v[88:89], %[lz0] offset:43072\n\t" \
"ds_read_b64_tr_b16 v[90:91], %[lz0] offset:43968\n\t" \
"v_mul_f32 v131, %[scale], v131\n\t" \
"v_mfma_f32_16x16x32_bf16 v[140:143], a[44:47], v[108:111], v[140:143]\n\t" \
"ds_read_b64_tr_b16 v[92:93], %[lz0] offset:50240\n\t" \
"ds_read_b64_tr_b16 v[94:95], %[lz0] offset:51136\n\t" \
"v_mul_f32 v132, %[scale], v132\n\t" \
"v_mfma_f32_16x16x32_bf16 v[136:139], a[16:19], v[112:115], v[136:139]\n\t" \
"v_mul_f32 v133, %[scale], v133\n\t" \
"v_mul_f32 v134, %[scale], v134\n\t" \
"v_mfma_f32_16x16x32_bf16 v[140:143], a[48:51], v[112:115], v[140:143]\n\t" \
"v_mul_f32 v135, %[scale], v135\n\t" \
"v_cvt_pk_bf16_f32 v144, v128, v129\n\t" \
"v_mfma_f32_16x16x32_bf16 v[136:139], a[20:23], v[116:119], v[136:139]\n\t" \
"v_cvt_pk_bf16_f32 v146, v130, v131\n\t" \
"v_cvt_pk_bf16_f32 v145, v132, v133\n\t" \
"v_mfma_f32_16x16x32_bf16 v[140:143], a[52:55], v[116:119], v[140:143]\n\t" \
"v_cvt_pk_bf16_f32 v147, v134, v135\n\t" \
"buffer_store_dwordx4 v[144:147], %[vst], %[rw], s91 offen sc1\n\t" \
"s_add_u32 s91, s91, %[ss0]\n\t" \
"v_mfma_f32_16x16x32_bf16 v[136:139], a[24:27], v[120:123], v[136:139]\n\t" \
"v_mfma_f32_16x16x32_bf16 v[140:143], a[56:59], v[120:123], v[140:143]\n\t" \
"v_mfma_f32_16x16x32_bf16 v[136:139], a[28:31], v[124:127], v[136:139]\n\t" \
"v_mfma_f32_16x16x32_bf16 v[140:143], a[60:63], v[124:127], v[140:143]\n\t" \
"s_waitcnt lgkmcnt(0)\n\t" \
"v_mfma_f32_16x16x32_bf16 v[128:131], a[0:3], v[64:67], 0\n\t" \
"ds_read_b64_tr_b16 v[96:97], %[lz0] offset:96\n\t" \
"ds_read_b64_tr_b16 v[98:99], %[lz0] offset:992\n\t" \
"v_mfma_f32_16x16x32_bf16 v[132:135], a[32:35], v[64:67], 0\n\t" \
"ds_read_b64_tr_b16 v[100:101], %[lz0] offset:7264\n\t" \
"ds_read_b64_tr_b16 v[102:103], %[lz0] offset:8160\n\t" \
"v_mfma_f32_16x16x32_bf16 v[128:131], a[4:7], v[68:71], v[128:131]\n\t" \
"ds_read_b64_tr_b16 v[104:105], %[lz0] offset:14432\n\t" \
"ds_read_b64_tr_b16 v[106:107], %[lz0] offset:15328\n\t" \
"v_mul_f32 v136, %[scale], v136\n\t" \
"v_mfma_f32_16x16x32_bf16 v[132:135], a[36:39], v[68:71], v[132:135]\n\t" \
"ds_read_b64_tr_b16 v[108:109], %[lz0] offset:21600\n\t" \
"ds_read_b64_tr_b16 v[110:111], %[lz0] offset:22496\n\t" \
"v_mul_f32 v137, %[scale], v137\n\t" \
"v_mfma_f32_16x16x32_bf16 v[128:131], a[8:11], v[72:75], v[128:131]\n\t" \
"ds_read_b64_tr_b16 v[112:113], %[lz0] offset:28768\n\t" \
"ds_read_b64_tr_b16 v[114:115], %[lz0] offset:29664\n\t" \
"v_mul_f32 v138, %[scale], v138\n\t" \
"v_mfma_f32_16x16x32_bf16 v[132:135], a[40:43], v[72:75], v[132:135]\n\t" \
"ds_read_b64_tr_b16 v[116:117], %[lz0] offset:35936\n\t" \
"ds_read_b64_tr_b16 v[118:119], %[lz0] offset:36832\n\t" \
"v_mul_f32 v139, %[scale], v139\n\t" \
"v_mfma_f32_16x16x32_bf16 v[128:131], a[12:15], v[76:79], v[128:131]\n\t" \
"ds_read_b64_tr_b16 v[120:121], %[lz0] offset:43104\n\t" \
"ds_read_b64_tr_b16 v[122:123], %[lz0] offset:44000\n\t" \
"v_mul_f32 v140, %[scale], v140\n\t" \
"v_mfma_f32_16x16x32_bf16 v[132:135], a[44:47], v[76:79], v[132:135]\n\t" \
"ds_read_b64_tr_b16 v[124:125], %[lz0] offset:50272\n\t" \
"ds_read_b64_tr_b16 v[126:127], %[lz0] offset:51168\n\t" \
"v_mul_f32 v141, %[scale], v141\n\t" \
"v_mfma_f32_16x16x32_bf16 v[128:131], a[16:19], v[80:83], v[128:131]\n\t" \
"v_mul_f32 v142, %[scale], v142\n\t" \
"v_mul_f32 v143, %[scale], v143\n\t" \
"v_mfma_f32_16x16x32_bf16 v[132:135], a[48:51], v[80:83], v[132:135]\n\t" \
"v_cvt_pk_bf16_f32 v148, v136, v137\n\t" \
"v_cvt_pk_bf16_f32 v150, v138, v139\n\t" \
"v_mfma_f32_16x16x32_bf16 v[128:131], a[20:23], v[84:87], v[128:131]\n\t" \
"v_cvt_pk_bf16_f32 v149, v140, v141\n\t" \
"v_cvt_pk_bf16_f32 v151, v142, v143\n\t" \
"v_mfma_f32_16x16x32_bf16 v[132:135], a[52:55], v[84:87], v[132:135]\n\t" \
"buffer_store_dwordx4 v[148:151], %[vst], %[rw], s91 offen sc1\n\t" \
"s_add_u32 s91, s91, %[ss0]\n\t" \
"v_mfma_f32_16x16x32_bf16 v[128:131], a[24:27], v[88:91], v[128:131]\n\t" \
"v_mfma_f32_16x16x32_bf16 v[132:135], a[56:59], v[88:91], v[132:135]\n\t" \
"v_mfma_f32_16x16x32_bf16 v[128:131], a[28:31], v[92:95], v[128:131]\n\t" \
"v_mfma_f32_16x16x32_bf16 v[132:135], a[60:63], v[92:95], v[132:135]\n\t" \
"s_waitcnt lgkmcnt(0)\n\t" \
"v_mfma_f32_16x16x32_bf16 v[136:139], a[0:3], v[96:99], 0\n\t" \
"ds_read_b64_tr_b16 v[64:65], %[lz0] offset:128\n\t" \
"ds_read_b64_tr_b16 v[66:67], %[lz0] offset:1024\n\t" \
"v_mfma_f32_16x16x32_bf16 v[140:143], a[32:35], v[96:99], 0\n\t" \
"ds_read_b64_tr_b16 v[68:69], %[lz0] offset:7296\n\t" \
"ds_read_b64_tr_b16 v[70:71], %[lz0] offset:8192\n\t" \
"v_mfma_f32_16x16x32_bf16 v[136:139], a[4:7], v[100:103], v[136:139]\n\t" \
"ds_read_b64_tr_b16 v[72:73], %[lz0] offset:14464\n\t" \
"ds_read_b64_tr_b16 v[74:75], %[lz0] offset:15360\n\t" \
"v_mul_f32 v128, %[scale], v128\n\t" \
"v_mfma_f32_16x16x32_bf16 v[140:143], a[36:39], v[100:103], v[140:143]\n\t" \
"ds_read_b64_tr_b16 v[76:77], %[lz0] offset:21632\n\t" \
"ds_read_b64_tr_b16 v[78:79], %[lz0] offset:22528\n\t" \
"v_mul_f32 v129, %[scale], v129\n\t" \
"v_mfma_f32_16x16x32_bf16 v[136:139], a[8:11], v[104:107], v[136:139]\n\t" \
"ds_read_b64_tr_b16 v[80:81], %[lz0] offset:28800\n\t" \
"ds_read_b64_tr_b16 v[82:83], %[lz0] offset:29696\n\t" \
"v_mul_f32 v130, %[scale], v130\n\t" \
"v_mfma_f32_16x16x32_bf16 v[140:143], a[40:43], v[104:107], v[140:143]\n\t" \
"ds_read_b64_tr_b16 v[84:85], %[lz0] offset:35968\n\t" \
"ds_read_b64_tr_b16 v[86:87], %[lz0] offset:36864\n\t" \
"v_mul_f32 v131, %[scale], v131\n\t" \
"v_mfma_f32_16x16x32_bf16 v[136:139], a[12:15], v[108:111], v[136:139]\n\t" \
"ds_read_b64_tr_b16 v[88:89], %[lz0] offset:43136\n\t" \
"ds_read_b64_tr_b16 v[90:91], %[lz0] offset:44032\n\t" \
"v_mul_f32 v132, %[scale], v132\n\t" \
"v_mfma_f32_16x16x32_bf16 v[140:143], a[44:47], v[108:111], v[140:143]\n\t" \
"ds_read_b64_tr_b16 v[92:93], %[lz0] offset:50304\n\t" \
"ds_read_b64_tr_b16 v[94:95], %[lz0] offset:51200\n\t" \
"v_mul_f32 v133, %[scale], v133\n\t" \
"v_mfma_f32_16x16x32_bf16 v[136:139], a[16:19], v[112:115], v[136:139]\n\t" \
"v_mul_f32 v134, %[scale], v134\n\t" \
"v_mul_f32 v135, %[scale], v135\n\t" \
"v_mfma_f32_16x16x32_bf16 v[140:143], a[48:51], v[112:115], v[140:143]\n\t" \
"v_cvt_pk_bf16_f32 v144, v128, v129\n\t" \
"v_cvt_pk_bf16_f32 v146, v130, v131\n\t" \
"v_mfma_f32_16x16x32_bf16 v[136:139], a[20:23], v[116:119], v[136:139]\n\t" \
"v_cvt_pk_bf16_f32 v145, v132, v133\n\t" \
"v_cvt_pk_bf16_f32 v147, v134, v135\n\t" \
"v_mfma_f32_16x16x32_bf16 v[140:143], a[52:55], v[116:119], v[140:143]\n\t" \
"buffer_store_dwordx4 v[144:147], %[vst], %[rw], s91 offen sc1\n\t" \
"s_add_u32 s91, s91, %[ss0]\n\t" \
"v_mfma_f32_16x16x32_bf16 v[136:139], a[24:27], v[120:123], v[136:139]\n\t" \
"v_mfma_f32_16x16x32_bf16 v[140:143], a[56:59], v[120:123], v[140:143]\n\t" \
"v_mfma_f32_16x16x32_bf16 v[136:139], a[28:31], v[124:127], v[136:139]\n\t" \
"v_mfma_f32_16x16x32_bf16 v[140:143], a[60:63], v[124:127], v[140:143]\n\t" \
"s_waitcnt lgkmcnt(0)\n\t" \
"v_mfma_f32_16x16x32_bf16 v[128:131], a[0:3], v[64:67], 0\n\t" \
"ds_read_b64_tr_b16 v[96:97], %[lz0] offset:160\n\t" \
"ds_read_b64_tr_b16 v[98:99], %[lz0] offset:1056\n\t" \
"v_mfma_f32_16x16x32_bf16 v[132:135], a[32:35], v[64:67], 0\n\t" \
"ds_read_b64_tr_b16 v[100:101], %[lz0] offset:7328\n\t" \
"ds_read_b64_tr_b16 v[102:103], %[lz0] offset:8224\n\t" \
"v_mfma_f32_16x16x32_bf16 v[128:131], a[4:7], v[68:71], v[128:131]\n\t" \
"ds_read_b64_tr_b16 v[104:105], %[lz0] offset:14496\n\t" \
"ds_read_b64_tr_b16 v[106:107], %[lz0] offset:15392\n\t" \
"v_mul_f32 v136, %[scale], v136\n\t" \
"v_mfma_f32_16x16x32_bf16 v[132:135], a[36:39], v[68:71], v[132:135]\n\t" \
"ds_read_b64_tr_b16 v[108:109], %[lz0] offset:21664\n\t" \
"ds_read_b64_tr_b16 v[110:111], %[lz0] offset:22560\n\t" \
"v_mul_f32 v137, %[scale], v137\n\t" \
"v_mfma_f32_16x16x32_bf16 v[128:131], a[8:11], v[72:75], v[128:131]\n\t" \
"ds_read_b64_tr_b16 v[112:113], %[lz0] offset:28832\n\t" \
"ds_read_b64_tr_b16 v[114:115], %[lz0] offset:29728\n\t" \
"v_mul_f32 v138, %[scale], v138\n\t" \
"v_mfma_f32_16x16x32_bf16 v[132:135], a[40:43], v[72:75], v[132:135]\n\t" \
"ds_read_b64_tr_b16 v[116:117], %[lz0] offset:36000\n\t" \
"ds_read_b64_tr_b16 v[118:119], %[lz0] offset:36896\n\t" \
"v_mul_f32 v139, %[scale], v139\n\t" \
"v_mfma_f32_16x16x32_bf16 v[128:131], a[12:15], v[76:79], v[128:131]\n\t" \
"ds_read_b64_tr_b16 v[120:121], %[lz0] offset:43168\n\t" \
"ds_read_b64_tr_b16 v[122:123], %[lz0] offset:44064\n\t" \
"v_mul_f32 v140, %[scale], v140\n\t" \
"v_mfma_f32_16x16x32_bf16 v[132:135], a[44:47], v[76:79], v[132:135]\n\t" \
"ds_read_b64_tr_b16 v[124:125], %[lz0] offset:50336\n\t" \
"ds_read_b64_tr_b16 v[126:127], %[lz0] offset:51232\n\t" \
"v_mul_f32 v141, %[scale], v141\n\t" \
"v_mfma_f32_16x16x32_bf16 v[128:131], a[16:19], v[80:83], v[128:131]\n\t" \
"v_mul_f32 v142, %[scale], v142\n\t" \
"v_mul_f32 v143, %[scale], v143\n\t" \
"v_mfma_f32_16x16x32_bf16 v[132:135], a[48:51], v[80:83], v[132:135]\n\t" \
"v_cvt_pk_bf16_f32 v148, v136, v137\n\t" \
"v_cvt_pk_bf16_f32 v150, v138, v139\n\t" \
"v_mfma_f32_16x16x32_bf16 v[128:131], a[20:23], v[84:87], v[128:131]\n\t" \
"v_cvt_pk_bf16_f32 v149, v140, v141\n\t" \
"v_cvt_pk_bf16_f32 v151, v142, v143\n\t" \
"v_mfma_f32_16x16x32_bf16 v[132:135], a[52:55], v[84:87], v[132:135]\n\t" \
"buffer_store_dwordx4 v[148:151], %[vst], %[rw], s91 offen sc1\n\t" \
"s_add_u32 s91, s91, %[ss0]\n\t" \
"v_mfma_f32_16x16x32_bf16 v[128:131], a[24:27], v[88:91], v[128:131]\n\t" \
"v_mfma_f32_16x16x32_bf16 v[132:135], a[56:59], v[88:91], v[132:135]\n\t" \
"v_mfma_f32_16x16x32_bf16 v[128:131], a[28:31], v[92:95], v[128:131]\n\t" \
"v_mfma_f32_16x16x32_bf16 v[132:135], a[60:63], v[92:95], v[132:135]\n\t" \
"s_waitcnt lgkmcnt(0)\n\t" \
"v_mfma_f32_16x16x32_bf16 v[136:139], a[0:3], v[96:99], 0\n\t" \
"ds_read_b64_tr_b16 v[64:65], %[lz0] offset:192\n\t" \
"ds_read_b64_tr_b16 v[66:67], %[lz0] offset:1088\n\t" \
"v_mfma_f32_16x16x32_bf16 v[140:143], a[32:35], v[96:99], 0\n\t" \
"ds_read_b64_tr_b16 v[68:69], %[lz0] offset:7360\n\t" \
"ds_read_b64_tr_b16 v[70:71], %[lz0] offset:8256\n\t" \
"v_mfma_f32_16x16x32_bf16 v[136:139], a[4:7], v[100:103], v[136:139]\n\t" \
"ds_read_b64_tr_b16 v[72:73], %[lz0] offset:14528\n\t" \
"ds_read_b64_tr_b16 v[74:75], %[lz0] offset:15424\n\t" \
"v_mul_f32 v128, %[scale], v128\n\t" \
"v_mfma_f32_16x16x32_bf16 v[140:143], a[36:39], v[100:103], v[140:143]\n\t" \
"ds_read_b64_tr_b16 v[76:77], %[lz0] offset:21696\n\t" \
"ds_read_b64_tr_b16 v[78:79], %[lz0] offset:22592\n\t" \
"v_mul_f32 v129, %[scale], v129\n\t" \
"v_mfma_f32_16x16x32_bf16 v[136:139], a[8:11], v[104:107], v[136:139]\n\t" \
"ds_read_b64_tr_b16 v[80:81], %[lz0] offset:28864\n\t" \
"ds_read_b64_tr_b16 v[82:83], %[lz0] offset:29760\n\t" \
"v_mul_f32 v130, %[scale], v130\n\t" \
"v_mfma_f32_16x16x32_bf16 v[140:143], a[40:43], v[104:107], v[140:143]\n\t" \
"ds_read_b64_tr_b16 v[84:85], %[lz0] offset:36032\n\t" \
"ds_read_b64_tr_b16 v[86:87], %[lz0] offset:36928\n\t" \
"v_mul_f32 v131, %[scale], v131\n\t" \
"v_mfma_f32_16x16x32_bf16 v[136:139], a[12:15], v[108:111], v[136:139]\n\t" \
"ds_read_b64_tr_b16 v[88:89], %[lz0] offset:43200\n\t" \
"ds_read_b64_tr_b16 v[90:91], %[lz0] offset:44096\n\t" \
"v_mul_f32 v132, %[scale], v132\n\t" \
"v_mfma_f32_16x16x32_bf16 v[140:143], a[44:47], v[108:111], v[140:143]\n\t" \
"ds_read_b64_tr_b16 v[92:93], %[lz0] offset:50368\n\t" \
"ds_read_b64_tr_b16 v[94:95], %[lz0] offset:51264\n\t" \
"v_mul_f32 v133, %[scale], v133\n\t" \
"v_mfma_f32_16x16x32_bf16 v[136:139], a[16:19], v[112:115], v[136:139]\n\t" \
"v_mul_f32 v134, %[scale], v134\n\t" \
"v_mul_f32 v135, %[scale], v135\n\t" \
"v_mfma_f32_16x16x32_bf16 v[140:143], a[48:51], v[112:115], v[140:143]\n\t" \
"v_cvt_pk_bf16_f32 v144, v128, v129\n\t" \
"v_cvt_pk_bf16_f32 v146, v130, v131\n\t" \
"v_mfma_f32_16x16x32_bf16 v[136:139], a[20:23], v[116:119], v[136:139]\n\t" \
"v_cvt_pk_bf16_f32 v145, v132, v133\n\t" \
"v_cvt_pk_bf16_f32 v147, v134, v135\n\t" \
"v_mfma_f32_16x16x32_bf16 v[140:143], a[52:55], v[116:119], v[140:143]\n\t" \
"buffer_store_dwordx4 v[144:147], %[vst], %[rw], s91 offen sc1\n\t" \
"s_add_u32 s91, s91, %[ss0]\n\t" \
"v_mfma_f32_16x16x32_bf16 v[136:139], a[24:27], v[120:123], v[136:139]\n\t" \
"v_mfma_f32_16x16x32_bf16 v[140:143], a[56:59], v[120:123], v[140:143]\n\t" \
"v_mfma_f32_16x16x32_bf16 v[136:139], a[28:31], v[124:127], v[136:139]\n\t" \
"v_mfma_f32_16x16x32_bf16 v[140:143], a[60:63], v[124:127], v[140:143]\n\t" \
"s_waitcnt lgkmcnt(0)\n\t" \
"v_mfma_f32_16x16x32_bf16 v[128:131], a[0:3], v[64:67], 0\n\t" \
"v_mfma_f32_16x16x32_bf16 v[132:135], a[32:35], v[64:67], 0\n\t" \
"v_mfma_f32_16x16x32_bf16 v[128:131], a[4:7], v[68:71], v[128:131]\n\t" \
"s_nop 2\n\t" \
"v_mul_f32 v136, %[scale], v136\n\t" \
"v_mfma_f32_16x16x32_bf16 v[132:135], a[36:39], v[68:71], v[132:135]\n\t" \
"v_mul_f32 v137, %[scale], v137\n\t" \
"v_mfma_f32_16x16x32_bf16 v[128:131], a[8:11], v[72:75], v[128:131]\n\t" \
"v_mul_f32 v138, %[scale], v138\n\t" \
"v_mfma_f32_16x16x32_bf16 v[132:135], a[40:43], v[72:75], v[132:135]\n\t" \
"v_mul_f32 v139, %[scale], v139\n\t" \
"v_mfma_f32_16x16x32_bf16 v[128:131], a[12:15], v[76:79], v[128:131]\n\t" \
"v_mul_f32 v140, %[scale], v140\n\t" \
"v_mfma_f32_16x16x32_bf16 v[132:135], a[44:47], v[76:79], v[132:135]\n\t" \
"v_mul_f32 v141, %[scale], v141\n\t" \
"v_mfma_f32_16x16x32_bf16 v[128:131], a[16:19], v[80:83], v[128:131]\n\t" \
"v_mul_f32 v142, %[scale], v142\n\t" \
"v_mul_f32 v143, %[scale], v143\n\t" \
"v_mfma_f32_16x16x32_bf16 v[132:135], a[48:51], v[80:83], v[132:135]\n\t" \
"v_cvt_pk_bf16_f32 v148, v136, v137\n\t" \
"v_cvt_pk_bf16_f32 v150, v138, v139\n\t" \
"v_mfma_f32_16x16x32_bf16 v[128:131], a[20:23], v[84:87], v[128:131]\n\t" \
"v_cvt_pk_bf16_f32 v149, v140, v141\n\t" \
"v_cvt_pk_bf16_f32 v151, v142, v143\n\t" \
"v_mfma_f32_16x16x32_bf16 v[132:135], a[52:55], v[84:87], v[132:135]\n\t" \
"buffer_store_dwordx4 v[148:151], %[vst], %[rw], s91 offen sc1\n\t" \
"s_add_u32 s91, s91, %[ss0]\n\t" \
"v_mfma_f32_16x16x32_bf16 v[128:131], a[24:27], v[88:91], v[128:131]\n\t" \
"v_mfma_f32_16x16x32_bf16 v[132:135], a[56:59], v[88:91], v[132:135]\n\t" \
"v_mfma_f32_16x16x32_bf16 v[128:131], a[28:31], v[92:95], v[128:131]\n\t" \
"v_mfma_f32_16x16x32_bf16 v[132:135], a[60:63], v[92:95], v[132:135]\n\t" \
"s_nop 6\n\t" \
"v_mul_f32 v128, %[scale], v128\n\t" \
"v_mul_f32 v129, %[scale], v129\n\t" \
"v_mul_f32 v130, %[scale], v130\n\t" \
"v_mul_f32 v131, %[scale], v131\n\t" \
"v_mul_f32 v132, %[scale], v132\n\t" \
"v_mul_f32 v133, %[scale], v133\n\t" \
"v_mul_f32 v134, %[scale], v134\n\t" \
"v_mul_f32 v135, %[scale], v135\n\t" \
"v_cvt_pk_bf16_f32 v144, v128, v129\n\t" \
"v_cvt_pk_bf16_f32 v146, v130, v131\n\t" \
"v_cvt_pk_bf16_f32 v145, v132, v133\n\t" \
"v_cvt_pk_bf16_f32 v147, v134, v135\n\t" \
"buffer_store_dwordx4 v[144:147], %[vst], %[rw], s91 offen sc1\n\t" \
"s_add_u32 s91, s91, %[ss0]\n\t" \
"s_waitcnt vmcnt(0) lgkmcnt(0)\n\t" \
"s_barrier\n\t"

#define ASM_P1M11_CLOB "v64", "v65", "v66", "v67", "v68", "v69", "v70", "v71", "v72", "v73", "v74", "v75", "v76", "v77", "v78", "v79", "v80", "v81", "v82", "v83", "v84", "v85", "v86", "v87", "v88", "v89", "v90", "v91", "v92", "v93", "v94", "v95", "v96", "v97", "v98", "v99", "v100", "v101", "v102", "v103", "v104", "v105", "v106", "v107", "v108", "v109", "v110", "v111", "v112", "v113", "v114", "v115", "v116", "v117", "v118", "v119", "v120", "v121", "v122", "v123", "v124", "v125", "v126", "v127", "v128", "v129", "v130", "v131", "v132", "v133", "v134", "v135", "v136", "v137", "v138", "v139", "v140", "v141", "v142", "v143", "v144", "v145", "v146", "v147", "v148", "v149", "v150", "v151", "a0", "a1", "a2", "a3", "a4", "a5", "a6", "a7", "a8", "a9", "a10", "a11", "a12", "a13", "a14", "a15", "a16", "a17", "a18", "a19", "a20", "a21", "a22", "a23", "a24", "a25", "a26", "a27", "a28", "a29", "a30", "a31", "a32", "a33", "a34", "a35", "a36", "a37", "a38", "a39", "a40", "a41", "a42", "a43", "a44", "a45", "a46", "a47", "a48", "a49", "a50", "a51", "a52", "a53", "a54", "a55", "a56", "a57", "a58", "a59", "a60", "a61", "a62", "a63", "s90", "s91", "scc", "memory"
#define DBGWS p.ws
template<int NR> __device__ __forceinline__ void wave_sum_n(float (&x)[NR]) {
    SB();
#pragma unroll
    for (int r = 0; r < NR; ++r) x[r] += dppf<0xB1>(x[r]);
    SB();
#pragma unroll
    for (int r = 0; r < NR; ++r) x[r] += dppf<0x4E>(x[r]);
    SB();
#pragma unroll
    for (int r = 0; r < NR; ++r) x[r] += dppf<0x141>(x[r]);
    SB();
#pragma unroll
    for (int r = 0; r < NR; ++r) x[r] += dppf<0x140>(x[r]);
    SB();
#pragma unroll
    for (int r = 0; r < NR; ++r) x[r] = add_x16(x[r]);
    SB();
#pragma unroll
    for (int r = 0; r < NR; ++r) x[r] = add_x32(x[r]);
    SB();
}
template<int NR>
__device__ __forceinline__ void ln_rows(v4u (&xv)[NR], const float (&ms)[8], const float (&mc)[8], int lane) {
    float sum[NR], sq[NR];
#pragma unroll
    for (int r = 0; r < NR; ++r) {
        sum[r] = ((bflo(xv[r].x) + bfhi(xv[r].x)) + (bflo(xv[r].y) + bfhi(xv[r].y))) + ((bflo(xv[r].z) + bfhi(xv[r].z)) + (bflo(xv[r].w) + bfhi(xv[r].w)));
    }
    wave_sum_n<NR>(sum);
#pragma unroll
    for (int r = 0; r < NR; ++r) sum[r] *= (1.f / D);
#pragma unroll
    for (int r = 0; r < NR; ++r) {
        float v[8] = {bflo(xv[r].x), bfhi(xv[r].x), bflo(xv[r].y), bfhi(xv[r].y), bflo(xv[r].z), bfhi(xv[r].z), bflo(xv[r].w), bfhi(xv[r].w)};
        float q = 0;
#pragma unroll
        for (int i = 0; i < 8; ++i) { float d = v[i] - sum[r]; q += d * d; }
        sq[r] = lane < 56 ? q : 0.f;
    }
    wave_sum_n<NR>(sq);
#pragma unroll
    for (int r = 0; r < NR; ++r) {
        float v[8] = {bflo(xv[r].x), bfhi(xv[r].x), bflo(xv[r].y), bfhi(xv[r].y), bflo(xv[r].z), bfhi(xv[r].z), bflo(xv[r].w), bfhi(xv[r].w)};
        float iden = __builtin_amdgcn_rsqf(sq[r] * (1.f / D) + 1e-5f);
        float o[8];
#pragma unroll
        for (int i = 0; i < 8; ++i) o[i] = ((v[i] - sum[r]) * iden) * mc[i] + ms[i];
        xv[r].x = pk2(o[0], o[1]); xv[r].y = pk2(o[2], o[3]); xv[r].z = pk2(o[4], o[5]); xv[r].w = pk2(o[6], o[7]);
    }
}
__device__ __forceinline__ f32x4 load_mod4(rsrc_t rw, int e) {
    v4u a, b;
    for (int spins = 0; spins < (1 << 22); ++spins) {
        a = LD16(rw, (int)O_MOD + e * 8); b = LD16(rw, (int)O_MOD + e * 8 + 16);
        asm volatile("" ::: "memory");
        if ((a.y & a.w & b.y & b.w) == 1u) break;
        __builtin_amdgcn_s_sleep(4);
    }
    return (f32x4){__uint_as_float(a.x), __uint_as_float(a.z), __uint_as_float(b.x), __uint_as_float(b.z)};
}
__device__ __forceinline__ void mod_wave(const Ptrs& p, int v, int lane) {
    const rsrc_t rw = mkrs(p.ws);
    const int c2 = lane & 1, ks = lane >> 1;
    v4u wv[14]; u16 cv[14];
#pragma unroll
    for (int i = 0; i < 14; ++i) { wv[i] = *(const v4u*)(p.wmod + (size_t)(ks * 14 + i) * (6 * D) + v * 16 + c2 * 8); cv[i] = p.c[ks * 14 + i]; }
    SB();
    __builtin_amdgcn_s_barrier();
    SB();
    float a[8] = {0, 0, 0, 0, 0, 0, 0, 0};
#pragma unroll
    for (int i = 0; i < 14; ++i) {
        float c = bf2f(cv[i]);
        float sl = rbf(c / (1.f + __expf(-c)));
        a[0] += sl * bflo(wv[i].x); a[1] += sl * bfhi(wv[i].x); a[2] += sl * bflo(wv[i].y); a[3] += sl * bfhi(wv[i].y);
        a[4] += sl * bflo(wv[i].z); a[5] += sl * bfhi(wv[i].z); a[6] += sl * bflo(wv[i].w); a[7] += sl * bfhi(wv[i].w);
    }
#pragma unroll
    for (int i = 0; i < 8; ++i) a[i] += dppf<0x4E>(a[i]);
#pragma unroll
    for (int i = 0; i < 8; ++i) a[i] += dppf<0x124>(a[i]);
#pragma unroll
    for (int i = 0; i < 8; ++i) a[i] += dppf<0x128>(a[i]);
#pragma unroll
    for (int i = 0; i < 8; ++i) a[i] = add_x32(add_x16(a[i]));
    const int e = (lane >> 1) & 7;
    float r = a[0];
#pragma unroll
    for (int i = 1; i < 8; ++i) r = e == i ? a[i] : r;
    if (lane < 16) { v2u gr = {__float_as_uint(rbf(r)), 1u}; ST8(gr, rw, (int)O_MOD + (v * 16 + c2 * 8 + e) * 8); }
}
__device__ __forceinline__ void ln_tile(const Ptrs& p, char* lds, v4u (&xv)[2], int v, int w, int lane) {
    const rsrc_t rw = mkrs(p.ws);
    const int tid = w * 64 + lane;
    float ms[8], mc[8];
    {
        const int c0 = lane < 56 ? lane * 8 : 0;
        v4u m[8];
        for (int spins = 0; spins < (1 << 22); ++spins) {
#pragma unroll
            for (int i = 0; i < 8; ++i) m[i] = LD16(rw, (int)O_MOD + ((i >> 2) * D + c0) * 8 + (i & 3) * 16);
            asm volatile("" ::: "memory");
            unsigned t = 1;
#pragma unroll
            for (int i = 0; i < 8; ++i) t &= m[i].y & m[i].w;
            if (__all(t == 1u)) break;
            __builtin_amdgcn_s_sleep(4);
        }
#pragma unroll
        for (int i = 0; i < 4; ++i) {
            ms[2 * i] = __uint_as_float(m[i].x); ms[2 * i + 1] = __uint_as_float(m[i].z);
            mc[2 * i] = rbf(1.f + __uint_as_float(m[4 + i].x)); mc[2 * i + 1] = rbf(1.f + __uint_as_float(m[4 + i].z));
        }
    }
    ln_rows<2>(xv, ms, mc, lane);
    if (lane < 56) {
#pragma unroll
        for (int r = 0; r < 2; ++r) {
            const int tok = w * 2 + r;
            *(v4u*)(lds + lane * 144 + tok * 16) = xv[r];
            const int sel = lane >= 28, l8 = lane - 28 * sel;
            unsigned* hfp = (unsigned*)(lds + 8064 + (2 * l8) * 144 + tok * 16) + sel;
            hfp[0] = xv[r].x; hfp[2] = xv[r].y; hfp[36] = xv[r].z; hfp[38] = xv[r].w;
        }
    }
    __syncthreads();
#pragma unroll
    for (int k = 0; k < 4; ++k) {
        int i = k * 256 + tid;
        if (i < 896) {
            v4u q = *(const v4u*)(lds + (i >> 3) * 144 + (i & 7) * 16);
            const int row = i >> 3, rr = row < 56 ? row : row - 56;
            ST16(q, rw, (row < 56 ? (int)O_HN : (int)O_HF) + (v >> 1) * 14336 + (rr * 16 + (v & 1) * 8 + (i & 7)) * 16);
        }
    }
}
__device__ __forceinline__ void fill_step(const Ptrs& p, rsrc_t rw, rsrc_t rx, unsigned parmask, int s, int k0, int stride, int kend) {
    const v4u sv = {SENT, SENT, SENT, SENT};
    const int base = step_base(s, parmask);
    for (int k = k0; k < kend; k += stride) {
        int off;
        if (k < 456) off = k * 16;
        else if (k < 968) off = SMM_OFF + (k - 456) * 16;
        else if (k < 1096) off = ML_OFF + (k - 968) * 16;
        else { int k2 = k - 1096; off = OX_OFF + (k2 >> 9) * 16384 + (k2 & 511) * 16; }
        v4u q = sv;
        if (s == 0 && k < 448) {
            int l2 = k & 63, c = k >> 6, r = l2 & 15, ga = l2 >> 4;
            q = *(const v4u*)(p.mem0 + (r & 7) * D + 224 * (r >> 3) + 32 * c + 8 * ga);
            q.x = min(q.x, 0xfffffffeu); q.y = min(q.y, 0xfffffffeu); q.z = min(q.z, 0xfffffffeu); q.w = min(q.w, 0xfffffffeu);
        }
        ST16(q, rx, base + off);
    }
    if (kend > 1096) for (int k = k0; k < 7168; k += stride) ST16(sv, rw, (int)O_GM + s * 114688 + k * 16);
}
__device__ __forceinline__ void calib_wave(const Ptrs& p, char* xbuf, int r, int sub, int lane, unsigned far_side) {
    unsigned* flags = (unsigned*)(p.ws + O_FLAG);
    const rsrc_t rx = mkrs(xbuf);
    {
        const int k = lane >> 3;
        v4u z = {0, 0, 0, 0};
        if (lane < 64) ST16(z, rx, r * (2 << 20) + 126 * 16384 + (k & 1) * 8192 + (k >> 1) * 2048 + sub * 256 + (lane & 7) * 16);
        DRAIN();
    }
    if (lane == 0) {
        unsigned t0 = 0xffffffffu, t1 = 0xffffffffu;
#pragma unroll 1
        for (int k = 0; k < 6; ++k) {
            unsigned long long c0 = __builtin_amdgcn_s_memtime();
            unsigned q = LD4(rx, r * (2 << 20) + 126 * 16384 + (k & 1) * 8192 + (k >> 1) * 2048 + sub * 256);
            asm volatile("s_waitcnt vmcnt(0)" :: "v"(q) : "memory");
            unsigned dt = (unsigned)(__builtin_amdgcn_s_memtime() - c0);
            if (k >= 2) { if (k & 1) t1 = min(t1, dt); else t0 = min(t0, dt); }
        }
        unsigned o = __hip_atomic_fetch_add(flags + F_VOTE + r, (t1 < t0 ? 1u : 0u) ^ far_side, __ATOMIC_RELAXED, __HIP_MEMORY_SCOPE_AGENT);
        asm volatile("" :: "v"(o) : "memory");
        unsigned cnt = __hip_atomic_fetch_add(flags + F_PAR, 1u, __ATOMIC_RELAXED, __HIP_MEMORY_SCOPE_AGENT);
        if (cnt + 1 == 8 * NREG) {
            unsigned vt[NREG]; int agree = 0;
#pragma unroll
            for (int q = 0; q < NREG; ++q) vt[q] = __hip_atomic_load(flags + F_VOTE + q, __ATOMIC_RELAXED, __HIP_MEMORY_SCOPE_AGENT);
#pragma unroll
            for (int q = 0; q < NREG; ++q) agree += (q & 1) ? (int)vt[q] : 8 - (int)vt[q];
            const unsigned p0 = agree >= 4 * NREG ? 0u : 1u;
            unsigned mask = 0;
#pragma unroll
            for (int q = 0; q < NREG; ++q) { unsigned bit = vt[q] >= 7 ? 1u : vt[q] <= 1 ? 0u : (p0 ^ (q & 1)); mask |= bit << q; }
            __hip_atomic_store(flags + R_PAR, mask | (1u << 24), __ATOMIC_RELAXED, __HIP_MEMORY_SCOPE_AGENT);
        }
    }
}
__device__ __forceinline__ void startup_fill(const Ptrs& p, char* xbuf, int v, int w, int lane) {
    unsigned* flags = (unsigned*)(p.ws + O_FLAG);
    const rsrc_t rw = mkrs(p.ws), rx = mkrs(xbuf);
    const int tid = w * 64 + lane;
    wg_wait(flags, R_PAR, 1, w);
    const unsigned parmask = __hip_atomic_load(flags + R_PAR, __ATOMIC_RELAXED, __HIP_MEMORY_SCOPE_AGENT) & 0xffffffu;
#pragma unroll 1
    for (int q = 0; q < 4; ++q) fill_step(p, rw, rx, parmask, q, v * 256 + tid, 60 * 256, 15432);
    fill_step(p, rw, rx, parmask, 4 + v, tid, 256, 1096);
    wg_arrive(flags, F_FILL, 60, R_FILL);
}
#define SCHED 1
#define STAGGER 1
__device__ __forceinline__ void glds16(const void* gsrc, unsigned lds_dst) {
    unsigned keep;
    asm volatile("s_mov_b32 %0, m0\n\ts_mov_b32 m0, %2\n\ts_nop 0\n\tglobal_load_lds_dwordx4 %1, off\n\ts_mov_b32 m0, %0" : "=&s"(keep) : "v"(gsrc), "s"(lds_dst) : "memory");
}
template<int MTX>
__device__ __forceinline__ void p1_task(const Ptrs& p, char* lds, int h, int qd, int eh, int w, int lane, int modv, int lnv) {
    unsigned* flags = (unsigned*)(p.ws + O_FLAG);
    const rsrc_t rw = mkrs(p.ws);
    const int g = lane >> 4, n = lane & 15;
    const unsigned l0 = (unsigned)(size_t)(LAS char*)lds;
    v4u xv[2];
#pragma unroll
    for (int r = 0; r < 2; ++r) { xv[r] = (v4u){0, 0, 0, 0}; if (lane < 56) xv[r] = *(const v4u*)(p.x + (size_t)(lnv * 8 + w * 2 + r) * D + lane * 8); }
#pragma unroll
    for (int k = 0; k < 14; ++k) {
        if (MTX == 0) {
            const int q = 4 * k + w, row = 2 * q + (lane >> 5), ch = (lane & 31) ^ (row & 15);
            glds16(p.Wq + ((size_t)(112 * qd + row) * H + h) * DH + 8 * ch, l0 + 40960 + q * 1024);
        } else {
            const int u = (4 * k + w) * 64 + lane, f = u / 14, c8 = u % 14;
            glds16(p.Wo + ((size_t)h * DH + f) * D + 112 * qd + 8 * c8, l0 + 40960 + (4 * k + w) * 1024);
        }
    }
    const rsrc_t rx = mkrs(MTX == 0 ? p.Wk : p.Wv);
    const int x0 = 7 * eh + 2 * w;
    const int voff = (((16 * x0 + n) * H + h) * DH + 8 * g) * 2;
    if (w < 3) asm volatile(ASM_P1L2_TEXT :: [voff] "v"(voff), [rx] "s"(rx) : ASM_P1L2_CLOB);
    else asm volatile(ASM_P1L1_TEXT :: [voff] "v"(voff), [rx] "s"(rx) : ASM_P1L1_CLOB);
    if (MTX == 0 && w == 0 && lnv < 64) TS(5120 + lnv * 4 + 1);
    ln_tile(p, lds, xv, lnv, w, lane);
    DRAIN();
    __builtin_amdgcn_s_barrier();
    if (w == 3 && lane == 0) arrive(flags, F_LN, 256, R_LN);
    if (MTX == 0 && w == 0 && lnv < 64) TS(5120 + lnv * 4 + 2);
    if (MTX == 0 && w == 0 && modv < 64) TS(5120 + modv * 4 + 3);
    const float scale = MTX == 0 ? 0.0625f * 1.4426950408889634f : 1.f;
    unsigned lz[4];
#pragma unroll
    for (int q = 0; q < 4; ++q) lz[q] = MTX == 0 ? l0 + 40960 + n * 512 + ((q ^ (n >> 2)) * 64) + ((g ^ (n & 3)) * 16) : l0 + 40960 + (8 * g + (n >> 2)) * 224 + (n & 3) * 8;
    int sb[2], ss[2];
#pragma unroll
    for (int q = 0; q < 2; ++q) {
        const int x = x0 + q, c7 = x >> 1, gh = x & 1;
        if (MTX == 0) {
            const int hf = qd >> 1, tt0 = 7 * (qd & 1);
            const int op0 = c7 < 6 ? (c7 >> 1) * 28 + tt0 * 2 + (c7 & 1) : 84 + tt0;
            sb[q] = (int)O_WAF + (((2 * h + hf) * 98 + op0) * 2 + gh) * 1024; ss[q] = c7 < 6 ? 4096 : 2048;
        } else { sb[q] = (int)O_WBF + ((7 * qd * 112 + h * 7 + c7) * 2 + gh) * 1024; ss[q] = 112 * 2048; }
    }
    const int vst = lane * 16;
#define P1M_INS [lz0] "v"(lz[0]), [lz1] "v"(lz[1]), [lz2] "v"(lz[2]), [lz3] "v"(lz[3]), [vst] "v"(vst), [rw] "s"(rw), [scale] "s"(scale), [sb0] "s"(sb[0]), [sb1] "s"(sb[1]), [ss0] "s"(ss[0]), [ss1] "s"(ss[1])
    if (MTX == 0) { if (w < 3) asm volatile(ASM_P1M20_TEXT :: P1M_INS : ASM_P1M20_CLOB); else asm volatile(ASM_P1M10_TEXT :: P1M_INS : ASM_P1M10_CLOB); }
    else { if (w < 3) asm volatile(ASM_P1M21_TEXT :: P1M_INS : ASM_P1M21_CLOB); else asm volatile(ASM_P1M11_TEXT :: P1M_INS : ASM_P1M11_CLOB); }
    if (w == 0 && lane == 0) {
        __hip_atomic_fetch_add(flags + (MTX == 0 ? F_WA : F_WB2) + (2 * h + (qd >> 1)) * 32, 1u, __ATOMIC_RELAXED, __HIP_MEMORY_SCOPE_AGENT);
        if (MTX == 1) __hip_atomic_fetch_add(flags + F_WB + qd * 32, 1u, __ATOMIC_RELAXED, __HIP_MEMORY_SCOPE_AGENT);
    }
}
template<int MTX>
__device__ __forceinline__ void p3_role(const Ptrs& p, char* lds, int h, int hf, int par, int w, int lane) {
    unsigned* flags = (unsigned*)(p.ws + O_FLAG);
    const int g = lane >> 4, n = lane & 15;
    const unsigned l0 = (unsigned)(size_t)(LAS char*)lds;
    const rsrc_t rw = mkrs(p.ws), rh = mkrs(p.ws + O_HF), ro = mkrs(p.ws + (MTX == 0 ? O_GXF : O_UXF)), rc = mkrs(flags + F_GU);
    const int tt0 = w < 2 ? 4 * w : 8 + 3 * (w - 2), T0 = 14 * hf + tt0;
    const int pc0 = (w < 2 ? 4 * w : 8 + 10 * (w - 2)) * 1024;
    const unsigned lr = l0 + lane * 16, lg = l0 + pc0;
    const int vg = pc0 + lane * 16, vw = lane * 16;
    const int src0 = par * 28672, so0 = (par - 2) * 458752, ao0 = (par * 16 + h) * 4;
    const int wa = MTX == 0 ? (int)O_WAF + (2 * h + hf) * 98 * 2048 + tt0 * 4096 : (int)O_WBF + (T0 * 112 + h * 7) * 2048;
    const int wb = (int)O_WAF + ((2 * h + hf) * 98 + 84 + tt0) * 2048;
    int vs[2];
#pragma unroll
    for (int kap = 0; kap < 2; ++kap)
        vs[kap] = MTX == 0 ? ((((h * 2 + kap) * 14 + (T0 >> 1)) * 64 + (2 * (T0 & 1) + (g >> 1)) * 16 + n) * 16 + 8 * (g & 1)) : ((h * 28 + T0) * 64 + lane) * 16;
    if (w == 0) { poll_ge(flags, R_LN, 1, 0); __builtin_amdgcn_fence(__ATOMIC_ACQUIRE, "agent"); DRAIN(); }
    __syncthreads();
    if (w == 0) TS(1024 + (par * 64 + MTX * 32 + h * 2 + hf) * 8 + 3);
    if (w >= 2) asm volatile(ASM_P3D_TEXT :: [lg] "s"(lg), [vg] "v"(vg), [rh] "s"(rh), [src0] "s"(src0) : ASM_P3D_CLOB);
    else asm volatile(ASM_P3D4_TEXT :: [lg] "s"(lg), [vg] "v"(vg), [rh] "s"(rh), [src0] "s"(src0) : ASM_P3D_CLOB);
    if (w == 0) poll_ge(flags, (MTX == 0 ? F_WA : F_WB2) + (2 * h + hf) * 32, 4, 0);
    __builtin_amdgcn_s_barrier();
    if (w == 0) TS(1024 + (par * 64 + MTX * 32 + h * 2 + hf) * 8 + 2);
#define P3W_INS [vw] "v"(vw), [rw] "s"(rw), [wa] "s"(wa), [wb] "s"(wb)
    if (MTX == 0) { if (w < 2) asm volatile(ASM_P3W_04_TEXT :: P3W_INS : ASM_P3W_04_CLOB); else asm volatile(ASM_P3W_03_TEXT :: P3W_INS : ASM_P3W_03_CLOB); }
    else { if (w < 2) asm volatile(ASM_P3W_14_TEXT :: P3W_INS : ASM_P3W_14_CLOB); else asm volatile(ASM_P3W_13_TEXT :: P3W_INS : ASM_P3W_13_CLOB); }
#define P3_INS [lr] "v"(lr), [lg] "s"(lg), [vg] "v"(vg), [vw] "v"(vw), [rw] "s"(rw), [rh] "s"(rh), [ro] "s"(ro), [rc] "s"(rc), [src0] "s"(src0), [so0] "s"(so0), [ao0] "s"(ao0), \
        [wa] "s"(wa), [wb] "s"(wb), [vs0] "v"(vs[0]), [vs1] "v"(vs[1])
    if (MTX == 0) {
        if (w == 0) asm volatile(ASM_P3_0W0_TEXT :: P3_INS : ASM_P3_0W0_CLOB);
        else if (w == 1) asm volatile(ASM_P3_0W1_TEXT :: P3_INS : ASM_P3_0W1_CLOB);
        else asm volatile(ASM_P3_0W2_TEXT :: P3_INS : ASM_P3_0W2_CLOB);
    } else {
        if (w == 0) asm volatile(ASM_P3_1W0_TEXT :: P3_INS : ASM_P3_1W0_CLOB);
        else if (w == 1) asm volatile(ASM_P3_1W1_TEXT :: P3_INS : ASM_P3_1W1_CLOB);
        else asm volatile(ASM_P3_1W2_TEXT :: P3_INS : ASM_P3_1W2_CLOB);
    }
    if (w == 0) TS(1024 + (par * 64 + MTX * 32 + h * 2 + hf) * 8 + 4);
}

__device__ __forceinline__ float gelu_tanh(float a) {
    float u = 0.7978845608028654f * (a + 0.044715f * a * a * a);
    float e = __expf(-2.f * u);
    return a * __builtin_amdgcn_rcpf(1.f + e);
}

__device__ __forceinline__ unsigned mx4(v4u v) { return max(max(v.x, v.y), max(v.z, v.w)); }
__device__ __forceinline__ v4u cn4(v4u v) { v.x = min(v.x, 0xfffffffeu); v.y = min(v.y, 0xfffffffeu); v.z = min(v.z, 0xfffffffeu); v.w = min(v.w, 0xfffffffeu); return v; }
__device__ __forceinline__ v2u cn2(v2u v) { v.x = min(v.x, 0xfffffffeu); v.y = min(v.y, 0xfffffffeu); return v; }
__device__ __forceinline__ float dot2bf(unsigned a, unsigned b, float c) { return __builtin_amdgcn_fdot2_f32_bf16(__builtin_bit_cast(bf16x2, a), __builtin_bit_cast(bf16x2, b), c, false); }
#define PIN_A(x) asm volatile("" : "+a"(x))
#define RETRY_TAIL(bad) if (!__any(bad)) break; __builtin_amdgcn_s_sleep(2); if (++spins > (1 << 20)) { __hip_atomic_store(flags + F_ABORT, 1u, __ATOMIC_RELAXED, __HIP_MEMORY_SCOPE_AGENT); break; } \
    if ((spins & 255) == 255 && __builtin_amdgcn_readfirstlane(__hip_atomic_load(flags + F_ABORT, __ATOMIC_RELAXED, __HIP_MEMORY_SCOPE_AGENT))) break;
template<int SLP = 0>
__device__ __forceinline__ void probe(unsigned* flags, rsrc_t rw, int off) {
    for (int spins = 0;;) {
        unsigned v = LD4(rw, off);
        asm volatile("" ::: "memory");
        if (SLP && __any(v == SENT)) __builtin_amdgcn_s_sleep(SLP);
        RETRY_TAIL(v == SENT)
    }
}
typedef short s16x4 __attribute__((ext_vector_type(4)));
__device__ __forceinline__ v2u tr_read(const char* q) { return __builtin_bit_cast(v2u, __builtin_amdgcn_ds_read_tr16_b64_v4i16((LAS s16x4*)q)); }
__device__ __forceinline__ void build_ffn(const Ptrs& p, char* lds, int j, int w, int lane) {
    unsigned* flags = (unsigned*)(p.ws + O_FLAG);
    const rsrc_t rw = mkrs(p.ws);
    const int g = lane >> 4, n = lane & 15, tid = w * 64 + lane;
    const int fo = (int)O_FFW + j * 65 * 4096 + tid * 16;
    float csv[2][4], b1v[2][4];
    const unsigned l0 = (unsigned)(size_t)(LAS char*)lds;
    const int jj = j;
#pragma unroll
    for (int k = 0; k < 28; ++k) { int i = k * 256 + tid, d = i >> 4, c8 = i & 15; glds16(p.w1 + (size_t)d * F + 128 * jj + 8 * c8, l0 + (k * 4 + w) * 1024); }
    {
        float* mt = (float*)(lds + 150528);
        float* tr = (float*)(lds + 154624);
        for (int i = tid; i < 224; i += 256) {
            f32x4 f = load_mod4(rw, 3 * D + i * 4);
            if (i >= 112) { f[0] = rbf(1.f + f[0]); f[1] = rbf(1.f + f[1]); f[2] = rbf(1.f + f[2]); f[3] = rbf(1.f + f[3]); }
            *(f32x4*)(mt + i * 4) = f;
        }
        DRAIN();
        __syncthreads();
        float ab[2] = {0, 0}, ac[2] = {0, 0};
#pragma unroll
        for (int c = 0; c < 14; ++c) {
            f32x4 s0 = *(const f32x4*)(mt + 32 * c + 8 * g), s1 = *(const f32x4*)(mt + 32 * c + 8 * g + 4);
            f32x4 c0 = *(const f32x4*)(mt + 448 + 32 * c + 8 * g), c1 = *(const f32x4*)(mt + 448 + 32 * c + 8 * g + 4);
            v2u v[2][2];
#pragma unroll
            for (int t = 0; t < 2; ++t)
#pragma unroll
                for (int hh = 0; hh < 2; ++hh) v[t][hh] = tr_read(lds + (32 * c + 8 * g + 4 * hh + (n >> 2)) * 256 + (16 * (2 * w + t) + 4 * (n & 3)) * 2);
#pragma unroll
            for (int t = 0; t < 2; ++t) {
                float wv[8];
#pragma unroll
                for (int q = 0; q < 8; ++q) { unsigned dw = (q & 2) ? v[t][q >> 2].y : v[t][q >> 2].x; wv[q] = (q & 1) ? bfhi(dw) : bflo(dw); ab[t] += wv[q] * (q < 4 ? s0[q & 3] : s1[q & 3]); wv[q] *= (q < 4 ? c0[q & 3] : c1[q & 3]); }
                v4u o = {pk2(wv[0], wv[1]), pk2(wv[2], wv[3]), pk2(wv[4], wv[5]), pk2(wv[6], wv[7])};
                ac[t] = dot2bf(o.x, 0x3f803f80u, ac[t]); ac[t] = dot2bf(o.y, 0x3f803f80u, ac[t]); ac[t] = dot2bf(o.z, 0x3f803f80u, ac[t]); ac[t] = dot2bf(o.w, 0x3f803f80u, ac[t]);
                ST16(o, rw, fo + (t * 14 + c) * 4096);
            }
            if (c & 1) SB();
        }
#pragma unroll
        for (int t = 0; t < 2; ++t) {
            ab[t] = add_x32(add_x16(ab[t])); ac[t] = add_x32(add_x16(ac[t]));
            if (g == 0) { tr[((w * 2 + t) * 2 + 0) * 16 + n] = ab[t]; tr[((w * 2 + t) * 2 + 1) * 16 + n] = ac[t]; }
        }
        __syncthreads();
#pragma unroll
        for (int k = 0; k < 28; ++k) { int i = (w * 28 + k) * 64 + lane, f = i >> 2, c8 = i & 3; glds16(p.w2 + (size_t)f * D + 32 * jj + 8 * c8, l0 + (w * 28 + k) * 1024); }
#pragma unroll
        for (int t = 0; t < 2; ++t) {
            v2u bv = *(const v2u*)(p.b1 + 16 * (8 * jj + 2 * w + t) + 4 * g);
            f32x4 xb = *(const f32x4*)(tr + ((w * 2 + t) * 2 + 0) * 16 + 4 * g), xc = *(const f32x4*)(tr + ((w * 2 + t) * 2 + 1) * 16 + 4 * g);
            b1v[t][0] = bflo(bv.x) + xb[0]; b1v[t][1] = bfhi(bv.x) + xb[1]; b1v[t][2] = bflo(bv.y) + xb[2]; b1v[t][3] = bfhi(bv.y) + xb[3];
            csv[t][0] = xc[0]; csv[t][1] = xc[1]; csv[t][2] = xc[2]; csv[t][3] = xc[3];
        }
        DRAIN();
#pragma unroll
        for (int c = 0; c < 14; ++c) {
#pragma unroll
            for (int t = 0; t < 2; ++t) {
                v2u a = tr_read(lds + (32 * (14 * w + c) + 4 * g + (n >> 2)) * 64 + (16 * t + 4 * (n & 3)) * 2);
                v2u b = tr_read(lds + (32 * (14 * w + c) + 16 + 4 * g + (n >> 2)) * 64 + (16 * t + 4 * (n & 3)) * 2);
                ST16(((v4u){a.x, a.y, b.x, b.y}), rw, fo + (28 + t * 14 + c) * 4096);
            }
        }
    }
#pragma unroll
    for (int t = 0; t < 2; ++t) {
        ST16(((v4u){__float_as_uint(csv[t][0]), __float_as_uint(csv[t][1]), __float_as_uint(csv[t][2]), __float_as_uint(csv[t][3])}), rw, fo + (56 + t) * 4096);
        ST16(((v4u){__float_as_uint(b1v[t][0]), __float_as_uint(b1v[t][1]), __float_as_uint(b1v[t][2]), __float_as_uint(b1v[t][3])}), rw, fo + (58 + t) * 4096);
    }
    {
        v2u b2v[2];
#pragma unroll
        for (int t = 0; t < 2; ++t) {
            const int oc = 16 * (2 * j + t) + 4 * g;
            ST16(__builtin_bit_cast(v4u, load_mod4(rw, 2 * D + oc)), rw, fo + (60 + t) * 4096);
            ST16(__builtin_bit_cast(v4u, load_mod4(rw, 5 * D + oc)), rw, fo + (62 + t) * 4096);
            b2v[t] = *(const v2u*)(p.b2 + oc);
        }
        ST16(((v4u){b2v[0].x, b2v[0].y, b2v[1].x, b2v[1].y}), rw, fo + 64 * 4096);
    }
    asm volatile("Lcode_mark_c:");
    wg_signal(flags, F_FFW);
}
template<int NR>
__device__ __forceinline__ void x_pass(const Ptrs& p, unsigned* flags, rsrc_t rw, rsrc_t rgx, rsrc_t rux, rsrc_t rxb, unsigned parmask, v4u (&W1)[2][14], v4u (&W2)[2][14],
                                       v4u* cst, float* red, char* xl, float* stl, int RB, int j, int jj, bool act, int w, int lane) {
        const int g = lane >> 4, n = lane & 15, SG0 = RB >> 1;
#pragma unroll
        for (int t = 0; t < 2; ++t)
#pragma unroll
            for (int c = 0; c < 14; ++c) { PIN_A(W1[t][c]); PIN_A(W2[t][c]); }
        if (j == 0 && w == 0) TS(4096 + (RB >> 2) * 8 + 0);
        const int wr = NR == 4 ? w : (w & 1);
        const bool rowact = NR == 4 || w < 2;
        const int sw = SG0 + (wr >> 1), qt = wr & 1;
        const size_t ro = (size_t)(RB * 16 + wr * 16 + n) * D + 32 * jj + 4 * g;
        v4u U[2][4][2]; v2u xv[2];
        const unsigned prog = __hip_atomic_load(flags + R_FS, __ATOMIC_RELAXED, __HIP_MEMORY_SCOPE_AGENT);
        {
            const int qo = (int)O_HN + ((RB + wr) * 14 * 64 + lane) * 16;
            const int ko = ((sw * 16 + j) * 28 * 64 + lane) * 16;
            v4u q[14];
            f32x4 S0 = {0, 0, 0, 0}, S1 = {0, 0, 0, 0}, Sm = {0, 0, 0, 0};
            {
                v4u k0[14];
#pragma unroll
                for (int c = 0; c < 14; ++c) { q[c] = LD16(rw, qo + c * 1024); k0[c] = LD16(rgx, ko + c * 1024); }
                if (qt) {
                    v4u k1[14];
#pragma unroll
                    for (int c = 0; c < 14; ++c) k1[c] = LD16(rgx, ko + (14 + c) * 1024);
                    SB();
#pragma unroll
                    for (int c = 0; c < 14; ++c) S1 = mf32(k1[c], q[c], S1);
                    asm volatile("s_nop 7\n\ts_nop 3" : "+a"(S1));
                }
                SB();
#pragma unroll
                for (int c = 0; c < 14; ++c) S0 = mf32(k0[c], q[c], S0);
                asm volatile("s_nop 7\n\ts_nop 3" : "+a"(S0));
            }
            SB();
            {
                const int mo = (int)O_GM + (sw * 16 + j) * 7168;
                const bool ahead = (int)__builtin_amdgcn_readfirstlane(prog) > sw;
                if (!ahead) {
#pragma unroll
                    for (int sg = 0; sg < NR / 2; ++sg)
#pragma unroll
                        for (int k = 0; k < 4; ++k)
#pragma unroll
                            for (int t = 0; t < 2; ++t) U[sg][k][t] = LD16(rux, ((((SG0 + sg) * 16 + 4 * w + k) * 28 + 2 * jj + t) * 64 + lane) * 16);
                    if (sw >= 4) poll_ge(flags, R_FS, sw - 2, 0);
                    probe<4>(flags, rw, mo + (lane < 14 ? lane : 0) * 512 + 508);
                }
                if (j == 0 && w == 3) TS(4096 + (RB >> 2) * 8 + 1);
                v4u km[14];
                for (int spins = 0;;) {
                    unsigned mx = 0;
#pragma unroll
                    for (int c = 0; c < 14; ++c) km[c] = LD16(rw, mo + ((c * 4 + g) * 8 + (n & 7)) * 16);
                    asm volatile("" ::: "memory");
                    SB();
#pragma unroll
                    for (int c = 0; c < 14; ++c) mx = max(mx, mx4(km[c]));
                    RETRY_TAIL(mx == SENT)
                }
                if (ahead) {
#pragma unroll
                    for (int sg = 0; sg < NR / 2; ++sg)
#pragma unroll
                        for (int k = 0; k < 4; ++k)
#pragma unroll
                            for (int t = 0; t < 2; ++t) U[sg][k][t] = LD16(rux, ((((SG0 + sg) * 16 + 4 * w + k) * 28 + 2 * jj + t) * 64 + lane) * 16);
                }
                xv[0] = *(const v2u*)(p.x + ro); xv[1] = *(const v2u*)(p.x + ro + 16);
                SB();
                f32x4 Sb = {0, 0, 0, 0};
#pragma unroll
                for (int c = 0; c < 7; ++c) { Sm = mf32(km[2 * c], q[2 * c], Sm); Sb = mf32(km[2 * c + 1], q[2 * c + 1], Sb); }
                Sm += Sb;
            }
            const float NEG = -1e30f;
            float v[12];
#pragma unroll
            for (int i = 0; i < 4; ++i) {
                int kk = 4 * g + i;
                v[i] = g < 2 ? Sm[i] : NEG;
                v[4 + i] = (qt == 1 || kk <= n) ? S0[i] : NEG;
                v[8 + i] = (qt == 1 && kk <= n) ? S1[i] : NEG;
            }
            float m = v[0];
#pragma unroll
            for (int i = 1; i < 12; ++i) m = fmaxf(m, v[i]);
            m = max_x16(m); m = max_x32(m);
            float l = 0;
#pragma unroll
            for (int i = 0; i < 12; ++i) { v[i] = __builtin_amdgcn_exp2f(v[i] - m); l += v[i]; }
            l = add_x16(l); l = add_x32(l);
            float il = __builtin_amdgcn_rcpf(l);
            v4u px; px.x = pk2(v[4] * il, v[5] * il); px.y = pk2(v[6] * il, v[7] * il); px.z = pk2(v[8] * il, v[9] * il); px.w = pk2(v[10] * il, v[11] * il);
            if (rowact) ST16(cn4(px), rw, (int)O_PX + (((sw * 16 + j) * 2 + qt) * 64 + lane) * 16);
            v2u pm; pm.x = pk2(v[0] * il, v[1] * il); pm.y = pk2(v[2] * il, v[3] * il);
            if (rowact && g < 2) ST8(cn2(pm), rw, (int)O_PM + ((sw * 2 + qt) * 16 + j) * 256 + n * 16 + 8 * g);
        }
        if (!act) return;
        v2u o1[2];
        {
            v4u Y[2][2], P[4][4], PMv[4];
            { const int k = lane & 3, R = (lane >> 2) & (NR - 1);
              const int a0 = (int)O_PX + ((((SG0 + (R >> 1)) * 16 + 4 * w + k) * 2 + (R & 1)) * 64 + 63) * 16 + 12;
              const int a1 = (int)O_PM + ((RB + R) * 16 + 4 * w + k) * 256 + 252;
              probe(flags, rw, (lane & 16) ? a1 : a0); }
            for (int spins = 0;;) {
                unsigned mx = 0;
#pragma unroll
                for (int R = 0; R < NR; ++R) {
#pragma unroll
                    for (int k = 0; k < 4; ++k) P[R][k] = LD16(rw, (int)O_PX + ((((SG0 + (R >> 1)) * 16 + 4 * w + k) * 2 + (R & 1)) * 64 + lane) * 16);
                    PMv[R] = LD16(rw, (int)O_PM + ((RB + R) * 16 + 4 * w + g) * 256 + n * 16);
                    Y[R >> 1][R & 1] = LD16(rxb, step_base(SG0 + (R >> 1), parmask) + yx_off(2 * j + (R & 1)) + ((4 * w + g) * 16 + n) * 16);
                }
                asm volatile("" ::: "memory");
                SB();
#pragma unroll
                for (int R = 0; R < NR; ++R) {
#pragma unroll
                    for (int k = 0; k < 4; ++k) mx = max(mx, mx4(P[R][k]));
                    mx = max(mx, max(mx4(PMv[R]), mx4(Y[R >> 1][R & 1])));
                }
                RETRY_TAIL(mx == SENT)
            }
            if (j == 0 && w == 0) TS(4096 + (RB >> 2) * 8 + 2);
#pragma unroll
            for (int R = 0; R < NR; ++R) {
                f32x4 a0 = {0, 0, 0, 0}, a1 = {0, 0, 0, 0};
#pragma unroll
                for (int k = 0; k < 4; ++k) { a0 = mf32(U[R >> 1][k][0], P[R][k], a0); a1 = mf32(U[R >> 1][k][1], P[R][k], a1); }
                a0 = mf32(Y[R >> 1][0], PMv[R], a0); a1 = mf32(Y[R >> 1][1], PMv[R], a1);
                *(f32x4*)(red + ((w * 8 + R * 2) * 64 + lane) * 4) = a0;
                *(f32x4*)(red + ((w * 8 + R * 2 + 1) * 64 + lane) * 4) = a1;
            }
            __syncthreads();
#pragma unroll
            for (int t = 0; t < 2; ++t) {
                f32x4 a = (*(const f32x4*)(red + ((0 * 8 + wr * 2 + t) * 64 + lane) * 4) + *(const f32x4*)(red + ((1 * 8 + wr * 2 + t) * 64 + lane) * 4))
                        + (*(const f32x4*)(red + ((2 * 8 + wr * 2 + t) * 64 + lane) * 4) + *(const f32x4*)(red + ((3 * 8 + wr * 2 + t) * 64 + lane) * 4));
                v4u gq = cst[t * 256];
                float r0 = bflo(xv[t].x) + rbf(__uint_as_float(gq.x) * rbf(a[0])), r1 = bfhi(xv[t].x) + rbf(__uint_as_float(gq.y) * rbf(a[1]));
                float r2 = bflo(xv[t].y) + rbf(__uint_as_float(gq.z) * rbf(a[2])), r3 = bfhi(xv[t].y) + rbf(__uint_as_float(gq.w) * rbf(a[3]));
                o1[t].x = pk2(r0, r1); o1[t].y = pk2(r2, r3);
                if (rowact) ST8(cn2(o1[t]), rw, (int)O_X1 + (((RB + wr) * 14 + j) * 64 + (2 * t + (g >> 1)) * 16 + n) * 16 + 8 * (g & 1));
            }
        }
        {
            v4u xr[14];
            const int xo = (int)O_X1 + ((RB + wr) * 14 * 64) * 16;
            { const int c = lane < 28 ? lane : 0; probe(flags, rw, xo + c * 512 + 508); }
            for (int spins = 0;;) {
                unsigned mx = 0;
#pragma unroll
                for (int c = 0; c < 14; ++c) xr[c] = LD16(rw, xo + (c * 64 + lane) * 16);
                asm volatile("" ::: "memory");
                SB();
#pragma unroll
                for (int c = 0; c < 14; ++c) mx = max(mx, mx4(xr[c]));
                RETRY_TAIL(mx == SENT)
            }
            if (j == 0 && w == 0) TS(4096 + (RB >> 2) * 8 + 3);
#pragma unroll
            for (int c = 0; c < 14; ++c) *(v4u*)(xl + ((w * 14 + c) * 64 + lane) * 16) = xr[c];
            float s0 = 0, s1 = 0, q0 = 0, q1 = 0;
#pragma unroll
            for (int c = 0; c < 14; ++c) {
                s0 = dot2bf(xr[c].x, 0x3f803f80u, s0); s1 = dot2bf(xr[c].y, 0x3f803f80u, s1); s0 = dot2bf(xr[c].z, 0x3f803f80u, s0); s1 = dot2bf(xr[c].w, 0x3f803f80u, s1);
                q0 = dot2bf(xr[c].x, xr[c].x, q0); q1 = dot2bf(xr[c].y, xr[c].y, q1); q0 = dot2bf(xr[c].z, xr[c].z, q0); q1 = dot2bf(xr[c].w, xr[c].w, q1);
            }
            float mean = add_x32(add_x16(s0 + s1)) * (1.f / D);
            float ex2 = add_x32(add_x16(q0 + q1)) * (1.f / D);
            float rstd = __builtin_amdgcn_rsqf(fmaxf(ex2 - mean * mean, 0.f) + 1e-5f);
            if (g == 0) { stl[(w * 16 + n) * 2] = mean; stl[(w * 16 + n) * 2 + 1] = rstd; }
        }
        __syncthreads();
#pragma unroll
        for (int Rp = 0; Rp < NR / 2; ++Rp) {
            f32x4 f[2][2] = {{{0, 0, 0, 0}, {0, 0, 0, 0}}, {{0, 0, 0, 0}, {0, 0, 0, 0}}};
            v4u hb[28];
#pragma unroll
            for (int c = 0; c < 14; ++c) { hb[2 * c] = *(const v4u*)(xl + (((2 * Rp) * 14 + c) * 64 + lane) * 16); hb[2 * c + 1] = *(const v4u*)(xl + (((2 * Rp + 1) * 14 + c) * 64 + lane) * 16); }
            float mu[2], rs[2];
            f32x4 cq[2] = {__builtin_bit_cast(f32x4, cst[5 * 256]), __builtin_bit_cast(f32x4, cst[6 * 256])}, bq[2] = {__builtin_bit_cast(f32x4, cst[7 * 256]), __builtin_bit_cast(f32x4, cst[8 * 256])};
#pragma unroll
            for (int rt = 0; rt < 2; ++rt) { mu[rt] = stl[((2 * Rp + rt) * 16 + n) * 2]; rs[rt] = stl[((2 * Rp + rt) * 16 + n) * 2 + 1]; }
            SB();
#pragma unroll
            for (int c = 0; c < 14; ++c)
#pragma unroll
                for (int t = 0; t < 2; ++t) { f[t][0] = mf32(W1[t][c], hb[2 * c], f[t][0]); f[t][1] = mf32(W1[t][c], hb[2 * c + 1], f[t][1]); }
#pragma unroll
            for (int rt = 0; rt < 2; ++rt) {
                float r[8];
#pragma unroll
                for (int t = 0; t < 2; ++t)
#pragma unroll
                    for (int i = 0; i < 4; ++i) r[4 * t + i] = gelu_tanh(rbf((f[t][rt][i] - mu[rt] * cq[t][i]) * rs[rt] + bq[t][i]));
                v4u hv = {pk2(r[0], r[1]), pk2(r[2], r[3]), pk2(r[4], r[5]), pk2(r[6], r[7])};
                ST16(cn4(hv), rw, (int)O_HID + (((RB + 2 * Rp + rt) * 56 + 4 * j + w) * 64 + lane) * 16);
            }
        }
        if (j == 0 && w == 0) TS(4096 + (RB >> 2) * 8 + 4);
        if constexpr (NR == 4) asm volatile("Lcode_mark_b:");
        {
            { const int c = lane < 14 * NR ? lane : 0; probe(flags, rw, (int)O_HID + (((RB + c / 14) * 56 + 14 * w + c % 14) * 64) * 16 + 1020); }
            if (j == 0 && w == 0) TS(4096 + (RB >> 2) * 8 + 5);
#pragma unroll
            for (int Rp = 0; Rp < NR / 2; ++Rp) {
                v4u hv[2][14];
                for (int spins = 0;;) {
                    unsigned mx = 0;
#pragma unroll
                    for (int rt = 0; rt < 2; ++rt)
#pragma unroll
                        for (int c = 0; c < 14; ++c) hv[rt][c] = LD16(rw, (int)O_HID + (((RB + 2 * Rp + rt) * 56 + 14 * w + c) * 64 + lane) * 16);
                    asm volatile("" ::: "memory");
                    SB();
#pragma unroll
                    for (int rt = 0; rt < 2; ++rt)
#pragma unroll
                        for (int c = 0; c < 14; ++c) mx = max(mx, mx4(hv[rt][c]));
                    RETRY_TAIL(mx == SENT)
                }
                f32x4 acc[2][2] = {{{0, 0, 0, 0}, {0, 0, 0, 0}}, {{0, 0, 0, 0}, {0, 0, 0, 0}}};
#pragma unroll
                for (int c = 0; c < 14; ++c)
#pragma unroll
                    for (int t = 0; t < 2; ++t) { acc[t][0] = mf32(W2[t][c], hv[0][c], acc[t][0]); acc[t][1] = mf32(W2[t][c], hv[1][c], acc[t][1]); }
#pragma unroll
                for (int rt = 0; rt < 2; ++rt)
#pragma unroll
                    for (int t = 0; t < 2; ++t) *(f32x4*)(red + ((w * 8 + (2 * Rp + rt) * 2 + t) * 64 + lane) * 4) = acc[t][rt];
            }
            __syncthreads();
#pragma unroll
            for (int t = 0; t < 2; ++t) {
                f32x4 a2 = (*(const f32x4*)(red + ((0 * 8 + wr * 2 + t) * 64 + lane) * 4) + *(const f32x4*)(red + ((1 * 8 + wr * 2 + t) * 64 + lane) * 4))
                         + (*(const f32x4*)(red + ((2 * 8 + wr * 2 + t) * 64 + lane) * 4) + *(const f32x4*)(red + ((3 * 8 + wr * 2 + t) * 64 + lane) * 4));
                v4u gq = cst[(2 + t) * 256], b4 = cst[4 * 256]; unsigned bx = t ? b4.z : b4.x, by = t ? b4.w : b4.y;
                float q0 = bflo(o1[t].x) + rbf(__uint_as_float(gq.x) * rbf(rbf(a2[0]) + bflo(bx))), q1 = bfhi(o1[t].x) + rbf(__uint_as_float(gq.y) * rbf(rbf(a2[1]) + bfhi(bx)));
                float q2 = bflo(o1[t].y) + rbf(__uint_as_float(gq.z) * rbf(rbf(a2[2]) + bflo(by))), q3 = bfhi(o1[t].y) + rbf(__uint_as_float(gq.w) * rbf(rbf(a2[3]) + bfhi(by)));
                v2u oo; oo.x = pk2(q0, q1); oo.y = pk2(q2, q3);
                if (rowact) *(v2u*)(p.out + ro + 16 * t) = oo;
            }
            if (j == 0 && w == 0) TS(4096 + (RB >> 2) * 8 + 6);
            if constexpr (NR == 2) asm volatile("Lcode_mark_a:");
            __syncthreads();
        }
}
__device__ __forceinline__ void stage_x(const Ptrs& p, char* xbuf, char* lds, int grp, int j, int w, int lane) {
    unsigned* flags = (unsigned*)(p.ws + O_FLAG);
    const rsrc_t rw = mkrs(p.ws);
    const rsrc_t rgx = mkrs(p.ws + O_GXF), rux = mkrs(p.ws + O_UXF);
    const int g = lane >> 4, n = lane & 15, tid = w * 64 + lane;
    const bool act = j < 14;
    if (tid == 192) arrive(flags, F_P3, 128, R_P3);
    const int jj = act ? j : 0;
    constexpr int NPAIR = 31; const int sgl = grp == 7 ? 63 : grp == 0 ? 62 : -1, nsgl = sgl >= 0 ? 1 : 0;
    wg_wait(flags, F_FFW, 14, w);
    v4u W1[2][14], W2[2][14], cq4[9];
    {
        const int fo = (int)O_FFW + jj * 65 * 4096 + tid * 16;
#pragma unroll
        for (int t = 0; t < 2; ++t)
#pragma unroll
            for (int c = 0; c < 14; ++c) { W1[t][c] = LD16(rw, fo + (t * 14 + c) * 4096); W2[t][c] = LD16(rw, fo + (28 + t * 14 + c) * 4096); }
#pragma unroll
        for (int q = 0; q < 9; ++q) cq4[q] = LD16(rw, fo + (56 + q) * 4096);
    }
    {
        v4u sv = {SENT, SENT, SENT, SENT};
        const int npr = 2 * ((NPAIR - grp + 7) / 8), nsg = npr + nsgl;
        for (int i = j * 256 + tid; i < nsg * 11520; i += 4096) {
            int q = i / 11520, k = i % 11520, off;
            int sg = q >= npr ? sgl + 2 * (q - npr) : 2 * (grp + 8 * (q >> 1)) + (q & 1);
            if (k < 2048) off = (int)O_PX + sg * 32768 + k * 16;
            else if (k < 2560) off = (int)O_PM + sg * 8192 + (k - 2048) * 16;
            else if (k < 4352) off = (int)O_X1 + sg * 28672 + (k - 2560) * 16;
            else off = (int)O_HID + sg * 114688 + (k - 4352) * 16;
            ST16(sv, rw, off);
        }
    }
    v4u* cst = (v4u*)(lds + 98304) + tid;
#pragma unroll
    for (int q = 0; q < 5; ++q) cst[q * 256] = cq4[4 + q];
#pragma unroll
    for (int q = 0; q < 4; ++q) cst[(5 + q) * 256] = cq4[q];
    float* red = (float*)lds;
    char* xl = lds + 32768;
    float* stl = (float*)(lds + 32768 + 57344);
    wg_signal(flags, F_XG + grp * 32);
    if (w == 0) poll_ge(flags, F_XG + grp * 32, 16, 0); else if (w == 1) poll_ge(flags, R_P3, 1, 0); else if (w == 2) poll_ge(flags, R_FILL, 1, 0);
    __syncthreads();

    const unsigned parmask = __builtin_amdgcn_readfirstlane(poll_ge(flags, R_PAR, 1, 0)) & 0xffffffu;
    const rsrc_t rxb = mkrs(xbuf);
    for (int d = 0; d < (SCHED == 2 ? 7 - grp : grp) * STAGGER; ++d) __builtin_amdgcn_s_sleep(32);
#pragma unroll 1
    for (int pr = grp; pr < NPAIR; pr += 8) x_pass<4>(p, flags, rw, rgx, rux, rxb, parmask, W1, W2, cst, red, xl, stl, pr * 4, j, jj, act, w, lane);
#pragma unroll 1
    for (int q = 0; q < nsgl; ++q) x_pass<2>(p, flags, rw, rgx, rux, rxb, parmask, W1, W2, cst, red, xl, stl, 2 * (sgl + 2 * q), j, jj, act, w, lane);
}
#undef DBGWS
#define DBGWS a.ws
constexpr int SPIN_CAP = 1 << 18;
__device__ __forceinline__ unsigned now32() { return (unsigned)__builtin_amdgcn_s_memtime(); }
#define GAP_M 300
#define TC_LEAD 1600
#define PER0 4100
#define PEN0 700
#define STRK 3
#define PFLOOR 4100
#define PEX_UP 150
#define PB_UP 60
#define PDN 2
__device__ __forceinline__ unsigned sync_clock() {
    unsigned best, first;
    asm volatile(
        "s_mov_b32 s93, 0\n\t"
        "s_mov_b32 s92, 200\n\t"
        "s_memrealtime s[84:85]\n\t"
        "s_waitcnt lgkmcnt(0)\n\t"
        "Ls%=:\n\t"
        "s_memrealtime s[86:87]\n\t"
        "s_memtime s[88:89]\n\t"
        "s_sub_u32 s92, s92, 1\n\t"
        "s_waitcnt lgkmcnt(0)\n\t"
        "s_cbranch_scc1 Ld%=\n\t"
        "s_cmp_eq_u32 s86, s84\n\t"
        "s_cbranch_scc1 Ls%=\n\t"
        "s_mov_b32 s84, s86\n\t"
        "s_mul_i32 s90, s86, 24\n\t"
        "s_sub_u32 s90, s90, s88\n\t"
        "s_cmp_eq_u32 s93, 0\n\t"
        "s_cselect_b32 %1, s90, %1\n\t"
        "s_cselect_b32 %0, 0, %0\n\t"
        "s_sub_u32 s90, s90, %1\n\t"
        "s_max_i32 %0, %0, s90\n\t"
        "s_add_u32 s93, s93, 1\n\t"
        "s_cmp_lt_u32 s93, 4\n\t"
        "s_cbranch_scc1 Ls%=\n\t"
        "Ld%=:\n\t"
        : "=&s"(best), "=&s"(first) :: "s84", "s85", "s86", "s87", "s88", "s89", "s90", "s92", "s93", "scc", "memory");
    return first + best;
}
__device__ __forceinline__ unsigned read_plan(__amdgpu_buffer_rsrc_t rs, int off, int slp) {
    unsigned x; int spins = SPIN_CAP * 4;
    for (;;) {
        x = __builtin_amdgcn_readfirstlane(LD4(rs, off));
        asm volatile("" ::: "memory");
        if (x != SENT || --spins <= 0) break;
        if (slp) __builtin_amdgcn_s_sleep(4);
    }
    return x;
}
__device__ __forceinline__ unsigned sane(unsigned t) { unsigned n = now32(); return (int)(t - n) > 12000 ? n : t; }
__device__ __forceinline__ int gap_ctl(int k, unsigned nret) { return nret == 1 ? min(k + 24, GAP_M + 400) : nret == 0 ? max(k - 2, GAP_M - 60) : k; }
__device__ __forceinline__ void tlb_touch(rsrc_t rs, int s, unsigned parmask, int lane) {
    if (s < NSEG) {
        unsigned q = __builtin_amdgcn_raw_buffer_load_b32(rs, (step_base(s, parmask) ^ 8192) + (lane < 36 ? lane : 0) * 16384, 0, 0);
        asm volatile("" :: "v"(q));
    }
}
__device__ __forceinline__ void load_wA(const Ptrs& a, int r, int w, int lane) {
    unsigned* flags = (unsigned*)(a.ws + O_FLAG);
    if (w == 0) TS(3072 + r * 4 + 0);
    wg_wait(flags, F_WA + r * 32, 4, w);
    if (w == 0) TS(3072 + r * 4 + 1);
    const rsrc_t rw = mkrs(a.ws + O_WAF);
    int soff = (r * 98 + w * 28) * 2048, voff = lane * 16;
    if (w < 3) asm volatile(ASM_LW28_TEXT :: [soff] "s"(soff), [voff] "v"(voff), [rs] "s"(rw) : ASM_LW28_CLOB);
    else asm volatile(ASM_LW14_TEXT :: [soff] "s"(soff), [voff] "v"(voff), [rs] "s"(rw) : ASM_LW14_CLOB);
    if (w == 0) TS(3072 + r * 4 + 2);
}
__device__ __forceinline__ void load_wY(const Ptrs& a, int ct, int w, int lane) {
    unsigned* flags = (unsigned*)(a.ws + O_FLAG);
    wg_wait(flags, F_WB + (ct / 7) * 32, 32, w);
    const rsrc_t rw = mkrs(a.ws + O_WBF);
    int soff = (ct * 112 + w * 28) * 2048, voff = lane * 16;
    asm volatile(ASM_LW28_TEXT :: [soff] "s"(soff), [voff] "v"(voff), [rs] "s"(rw) : ASM_LW28_CLOB);
    if (w == 0) TS(3328 + ct);
}
template<int NC>
__device__ __forceinline__ void roleA_wave(const Ptrs& a, char* xbuf, unsigned parmask, int r, int w, int lane, char* lds) {
    const int h = r >> 1, half = r & 1;
    const __amdgpu_buffer_rsrc_t rs = mkrs(xbuf);
    const __amdgpu_buffer_rsrc_t rg = mkrs(a.ws + O_GM);
    unsigned* flags = (unsigned*)(a.ws + O_FLAG);
    const int idx = (lane & 8) ? 0xEEEEEEEE : 0x44444444;
    const int g = lane >> 4, n = lane & 15;
    v4u J; {
        unsigned e[8];
#pragma unroll
        for (int j = 0; j < 8; ++j) e[j] = (n == 8 * (j >> 2) + ((4 * g + (j & 3)) & 7)) ? 0x3f80u : 0u;
        J.x = e[0] | (e[1] << 16); J.y = e[2] | (e[3] << 16); J.z = e[4] | (e[5] << 16); J.w = e[6] | (e[7] << 16);
    }
    const unsigned l0 = (unsigned)(size_t)(LAS char*)lds;
    float* gred = (float*)(lds + 20480);
    const int go = 2 * w * 1024 + lane * 16;
    const int amo = ((((lane >> 3) & 1) * 2 + (g >> 1)) * 16 + 8 * half + (lane & 7)) * 16 + 8 * (g & 1);
    const unsigned lred = l0 + 16384 + (w * 64 + lane) * 16;
    const unsigned lrd = l0 + 16384 + ((((lane >> 5)) * 16 + (lane & 7)) * 4 + ((lane >> 3) & 3)) * 4;
    const unsigned lrd2 = lrd + 640;
    const unsigned lg = l0 + 20480 + (w * 7 * 64 + lane) * 16;
    const int smo = SMM_OFF + ((((h >> 2) * 2 + ((lane >> 2) & 1)) * 64 + (h & 3) * 16 + half * 8 + (lane >> 3)) * 4 + (lane & 3)) * 4;
    const int gmo = h * 7168 + (((7 * half + 2 * w) * 4 + 2 * (n >> 3) + (g >> 1)) * 8 + (n & 7)) * 16 + 8 * (g & 1);
    wg_wait(flags, R_FILL, 1, w);
    if (w == 0) TS(3072 + r * 4 + 3);
    int k2 = GAP_M;
    unsigned tpub, tgt = now32();
    unsigned fails = 0;
    for (int s = 0; s < NSEG; ++s) {
        const int base = step_base(s, parmask);
        const unsigned mb = l0 + (s & 1) * 8192;
        unsigned nret, pl; const int spl = base + PLAN_OFF;
        tgt = __builtin_amdgcn_readfirstlane(tgt);
        int voff = base + go, so = base + smo; unsigned lw = mb + go, lam = mb + amo;
        if (NC == 2) asm volatile(ASM_A2_TEXT : [tpub] "=s"(tpub), [nret] "=s"(nret), [plan] "=&s"(pl)
            : [spl] "s"(spl), [rs] "s"(rs), [tgt] "s"(tgt), [voff] "v"(voff), [idx] "v"(idx), [J] "v"(J), [lw] "v"(lw), [lam] "v"(lam), [lred] "v"(lred), [lg] "v"(lg) : ASM_A2_CLOB);
        else asm volatile(ASM_A1_TEXT : [tpub] "=s"(tpub), [nret] "=s"(nret), [plan] "=&s"(pl)
            : [spl] "s"(spl), [rs] "s"(rs), [tgt] "s"(tgt), [voff] "v"(voff), [idx] "v"(idx), [J] "v"(J), [lw] "v"(lw), [lam] "v"(lam), [lred] "v"(lred), [lg] "v"(lg), [lrd] "v"(lrd), [lrd2] "v"(lrd2), [so] "v"(so) : ASM_A1_CLOB);
        fails |= nret >> 18;
        if (r == 0 && w == 1) TS(512 + s);
        __syncthreads();
#pragma unroll
        for (int p = 0; p < NC; ++p) {
            f32x4 t0 = *(const f32x4*)(gred + ((0 * 7 + 2 * w + p) * 64 + lane) * 4), t1 = *(const f32x4*)(gred + ((1 * 7 + 2 * w + p) * 64 + lane) * 4);
            f32x4 t2 = *(const f32x4*)(gred + ((2 * 7 + 2 * w + p) * 64 + lane) * 4), t3 = *(const f32x4*)(gred + ((3 * 7 + 2 * w + p) * 64 + lane) * 4);
            f32x4 t = (t0 + t1) + (t2 + t3);
            v2u o; o.x = min(pk2(t[0], t[1]), 0xfffffffeu); o.y = min(pk2(t[2], t[3]), 0xfffffffeu);
            ST8(o, rg, s * (16 * 8 * 448 * 2) + gmo + 512 * p);
            if (s + 4 < NSEG) ST8(((v2u){SENT, SENT}), rg, (s + 4) * (16 * 8 * 448 * 2) + gmo + 512 * p);
        }
        if (false) tlb_touch(rs, s + 2, parmask, lane);
        if (s > 2) k2 = gap_ctl(k2, nret);
        if (s > 0) { if (pl == SENT) pl = read_plan(rs, base + PLAN_OFF, 0); tgt = sane(pl + k2 - sync_clock()); } else tgt = tpub + 1500;
    }
    if (fails) __hip_atomic_store(flags + F_ABORT, 1u, __ATOMIC_RELAXED, __HIP_MEMORY_SCOPE_AGENT);
}

__device__ __forceinline__ void roleY(const Ptrs& a, char* xbuf, unsigned parmask, int ct, int w, int lane, char* lds) {
    const int tid = w * 64 + lane;
    const __amdgpu_buffer_rsrc_t rs = mkrs(xbuf);
    unsigned* flags = (unsigned*)(a.ws + O_FLAG);
    const int idx = (lane & 8) ? 0xEEEEEEEE : 0x44444444;
    const int g = lane >> 4, n = lane & 15, hh = 4 * w + g;
    const unsigned l0 = (unsigned)(size_t)(LAS char*)lds;
    const unsigned lyw = l0 + 20480 + w * 4096 + lane * 16;
    const unsigned lyr = l0 + 20480 + w * 4096 + g * 1024 + n * 16;
    const int go0 = tid * 16, go1 = tid < 192 ? 4096 + tid * 16 : tid * 16;
    const int yxo = yx_off(ct) + (hh * 16 + n) * 16;
    wg_wait(flags, R_FILL, 1, w);
    int d1 = GAP_M;
    unsigned tpub, tg = now32(), fails = 0;
    for (int s = 0; s < NSEG; ++s) {
        const int base = step_base(s, parmask);
        const unsigned mb = l0 + (s & 1) * 8192; unsigned lm = mb + lane * 16, lw0 = mb + go0, lw1 = mb + go1, tgt = __builtin_amdgcn_readfirstlane(tg), nr1; int vg0 = base + go0, vg1 = base + go1, vyx = base + yxo; unsigned pl; const int spl = base + PLAN_OFF;
        asm volatile(ASM_CY_TEXT : [tpub] "=s"(tpub), [nret] "=s"(nr1), [plan] "=&s"(pl)
            : [spl] "s"(spl), [rs] "s"(rs), [tgt] "s"(tgt), [vg0] "v"(vg0), [vg1] "v"(vg1), [lw0] "v"(lw0), [lw1] "v"(lw1), [lm] "v"(lm), [idx] "v"(idx), [lyw] "v"(lyw), [lyr] "v"(lyr), [vyx] "v"(vyx) : ASM_CY_CLOB);
        fails |= nr1 >> 18;
        if (s + 4 < NSEG) ST16(((v4u){SENT, SENT, SENT, SENT}), rs, step_base(s + 4, parmask) + yxo);
        if (false) tlb_touch(rs, s + 2, parmask, lane);
        if (s > 2) d1 = gap_ctl(d1, nr1);
        if (s > 0) { if (pl == SENT) pl = read_plan(rs, base + PLAN_OFF, 0); tg = sane(pl + d1 - sync_clock()); } else tg = tpub + 1500;
    }
    if (fails) __hip_atomic_store(flags + F_ABORT, 1u, __ATOMIC_RELAXED, __HIP_MEMORY_SCOPE_AGENT);
}
template<bool W0>
__device__ __forceinline__ void roleC(const Ptrs& a, char* xbuf, unsigned parmask, int ct, int w, int lane, char* lds) {
    const int tid = w * 64 + lane;
    const __amdgpu_buffer_rsrc_t rs = mkrs(xbuf);
    const int g = lane >> 4, n = lane & 15;
    const int aa = lane & 7, pp = (lane >> 3) & 1, hh = 4 * w + g;
    const unsigned l0 = (unsigned)(size_t)(LAS char*)lds;
    const int so = SMM_OFF + (w * 128 + lane) * 16;
    const int mlo = ML_OFF + hh * 128 + aa * 8;
    const int oxo = ox_off(ct) + (hh * 16 + n) * 16;
    const unsigned dm0 = pp ? 0u : (aa == 0 ? 0xffffu : aa == 1 ? 0xffff0000u : 0u), dm1 = pp ? 0u : (aa == 2 ? 0xffffu : aa == 3 ? 0xffff0000u : 0u);
    const unsigned dm2 = pp ? 0u : (aa == 4 ? 0xffffu : aa == 5 ? 0xffff0000u : 0u), dm3 = pp ? 0u : (aa == 6 ? 0xffffu : aa == 7 ? 0xffff0000u : 0u);
    int pubo; { int col = 16 * ct + 4 * g; int rho = col >= 224; int lc = col - 224 * rho; int c = lc >> 5, ga = (lc >> 3) & 3, ja = lc & 7; pubo = ((c * 64 + ga * 16 + rho * 8 + aa) * 8 + ja) * 2; }
    if (lane == 8) pubo = PLAN_OFF;
    const unsigned emask = ct == 0 ? 0x00ff01ffu : 0x00ff00ffu;
    unsigned* flags = (unsigned*)(a.ws + O_FLAG);
    wg_wait(flags, R_FILL, 1, w);
    if (w == 0) read_plan(rs, step_base(0, parmask) + SMM_OFF + 8188, 1);
    __syncthreads();
    int k1 = TC_LEAD, per = PER0, pb = PER0, pex = 0, streak = 0;
    unsigned tpub, tg = now32(), coff = sync_clock(), plan = tg + coff; int late = 0;
    unsigned fails = 0, nret = 0;
    for (int s = 0; s < NSEG - 1; ++s) {
        const int base = step_base(s, parmask);
        const int base1 = step_base(s + 1, parmask);
        const unsigned lred = l0 + (s & 1) * 4096 + tid * 16;
        unsigned tgt = __builtin_amdgcn_readfirstlane(tg), cf = __builtin_amdgcn_readfirstlane(coff + 150), prev = __builtin_amdgcn_readfirstlane(plan); int pen = PEN0, eps = 60, pr = __builtin_amdgcn_readfirstlane(per);
        int vso = base + so, vml = base + mlo, vox = base + oxo, vyx = base + oxo + 4096, vpub = base1 + pubo;
        if (W0) asm volatile(ASM_C0_TEXT : [tpub] "=s"(tpub), [nret] "=s"(nret), [plan] "=s"(plan), [late] "=&s"(late)
            : [rs] "s"(rs), [tgt] "s"(tgt), [vso] "v"(vso), [vml] "v"(vml), [vox] "v"(vox), [vyx] "v"(vyx), [dm0] "v"(dm0), [dm1] "v"(dm1), [dm2] "v"(dm2), [dm3] "v"(dm3), [lred] "v"(lred), [vpub] "v"(vpub), [emask] "s"(emask), [coff] "s"(cf), [prev] "s"(prev), [pen] "s"(pen), [eps] "s"(eps), [per] "s"(pr) : ASM_C0_CLOB);
        else asm volatile(ASM_C1_TEXT : [tpub] "=s"(tpub), [nret] "=s"(nret)
            : [rs] "s"(rs), [tgt] "s"(tgt), [vso] "v"(vso), [vml] "v"(vml), [vox] "v"(vox), [vyx] "v"(vyx), [dm0] "v"(dm0), [dm1] "v"(dm1), [dm2] "v"(dm2), [dm3] "v"(dm3), [lred] "v"(lred) : ASM_C1_CLOB);
        fails |= nret >> 18;
        {
            const int e = (int)(tpub + coff - prev) + (W0 ? 0 : 190);
            int dk = 0;
            if (s > 1 && e < 250 && !nret) dk = e > -60 ? min(e + 60, 48) : max((e + 60) >> 2, -12);
            k1 += dk;
            if (W0 && ct == 0) {
                if (lane == 0) __hip_atomic_store(flags + R_FS, (unsigned)s + 1u, __ATOMIC_RELAXED, __HIP_MEMORY_SCOPE_AGENT);
                if (s > 2) {
                    if (late > 250) { pex += PEX_UP; pb += PB_UP; streak = 0; } else { pex = (pex * 3) >> 2; ++streak; pb += dk - (pb > PFLOOR ? min(PDN + streak * STRK, 40) : PDN); }
                    per = min(pb + pex, 9000);
                }
            }
            else { __builtin_amdgcn_s_sleep(10); plan = read_plan(rs, base1 + PLAN_OFF, 0); }
            if (false) tlb_touch(rs, s + 3, parmask, lane);
            coff = sync_clock();
            tg = sane(plan - k1 - coff);
        }
    }
    if (fails) __hip_atomic_store(flags + F_ABORT, 1u, __ATOMIC_RELAXED, __HIP_MEMORY_SCOPE_AGENT);
}

template<int WV>
__device__ __forceinline__ void roleH(const Ptrs& a, char* xbuf, unsigned parmask, int set, int h, int w, int lane, char* lds) {
    const __amdgpu_buffer_rsrc_t rs = mkrs(xbuf);
    const int g = lane >> 4, n = lane & 15;
    const unsigned l0 = (unsigned)(size_t)(LAS char*)lds;
    const int c0 = 2 * w, c1 = (w < 3) ? 2 * w + 1 : 6;
    const int oxo = ox_off(7 * w + g) + (h * 16 + n) * 16, oxo1 = ox_off(min(7 * w + 4 + g, 27)) + (h * 16 + n) * 16;
    unsigned* flags = (unsigned*)(a.ws + O_FLAG);
    const rsrc_t rgx = mkrs(a.ws + O_GXF), rux = mkrs(a.ws + O_UXF);
    wg_wait(flags, R_FILL, 1, w);
    int it = 0;
    unsigned fails = 0;
    int k2 = GAP_M;
    for (int s = set; s < NSEG - 1; s += NHSETS, ++it) {
        const int base = step_base(s, parmask);
        v4u Gf[2][2][2], Uf[7];
        const int fo = ((s * 16 + h) * 28 * 64 + lane) * 16;
        wg_wait(flags, F_GU + s * 16 + h, 4, w);
#pragma unroll
        for (int kap = 0; kap < 2; ++kap)
#pragma unroll
            for (int rho = 0; rho < 2; ++rho) {
                Gf[kap][rho][0] = LD16(rgx, fo + ((kap * 2 + rho) * 7 + c0) * 1024);
                Gf[kap][rho][1] = LD16(rgx, fo + ((kap * 2 + rho) * 7 + c1) * 1024);
            }
#pragma unroll
        for (int i = 0; i < 7; ++i) Uf[i] = LD16(rux, fo + (7 * w + i) * 1024);
        if (w == 3) {
#pragma unroll
            for (int kap = 0; kap < 2; ++kap)
#pragma unroll
                for (int rho = 0; rho < 2; ++rho) Gf[kap][rho][1] = (v4u){0, 0, 0, 0};
        }
        if (false) tlb_touch(rs, s, parmask, lane);
        unsigned tgt;
        if (s > 1) { unsigned pl = read_plan(rs, step_base(s - 1, parmask) + PLAN_OFF, 1); tgt = sane(pl + k2 - sync_clock()); }
        else tgt = now32() + 0;
        tgt = __builtin_amdgcn_readfirstlane(tgt);
        unsigned nret;
        int vm0 = base + c0 * 1024 + lane * 16, vm1 = base + c1 * 1024 + lane * 16, vo0 = base + oxo, vo1 = base + oxo1, vml = base + ML_OFF + h * 128 + lane * 8;
        unsigned lw = l0 + (it & 1) * 4096 + (w * 64 + lane) * 16, lr = l0 + (it & 1) * 4096 + lane * 16;
#define H_INS [rs] "s"(rs), [tgt] "s"(tgt), [vm0] "v"(vm0), [vm1] "v"(vm1), [vo0] "v"(vo0), [vo1] "v"(vo1), [vml] "v"(vml), [lw] "v"(lw), [lr] "v"(lr), \
            [g000] "v"(Gf[0][0][0]), [g001] "v"(Gf[0][0][1]), [g010] "v"(Gf[0][1][0]), [g011] "v"(Gf[0][1][1]), [g100] "v"(Gf[1][0][0]), [g101] "v"(Gf[1][0][1]), [g110] "v"(Gf[1][1][0]), [g111] "v"(Gf[1][1][1]), \
            [u0] "v"(Uf[0]), [u1] "v"(Uf[1]), [u2] "v"(Uf[2]), [u3] "v"(Uf[3]), [u4] "v"(Uf[4]), [u5] "v"(Uf[5]), [u6] "v"(Uf[6])
        if (WV == 0) asm volatile(ASM_H0_TEXT : [nret] "=s"(nret) : H_INS : ASM_H0_CLOB);
        else asm volatile(ASM_H1_TEXT : [nret] "=s"(nret) : H_INS : ASM_H1_CLOB);
        fails |= nret >> 18;
        if (s > 2) k2 = gap_ctl(k2, nret);
        if (s + 4 < NSEG) {
            const int b4 = step_base(s + 4, parmask);
            ST16(((v4u){SENT, SENT, SENT, SENT}), rs, b4 + oxo);
            if (g < 3) ST16(((v4u){SENT, SENT, SENT, SENT}), rs, b4 + oxo1);
        }
    }
    if (fails) __hip_atomic_store(flags + F_ABORT, 1u, __ATOMIC_RELAXED, __HIP_MEMORY_SCOPE_AGENT);
}

#undef DBGWS
#define DBGWS p.ws
__global__ void __launch_bounds__(256, 1) k_mega(Ptrs p) {
    extern __shared__ __attribute__((aligned(16))) char lds[];
    const int b = blockIdx.x;
    const int w = __builtin_amdgcn_readfirstlane(threadIdx.x >> 6), lane = threadIdx.x & 63;
    unsigned* flags = (unsigned*)(p.ws + O_FLAG);
    char* xbuf = (char*)(((size_t)(p.ws + O_XB) + (2u << 20) - 1) & ~(size_t)((2u << 20) - 1));
    if (w == 0) TS(b);
    {
        unsigned long long pc; unsigned len;
        int l1, l2, l3;
        asm volatile("s_getpc_b64 %0\nLpc%=:\n\ts_mov_b32 %1, Lcode_mark_a-Lpc%=\n\ts_mov_b32 %2, Lcode_mark_b-Lpc%=\n\ts_mov_b32 %3, Lcode_mark_c-Lpc%=" : "=s"(pc), "=s"(l1), "=s"(l2), "=s"(l3));
        len = (unsigned)max(max(l1, l2), max(l3, 0));
        const unsigned off = ((unsigned)(b >> 3) * 256u + threadIdx.x) * 64u;
        if (__builtin_amdgcn_readfirstlane(off) + 4096u <= len) {
            unsigned keep; const unsigned dst = (unsigned)(size_t)(LAS char*)lds + 156672 + w * 256;
            asm volatile("s_mov_b32 %0, m0\n\ts_mov_b32 m0, %2\n\ts_nop 0\n\tglobal_load_lds_dword %1, off\n\ts_mov_b32 m0, %0" : "=&s"(keep) : "v"(pc + off), "s"(dst) : "memory");
        }
    }
    unsigned tk = 0;
    const unsigned xcc = (unsigned)__builtin_amdgcn_s_getreg(20 | (3 << 11)) & 7u;
    {
        const int task = (b & 7) * 32 + (b >> 3), mh = task >> 3;
        if (w == 3) {
            if (lane == 0) tk = __hip_atomic_fetch_add(flags + F_TK + xcc * 32, 1u, __ATOMIC_RELAXED, __HIP_MEMORY_SCOPE_AGENT);
            if (task < 168) { mod_wave(p, task, lane); if (task < 42) TS(5120 + task * 4 + 0); }
            else __builtin_amdgcn_s_barrier();
        } else __builtin_amdgcn_s_barrier();
        if (mh < 16) p1_task<0>(p, lds, mh, (task >> 1) & 3, task & 1, w, lane, task, task);
        else p1_task<1>(p, lds, mh - 16, (task >> 1) & 3, task & 1, w, lane, task, task);
        if (w == 0) TS(6144 + task);
    }
    if (threadIdx.x == 192) {
        unsigned slot = xcc * 32 + tk;
        if (tk >= 32) {
            unsigned c[8], tot = 0;
            for (int spins = 0; spins < (1 << 22); ++spins) {
                tot = 0;
#pragma unroll
                for (int q = 0; q < 8; ++q) { c[q] = __hip_atomic_load(flags + F_TK + q * 32, __ATOMIC_RELAXED, __HIP_MEMORY_SCOPE_AGENT); tot += c[q]; }
                if (tot == 256) break;
                __builtin_amdgcn_s_sleep(8);
            }
            unsigned k = __hip_atomic_fetch_add(flags + F_OV, 1u, __ATOMIC_RELAXED, __HIP_MEMORY_SCOPE_AGENT);
            slot = 0;
#pragma unroll
            for (int q = 0; q < 8; ++q) {
                const unsigned fr = c[q] < 32 ? 32 - c[q] : 0;
                if (k < fr) { slot = q * 32 + c[q] + k; k = 0xffffu; } else if (k != 0xffffu) k -= fr;
            }
        }
        *(unsigned*)lds = slot;
    }
    __syncthreads();
    const int slot = __builtin_amdgcn_readfirstlane(*(const unsigned*)lds);
    __syncthreads();
    const int x = slot >> 5, idx = slot & 31;
    if (w == 0) TS(256 + b);
    const int v = (x >> 1) * 28 + idx;
    if (x >= 4) {
        const int id = (x - 4) * 32 + idx, par = id & 1, sl = id >> 1;
        if (sl < 32) p3_role<0>(p, lds, sl >> 1, sl & 1, par, w, lane);
        else p3_role<1>(p, lds, (sl >> 1) & 15, sl & 1, par, w, lane);
        stage_x(p, xbuf, lds, id >> 4, id & 15, w, lane);
        return;
    }
    if ((x == 1 || x == 2) && idx >= 28) {
        const int q = (x - 1) * 4 + idx - 28;
#pragma unroll 1
        for (int j = q; j < 14; j += 8) build_ffn(p, lds, j, w, lane);
        return;
    }
    if (x & 1) {
        if (v < 44) calib_wave(p, xbuf, v >> 1, (v & 1) * 4 + w, lane, 0);
        startup_fill(p, xbuf, v, w, lane);
    }
    if (x == 0) load_wA(p, idx, w, lane); else if (x == 2 && idx < 28) load_wY(p, idx, w, lane);
    wg_wait(flags, R_PAR, 1, w);
    const unsigned parmask = __builtin_amdgcn_readfirstlane(__hip_atomic_load(flags + R_PAR, __ATOMIC_RELAXED, __HIP_MEMORY_SCOPE_AGENT)) & 0xffffffu;
    if (x == 0) { if (w < 3) roleA_wave<2>(p, xbuf, parmask, idx, w, lane, lds); else roleA_wave<1>(p, xbuf, parmask, idx, w, lane, lds); }
    else if (x == 1) {
        if (idx < 28) { if (w == 0) roleC<true>(p, xbuf, parmask, idx, w, lane, lds); else roleC<false>(p, xbuf, parmask, idx, w, lane, lds); }
    } else if (x == 2) {
        if (idx < 28) roleY(p, xbuf, parmask, idx, w, lane, lds);
    } else {
        if (w == 0) roleH<0>(p, xbuf, parmask, idx >> 4, idx & 15, w, lane, lds); else roleH<1>(p, xbuf, parmask, idx >> 4, idx & 15, w, lane, lds);
    }
}

extern "C" void kernel_launch(void* const* d_in, const int* in_sizes, int n_in,
                              void* d_out, int out_size, void* d_ws, size_t ws_size,
                              hipStream_t stream) {
    Ptrs p;
    p.x = (const u16*)d_in[0]; p.mem0 = (const u16*)d_in[1];
    p.Wq = (const u16*)d_in[2]; p.Wk = (const u16*)d_in[3]; p.Wv = (const u16*)d_in[4]; p.Wo = (const u16*)d_in[5];
    p.w1 = (const u16*)d_in[8]; p.b1 = (const u16*)d_in[9]; p.w2 = (const u16*)d_in[10]; p.b2 = (const u16*)d_in[11];
    p.c = (const u16*)d_in[12]; p.wmod = (const u16*)d_in[13];
    p.out = (u16*)d_out;
    p.ws = (char*)d_ws;
    (void)hipFuncSetAttribute((const void*)k_mega, hipFuncAttributeMaxDynamicSharedMemorySize, 157696);
    (void)hipMemsetAsync(d_ws, 0, FLAG_BYTES, stream);
    k_mega<<<256, 256, 157696, stream>>>(p);
}
```
